```python
import torch

def _softmax(x, axis=-1):
    return torch.softmax(x, dim=axis)
def _gelu(x):
    return torch.nn.functional.gelu(x, approximate="tanh")
def _silu(x):
    return torch.nn.functional.silu(x)
def _sigmoid(x):
    return torch.sigmoid(x)
def _layernorm(x, axis=-1, eps=1e-5):
    m = x.mean(dim=axis, keepdim=True)
    v = x.var(dim=axis, keepdim=True, unbiased=False)
    return (x - m) / torch.sqrt(v + eps)
def _scatter_add(tgt, idx, src, axis=0):
    return tgt.clone().index_add_(axis, idx, src)
def _segment_sum(src, seg, n):
    out = torch.zeros((n,) + src.shape[1:], dtype=src.dtype)
    return out.index_add_(0, seg, src)
def _pad(x, pw):
    flat = tuple(v for pair in reversed(pw) for v in pair)
    return torch.nn.functional.pad(x, flat)

kernel_name = 'recurrent_scan__sliding_block_cached__glu_gated_bilinear__prompt'

def setup_inputs():

    _w = (lambda a: (lambda t: t.to(torch.bfloat16) if t.is_floating_point() else t)(torch.as_tensor(a)))
    def _A_setup():
        B, Sq, D = 1, 2048, 640
        Hq, Hkv, Sk, Dh = 32, 32, 2048, 192
        assert Hq % Hkv == 0, (Hq, Hkv)
        return {
            "x": _w((lambda *s: torch.randn(*s, dtype=torch.bfloat16) * 0.05)(B, Sq, D)),
            "k_cache": _w((lambda *s: torch.randn(*s, dtype=torch.bfloat16) * 1.6)(B, Hkv, Sk, Dh)),
            "v_cache": _w((lambda *s: torch.randn(*s, dtype=torch.bfloat16) * 1.6)(B, Hkv, Sk, Dh)),
            "wq": _w((lambda *s: torch.randn(*s, dtype=torch.bfloat16) * 1.6)(D, Hq * Dh) * (D ** -0.5)),
            "wo": _w((lambda *s: torch.randn(*s, dtype=torch.bfloat16) * 1.6)(Hq * Dh, D) * ((Hq * Dh) ** -0.5)),
        }
    _A_in = _A_setup()

    def _B_setup():
        B, S, D, F = 1, 2048, 640, 1536
        return {
            "x": _w((lambda *s: torch.randn(*s, dtype=torch.bfloat16) * 0.05)(B, S, D)),
            "w_gate": _w((lambda *s: torch.randn(*s, dtype=torch.bfloat16) * 0.2)(D, F) * (D ** -0.5)),
            "w_up": _w((lambda *s: torch.randn(*s, dtype=torch.bfloat16) * 0.2)(D, F) * (D ** -0.5)),
            "w_down": _w((lambda *s: torch.randn(*s, dtype=torch.bfloat16) * 0.2)(F, D) * (F ** -0.5)),
        }
    _B_in = _B_setup()

    return {
        "A_x": _A_in["x"],
        "A_k_cache": _A_in["k_cache"],
        "A_v_cache": _A_in["v_cache"],
        "A_wq": _A_in["wq"],
        "A_wo": _A_in["wo"],
        "B_x": _B_in["x"],
        "B_w_gate": _B_in["w_gate"],
        "B_w_up": _B_in["w_up"],
        "B_w_down": _B_in["w_down"],
    }

def reference(A_x, A_k_cache, A_v_cache, A_wq, A_wo, B_x, B_w_gate, B_w_up, B_w_down):
    def _A_ref(x, k_cache, v_cache, wq, wo):
        B, Sq, D = x.shape
        Hq, Dh = 32, 192
        Hkv, Sk = k_cache.shape[1], k_cache.shape[2]
        W = min(4096, max(256, Sk // 4))
        rep = Hq // Hkv
        q = (lambda x, *d: x.permute(*d))((x @ wq).reshape(B, Sq, Hq, Dh), 0, 2, 1, 3)
        k = (lambda x, n, axis: torch.repeat_interleave(x, n, dim=axis))(k_cache[:, :, Sk - W:, :], rep, axis=1)
        v = (lambda x, n, axis: torch.repeat_interleave(x, n, dim=axis))(v_cache[:, :, Sk - W:, :], rep, axis=1)
        scores = torch.einsum("bhqd,bhkd->bhqk", q, k) * (Dh ** -0.5)
        p = _softmax(scores, axis=-1)
        o = torch.einsum("bhqk,bhkd->bhqd", p, v)
        return (lambda x, *d: x.permute(*d))(o, 0, 2, 1, 3).reshape(B, Sq, Hq * Dh) @ wo

    def _B_ref(x, w_gate, w_up, w_down):
        g = torch.einsum("bsd,df->bsf", x, w_gate)
        u = torch.einsum("bsd,df->bsf", x, w_up)
        h = g * u
        return torch.einsum("bsf,fd->bsd", h, w_down)

    x = A_x
    xn = _layernorm(x, axis=-1)
    s = _A_ref(xn, A_k_cache, A_v_cache, A_wq, A_wo)
    g = _silu(_B_ref(xn, B_w_gate, B_w_up, B_w_down))
    return x + s * g
```

```cpp
#include <hip/hip_runtime.h>
#include <hip/hip_bf16.h>
#include <stdint.h>
typedef unsigned short u16;
typedef float f32x4 __attribute__((ext_vector_type(4)));
typedef __bf16 bf16x8 __attribute__((ext_vector_type(8)));
static __device__ __forceinline__ float bf2f(u16 v){ return __uint_as_float(((unsigned)v)<<16); }
static __device__ __forceinline__ u16 f2bf(float f){ __bf16 b=(__bf16)f; return __builtin_bit_cast(u16,b); }

__global__ void k_ln(const u16* x, u16* xn){
  int row=blockIdx.x; int t=threadIdx.x;
  float v[10]; float s=0;
  for(int i=0;i<10;i++){ v[i]=bf2f(x[row*640+t+64*i]); s+=v[i]; }
  for(int o=32;o;o>>=1) s+=__shfl_xor(s,o);
  float m=s/640.f; float q=0;
  for(int i=0;i<10;i++){ float d=v[i]-m; q+=d*d; }
  for(int o=32;o;o>>=1) q+=__shfl_xor(q,o);
  float r=rsqrtf(q/640.f+1e-5f);
  for(int i=0;i<10;i++) xn[row*640+t+64*i]=f2bf((v[i]-m)*r);
}
template<bool BT>
__global__ void k_gemm(const u16* A,int lda,long sa,const u16* B,int ldb,long sb,u16* C,float* Cf,int ldc,long sc,int K,float alpha){
  __shared__ u16 As[64][40]; __shared__ u16 Bs[64][40];
  A+=sa*blockIdx.z; B+=sb*blockIdx.z;
  int m0=blockIdx.y*64,n0=blockIdx.x*64; int t=threadIdx.x,w=t>>6,l=t&63;
  f32x4 acc[4]; for(int i=0;i<4;i++) acc[i]=(f32x4){0,0,0,0};
  for(int k0=0;k0<K;k0+=32){
    { int r=t>>2,c=(t&3)*8; *(uint4*)&As[r][c]=*(const uint4*)&A[(long)(m0+r)*lda+k0+c]; }
    if(BT){ int r=t>>2,c=(t&3)*8; *(uint4*)&Bs[r][c]=*(const uint4*)&B[(long)(n0+r)*ldb+k0+c]; }
    else { int kk=t>>3,c=(t&7)*8; uint4 v=*(const uint4*)&B[(long)(k0+kk)*ldb+n0+c]; u16* p=(u16*)&v; for(int j=0;j<8;j++) Bs[c+j][kk]=p[j]; }
    __syncthreads();
    bf16x8 a=*(bf16x8*)&As[w*16+(l&15)][(l>>4)*8];
    for(int i=0;i<4;i++){ bf16x8 b=*(bf16x8*)&Bs[i*16+(l&15)][(l>>4)*8]; acc[i]=__builtin_amdgcn_mfma_f32_16x16x32_bf16(a,b,acc[i],0,0,0);}
    __syncthreads();
  }
  for(int i=0;i<4;i++) for(int j=0;j<4;j++){ long o=sc*blockIdx.z+(long)(m0+w*16+(l>>4)*4+j)*ldc+n0+i*16+(l&15); if(C) C[o]=f2bf(acc[i][j]*alpha); else Cf[o]=acc[i][j]*alpha; }
}
__global__ void k_softmax(const float* S,u16* P){
  long row=blockIdx.x; int t=threadIdx.x; float v[8]; float m=-1e30f;
  for(int i=0;i<8;i++){ v[i]=S[row*512+t+64*i]; m=fmaxf(m,v[i]); }
  for(int o=32;o;o>>=1) m=fmaxf(m,__shfl_xor(m,o));
  float s=0; for(int i=0;i<8;i++){ v[i]=__expf(v[i]-m); s+=v[i]; }
  for(int o=32;o;o>>=1) s+=__shfl_xor(s,o);
  for(int i=0;i<8;i++) P[row*512+t+64*i]=f2bf(v[i]/s);
}
__global__ void k_mul(const u16* g,const u16* u,u16* h,int n){ int i=blockIdx.x*256+threadIdx.x; if(i<n) h[i]=f2bf(bf2f(g[i])*bf2f(u[i])); }
__global__ void k_fin(const u16* x,const u16* s,const u16* d,u16* o,int n){ int i=blockIdx.x*256+threadIdx.x; if(i<n){ float dd=bf2f(d[i]); float g=dd/(1.f+__expf(-dd)); o[i]=f2bf(bf2f(x[i])+bf2f(s[i])*g);} }

extern "C" void kernel_launch(void* const* d_in, const int* in_sizes, int n_in, void* d_out, int out_size, void* d_ws, size_t ws_size, hipStream_t stream){
  const u16* x=(const u16*)d_in[0]; const u16* kc=(const u16*)d_in[1]; const u16* vc=(const u16*)d_in[2];
  const u16* wq=(const u16*)d_in[3]; const u16* wo=(const u16*)d_in[4];
  const u16* wg=(const u16*)d_in[6]; const u16* wu=(const u16*)d_in[7]; const u16* wd=(const u16*)d_in[8];
  char* ws=(char*)d_ws; size_t off=0; auto alloc=[&](size_t b){ char* p=ws+off; off+=(b+255)&~(size_t)255; return p; };
  u16* xn=(u16*)alloc(2048*640*2); u16* q=(u16*)alloc(2048*6144*2);
  u16* P=(u16*)alloc((size_t)32*2048*512*2);
  float* S=(float*)alloc((size_t)32*2048*512*4);
  off=(char*)S-ws;
  u16* o=(u16*)alloc(2048*6144*2);
  u16* g=(u16*)alloc(2048*1536*2); u16* u=(u16*)alloc(2048*1536*2); u16* h=(u16*)alloc(2048*1536*2);
  u16* s=(u16*)alloc(2048*640*2); u16* d=(u16*)alloc(2048*640*2);
  k_ln<<<2048,64,0,stream>>>(x,xn);
  k_gemm<false><<<dim3(96,32,1),256,0,stream>>>(xn,640,0,wq,6144,0,q,nullptr,6144,0,640,1.f);
  k_gemm<true><<<dim3(8,32,32),256,0,stream>>>(q,6144,192,kc+1536*192,192,2048*192,nullptr,S,512,2048*512,192,0.07216878364870322f);
  k_softmax<<<32*2048,64,0,stream>>>(S,P);
  k_gemm<false><<<dim3(3,32,32),256,0,stream>>>(P,512,2048*512,vc+1536*192,192,2048*192,o,nullptr,6144,192,512,1.f);
  k_gemm<false><<<dim3(10,32,1),256,0,stream>>>(o,6144,0,wo,640,0,s,nullptr,640,0,6144,1.f);
  k_gemm<false><<<dim3(24,32,1),256,0,stream>>>(xn,640,0,wg,1536,0,g,nullptr,1536,0,640,1.f);
  k_gemm<false><<<dim3(24,32,1),256,0,stream>>>(xn,640,0,wu,1536,0,u,nullptr,1536,0,640,1.f);
  k_mul<<<2048*1536/256,256,0,stream>>>(g,u,h,2048*1536);
  k_gemm<false><<<dim3(10,32,1),256,0,stream>>>(h,1536,0,wd,640,0,d,nullptr,640,0,1536,1.f);
  k_fin<<<2048*640/256,256,0,stream>>>(x,s,d,(u16*)d_out,2048*640);
}
```

```cpp
#include <hip/hip_runtime.h>
#include <hip/hip_bf16.h>
#include <stdint.h>
typedef unsigned short u16;
typedef float f32x4 __attribute__((ext_vector_type(4)));
typedef __bf16 bf16x8 __attribute__((ext_vector_type(8)));
static __device__ __forceinline__ float bf2f(u16 v){ return __uint_as_float(((unsigned)v)<<16); }
static __device__ __forceinline__ u16 f2bf(float f){ __bf16 b=(__bf16)f; return __builtin_bit_cast(u16,b); }

__global__ void k_ln(const u16* x, u16* xn){
  int row=blockIdx.x; int t=threadIdx.x;
  float v[10]; float s=0;
  for(int i=0;i<10;i++){ v[i]=bf2f(x[row*640+t+64*i]); s+=v[i]; }
  for(int o=32;o;o>>=1) s+=__shfl_xor(s,o);
  float m=s/640.f; float q=0;
  for(int i=0;i<10;i++){ float d=v[i]-m; q+=d*d; }
  for(int o=32;o;o>>=1) q+=__shfl_xor(q,o);
  float r=rsqrtf(q/640.f+1e-5f);
  for(int i=0;i<10;i++) xn[row*640+t+64*i]=f2bf((v[i]-m)*r);
}
template<bool BT>
__global__ void k_gemm(const u16* A,int lda,long sa,const u16* B,int ldb,long sb,u16* C,float* Cf,int ldc,long sc,int K,float alpha){
  __shared__ u16 As[64][40]; __shared__ u16 Bs[64][40];
  A+=sa*blockIdx.z; B+=sb*blockIdx.z;
  int m0=blockIdx.y*64,n0=blockIdx.x*64; int t=threadIdx.x,w=t>>6,l=t&63;
  f32x4 acc[4]; for(int i=0;i<4;i++) acc[i]=(f32x4){0,0,0,0};
  for(int k0=0;k0<K;k0+=32){
    { int r=t>>2,c=(t&3)*8; *(uint4*)&As[r][c]=*(const uint4*)&A[(long)(m0+r)*lda+k0+c]; }
    if(BT){ int r=t>>2,c=(t&3)*8; *(uint4*)&Bs[r][c]=*(const uint4*)&B[(long)(n0+r)*ldb+k0+c]; }
    else { int kk=t>>3,c=(t&7)*8; uint4 v=*(const uint4*)&B[(long)(k0+kk)*ldb+n0+c]; u16* p=(u16*)&v; for(int j=0;j<8;j++) Bs[c+j][kk]=p[j]; }
    __syncthreads();
    bf16x8 a=*(bf16x8*)&As[w*16+(l&15)][(l>>4)*8];
    for(int i=0;i<4;i++){ bf16x8 b=*(bf16x8*)&Bs[i*16+(l&15)][(l>>4)*8]; acc[i]=__builtin_amdgcn_mfma_f32_16x16x32_bf16(a,b,acc[i],0,0,0);}
    __syncthreads();
  }
  for(int i=0;i<4;i++) for(int j=0;j<4;j++){ long o=sc*blockIdx.z+(long)(m0+w*16+(l>>4)*4+j)*ldc+n0+i*16+(l&15); if(C) C[o]=f2bf(acc[i][j]*alpha); else Cf[o]=acc[i][j]*alpha; }
}
__global__ void k_softmax(const float* S,u16* P){
  long row=blockIdx.x; int t=threadIdx.x; float v[8]; float m=-1e30f;
  for(int i=0;i<8;i++){ v[i]=S[row*512+t+64*i]; m=fmaxf(m,v[i]); }
  for(int o=32;o;o>>=1) m=fmaxf(m,__shfl_xor(m,o));
  float s=0; for(int i=0;i<8;i++){ v[i]=__expf(v[i]-m); s+=v[i]; }
  for(int o=32;o;o>>=1) s+=__shfl_xor(s,o);
  for(int i=0;i<8;i++) P[row*512+t+64*i]=f2bf(v[i]/s);
}
__global__ void k_mul(const u16* g,const u16* u,u16* h,int n){ int i=blockIdx.x*256+threadIdx.x; if(i<n) h[i]=f2bf(bf2f(g[i])*bf2f(u[i])); }
__global__ void k_fin(const u16* x,const u16* s,const u16* d,u16* o,int n){ int i=blockIdx.x*256+threadIdx.x; if(i<n){ float dd=bf2f(d[i]); float g=dd/(1.f+__expf(-dd)); o[i]=f2bf(bf2f(x[i])+bf2f(s[i])*g);} }


__global__ __launch_bounds__(256) void k_probe(const uint4* a, long na, const uint4* b, long nb, const uint4* c, long nc, const uint4* k, const uint4* v, unsigned* sink){
  unsigned acc=0; long t=blockIdx.x*256L+threadIdx.x, T=gridDim.x*256L;
  for(long i=t;i<na;i+=T){ uint4 x=a[i]; uint4 y=b[i]; acc^=x.x^y.y; }
  for(long i=t;i<32L*512*24;i+=T){ long h=i/(512*24), r=i%(512*24); long o=h*2048*24+1536*24+r; uint4 x=k[o]; uint4 y=v[o]; acc^=x.x^y.y; }
  if(acc==0x12345) sink[0]=acc;
}
extern "C" void kernel_launch(void* const* d_in, const int* in_sizes, int n_in, void* d_out, int out_size, void* d_ws, size_t ws_size, hipStream_t stream){
  const u16* x=(const u16*)d_in[0]; const u16* kc=(const u16*)d_in[1]; const u16* vc=(const u16*)d_in[2];
  const u16* wq=(const u16*)d_in[3]; const u16* wo=(const u16*)d_in[4];
  const u16* wg=(const u16*)d_in[6]; const u16* wu=(const u16*)d_in[7]; const u16* wd=(const u16*)d_in[8];
  char* ws=(char*)d_ws; size_t off=0; auto alloc=[&](size_t b){ char* p=ws+off; off+=(b+255)&~(size_t)255; return p; };
  u16* xn=(u16*)alloc(2048*640*2); u16* q=(u16*)alloc(2048*6144*2);
  u16* P=(u16*)alloc((size_t)32*2048*512*2);
  float* S=(float*)alloc((size_t)32*2048*512*4);
  off=(char*)S-ws;
  u16* o=(u16*)alloc(2048*6144*2);
  u16* g=(u16*)alloc(2048*1536*2); u16* u=(u16*)alloc(2048*1536*2); u16* h=(u16*)alloc(2048*1536*2);
  u16* s=(u16*)alloc(2048*640*2); u16* d=(u16*)alloc(2048*640*2);
  k_ln<<<2048,64,0,stream>>>(x,xn);
  k_gemm<false><<<dim3(96,32,1),256,0,stream>>>(xn,640,0,wq,6144,0,q,nullptr,6144,0,640,1.f);
  k_gemm<true><<<dim3(8,32,32),256,0,stream>>>(q,6144,192,kc+1536*192,192,2048*192,nullptr,S,512,2048*512,192,0.07216878364870322f);
  k_softmax<<<32*2048,64,0,stream>>>(S,P);
  k_gemm<false><<<dim3(3,32,32),256,0,stream>>>(P,512,2048*512,vc+1536*192,192,2048*192,o,nullptr,6144,192,512,1.f);
  k_gemm<false><<<dim3(10,32,1),256,0,stream>>>(o,6144,0,wo,640,0,s,nullptr,640,0,6144,1.f);
  k_gemm<false><<<dim3(24,32,1),256,0,stream>>>(xn,640,0,wg,1536,0,g,nullptr,1536,0,640,1.f);
  k_gemm<false><<<dim3(24,32,1),256,0,stream>>>(xn,640,0,wu,1536,0,u,nullptr,1536,0,640,1.f);
  k_mul<<<2048*1536/256,256,0,stream>>>(g,u,h,2048*1536);
  k_gemm<false><<<dim3(10,32,1),256,0,stream>>>(h,1536,0,wd,640,0,d,nullptr,640,0,1536,1.f);
  k_fin<<<2048*640/256,256,0,stream>>>(x,s,d,(u16*)d_out,2048*640);
}
```

```cpp
#include <hip/hip_runtime.h>
#include <hip/hip_bf16.h>
#include <stdint.h>
typedef unsigned short u16;
typedef float f32x4 __attribute__((ext_vector_type(4)));
typedef __bf16 bf16x8 __attribute__((ext_vector_type(8)));
static __device__ __forceinline__ float bf2f(u16 v){ return __uint_as_float(((unsigned)v)<<16); }
static __device__ __forceinline__ u16 f2bf(float f){ __bf16 b=(__bf16)f; return __builtin_bit_cast(u16,b); }

__global__ void k_ln(const u16* x, u16* xn){
  int row=blockIdx.x; int t=threadIdx.x;
  float v[10]; float s=0;
  for(int i=0;i<10;i++){ v[i]=bf2f(x[row*640+t+64*i]); s+=v[i]; }
  for(int o=32;o;o>>=1) s+=__shfl_xor(s,o);
  float m=s/640.f; float q=0;
  for(int i=0;i<10;i++){ float d=v[i]-m; q+=d*d; }
  for(int o=32;o;o>>=1) q+=__shfl_xor(q,o);
  float r=rsqrtf(q/640.f+1e-5f);
  for(int i=0;i<10;i++) xn[row*640+t+64*i]=f2bf((v[i]-m)*r);
}
template<bool BT>
__global__ void k_gemm(const u16* A,int lda,long sa,const u16* B,int ldb,long sb,u16* C,float* Cf,int ldc,long sc,int K,float alpha){
  __shared__ u16 As[64][40]; __shared__ u16 Bs[64][40];
  A+=sa*blockIdx.z; B+=sb*blockIdx.z;
  int m0=blockIdx.y*64,n0=blockIdx.x*64; int t=threadIdx.x,w=t>>6,l=t&63;
  f32x4 acc[4]; for(int i=0;i<4;i++) acc[i]=(f32x4){0,0,0,0};
  for(int k0=0;k0<K;k0+=32){
    { int r=t>>2,c=(t&3)*8; *(uint4*)&As[r][c]=*(const uint4*)&A[(long)(m0+r)*lda+k0+c]; }
    if(BT){ int r=t>>2,c=(t&3)*8; *(uint4*)&Bs[r][c]=*(const uint4*)&B[(long)(n0+r)*ldb+k0+c]; }
    else { int kk=t>>3,c=(t&7)*8; uint4 v=*(const uint4*)&B[(long)(k0+kk)*ldb+n0+c]; u16* p=(u16*)&v; for(int j=0;j<8;j++) Bs[c+j][kk]=p[j]; }
    __syncthreads();
    bf16x8 a=*(bf16x8*)&As[w*16+(l&15)][(l>>4)*8];
    for(int i=0;i<4;i++){ bf16x8 b=*(bf16x8*)&Bs[i*16+(l&15)][(l>>4)*8]; acc[i]=__builtin_amdgcn_mfma_f32_16x16x32_bf16(a,b,acc[i],0,0,0);}
    __syncthreads();
  }
  for(int i=0;i<4;i++) for(int j=0;j<4;j++){ long o=sc*blockIdx.z+(long)(m0+w*16+(l>>4)*4+j)*ldc+n0+i*16+(l&15); if(C) C[o]=f2bf(acc[i][j]*alpha); else Cf[o]=acc[i][j]*alpha; }
}
__global__ void k_softmax(const float* S,u16* P){
  long row=blockIdx.x; int t=threadIdx.x; float v[8]; float m=-1e30f;
  for(int i=0;i<8;i++){ v[i]=S[row*512+t+64*i]; m=fmaxf(m,v[i]); }
  for(int o=32;o;o>>=1) m=fmaxf(m,__shfl_xor(m,o));
  float s=0; for(int i=0;i<8;i++){ v[i]=__expf(v[i]-m); s+=v[i]; }
  for(int o=32;o;o>>=1) s+=__shfl_xor(s,o);
  for(int i=0;i<8;i++) P[row*512+t+64*i]=f2bf(v[i]/s);
}
__global__ void k_mul(const u16* g,const u16* u,u16* h,int n){ int i=blockIdx.x*256+threadIdx.x; if(i<n) h[i]=f2bf(bf2f(g[i])*bf2f(u[i])); }
__global__ void k_fin(const u16* x,const u16* s,const u16* d,u16* o,int n){ int i=blockIdx.x*256+threadIdx.x; if(i<n){ float dd=bf2f(d[i]); float g=dd/(1.f+__expf(-dd)); o[i]=f2bf(bf2f(x[i])+bf2f(s[i])*g);} }


typedef unsigned long long u64;
typedef unsigned u32x4 __attribute__((ext_vector_type(4)));
__device__ __forceinline__ u64 rt(){ u64 t; asm volatile("s_memrealtime %0\n\ts_waitcnt lgkmcnt(0)":"=s"(t)::"memory"); return t; }
__global__ __launch_bounds__(256) void k_probe(const u32x4* a, const u32x4* b, const u32x4* k, const u32x4* v, u64* st){
  u64 t0=rt();
  u32x4 acc={0,0,0,0}; int t=threadIdx.x; long B=blockIdx.x;
  u32x4 r[28];
  #pragma unroll
  for(int i=0;i<7;i++){ r[i]=a[B*1920+i*256+t]; r[7+i]=b[B*1920+i*256+t]; }
  #pragma unroll
  for(int i=0;i<6;i++){ long j=B*1536+i*256+t; long h=j/(512*24), rr=j%(512*24); long o=h*2048*24+1536*24+rr; r[14+i]=k[o]; r[20+i]=v[o]; }
  #pragma unroll
  for(int i=0;i<26;i++) acc^=r[i];
  if(acc.x==0x12345 && acc.y==0x54321) st[4096]=acc.z;
  asm volatile("s_waitcnt vmcnt(0)":::"memory");
  __syncthreads();
  u64 t1=rt();
  if(t==0){ st[B*2]=t0; st[B*2+1]=t1; }
}
__global__ void k_spin(const u64* st, int mode){
  u64 mn=~0ull,mx=0,mx0=0;
  for(int b=0;b<256;b++){ u64 a=st[b*2], e=st[b*2+1]; mn=a<mn?a:mn; mx=e>mx?e:mx; mx0=a>mx0?a:mx0; }
  u64 d= mode==0? (mx-mn) : (mx0-mn);
  if(d>10000) d=10000;
  u64 t0=rt(); while(rt()-t0 < d*50);
}
extern "C" void kernel_launch(void* const* d_in, const int* in_sizes, int n_in, void* d_out, int out_size, void* d_ws, size_t ws_size, hipStream_t stream){
  const u16* x=(const u16*)d_in[0]; const u16* kc=(const u16*)d_in[1]; const u16* vc=(const u16*)d_in[2];
  const u16* wq=(const u16*)d_in[3]; const u16* wo=(const u16*)d_in[4];
  const u16* wg=(const u16*)d_in[6]; const u16* wu=(const u16*)d_in[7]; const u16* wd=(const u16*)d_in[8];
  char* ws=(char*)d_ws; size_t off=0; auto alloc=[&](size_t b){ char* p=ws+off; off+=(b+255)&~(size_t)255; return p; };
  u16* xn=(u16*)alloc(2048*640*2); u16* q=(u16*)alloc(2048*6144*2);
  u16* P=(u16*)alloc((size_t)32*2048*512*2);
  float* S=(float*)alloc((size_t)32*2048*512*4);
  off=(char*)S-ws;
  u16* o=(u16*)alloc(2048*6144*2);
  u16* g=(u16*)alloc(2048*1536*2); u16* u=(u16*)alloc(2048*1536*2); u16* h=(u16*)alloc(2048*1536*2);
  u16* s=(u16*)alloc(2048*640*2); u16* d=(u16*)alloc(2048*640*2);
  u64* st=(u64*)(ws+ws_size-65536);
  k_probe<<<256,256,0,stream>>>((const u32x4*)wq,(const u32x4*)wo,(const u32x4*)kc,(const u32x4*)vc,st);
  k_ln<<<2048,64,0,stream>>>(x,xn);
  k_gemm<false><<<dim3(96,32,1),256,0,stream>>>(xn,640,0,wq,6144,0,q,nullptr,6144,0,640,1.f);
  k_gemm<true><<<dim3(8,32,32),256,0,stream>>>(q,6144,192,kc+1536*192,192,2048*192,nullptr,S,512,2048*512,192,0.07216878364870322f);
  k_softmax<<<32*2048,64,0,stream>>>(S,P);
  k_gemm<false><<<dim3(3,32,32),256,0,stream>>>(P,512,2048*512,vc+1536*192,192,2048*192,o,nullptr,6144,192,512,1.f);
  k_gemm<false><<<dim3(10,32,1),256,0,stream>>>(o,6144,0,wo,640,0,s,nullptr,640,0,6144,1.f);
  k_gemm<false><<<dim3(24,32,1),256,0,stream>>>(xn,640,0,wg,1536,0,g,nullptr,1536,0,640,1.f);
  k_gemm<false><<<dim3(24,32,1),256,0,stream>>>(xn,640,0,wu,1536,0,u,nullptr,1536,0,640,1.f);
  k_mul<<<2048*1536/256,256,0,stream>>>(g,u,h,2048*1536);
  k_gemm<false><<<dim3(10,32,1),256,0,stream>>>(h,1536,0,wd,640,0,d,nullptr,640,0,1536,1.f);
  k_fin<<<2048*640/256,256,0,stream>>>(x,s,d,(u16*)d_out,2048*640);
  k_spin<<<1,64,0,stream>>>(st,1);
}
```

```cpp
#include <hip/hip_runtime.h>
#include <stdint.h>

typedef unsigned short u16;
constexpr unsigned AM_OFF = 4194304, AM_QB = 249856, PART_OFF = 41943040, PART_SL = 2625536, WS_USED = 62947328,
                   FL_OFF = 3407872, FL2_OFF = 3473408, FS = 6;

constexpr unsigned sigma(unsigned h, unsigned c) {
  const unsigned hh = h & 15, H = h >> 4;
  if (hh < 15) { const unsigned xx = hh / 5, i = hh % 5; return 32 * (3 * H + xx) + 5 * c + i + (c > 2 * xx) + (c > 2 * xx + 1); }
  return 32 * (3 * H + (c >> 1)) + 5 * c + 5 + (c & 1);
}
constexpr unsigned wrow(unsigned x, unsigned p) {
  if (x >= 6) return 32 * ((x - 6) * 24 + p);
  const unsigned H = x / 3, xx = x % 3, e = 10 * xx + 5;
  unsigned c = 0, hh = 0;
  if (p < e) { c = p / 5; hh = 5 * xx + p % 5; }
  else if (p == e) { c = 2 * xx; hh = 15; }
  else if (p < e + 6) { c = 2 * xx + 1; hh = 5 * xx + (p - e - 1); }
  else if (p == e + 6) { c = 2 * xx + 1; hh = 15; }
  else { c = (p - 2) / 5; hh = 5 * xx + (p - 2) % 5; }
  return 192 * (16 * H + hh) + 32 * c;
}

struct Tab { unsigned v[1024 * 36]; };
constexpr Tab make_tab() {
  Tab t{};
  const unsigned perm[8] = {6,0,7,1,2,3,4,5};
  for (unsigned e = 0; e < 1024; ++e) {
    const unsigned wgi = e >> 2, w = e & 3, j = wgi >> 3, x = perm[wgi & 7], head = 4 * (wgi & 7) + (j & 3), qb = j >> 2, nb = j & 3;
    const unsigned odd = head & 1, isd = x >= 6, am = AM_OFF + (qb * 16 + w * 4) * AM_QB, oob = 0x7f000000u;
    unsigned* v = t.v + e * 36;
    for (int c = 0; c < 6; ++c) v[c] = am + sigma(head, c) * 1024;
    v[6] = am + (192 + (3 * head + odd) / 2) * 1024;
    v[7] = am + (192 + (odd ? 3 * head : 3 * head + 2) / 2) * 1024 + 8 * odd;
    for (int s = 0; s < 8; ++s) v[8 + s] = (isd && s >= 6) ? oob : wrow(x, 4 * s + w) * 1280 + nb * 320;
    v[16] = am + (isd ? 192 + 24 * (x - 6) : 32 * x) * 1024;
    v[17] = PART_OFF + x * PART_SL + j * 81920 + w * 20480;
    v[18] = isd ? 2 : 8;
    v[19] = FL_OFF + (((qb * 4 + w) * 32 + head) << FS);
    v[20] = FL_OFF + ((qb * 4 + w) * 32 << FS);
    v[21] = isd ? 16 * (x - 6) : 16 * (x / 3) + 5 * (x % 3);
    v[22] = isd ? 16 : 5;
    v[23] = isd ? 16 * (x - 6) : 16 * (x / 3) + 15;
    v[24] = FL2_OFF + (((j * 4 + w) * 8 + x) << FS);
    v[25] = FL2_OFF + (((j * 4 + (x >> 1)) * 8) << FS);
    v[26] = (head * 2048 + 1536) * 384 + qb * 24576 + w * 6144;
    v[27] = (isd && j >= 24) ? oob : wrow(x, j) * 1280 + w * 10240;
    for (int k = 0; k < 3; ++k) {
      const unsigned u = w + 4 * k;
      v[28 + k] = u < 10 ? PART_OFF + j * 81920 + (10 * x + 5 * (u & 1) + (u >> 1)) * 1024 : oob;
      v[32 + k] = u < 10 ? (qb * 256 + (2 * x + (u & 1)) * 16) * 1280 + nb * 320 + (u >> 1) * 64 : oob;
    }
    v[31] = isd; v[35] = 0;
  }
  return t;
}
extern "C" __device__ __attribute__((used, visibility("protected"))) const Tab km_tab = make_tab();

extern "C" __global__ __launch_bounds__(256, 1) void km_main(char* ws, const u16* wq, const u16* wg, const u16* wu, const u16* kc, const u16* vc,
                                                   const u16* wo, const u16* wd, const u16* x, u16* out) {
  __shared__ __attribute__((aligned(1024))) char lds[163840];
  asm volatile("" :: "s"((unsigned)(size_t)lds));
  unsigned tid = threadIdx.x;
  asm volatile(
    "s_getpc_b64 s[86:87]\n"
    "v_lshlrev_b32 v17, 6, v0\n"
    "global_load_dword a245, v17, s[86:87]\n"
    "s_mov_b32 s92, 0\n"
    ".L_start_%=:\n"
    "s_load_dwordx2 s[68:69], %[ka], 0x40\n"
    "s_load_dwordx2 s[40:41], %[ka], 0x0\n"
    "s_load_dwordx2 s[44:45], %[ka], 0x8\n"
    "s_load_dwordx2 s[48:49], %[ka], 0x10\n"
    "s_load_dwordx2 s[52:53], %[ka], 0x18\n"
    "s_load_dwordx2 s[56:57], %[ka], 0x20\n"
    "s_load_dwordx2 s[60:61], %[ka], 0x28\n"
    "s_load_dwordx2 s[64:65], %[ka], 0x30\n"
    "s_load_dwordx2 s[38:39], %[ka], 0x38\n"
    "s_load_dwordx2 s[72:73], %[ka], 0x48\n"
    "v_readfirstlane_b32 s76, v0\n"
    "v_and_b32 v16, 63, v0\n"
    "s_and_b32 s86, %[bid], 7\n"
    "s_lshl_b32 s93, s86, 2\n"
    "s_bfe_u32 s86, %[bid], 0x20003\n"
    "s_add_u32 s93, s93, s86\n"
    "s_lshr_b32 s76, s76, 6\n"
    "v_lshlrev_b32 v0, 4, v16\n"
    "s_lshr_b32 s37, %[bid], 5\n"
    "s_lshl_b32 s86, s37, 5\n"
    "s_add_u32 s86, s86, s93\n"
    "s_lshl_b32 s86, s86, 2\n"
    "s_add_u32 s86, s86, s76\n"
    "s_mul_i32 s89, s86, 2560\n"
    "v_lshrrev_b32 v17, 5, v16\n"
    "v_and_b32 v18, 31, v16\n"
    "v_mul_u32_u24 v19, 1280, v17\n"
    "v_lshl_add_u32 v19, v18, 4, v19\n"
    "v_bfe_u32 v23, v16, 4, 1\n"
    "v_lshl_add_u32 v20, v23, 30, v19\n"
    "s_mov_b32 s70, 2621440\n"
    "s_mov_b32 s71, 0x20000\n"
    "s_mov_b32 s42, 62947328\n"
    "s_mov_b32 s43, 0x20000\n"
    "s_mov_b32 s46, 7864320\n"
    "s_mov_b32 s47, 0x20000\n"
    "s_lshl_b32 s90, s37, 6\n"
    "s_add_u32 s90, s90, 3141632\n"
    "s_waitcnt lgkmcnt(0)\n"
    "s_and_b32 s69, s69, 0xffff\n"
    "s_and_b32 s41, s41, 0xffff\n"
    "s_and_b32 s45, s45, 0xffff\n"
    "buffer_load_dwordx4 v[24:27], v19, s[68:71], s89 offen offset:0\n"
    "buffer_load_dwordx4 v[28:31], v19, s[68:71], s89 offen offset:512\n"
    "buffer_load_dwordx4 v[32:35], v20, s[68:71], s89 offen offset:1024\n"
    "buffer_load_dword v36, off, s[40:43], s90 sc1\n"
    "v_xor_b32 v23, 1, v23\n"
    "v_cvt_f32_u32 v23, v23\n"
    "s_lshl_b32 s86, s76, 1\n"
    "v_add_u32 v17, s86, v17\n"
    "v_and_b32 v22, 7, v16\n"
    "v_xor_b32 v17, v17, v22\n"
    "v_lshlrev_b32 v21, 7, v18\n"
    "v_lshl_add_u32 v21, v17, 4, v21\n"
    "v_add_u32 v21, 147712, v21\n"
    "v_lshrrev_b32 v17, 3, v16\n"
    "v_lshlrev_b32 v20, 4, v22\n"
    "v_lshl_add_u32 v20, v17, 8, v20\n"
    "s_lshl_b32 s86, s76, 3\n"
    "v_add_u32 v18, s86, v17\n"
    "v_mul_u32_u24 v1, 12288, v18\n"
    "v_bfe_u32 v19, v16, 4, 1\n"
    "v_lshlrev_b32 v19, 2, v19\n"
    "v_xor_b32 v18, v22, v19\n"
    "v_lshl_add_u32 v1, v18, 4, v1\n"
    "v_xor_b32 v22, v22, v17\n"
    "v_lshlrev_b32 v22, 4, v22\n"
    "v_lshl_add_u32 v22, v17, 7, v22\n"
    "s_lshl_b32 s86, s76, 10\n"
    "s_add_u32 s86, s86, 147712\n"
    "v_add_u32 v22, s86, v22\n"
    "s_lshr_b32 s86, s76, 1\n"
    "s_mul_i32 s77, s86, 12288\n"
    "s_and_b32 s86, s76, 1\n"
    "s_mul_i32 s86, s86, 24584\n"
    "s_add_u32 s77, s77, s86\n"
    "s_mul_i32 s79, s93, 384\n"
    "s_cmp_eq_u32 s76, 3\n"
    "s_cselect_b64 s[4:5], s[52:53], s[48:49]\n"
    "s_cmp_lt_u32 s76, 2\n"
    "s_cselect_b64 s[4:5], s[44:45], s[4:5]\n"
    "s_cselect_b64 vcc, -1, 0\n"
    "s_cselect_b32 s9, 4, 1\n"
    "s_and_b32 s5, s5, 0xffff\n"
    "s_mul_i32 s6, s9, 1966080\n"
    "s_mov_b32 s7, 0x20000\n"
    "s_mul_i32 s8, s37, 98304\n"
    "s_mul_i32 s86, s93, 96\n"
    "s_add_u32 s8, s8, s86\n"
    "s_mul_i32 s8, s8, s9\n"
    "s_mul_i32 s9, s9, 786432\n"
    "s_mul_i32 s86, s76, 48\n"
    "v_min_u32 v11, 47, v16\n"
    "v_add_u32 v11, s86, v11\n"
    "v_mul_u32_u24 v12, 171, v11\n"
    "v_lshrrev_b32 v12, 9, v12\n"
    "v_mul_u32_u24 v13, 3, v12\n"
    "v_sub_u32 v13, v11, v13\n"
    "v_mul_u32_u24 v12, 12288, v12\n"
    "v_lshl_add_u32 v11, v13, 7, v12\n"
    "v_lshrrev_b32 v12, 1, v16\n"
    "v_mul_u32_u24 v12, 3072, v12\n"
    "v_and_b32 v13, 1, v16\n"
    "v_mul_u32_u24 v13, 92, v13\n"
    "v_add_u32 v12, v12, v13\n"
    "v_cndmask_b32 v14, v12, v11, vcc\n"
    "s_add_u32 m0, s77, 0\n"
    "s_add_u32 s88, s79, 0\n"
    "buffer_load_dwordx4 v1, s[44:47], s88 offen offset:0 lds\n"
    "s_add_u32 m0, s77, 4096\n"
    "s_add_u32 s88, s79, 128\n"
    "buffer_load_dwordx4 v1, s[44:47], s88 offen offset:0 lds\n"
    "s_add_u32 m0, s77, 8192\n"
    "s_add_u32 s88, s79, 256\n"
    "buffer_load_dwordx4 v1, s[44:47], s88 offen offset:0 lds\n"
    "s_add_u32 m0, s77, 1024\n"
    "s_add_u32 s88, s79, 393216\n"
    "buffer_load_dwordx4 v1, s[44:47], s88 offen offset:0 lds\n"
    "s_add_u32 m0, s77, 5120\n"
    "s_add_u32 s88, s79, 393344\n"
    "buffer_load_dwordx4 v1, s[44:47], s88 offen offset:0 lds\n"
    "s_add_u32 m0, s77, 9216\n"
    "s_add_u32 s88, s79, 393472\n"
    "buffer_load_dwordx4 v1, s[44:47], s88 offen offset:0 lds\n"
    "buffer_load_dword a246, v14, s[4:7], s8 offen offset:0\n"
    "s_add_u32 m0, s77, 2048\n"
    "s_add_u32 s88, s79, 786432\n"
    "buffer_load_dwordx4 v1, s[44:47], s88 offen offset:0 lds\n"
    "s_add_u32 m0, s77, 6144\n"
    "s_add_u32 s88, s79, 786560\n"
    "buffer_load_dwordx4 v1, s[44:47], s88 offen offset:0 lds\n"
    "s_add_u32 m0, s77, 10240\n"
    "s_add_u32 s88, s79, 786688\n"
    "buffer_load_dwordx4 v1, s[44:47], s88 offen offset:0 lds\n"
    "s_lshr_b32 s94, %[bid], 3\n"
    "s_lshl2_add_u32 s94, s94, s76\n"
    "s_lshl_b32 s94, s94, 8\n"
    "v_bfe_u32 v17, v0, 4, 1\n"
    "v_lshl_add_u32 v17, v17, 7, s94\n"
    "v_min_u32 v17, .L_sgo_%=-.L_q1_%=-4, v17\n"
    "s_getpc_b64 s[86:87]\n"
    ".L_cw0_%=:\n"
    "s_add_u32 s86, s86, .L_q1_%=-.L_cw0_%=\n"
    "s_addc_u32 s87, s87, 0\n"
    "global_load_dword a252, v17, s[86:87]\n"
    "s_lshl_b32 s86, %[bid], 2\n"
    "s_add_u32 s86, s86, s76\n"
    "s_mul_i32 s86, s86, 144\n"
    "v_min_u32 v17, 35, v16\n"
    "v_lshl_add_u32 v17, v17, 2, s86\n"
    "s_getpc_b64 s[86:87]\n"
    "s_add_u32 s86, s86, km_tab@rel32@lo+4\n"
    "s_addc_u32 s87, s87, km_tab@rel32@hi+12\n"
    "global_load_dword v15, v17, s[86:87]\n"
    "s_lshl_b32 s86, s37, 4\n"
    "s_lshr_b32 s87, s93, 1\n"
    "s_add_u32 s86, s86, s87\n"
    "s_mul_i32 s90, s86, 20480\n"
    "s_and_b32 s86, s93, 1\n"
    "s_lshl_b32 s86, s86, 7\n"
    "s_add_u32 s90, s90, s86\n"
    "s_lshl_b32 s86, s76, 11\n"
    "s_add_u32 s90, s90, s86\n"
    "s_add_u32 s89, s90, 16384\n"
    "s_cmp_lt_u32 s76, 2\n"
    "s_cselect_b32 s89, s89, 0x7f000000\n"
    "s_lshl_b32 s91, s37, 11\n"
    "s_add_u32 s91, s91, 3538944\n"
    "s_lshl_b32 s86, s93, 6\n"
    "s_add_u32 s36, s91, s86\n"
    "s_cmp_eq_u32 s76, 0\n"
    "s_cselect_b32 s36, s36, 0x7f000000\n"
    "s_lshl_b32 s86, s76, 3\n"
    "v_and_b32 v18, 7, v16\n"
    "v_add_u32 v18, s86, v18\n"
    "v_lshlrev_b32 v18, 6, v18\n"
    "s_waitcnt vmcnt(13)\n"
    "v_lshlrev_b32 v40, 16, v24\n"
    "v_and_b32 v41, 0xffff0000, v24\n"
    "v_lshlrev_b32 v42, 16, v25\n"
    "v_and_b32 v43, 0xffff0000, v25\n"
    "v_lshlrev_b32 v44, 16, v26\n"
    "v_and_b32 v45, 0xffff0000, v26\n"
    "v_lshlrev_b32 v46, 16, v27\n"
    "v_and_b32 v47, 0xffff0000, v27\n"
    "v_lshlrev_b32 v48, 16, v28\n"
    "v_and_b32 v49, 0xffff0000, v28\n"
    "v_lshlrev_b32 v50, 16, v29\n"
    "v_and_b32 v51, 0xffff0000, v29\n"
    "v_lshlrev_b32 v52, 16, v30\n"
    "v_and_b32 v53, 0xffff0000, v30\n"
    "v_lshlrev_b32 v54, 16, v31\n"
    "v_and_b32 v55, 0xffff0000, v31\n"
    "v_lshlrev_b32 v56, 16, v32\n"
    "v_and_b32 v57, 0xffff0000, v32\n"
    "v_lshlrev_b32 v58, 16, v33\n"
    "v_and_b32 v59, 0xffff0000, v33\n"
    "v_lshlrev_b32 v60, 16, v34\n"
    "v_and_b32 v61, 0xffff0000, v34\n"
    "v_lshlrev_b32 v62, 16, v35\n"
    "v_and_b32 v63, 0xffff0000, v35\n"
    "v_pk_add_f32 v[64:65], v[40:41], v[44:45]\n"
    "v_pk_add_f32 v[66:67], v[42:43], v[46:47]\n"
    "v_pk_add_f32 v[64:65], v[64:65], v[48:49]\n"
    "v_pk_add_f32 v[66:67], v[66:67], v[50:51]\n"
    "v_pk_add_f32 v[64:65], v[64:65], v[52:53]\n"
    "v_pk_add_f32 v[66:67], v[66:67], v[54:55]\n"
    "v_pk_add_f32 v[64:65], v[64:65], v[56:57]\n"
    "v_pk_add_f32 v[66:67], v[66:67], v[58:59]\n"
    "v_pk_add_f32 v[64:65], v[64:65], v[60:61]\n"
    "v_pk_add_f32 v[66:67], v[66:67], v[62:63]\n"
    "v_pk_add_f32 v[64:65], v[64:65], v[66:67]\n"
    "v_add_f32 v69, v64, v65\n"
    "s_nop 1\n"
    "v_add_f32_dpp v69, v69, v69 quad_perm:[1,0,3,2] row_mask:0xf bank_mask:0xf\n"
    "s_nop 1\n"
    "v_add_f32_dpp v69, v69, v69 quad_perm:[2,3,0,1] row_mask:0xf bank_mask:0xf\n"
    "s_nop 1\n"
    "v_add_f32_dpp v69, v69, v69 row_ror:4 row_mask:0xf bank_mask:0xf\n"
    "s_nop 1\n"
    "v_add_f32_dpp v69, v69, v69 row_ror:8 row_mask:0xf bank_mask:0xf\n"
    "v_mov_b32 v68, v69\n"
    "s_nop 1\n"
    "v_permlane16_swap_b32 v69, v68\n"
    "s_nop 0\n"
    "v_add_f32 v69, v69, v68\n"
    "v_mul_f32 v69, 0xbacccccd, v69\n"
    "v_pk_add_f32 v[40:41], v[40:41], v[68:69] op_sel:[0,1] op_sel_hi:[1,1]\n"
    "v_pk_add_f32 v[42:43], v[42:43], v[68:69] op_sel:[0,1] op_sel_hi:[1,1]\n"
    "v_pk_add_f32 v[44:45], v[44:45], v[68:69] op_sel:[0,1] op_sel_hi:[1,1]\n"
    "v_pk_add_f32 v[46:47], v[46:47], v[68:69] op_sel:[0,1] op_sel_hi:[1,1]\n"
    "v_pk_add_f32 v[48:49], v[48:49], v[68:69] op_sel:[0,1] op_sel_hi:[1,1]\n"
    "v_pk_add_f32 v[50:51], v[50:51], v[68:69] op_sel:[0,1] op_sel_hi:[1,1]\n"
    "v_pk_add_f32 v[52:53], v[52:53], v[68:69] op_sel:[0,1] op_sel_hi:[1,1]\n"
    "v_pk_add_f32 v[54:55], v[54:55], v[68:69] op_sel:[0,1] op_sel_hi:[1,1]\n"
    "v_pk_add_f32 v[56:57], v[56:57], v[68:69] op_sel:[0,1] op_sel_hi:[1,1]\n"
    "v_pk_add_f32 v[58:59], v[58:59], v[68:69] op_sel:[0,1] op_sel_hi:[1,1]\n"
    "v_pk_add_f32 v[60:61], v[60:61], v[68:69] op_sel:[0,1] op_sel_hi:[1,1]\n"
    "v_pk_add_f32 v[62:63], v[62:63], v[68:69] op_sel:[0,1] op_sel_hi:[1,1]\n"
    "v_pk_mul_f32 v[56:57], v[56:57], v[22:23] op_sel:[0,1] op_sel_hi:[1,1]\n"
    "v_pk_mul_f32 v[58:59], v[58:59], v[22:23] op_sel:[0,1] op_sel_hi:[1,1]\n"
    "v_pk_mul_f32 v[60:61], v[60:61], v[22:23] op_sel:[0,1] op_sel_hi:[1,1]\n"
    "v_pk_mul_f32 v[62:63], v[62:63], v[22:23] op_sel:[0,1] op_sel_hi:[1,1]\n"
    "v_pk_mul_f32 v[64:65], v[40:41], v[40:41]\n"
    "v_pk_mul_f32 v[66:67], v[42:43], v[42:43]\n"
    "v_pk_fma_f32 v[64:65], v[44:45], v[44:45], v[64:65]\n"
    "v_pk_fma_f32 v[66:67], v[46:47], v[46:47], v[66:67]\n"
    "v_pk_fma_f32 v[64:65], v[48:49], v[48:49], v[64:65]\n"
    "v_pk_fma_f32 v[66:67], v[50:51], v[50:51], v[66:67]\n"
    "v_pk_fma_f32 v[64:65], v[52:53], v[52:53], v[64:65]\n"
    "v_pk_fma_f32 v[66:67], v[54:55], v[54:55], v[66:67]\n"
    "v_pk_fma_f32 v[64:65], v[56:57], v[56:57], v[64:65]\n"
    "v_pk_fma_f32 v[66:67], v[58:59], v[58:59], v[66:67]\n"
    "v_pk_fma_f32 v[64:65], v[60:61], v[60:61], v[64:65]\n"
    "v_pk_fma_f32 v[66:67], v[62:63], v[62:63], v[66:67]\n"
    "v_pk_add_f32 v[64:65], v[64:65], v[66:67]\n"
    "v_add_f32 v69, v64, v65\n"
    "s_nop 1\n"
    "v_add_f32_dpp v69, v69, v69 quad_perm:[1,0,3,2] row_mask:0xf bank_mask:0xf\n"
    "s_nop 1\n"
    "v_add_f32_dpp v69, v69, v69 quad_perm:[2,3,0,1] row_mask:0xf bank_mask:0xf\n"
    "s_nop 1\n"
    "v_add_f32_dpp v69, v69, v69 row_ror:4 row_mask:0xf bank_mask:0xf\n"
    "s_nop 1\n"
    "v_add_f32_dpp v69, v69, v69 row_ror:8 row_mask:0xf bank_mask:0xf\n"
    "v_mov_b32 v68, v69\n"
    "s_nop 1\n"
    "v_permlane16_swap_b32 v69, v68\n"
    "s_nop 0\n"
    "v_add_f32 v69, v69, v68\n"
    "v_mov_b32 v68, 0x3727c5ac\n"
    "v_fmac_f32 v68, 0x3acccccd, v69\n"
    "v_rsq_f32 v68, v68\n"
    "s_nop 0\n"
    "v_mul_f32 v69, 0x3dd53b92, v68\n"
    "v_pk_mul_f32 v[40:41], v[40:41], v[68:69] op_sel:[0,1] op_sel_hi:[1,1]\n"
    "v_pk_mul_f32 v[42:43], v[42:43], v[68:69] op_sel:[0,1] op_sel_hi:[1,1]\n"
    "v_pk_mul_f32 v[44:45], v[44:45], v[68:69] op_sel:[0,1] op_sel_hi:[1,1]\n"
    "v_pk_mul_f32 v[46:47], v[46:47], v[68:69] op_sel:[0,1] op_sel_hi:[1,1]\n"
    "v_pk_mul_f32 v[48:49], v[48:49], v[68:69] op_sel:[0,1] op_sel_hi:[1,1]\n"
    "v_pk_mul_f32 v[50:51], v[50:51], v[68:69] op_sel:[0,1] op_sel_hi:[1,1]\n"
    "v_pk_mul_f32 v[52:53], v[52:53], v[68:69] op_sel:[0,1] op_sel_hi:[1,1]\n"
    "v_pk_mul_f32 v[54:55], v[54:55], v[68:69] op_sel:[0,1] op_sel_hi:[1,1]\n"
    "v_pk_mul_f32 v[56:57], v[56:57], v[68:69] op_sel:[0,1] op_sel_hi:[1,1]\n"
    "v_pk_mul_f32 v[58:59], v[58:59], v[68:69] op_sel:[0,1] op_sel_hi:[1,1]\n"
    "v_pk_mul_f32 v[60:61], v[60:61], v[68:69] op_sel:[0,1] op_sel_hi:[1,1]\n"
    "v_pk_mul_f32 v[62:63], v[62:63], v[68:69] op_sel:[0,1] op_sel_hi:[1,1]\n"
    "v_cvt_pk_bf16_f32 v24, v40, v41\n"
    "v_cvt_pk_bf16_f32 v25, v42, v43\n"
    "v_cvt_pk_bf16_f32 v26, v44, v45\n"
    "v_cvt_pk_bf16_f32 v27, v46, v47\n"
    "v_cvt_pk_bf16_f32 v28, v48, v49\n"
    "v_cvt_pk_bf16_f32 v29, v50, v51\n"
    "v_cvt_pk_bf16_f32 v30, v52, v53\n"
    "v_cvt_pk_bf16_f32 v31, v54, v55\n"
    "v_cvt_pk_bf16_f32 v32, v56, v57\n"
    "v_cvt_pk_bf16_f32 v33, v58, v59\n"
    "v_cvt_pk_bf16_f32 v34, v60, v61\n"
    "v_cvt_pk_bf16_f32 v35, v62, v63\n"
    "ds_write_b128 v21, v[24:27] offset:0\n"
    "ds_write_b128 v21, v[28:31] offset:4096\n"
    "ds_write_b128 v21, v[32:35] offset:8192\n"
    "s_waitcnt lgkmcnt(0)\n"
    "s_barrier\n"
    "ds_read_b128 v[24:27], v22 offset:0\n"
    "ds_read_b128 v[28:31], v22 offset:4096\n"
    "ds_read_b128 v[32:35], v22 offset:8192\n"
    "s_waitcnt lgkmcnt(2)\n"
    "buffer_store_dwordx4 v[24:27], v20, s[40:43], s90 offen offset:0 sc1\n"
    "s_add_u32 s90, s90, 8192\n"
    "s_waitcnt lgkmcnt(1)\n"
    "buffer_store_dwordx4 v[28:31], v20, s[40:43], s90 offen offset:0 sc1\n"
    "s_waitcnt lgkmcnt(0)\n"
    "buffer_store_dwordx4 v[32:35], v20, s[40:43], s89 offen offset:0 sc1\n"
    "s_waitcnt vmcnt(15)\n"
    "v_readfirstlane_b32 s96, v36\n"
    "v_min_u32 v19, 1, v0\n"
    "v_lshlrev_b32 v19, 31, v19\n"
    "s_add_u32 s96, s96, 1\n"
    "s_mul_i32 s97, s96, 0x9e3779b1\n"
    "s_and_b32 s97, s97, 0xfffffff0\n"
    "s_add_u32 s94, s97, 1\n"
    "v_mov_b32 v38, s96\n"
    "v_mov_b32 v39, s94\n"
    "s_and_b32 s86, %[bid], 7\n"
    "s_lshl_b32 s86, s86, 2\n"
    "s_lshr_b32 s86, 0x54321706, s86\n"
    "s_and_b32 s86, s86, 15\n"
    "s_cmp_ge_u32 s86, 6\n"
    "s_cselect_b64 s[64:65], s[38:39], s[64:65]\n"
    "s_cselect_b32 s86, 1536, 0\n"
    "s_cselect_b32 s87, 0, 6144\n"
    "s_add_u32 s86, s86, s87\n"
    "s_mul_i32 s66, s86, 1280\n"
    "s_and_b32 s49, s49, 0xffff\n"
    "s_mov_b32 s50, 1966080\n"
    "s_mov_b32 s51, 0x20000\n"
    "s_and_b32 s53, s53, 0xffff\n"
    "s_mov_b32 s54, 1966080\n"
    "s_mov_b32 s55, 0x20000\n"
    "s_and_b32 s86, s93, 1\n"
    "s_lshl_b32 s86, s86, 1\n"
    "v_add_u32 v72, 0, v16\n"
    "v_mul_u32_u24 v73, 171, v72\n"
    "v_lshrrev_b32 v73, 10, v73\n"
    "v_mul_u32_u24 v74, 6, v73\n"
    "v_sub_u32 v74, v72, v74\n"
    "v_add_u32 v74, s86, v74\n"
    "v_subrev_u32 v71, 6, v74\n"
    "v_min_u32 v74, v74, v71\n"
    "v_bfe_u32 v70, v73, 2, 2\n"
    "v_lshlrev_b32 v70, 3, v70\n"
    "v_lshrrev_b32 v71, 4, v73\n"
    "v_lshl_add_u32 v70, v71, 2, v70\n"
    "v_and_b32 v71, 3, v73\n"
    "v_add_u32 v70, v70, v71\n"
    "v_mul_u32_u24 v70, 3072, v70\n"
    "v_lshl_add_u32 v2, v74, 4, v70\n"
    "v_add_u32 v72, 64, v16\n"
    "v_mul_u32_u24 v73, 171, v72\n"
    "v_lshrrev_b32 v73, 10, v73\n"
    "v_mul_u32_u24 v74, 6, v73\n"
    "v_sub_u32 v74, v72, v74\n"
    "v_add_u32 v74, s86, v74\n"
    "v_subrev_u32 v71, 6, v74\n"
    "v_min_u32 v74, v74, v71\n"
    "v_bfe_u32 v70, v73, 2, 2\n"
    "v_lshlrev_b32 v70, 3, v70\n"
    "v_lshrrev_b32 v71, 4, v73\n"
    "v_lshl_add_u32 v70, v71, 2, v70\n"
    "v_and_b32 v71, 3, v73\n"
    "v_add_u32 v70, v70, v71\n"
    "v_mul_u32_u24 v70, 3072, v70\n"
    "v_lshl_add_u32 v3, v74, 4, v70\n"
    "v_add_u32 v72, 128, v16\n"
    "v_mul_u32_u24 v73, 171, v72\n"
    "v_lshrrev_b32 v73, 10, v73\n"
    "v_mul_u32_u24 v74, 6, v73\n"
    "v_sub_u32 v74, v72, v74\n"
    "v_add_u32 v74, s86, v74\n"
    "v_subrev_u32 v71, 6, v74\n"
    "v_min_u32 v74, v74, v71\n"
    "v_bfe_u32 v70, v73, 2, 2\n"
    "v_lshlrev_b32 v70, 3, v70\n"
    "v_lshrrev_b32 v71, 4, v73\n"
    "v_lshl_add_u32 v70, v71, 2, v70\n"
    "v_and_b32 v71, 3, v73\n"
    "v_add_u32 v70, v70, v71\n"
    "v_mul_u32_u24 v70, 3072, v70\n"
    "v_lshl_add_u32 v4, v74, 4, v70\n"
    "s_mul_i32 s78, s76, 3072\n"
    "s_add_u32 s78, s78, 49168\n"
    "s_mul_i32 s80, s76, 98304\n"
    "s_mul_i32 s86, s93, 96\n"
    "s_add_u32 s80, s80, s86\n"
    "v_lshrrev_b32 v72, 4, v16\n"
    "v_bfe_u32 v73, v16, 2, 2\n"
    "v_and_b32 v74, 3, v16\n"
    "v_lshrrev_b32 v70, 1, v72\n"
    "v_mul_u32_u24 v70, 12288, v70\n"
    "v_and_b32 v71, 1, v72\n"
    "v_mul_u32_u24 v71, 24584, v71\n"
    "v_add_u32 v70, v70, v71\n"
    "v_lshl_add_u32 v70, v73, 7, v70\n"
    "v_lshrrev_b32 v71, 1, v73\n"
    "v_lshl_add_u32 v70, v71, 6, v70\n"
    "v_lshl_add_u32 v5, v74, 4, v70\n"
    "v_xor_b32 v6, 64, v5\n"
    "v_lshl_add_u32 v70, v72, 2, v73\n"
    "v_mul_u32_u24 v70, 96, v70\n"
    "v_lshl_add_u32 v70, v74, 3, v70\n"
    "v_add_u32 v7, 49168, v70\n"
    "s_lshl_b32 s86, s37, 2\n"
    "s_add_u32 s86, s86, s76\n"
    "s_mul_i32 s87, s86, 81920\n"
    "s_mov_b32 s81, 0\n"
    "s_mul_i32 s81, s81, 20480\n"
    "s_add_u32 s81, s81, s87\n"
    "s_mov_b32 s3, 1\n"
    "s_mul_i32 s3, s3, 20480\n"
    "s_add_u32 s3, s3, s87\n"
    "s_mov_b32 s39, 2\n"
    "s_mul_i32 s39, s39, 20480\n"
    "s_add_u32 s39, s39, s87\n"
    "s_mov_b32 s99, 3\n"
    "s_mul_i32 s99, s99, 20480\n"
    "s_add_u32 s99, s99, s87\n"
    "s_waitcnt vmcnt(0)\n"
    "s_barrier\n"
    "buffer_store_dwordx2 v[38:39], v19, s[40:43], s36 offen offset:0 sc1\n"
    "buffer_load_dwordx2 v[40:41], v18, s[40:43], s91 offen offset:0 sc1\n"
    "s_add_u32 m0, s77, 3072\n"
    "s_add_u32 s88, s79, 1179648\n"
    "buffer_load_dwordx4 v1, s[44:47], s88 offen offset:0 lds\n"
    "s_add_u32 m0, s77, 7168\n"
    "s_add_u32 s88, s79, 1179776\n"
    "buffer_load_dwordx4 v1, s[44:47], s88 offen offset:0 lds\n"
    "s_add_u32 m0, s77, 11264\n"
    "s_add_u32 s88, s79, 1179904\n"
    "buffer_load_dwordx4 v1, s[44:47], s88 offen offset:0 lds\n"
    "s_add_u32 s94, s97, 1\n"
    "s_waitcnt vmcnt(3)\n"
    "v_xor_b32 v42, s96, v40\n"
    "v_subrev_u32 v43, s94, v41\n"
    "v_lshrrev_b32 v43, 4, v43\n"
    "v_or_b32 v42, v42, v43\n"
    "v_cmp_ne_u32 vcc, 0, v42\n"
    "s_nop 0\n"
    "s_cbranch_vccnz .L_spin7_%=\n"
    ".L_back7_%=:\n"
    "buffer_load_dwordx4 a[0:3], v0, s[40:43], s81 offen offset:0 sc1\n"
    "buffer_load_dwordx4 a[16:19], v0, s[40:43], s3 offen offset:0 sc1\n"
    "buffer_load_dwordx4 a[32:35], v0, s[40:43], s39 offen offset:0 sc1\n"
    "buffer_load_dwordx4 a[48:51], v0, s[40:43], s99 offen offset:0 sc1\n"
    ".L_q0_%=:\n"
    "s_barrier\n"
    "ds_read_b64_tr_b16 a[128:129], v5 offset:0\n"
    "ds_read_b64_tr_b16 a[130:131], v5 offset:512\n"
    "ds_read_b64_tr_b16 a[132:133], v5 offset:8\n"
    "ds_read_b64_tr_b16 a[134:135], v5 offset:520\n"
    "ds_read_b64_tr_b16 a[136:137], v6 offset:0\n"
    "ds_read_b64_tr_b16 a[138:139], v6 offset:512\n"
    "s_waitcnt vmcnt(0) lgkmcnt(4)\n"
    "v_mfma_f32_16x16x32_bf16 v[80:83], a[128:131], a[0:3], 0\n"
    "buffer_load_dwordx4 a[4:7], v0, s[40:43], s81 offen offset:1024 sc1\n"
    "v_mfma_f32_16x16x32_bf16 v[84:87], a[128:131], a[16:19], 0\n"
    "ds_read_b64_tr_b16 a[140:141], v6 offset:8\n"
    "ds_read_b64_tr_b16 a[142:143], v6 offset:520\n"
    "v_mfma_f32_16x16x32_bf16 v[88:91], a[128:131], a[32:35], 0\n"
    "v_mfma_f32_16x16x32_bf16 v[92:95], a[128:131], a[48:51], 0\n"
    "s_waitcnt lgkmcnt(4)\n"
    "v_mfma_f32_16x16x32_bf16 v[96:99], a[132:135], a[0:3], 0\n"
    "buffer_load_dwordx4 a[20:23], v0, s[40:43], s3 offen offset:1024 sc1\n"
    "v_mfma_f32_16x16x32_bf16 v[100:103], a[132:135], a[16:19], 0\n"
    "ds_read_b64_tr_b16 a[144:145], v5 offset:4096\n"
    "ds_read_b64_tr_b16 a[146:147], v5 offset:4608\n"
    "v_mfma_f32_16x16x32_bf16 v[104:107], a[132:135], a[32:35], 0\n"
    "v_mfma_f32_16x16x32_bf16 v[108:111], a[132:135], a[48:51], 0\n"
    "s_waitcnt lgkmcnt(4)\n"
    "v_mfma_f32_16x16x32_bf16 a[160:163], a[136:139], a[0:3], 0\n"
    "buffer_load_dwordx4 a[36:39], v0, s[40:43], s39 offen offset:1024 sc1\n"
    "v_mfma_f32_16x16x32_bf16 a[164:167], a[136:139], a[16:19], 0\n"
    "ds_read_b64_tr_b16 a[148:149], v5 offset:4104\n"
    "ds_read_b64_tr_b16 a[150:151], v5 offset:4616\n"
    "v_mfma_f32_16x16x32_bf16 a[168:171], a[136:139], a[32:35], 0\n"
    "v_mfma_f32_16x16x32_bf16 a[172:175], a[136:139], a[48:51], 0\n"
    "s_waitcnt lgkmcnt(4)\n"
    "v_mfma_f32_16x16x32_bf16 a[176:179], a[140:143], a[0:3], 0\n"
    "buffer_load_dwordx4 a[52:55], v0, s[40:43], s99 offen offset:1024 sc1\n"
    "v_mfma_f32_16x16x32_bf16 a[180:183], a[140:143], a[16:19], 0\n"
    "ds_read_b64_tr_b16 a[152:153], v6 offset:4096\n"
    "ds_read_b64_tr_b16 a[154:155], v6 offset:4608\n"
    "v_mfma_f32_16x16x32_bf16 a[184:187], a[140:143], a[32:35], 0\n"
    "v_mfma_f32_16x16x32_bf16 a[188:191], a[140:143], a[48:51], 0\n"
    "s_waitcnt lgkmcnt(4)\n"
    "v_mfma_f32_16x16x32_bf16 a[192:195], a[144:147], a[0:3], 0\n"
    "buffer_load_dwordx4 a[8:11], v0, s[40:43], s81 offen offset:2048 sc1\n"
    "v_mfma_f32_16x16x32_bf16 a[196:199], a[144:147], a[16:19], 0\n"
    "ds_read_b64_tr_b16 a[156:157], v6 offset:4104\n"
    "ds_read_b64_tr_b16 a[158:159], v6 offset:4616\n"
    "v_mfma_f32_16x16x32_bf16 a[200:203], a[144:147], a[32:35], 0\n"
    "v_mfma_f32_16x16x32_bf16 a[204:207], a[144:147], a[48:51], 0\n"
    "s_waitcnt lgkmcnt(4)\n"
    "v_mfma_f32_16x16x32_bf16 v[112:115], a[148:151], a[0:3], 0\n"
    "buffer_load_dwordx4 a[24:27], v0, s[40:43], s3 offen offset:2048 sc1\n"
    "v_mfma_f32_16x16x32_bf16 v[116:119], a[148:151], a[16:19], 0\n"
    "ds_read_b64_tr_b16 a[128:129], v5 offset:8192\n"
    "ds_read_b64_tr_b16 a[130:131], v5 offset:8704\n"
    "v_mfma_f32_16x16x32_bf16 v[120:123], a[148:151], a[32:35], 0\n"
    "v_mfma_f32_16x16x32_bf16 v[124:127], a[148:151], a[48:51], 0\n"
    "s_waitcnt lgkmcnt(4)\n"
    "v_mfma_f32_16x16x32_bf16 v[128:131], a[152:155], a[0:3], 0\n"
    "ds_read_b64_tr_b16 a[132:133], v5 offset:8200\n"
    "ds_read_b64_tr_b16 a[134:135], v5 offset:8712\n"
    "v_mfma_f32_16x16x32_bf16 v[132:135], a[152:155], a[16:19], 0\n"
    "buffer_load_dwordx4 a[40:43], v0, s[40:43], s39 offen offset:2048 sc1\n"
    "v_mfma_f32_16x16x32_bf16 v[136:139], a[152:155], a[32:35], 0\n"
    "v_mfma_f32_16x16x32_bf16 v[140:143], a[152:155], a[48:51], 0\n"
    "s_waitcnt lgkmcnt(4)\n"
    "v_mfma_f32_16x16x32_bf16 v[144:147], a[156:159], a[0:3], 0\n"
    "ds_read_b64_tr_b16 a[136:137], v6 offset:8192\n"
    "ds_read_b64_tr_b16 a[138:139], v6 offset:8704\n"
    "v_mfma_f32_16x16x32_bf16 v[148:151], a[156:159], a[16:19], 0\n"
    "buffer_load_dwordx4 a[56:59], v0, s[40:43], s99 offen offset:2048 sc1\n"
    "v_mfma_f32_16x16x32_bf16 v[152:155], a[156:159], a[32:35], 0\n"
    "v_mfma_f32_16x16x32_bf16 v[156:159], a[156:159], a[48:51], 0\n"
    "s_waitcnt lgkmcnt(4)\n"
    "v_mfma_f32_16x16x32_bf16 v[160:163], a[128:131], a[0:3], 0\n"
    "ds_read_b64_tr_b16 a[140:141], v6 offset:8200\n"
    "ds_read_b64_tr_b16 a[142:143], v6 offset:8712\n"
    "v_mfma_f32_16x16x32_bf16 v[164:167], a[128:131], a[16:19], 0\n"
    "buffer_load_dwordx4 a[12:15], v0, s[40:43], s81 offen offset:3072 sc1\n"
    "v_mfma_f32_16x16x32_bf16 v[168:171], a[128:131], a[32:35], 0\n"
    "v_mfma_f32_16x16x32_bf16 v[172:175], a[128:131], a[48:51], 0\n"
    "s_waitcnt lgkmcnt(4)\n"
    "v_mfma_f32_16x16x32_bf16 v[176:179], a[132:135], a[0:3], 0\n"
    "ds_read_b64_tr_b16 a[144:145], v5 offset:1024\n"
    "ds_read_b64_tr_b16 a[146:147], v5 offset:1536\n"
    "v_mfma_f32_16x16x32_bf16 v[180:183], a[132:135], a[16:19], 0\n"
    "buffer_load_dwordx4 a[28:31], v0, s[40:43], s3 offen offset:3072 sc1\n"
    "v_mfma_f32_16x16x32_bf16 v[184:187], a[132:135], a[32:35], 0\n"
    "v_mfma_f32_16x16x32_bf16 v[188:191], a[132:135], a[48:51], 0\n"
    "s_waitcnt lgkmcnt(4)\n"
    "v_mfma_f32_16x16x32_bf16 v[192:195], a[136:139], a[0:3], 0\n"
    "ds_read_b64_tr_b16 a[148:149], v5 offset:1032\n"
    "ds_read_b64_tr_b16 a[150:151], v5 offset:1544\n"
    "v_mfma_f32_16x16x32_bf16 v[196:199], a[136:139], a[16:19], 0\n"
    "buffer_load_dwordx4 a[44:47], v0, s[40:43], s39 offen offset:3072 sc1\n"
    "v_mfma_f32_16x16x32_bf16 v[200:203], a[136:139], a[32:35], 0\n"
    "v_mfma_f32_16x16x32_bf16 v[204:207], a[136:139], a[48:51], 0\n"
    "s_waitcnt lgkmcnt(4)\n"
    "v_mfma_f32_16x16x32_bf16 v[208:211], a[140:143], a[0:3], 0\n"
    "ds_read_b64_tr_b16 a[152:153], v6 offset:1024\n"
    "ds_read_b64_tr_b16 a[154:155], v6 offset:1536\n"
    "v_mfma_f32_16x16x32_bf16 v[212:215], a[140:143], a[16:19], 0\n"
    "buffer_load_dwordx4 a[60:63], v0, s[40:43], s99 offen offset:3072 sc1\n"
    "v_mfma_f32_16x16x32_bf16 v[216:219], a[140:143], a[32:35], 0\n"
    "v_mfma_f32_16x16x32_bf16 v[220:223], a[140:143], a[48:51], 0\n"
    "s_waitcnt vmcnt(8) lgkmcnt(4)\n"
    "v_mfma_f32_16x16x32_bf16 v[80:83], a[144:147], a[4:7], v[80:83]\n"
    "ds_read_b64_tr_b16 a[156:157], v6 offset:1032\n"
    "ds_read_b64_tr_b16 a[158:159], v6 offset:1544\n"
    "v_mfma_f32_16x16x32_bf16 v[84:87], a[144:147], a[20:23], v[84:87]\n"
    "v_mfma_f32_16x16x32_bf16 v[88:91], a[144:147], a[36:39], v[88:91]\n"
    "v_mfma_f32_16x16x32_bf16 v[92:95], a[144:147], a[52:55], v[92:95]\n"
    "s_waitcnt lgkmcnt(4)\n"
    "s_add_u32 s8, s8, s9\n"
    "v_mfma_f32_16x16x32_bf16 v[96:99], a[148:151], a[4:7], v[96:99]\n"
    "ds_read_b64_tr_b16 a[128:129], v5 offset:5120\n"
    "ds_read_b64_tr_b16 a[130:131], v5 offset:5632\n"
    "v_mfma_f32_16x16x32_bf16 v[100:103], a[148:151], a[20:23], v[100:103]\n"
    "buffer_load_dword a247, v14, s[4:7], s8 offen offset:0\n"
    "v_mfma_f32_16x16x32_bf16 v[104:107], a[148:151], a[36:39], v[104:107]\n"
    "s_add_u32 m0, s78, 0\n"
    "v_mfma_f32_16x16x32_bf16 v[108:111], a[148:151], a[52:55], v[108:111]\n"
    "s_waitcnt lgkmcnt(4)\n"
    "v_mfma_f32_16x16x32_bf16 a[160:163], a[152:155], a[4:7], a[160:163]\n"
    "ds_read_b64_tr_b16 a[132:133], v5 offset:5128\n"
    "ds_read_b64_tr_b16 a[134:135], v5 offset:5640\n"
    "v_mfma_f32_16x16x32_bf16 a[164:167], a[152:155], a[20:23], a[164:167]\n"
    "s_add_u32 s88, s80, 0\n"
    "v_mfma_f32_16x16x32_bf16 a[168:171], a[152:155], a[36:39], a[168:171]\n"
    "v_mfma_f32_16x16x32_bf16 a[172:175], a[152:155], a[52:55], a[172:175]\n"
    "s_waitcnt lgkmcnt(4)\n"
    "v_mfma_f32_16x16x32_bf16 a[176:179], a[156:159], a[4:7], a[176:179]\n"
    "ds_read_b64_tr_b16 a[136:137], v6 offset:5120\n"
    "ds_read_b64_tr_b16 a[138:139], v6 offset:5632\n"
    "v_mfma_f32_16x16x32_bf16 a[180:183], a[156:159], a[20:23], a[180:183]\n"
    "buffer_load_dwordx4 v2, s[48:51], s88 offen offset:0 lds\n"
    "v_mfma_f32_16x16x32_bf16 a[184:187], a[156:159], a[36:39], a[184:187]\n"
    "v_mfma_f32_16x16x32_bf16 a[188:191], a[156:159], a[52:55], a[188:191]\n"
    "s_waitcnt lgkmcnt(4)\n"
    "s_add_u32 m0, s78, 1024\n"
    "v_mfma_f32_16x16x32_bf16 a[192:195], a[128:131], a[4:7], a[192:195]\n"
    "ds_read_b64_tr_b16 a[140:141], v6 offset:5128\n"
    "ds_read_b64_tr_b16 a[142:143], v6 offset:5640\n"
    "v_mfma_f32_16x16x32_bf16 a[196:199], a[128:131], a[20:23], a[196:199]\n"
    "v_mfma_f32_16x16x32_bf16 a[200:203], a[128:131], a[36:39], a[200:203]\n"
    "s_add_u32 s88, s80, 0\n"
    "v_mfma_f32_16x16x32_bf16 a[204:207], a[128:131], a[52:55], a[204:207]\n"
    "s_waitcnt lgkmcnt(4)\n"
    "v_mfma_f32_16x16x32_bf16 v[112:115], a[132:135], a[4:7], v[112:115]\n"
    "ds_read_b64_tr_b16 a[144:145], v5 offset:9216\n"
    "ds_read_b64_tr_b16 a[146:147], v5 offset:9728\n"
    "v_mfma_f32_16x16x32_bf16 v[116:119], a[132:135], a[20:23], v[116:119]\n"
    "buffer_load_dwordx4 v3, s[48:51], s88 offen offset:0 lds\n"
    "v_mfma_f32_16x16x32_bf16 v[120:123], a[132:135], a[36:39], v[120:123]\n"
    "v_mfma_f32_16x16x32_bf16 v[124:127], a[132:135], a[52:55], v[124:127]\n"
    "s_waitcnt lgkmcnt(4)\n"
    "v_mfma_f32_16x16x32_bf16 v[128:131], a[136:139], a[4:7], v[128:131]\n"
    "s_add_u32 m0, s78, 2048\n"
    "ds_read_b64_tr_b16 a[148:149], v5 offset:9224\n"
    "ds_read_b64_tr_b16 a[150:151], v5 offset:9736\n"
    "v_mfma_f32_16x16x32_bf16 v[132:135], a[136:139], a[20:23], v[132:135]\n"
    "v_mfma_f32_16x16x32_bf16 v[136:139], a[136:139], a[36:39], v[136:139]\n"
    "v_mfma_f32_16x16x32_bf16 v[140:143], a[136:139], a[52:55], v[140:143]\n"
    "s_waitcnt lgkmcnt(4)\n"
    "s_add_u32 s88, s80, 0\n"
    "v_mfma_f32_16x16x32_bf16 v[144:147], a[140:143], a[4:7], v[144:147]\n"
    "ds_read_b64_tr_b16 a[152:153], v6 offset:9216\n"
    "ds_read_b64_tr_b16 a[154:155], v6 offset:9728\n"
    "v_mfma_f32_16x16x32_bf16 v[148:151], a[140:143], a[20:23], v[148:151]\n"
    "v_mfma_f32_16x16x32_bf16 v[152:155], a[140:143], a[36:39], v[152:155]\n"
    "buffer_load_dwordx4 v4, s[48:51], s88 offen offset:0 lds\n"
    "v_mfma_f32_16x16x32_bf16 v[156:159], a[140:143], a[52:55], v[156:159]\n"
    "s_waitcnt lgkmcnt(4)\n"
    "v_mfma_f32_16x16x32_bf16 v[160:163], a[144:147], a[4:7], v[160:163]\n"
    "ds_read_b64_tr_b16 a[156:157], v6 offset:9224\n"
    "ds_read_b64_tr_b16 a[158:159], v6 offset:9736\n"
    "v_mfma_f32_16x16x32_bf16 v[164:167], a[144:147], a[20:23], v[164:167]\n"
    "v_mfma_f32_16x16x32_bf16 v[168:171], a[144:147], a[36:39], v[168:171]\n"
    "s_add_u32 m0, s78, 12288\n"
    "v_mfma_f32_16x16x32_bf16 v[172:175], a[144:147], a[52:55], v[172:175]\n"
    "s_waitcnt lgkmcnt(4)\n"
    "v_mfma_f32_16x16x32_bf16 v[176:179], a[148:151], a[4:7], v[176:179]\n"
    "s_barrier\n"
    "ds_read_b64_tr_b16 a[128:129], v5 offset:2048\n"
    "v_mfma_f32_16x16x32_bf16 v[180:183], a[148:151], a[20:23], v[180:183]\n"
    "s_add_u32 s88, s80, 0\n"
    "ds_read_b64_tr_b16 a[130:131], v5 offset:2560\n"
    "v_mfma_f32_16x16x32_bf16 v[184:187], a[148:151], a[36:39], v[184:187]\n"
    "v_mfma_f32_16x16x32_bf16 v[188:191], a[148:151], a[52:55], v[188:191]\n"
    "s_waitcnt lgkmcnt(4)\n"
    "v_mfma_f32_16x16x32_bf16 v[192:195], a[152:155], a[4:7], v[192:195]\n"
    "ds_read_b64_tr_b16 a[132:133], v5 offset:2056\n"
    "ds_read_b64_tr_b16 a[134:135], v5 offset:2568\n"
    "v_mfma_f32_16x16x32_bf16 v[196:199], a[152:155], a[20:23], v[196:199]\n"
    "buffer_load_dwordx4 v2, s[52:55], s88 offen offset:0 lds\n"
    "v_mfma_f32_16x16x32_bf16 v[200:203], a[152:155], a[36:39], v[200:203]\n"
    "v_mfma_f32_16x16x32_bf16 v[204:207], a[152:155], a[52:55], v[204:207]\n"
    "s_waitcnt lgkmcnt(4)\n"
    "s_add_u32 m0, s78, 13312\n"
    "v_mfma_f32_16x16x32_bf16 v[208:211], a[156:159], a[4:7], v[208:211]\n"
    "ds_read_b64_tr_b16 a[136:137], v6 offset:2048\n"
    "ds_read_b64_tr_b16 a[138:139], v6 offset:2560\n"
    "v_mfma_f32_16x16x32_bf16 v[212:215], a[156:159], a[20:23], v[212:215]\n"
    "v_mfma_f32_16x16x32_bf16 v[216:219], a[156:159], a[36:39], v[216:219]\n"
    "s_add_u32 s88, s80, 0\n"
    "v_mfma_f32_16x16x32_bf16 v[220:223], a[156:159], a[52:55], v[220:223]\n"
    "s_waitcnt vmcnt(9) lgkmcnt(4)\n"
    "v_mfma_f32_16x16x32_bf16 v[80:83], a[128:131], a[8:11], v[80:83]\n"
    "ds_read_b64_tr_b16 a[140:141], v6 offset:2056\n"
    "ds_read_b64_tr_b16 a[142:143], v6 offset:2568\n"
    "v_mfma_f32_16x16x32_bf16 v[84:87], a[128:131], a[24:27], v[84:87]\n"
    "buffer_load_dwordx4 v3, s[52:55], s88 offen offset:0 lds\n"
    "v_mfma_f32_16x16x32_bf16 v[88:91], a[128:131], a[40:43], v[88:91]\n"
    "v_mfma_f32_16x16x32_bf16 v[92:95], a[128:131], a[56:59], v[92:95]\n"
    "s_waitcnt lgkmcnt(4)\n"
    "v_mfma_f32_16x16x32_bf16 v[96:99], a[132:135], a[8:11], v[96:99]\n"
    "s_add_u32 m0, s78, 14336\n"
    "ds_read_b64_tr_b16 a[144:145], v5 offset:6144\n"
    "ds_read_b64_tr_b16 a[146:147], v5 offset:6656\n"
    "v_mfma_f32_16x16x32_bf16 v[100:103], a[132:135], a[24:27], v[100:103]\n"
    "v_add_u32 v8, 73744, v5\n"
    "v_add_u32 v9, 73744, v6\n"
    "v_mfma_f32_16x16x32_bf16 v[104:107], a[132:135], a[40:43], v[104:107]\n"
    "v_add_u32 v10, 73744, v7\n"
    "v_mfma_f32_16x16x32_bf16 v[108:111], a[132:135], a[56:59], v[108:111]\n"
    "s_waitcnt lgkmcnt(4)\n"
    "s_add_u32 s88, s80, 0\n"
    "v_mfma_f32_16x16x32_bf16 a[160:163], a[136:139], a[8:11], a[160:163]\n"
    "ds_read_b64_tr_b16 a[148:149], v5 offset:6152\n"
    "ds_read_b64_tr_b16 a[150:151], v5 offset:6664\n"
    "v_mfma_f32_16x16x32_bf16 a[164:167], a[136:139], a[24:27], a[164:167]\n"
    "v_mfma_f32_16x16x32_bf16 a[168:171], a[136:139], a[40:43], a[168:171]\n"
    "buffer_load_dwordx4 v4, s[52:55], s88 offen offset:0 lds\n"
    "v_mfma_f32_16x16x32_bf16 a[172:175], a[136:139], a[56:59], a[172:175]\n"
    "s_waitcnt lgkmcnt(4)\n"
    "v_mfma_f32_16x16x32_bf16 a[176:179], a[140:143], a[8:11], a[176:179]\n"
    "ds_read_b64_tr_b16 a[152:153], v6 offset:6144\n"
    "ds_read_b64_tr_b16 a[154:155], v6 offset:6656\n"
    "v_mfma_f32_16x16x32_bf16 a[180:183], a[140:143], a[24:27], a[180:183]\n"
    "v_mfma_f32_16x16x32_bf16 a[184:187], a[140:143], a[40:43], a[184:187]\n"
    "v_mfma_f32_16x16x32_bf16 a[188:191], a[140:143], a[56:59], a[188:191]\n"
    "s_waitcnt lgkmcnt(4)\n"
    "v_mfma_f32_16x16x32_bf16 a[192:195], a[144:147], a[8:11], a[192:195]\n"
    "ds_read_b64_tr_b16 a[156:157], v6 offset:6152\n"
    "ds_read_b64_tr_b16 a[158:159], v6 offset:6664\n"
    "v_mfma_f32_16x16x32_bf16 a[196:199], a[144:147], a[24:27], a[196:199]\n"
    "v_mfma_f32_16x16x32_bf16 a[200:203], a[144:147], a[40:43], a[200:203]\n"
    "s_add_u32 s89, s81, 4096\n"
    "v_mfma_f32_16x16x32_bf16 a[204:207], a[144:147], a[56:59], a[204:207]\n"
    "s_waitcnt lgkmcnt(4)\n"
    "v_mfma_f32_16x16x32_bf16 v[112:115], a[148:151], a[8:11], v[112:115]\n"
    "ds_read_b64_tr_b16 a[128:129], v5 offset:10240\n"
    "ds_read_b64_tr_b16 a[130:131], v5 offset:10752\n"
    "v_mfma_f32_16x16x32_bf16 v[116:119], a[148:151], a[24:27], v[116:119]\n"
    "buffer_load_dwordx4 a[64:67], v0, s[40:43], s89 offen offset:0 sc1\n"
    "v_mfma_f32_16x16x32_bf16 v[120:123], a[148:151], a[40:43], v[120:123]\n"
    "v_mfma_f32_16x16x32_bf16 v[124:127], a[148:151], a[56:59], v[124:127]\n"
    "s_waitcnt lgkmcnt(4)\n"
    "v_mfma_f32_16x16x32_bf16 v[128:131], a[152:155], a[8:11], v[128:131]\n"
    "buffer_load_dwordx4 a[68:71], v0, s[40:43], s89 offen offset:1024 sc1\n"
    "v_mfma_f32_16x16x32_bf16 v[132:135], a[152:155], a[24:27], v[132:135]\n"
    "ds_read_b64_tr_b16 a[132:133], v5 offset:10248\n"
    "ds_read_b64_tr_b16 a[134:135], v5 offset:10760\n"
    "v_mfma_f32_16x16x32_bf16 v[136:139], a[152:155], a[40:43], v[136:139]\n"
    "buffer_load_dwordx4 a[72:75], v0, s[40:43], s89 offen offset:2048 sc1\n"
    "v_mfma_f32_16x16x32_bf16 v[140:143], a[152:155], a[56:59], v[140:143]\n"
    "s_waitcnt lgkmcnt(4)\n"
    "v_mfma_f32_16x16x32_bf16 v[144:147], a[156:159], a[8:11], v[144:147]\n"
    "ds_read_b64_tr_b16 a[136:137], v6 offset:10240\n"
    "ds_read_b64_tr_b16 a[138:139], v6 offset:10752\n"
    "v_mfma_f32_16x16x32_bf16 v[148:151], a[156:159], a[24:27], v[148:151]\n"
    "buffer_load_dwordx4 a[76:79], v0, s[40:43], s89 offen offset:3072 sc1\n"
    "v_mfma_f32_16x16x32_bf16 v[152:155], a[156:159], a[40:43], v[152:155]\n"
    "v_mfma_f32_16x16x32_bf16 v[156:159], a[156:159], a[56:59], v[156:159]\n"
    "s_waitcnt lgkmcnt(4)\n"
    "s_add_u32 s89, s3, 4096\n"
    "v_mfma_f32_16x16x32_bf16 v[160:163], a[128:131], a[8:11], v[160:163]\n"
    "ds_read_b64_tr_b16 a[140:141], v6 offset:10248\n"
    "ds_read_b64_tr_b16 a[142:143], v6 offset:10760\n"
    "v_mfma_f32_16x16x32_bf16 v[164:167], a[128:131], a[24:27], v[164:167]\n"
    "buffer_load_dwordx4 a[80:83], v0, s[40:43], s89 offen offset:0 sc1\n"
    "v_mfma_f32_16x16x32_bf16 v[168:171], a[128:131], a[40:43], v[168:171]\n"
    "v_mfma_f32_16x16x32_bf16 v[172:175], a[128:131], a[56:59], v[172:175]\n"
    "s_waitcnt lgkmcnt(4)\n"
    "v_mfma_f32_16x16x32_bf16 v[176:179], a[132:135], a[8:11], v[176:179]\n"
    "s_barrier\n"
    "ds_read_b64_tr_b16 a[144:145], v5 offset:3072\n"
    "v_mfma_f32_16x16x32_bf16 v[180:183], a[132:135], a[24:27], v[180:183]\n"
    "buffer_load_dwordx4 a[84:87], v0, s[40:43], s89 offen offset:1024 sc1\n"
    "v_mfma_f32_16x16x32_bf16 v[184:187], a[132:135], a[40:43], v[184:187]\n"
    "ds_read_b64_tr_b16 a[146:147], v5 offset:3584\n"
    "v_mfma_f32_16x16x32_bf16 v[188:191], a[132:135], a[56:59], v[188:191]\n"
    "s_waitcnt lgkmcnt(4)\n"
    "v_mfma_f32_16x16x32_bf16 v[192:195], a[136:139], a[8:11], v[192:195]\n"
    "buffer_load_dwordx4 a[88:91], v0, s[40:43], s89 offen offset:2048 sc1\n"
    "v_mfma_f32_16x16x32_bf16 v[196:199], a[136:139], a[24:27], v[196:199]\n"
    "ds_read_b64_tr_b16 a[148:149], v5 offset:3080\n"
    "ds_read_b64_tr_b16 a[150:151], v5 offset:3592\n"
    "v_mfma_f32_16x16x32_bf16 v[200:203], a[136:139], a[40:43], v[200:203]\n"
    "buffer_load_dwordx4 a[92:95], v0, s[40:43], s89 offen offset:3072 sc1\n"
    "v_mfma_f32_16x16x32_bf16 v[204:207], a[136:139], a[56:59], v[204:207]\n"
    "s_waitcnt lgkmcnt(4)\n"
    "v_mfma_f32_16x16x32_bf16 v[208:211], a[140:143], a[8:11], v[208:211]\n"
    "s_add_u32 s89, s39, 4096\n"
    "ds_read_b64_tr_b16 a[152:153], v6 offset:3072\n"
    "ds_read_b64_tr_b16 a[154:155], v6 offset:3584\n"
    "v_mfma_f32_16x16x32_bf16 v[212:215], a[140:143], a[24:27], v[212:215]\n"
    "v_mfma_f32_16x16x32_bf16 v[216:219], a[140:143], a[40:43], v[216:219]\n"
    "v_mfma_f32_16x16x32_bf16 v[220:223], a[140:143], a[56:59], v[220:223]\n"
    "s_waitcnt vmcnt(15) lgkmcnt(4)\n"
    "v_mfma_f32_16x16x32_bf16 v[80:83], a[144:147], a[12:15], v[80:83]\n"
    "buffer_load_dwordx4 a[96:99], v0, s[40:43], s89 offen offset:0 sc1\n"
    "v_mfma_f32_16x16x32_bf16 v[84:87], a[144:147], a[28:31], v[84:87]\n"
    "ds_read_b64_tr_b16 a[156:157], v6 offset:3080\n"
    "ds_read_b64_tr_b16 a[158:159], v6 offset:3592\n"
    "v_mfma_f32_16x16x32_bf16 v[88:91], a[144:147], a[44:47], v[88:91]\n"
    "buffer_load_dwordx4 a[100:103], v0, s[40:43], s89 offen offset:1024 sc1\n"
    "v_mfma_f32_16x16x32_bf16 v[92:95], a[144:147], a[60:63], v[92:95]\n"
    "s_waitcnt lgkmcnt(4)\n"
    "v_mfma_f32_16x16x32_bf16 v[96:99], a[148:151], a[12:15], v[96:99]\n"
    "ds_read_b64_tr_b16 a[128:129], v5 offset:7168\n"
    "ds_read_b64_tr_b16 a[130:131], v5 offset:7680\n"
    "v_mfma_f32_16x16x32_bf16 v[100:103], a[148:151], a[28:31], v[100:103]\n"
    "buffer_load_dwordx4 a[104:107], v0, s[40:43], s89 offen offset:2048 sc1\n"
    "v_mfma_f32_16x16x32_bf16 v[104:107], a[148:151], a[44:47], v[104:107]\n"
    "v_mfma_f32_16x16x32_bf16 v[108:111], a[148:151], a[60:63], v[108:111]\n"
    "s_waitcnt lgkmcnt(4)\n"
    "v_mfma_f32_16x16x32_bf16 a[160:163], a[152:155], a[12:15], a[160:163]\n"
    "buffer_load_dwordx4 a[108:111], v0, s[40:43], s89 offen offset:3072 sc1\n"
    "v_mfma_f32_16x16x32_bf16 a[164:167], a[152:155], a[28:31], a[164:167]\n"
    "s_add_u32 m0, s77, 73744\n"
    "ds_read_b64_tr_b16 a[132:133], v5 offset:7176\n"
    "ds_read_b64_tr_b16 a[134:135], v5 offset:7688\n"
    "v_mfma_f32_16x16x32_bf16 a[168:171], a[152:155], a[44:47], a[168:171]\n"
    "v_mfma_f32_16x16x32_bf16 a[172:175], a[152:155], a[60:63], a[172:175]\n"
    "s_waitcnt lgkmcnt(4)\n"
    "v_mfma_f32_16x16x32_bf16 a[176:179], a[156:159], a[12:15], a[176:179]\n"
    "s_add_u32 s88, s79, 1572864\n"
    "ds_read_b64_tr_b16 a[136:137], v6 offset:7168\n"
    "ds_read_b64_tr_b16 a[138:139], v6 offset:7680\n"
    "v_mfma_f32_16x16x32_bf16 a[180:183], a[156:159], a[28:31], a[180:183]\n"
    "v_mfma_f32_16x16x32_bf16 a[184:187], a[156:159], a[44:47], a[184:187]\n"
    "buffer_load_dwordx4 v1, s[44:47], s88 offen offset:0 lds\n"
    "v_mfma_f32_16x16x32_bf16 a[188:191], a[156:159], a[60:63], a[188:191]\n"
    "s_waitcnt lgkmcnt(4)\n"
    "v_mfma_f32_16x16x32_bf16 a[192:195], a[128:131], a[12:15], a[192:195]\n"
    "ds_read_b64_tr_b16 a[140:141], v6 offset:7176\n"
    "ds_read_b64_tr_b16 a[142:143], v6 offset:7688\n"
    "v_mfma_f32_16x16x32_bf16 a[196:199], a[128:131], a[28:31], a[196:199]\n"
    "s_add_u32 m0, s77, 77840\n"
    "v_mfma_f32_16x16x32_bf16 a[200:203], a[128:131], a[44:47], a[200:203]\n"
    "v_mfma_f32_16x16x32_bf16 a[204:207], a[128:131], a[60:63], a[204:207]\n"
    "s_waitcnt lgkmcnt(4)\n"
    "v_mfma_f32_16x16x32_bf16 v[112:115], a[132:135], a[12:15], v[112:115]\n"
    "s_add_u32 s88, s79, 1572992\n"
    "ds_read_b64_tr_b16 a[144:145], v5 offset:11264\n"
    "ds_read_b64_tr_b16 a[146:147], v5 offset:11776\n"
    "v_mfma_f32_16x16x32_bf16 v[116:119], a[132:135], a[28:31], v[116:119]\n"
    "v_mfma_f32_16x16x32_bf16 v[120:123], a[132:135], a[44:47], v[120:123]\n"
    "buffer_load_dwordx4 v1, s[44:47], s88 offen offset:0 lds\n"
    "v_mfma_f32_16x16x32_bf16 v[124:127], a[132:135], a[60:63], v[124:127]\n"
    "s_waitcnt lgkmcnt(4)\n"
    "v_mfma_f32_16x16x32_bf16 v[128:131], a[136:139], a[12:15], v[128:131]\n"
    "ds_read_b64_tr_b16 a[148:149], v5 offset:11272\n"
    "ds_read_b64_tr_b16 a[150:151], v5 offset:11784\n"
    "v_mfma_f32_16x16x32_bf16 v[132:135], a[136:139], a[28:31], v[132:135]\n"
    "s_add_u32 m0, s77, 81936\n"
    "v_mfma_f32_16x16x32_bf16 v[136:139], a[136:139], a[44:47], v[136:139]\n"
    "v_mfma_f32_16x16x32_bf16 v[140:143], a[136:139], a[60:63], v[140:143]\n"
    "s_waitcnt lgkmcnt(4)\n"
    "v_mfma_f32_16x16x32_bf16 v[144:147], a[140:143], a[12:15], v[144:147]\n"
    "s_add_u32 s88, s79, 1573120\n"
    "ds_read_b64_tr_b16 a[152:153], v6 offset:11264\n"
    "ds_read_b64_tr_b16 a[154:155], v6 offset:11776\n"
    "v_mfma_f32_16x16x32_bf16 v[148:151], a[140:143], a[28:31], v[148:151]\n"
    "v_mfma_f32_16x16x32_bf16 v[152:155], a[140:143], a[44:47], v[152:155]\n"
    "buffer_load_dwordx4 v1, s[44:47], s88 offen offset:0 lds\n"
    "v_mfma_f32_16x16x32_bf16 v[156:159], a[140:143], a[60:63], v[156:159]\n"
    "s_waitcnt lgkmcnt(4)\n"
    "v_mfma_f32_16x16x32_bf16 v[160:163], a[144:147], a[12:15], v[160:163]\n"
    "ds_read_b64_tr_b16 a[156:157], v6 offset:11272\n"
    "ds_read_b64_tr_b16 a[158:159], v6 offset:11784\n"
    "v_mfma_f32_16x16x32_bf16 v[164:167], a[144:147], a[28:31], v[164:167]\n"
    "s_add_u32 m0, s77, 74768\n"
    "v_mfma_f32_16x16x32_bf16 v[168:171], a[144:147], a[44:47], v[168:171]\n"
    "v_mfma_f32_16x16x32_bf16 v[172:175], a[144:147], a[60:63], v[172:175]\n"
    "s_waitcnt lgkmcnt(4)\n"
    "v_mfma_f32_16x16x32_bf16 v[176:179], a[148:151], a[12:15], v[176:179]\n"
    "s_waitcnt vmcnt(15)\n"
    "s_barrier\n"
    "ds_read_b64_tr_b16 a[128:129], v7 offset:0\n"
    "v_mfma_f32_16x16x32_bf16 v[180:183], a[148:151], a[28:31], v[180:183]\n"
    "s_add_u32 s88, s79, 1966080\n"
    "ds_read_b64_tr_b16 a[130:131], v7 offset:1536\n"
    "v_mfma_f32_16x16x32_bf16 v[184:187], a[148:151], a[44:47], v[184:187]\n"
    "buffer_load_dwordx4 v1, s[44:47], s88 offen offset:0 lds\n"
    "v_mfma_f32_16x16x32_bf16 v[188:191], a[148:151], a[60:63], v[188:191]\n"
    "s_waitcnt lgkmcnt(4)\n"
    "v_mfma_f32_16x16x32_bf16 v[192:195], a[152:155], a[12:15], v[192:195]\n"
    "ds_read_b64_tr_b16 a[132:133], v7 offset:3072\n"
    "ds_read_b64_tr_b16 a[134:135], v7 offset:4608\n"
    "v_mfma_f32_16x16x32_bf16 v[196:199], a[152:155], a[28:31], v[196:199]\n"
    "s_add_u32 m0, s77, 78864\n"
    "v_mfma_f32_16x16x32_bf16 v[200:203], a[152:155], a[44:47], v[200:203]\n"
    "v_mfma_f32_16x16x32_bf16 v[204:207], a[152:155], a[60:63], v[204:207]\n"
    "s_waitcnt lgkmcnt(4)\n"
    "v_mfma_f32_16x16x32_bf16 v[208:211], a[156:159], a[12:15], v[208:211]\n"
    "s_add_u32 s88, s79, 1966208\n"
    "ds_read_b64_tr_b16 a[136:137], v7 offset:6144\n"
    "ds_read_b64_tr_b16 a[138:139], v7 offset:7680\n"
    "v_mfma_f32_16x16x32_bf16 v[212:215], a[156:159], a[28:31], v[212:215]\n"
    "v_mfma_f32_16x16x32_bf16 v[216:219], a[156:159], a[44:47], v[216:219]\n"
    "buffer_load_dwordx4 v1, s[44:47], s88 offen offset:0 lds\n"
    "v_mfma_f32_16x16x32_bf16 v[220:223], a[156:159], a[60:63], v[220:223]\n"
    "s_waitcnt lgkmcnt(4)\n"
    "v_mfma_f32_16x16x32_bf16 v[224:227], a[128:131], a[0:3], 0\n"
    "ds_read_b64_tr_b16 a[140:141], v7 offset:9216\n"
    "ds_read_b64_tr_b16 a[142:143], v7 offset:10752\n"
    "v_mfma_f32_16x16x32_bf16 v[228:231], a[128:131], a[16:19], 0\n"
    "s_add_u32 m0, s77, 82960\n"
    "v_mfma_f32_16x16x32_bf16 v[232:235], a[128:131], a[32:35], 0\n"
    "v_mfma_f32_16x16x32_bf16 v[236:239], a[128:131], a[48:51], 0\n"
    "s_waitcnt lgkmcnt(4)\n"
    "s_add_u32 s88, s79, 1966336\n"
    "v_mfma_f32_16x16x32_bf16 v[224:227], a[132:135], a[4:7], v[224:227]\n"
    "ds_read_b64_tr_b16 a[144:145], v7 offset:32\n"
    "ds_read_b64_tr_b16 a[146:147], v7 offset:1568\n"
    "v_mfma_f32_16x16x32_bf16 v[228:231], a[132:135], a[20:23], v[228:231]\n"
    "v_mfma_f32_16x16x32_bf16 v[232:235], a[132:135], a[36:39], v[232:235]\n"
    "buffer_load_dwordx4 v1, s[44:47], s88 offen offset:0 lds\n"
    "v_mfma_f32_16x16x32_bf16 v[236:239], a[132:135], a[52:55], v[236:239]\n"
    "s_waitcnt lgkmcnt(4)\n"
    "v_mfma_f32_16x16x32_bf16 v[224:227], a[136:139], a[8:11], v[224:227]\n"
    "ds_read_b64_tr_b16 a[148:149], v7 offset:3104\n"
    "ds_read_b64_tr_b16 a[150:151], v7 offset:4640\n"
    "v_mfma_f32_16x16x32_bf16 v[228:231], a[136:139], a[24:27], v[228:231]\n"
    "s_add_u32 m0, s77, 75792\n"
    "v_mfma_f32_16x16x32_bf16 v[232:235], a[136:139], a[40:43], v[232:235]\n"
    "v_mfma_f32_16x16x32_bf16 v[236:239], a[136:139], a[56:59], v[236:239]\n"
    "s_waitcnt lgkmcnt(4)\n"
    "s_add_u32 s88, s79, 2359296\n"
    "v_mfma_f32_16x16x32_bf16 v[224:227], a[140:143], a[12:15], v[224:227]\n"
    "ds_read_b64_tr_b16 a[152:153], v7 offset:6176\n"
    "ds_read_b64_tr_b16 a[154:155], v7 offset:7712\n"
    "v_mfma_f32_16x16x32_bf16 v[228:231], a[140:143], a[28:31], v[228:231]\n"
    "v_mfma_f32_16x16x32_bf16 v[232:235], a[140:143], a[44:47], v[232:235]\n"
    "buffer_load_dwordx4 v1, s[44:47], s88 offen offset:0 lds\n"
    "v_mfma_f32_16x16x32_bf16 v[236:239], a[140:143], a[60:63], v[236:239]\n"
    "s_waitcnt lgkmcnt(4)\n"
    "v_mfma_f32_16x16x32_bf16 v[240:243], a[144:147], a[0:3], 0\n"
    "ds_read_b64_tr_b16 a[156:157], v7 offset:9248\n"
    "ds_read_b64_tr_b16 a[158:159], v7 offset:10784\n"
    "v_mfma_f32_16x16x32_bf16 v[244:247], a[144:147], a[16:19], 0\n"
    "s_add_u32 m0, s77, 79888\n"
    "v_mfma_f32_16x16x32_bf16 v[248:251], a[144:147], a[32:35], 0\n"
    "v_mfma_f32_16x16x32_bf16 v[252:255], a[144:147], a[48:51], 0\n"
    "s_waitcnt lgkmcnt(4)\n"
    "s_add_u32 s88, s79, 2359424\n"
    "v_mfma_f32_16x16x32_bf16 v[240:243], a[148:151], a[4:7], v[240:243]\n"
    "ds_read_b64_tr_b16 a[128:129], v7 offset:64\n"
    "ds_read_b64_tr_b16 a[130:131], v7 offset:1600\n"
    "v_mfma_f32_16x16x32_bf16 v[244:247], a[148:151], a[20:23], v[244:247]\n"
    "v_mfma_f32_16x16x32_bf16 v[248:251], a[148:151], a[36:39], v[248:251]\n"
    "buffer_load_dwordx4 v1, s[44:47], s88 offen offset:0 lds\n"
    "v_mfma_f32_16x16x32_bf16 v[252:255], a[148:151], a[52:55], v[252:255]\n"
    "s_waitcnt lgkmcnt(4)\n"
    "v_mfma_f32_16x16x32_bf16 v[240:243], a[152:155], a[8:11], v[240:243]\n"
    "ds_read_b64_tr_b16 a[132:133], v7 offset:3136\n"
    "ds_read_b64_tr_b16 a[134:135], v7 offset:4672\n"
    "v_mfma_f32_16x16x32_bf16 v[244:247], a[152:155], a[24:27], v[244:247]\n"
    "s_add_u32 m0, s77, 83984\n"
    "v_mfma_f32_16x16x32_bf16 v[248:251], a[152:155], a[40:43], v[248:251]\n"
    "v_mfma_f32_16x16x32_bf16 v[252:255], a[152:155], a[56:59], v[252:255]\n"
    "s_waitcnt lgkmcnt(4)\n"
    "s_add_u32 s88, s79, 2359552\n"
    "v_mfma_f32_16x16x32_bf16 v[240:243], a[156:159], a[12:15], v[240:243]\n"
    "ds_read_b64_tr_b16 a[136:137], v7 offset:6208\n"
    "ds_read_b64_tr_b16 a[138:139], v7 offset:7744\n"
    "v_mfma_f32_16x16x32_bf16 v[244:247], a[156:159], a[28:31], v[244:247]\n"
    "v_mfma_f32_16x16x32_bf16 v[248:251], a[156:159], a[44:47], v[248:251]\n"
    "buffer_load_dwordx4 v1, s[44:47], s88 offen offset:0 lds\n"
    "v_mfma_f32_16x16x32_bf16 v[252:255], a[156:159], a[60:63], v[252:255]\n"
    "s_waitcnt lgkmcnt(4)\n"
    "v_mfma_f32_16x16x32_bf16 v[16:19], a[128:131], a[0:3], 0\n"
    "ds_read_b64_tr_b16 a[140:141], v7 offset:9280\n"
    "ds_read_b64_tr_b16 a[142:143], v7 offset:10816\n"
    "v_mfma_f32_16x16x32_bf16 v[20:23], a[128:131], a[16:19], 0\n"
    "s_add_u32 m0, s77, 76816\n"
    "v_mfma_f32_16x16x32_bf16 v[24:27], a[128:131], a[32:35], 0\n"
    "v_mfma_f32_16x16x32_bf16 v[28:31], a[128:131], a[48:51], 0\n"
    "s_waitcnt lgkmcnt(4)\n"
    "s_add_u32 s88, s79, 2752512\n"
    "v_mfma_f32_16x16x32_bf16 v[16:19], a[132:135], a[4:7], v[16:19]\n"
    "ds_read_b64_tr_b16 a[144:145], v7 offset:12288\n"
    "ds_read_b64_tr_b16 a[146:147], v7 offset:13824\n"
    "v_mfma_f32_16x16x32_bf16 v[20:23], a[132:135], a[20:23], v[20:23]\n"
    "v_mfma_f32_16x16x32_bf16 v[24:27], a[132:135], a[36:39], v[24:27]\n"
    "buffer_load_dwordx4 v1, s[44:47], s88 offen offset:0 lds\n"
    "v_mfma_f32_16x16x32_bf16 v[28:31], a[132:135], a[52:55], v[28:31]\n"
    "s_waitcnt lgkmcnt(4)\n"
    "v_mfma_f32_16x16x32_bf16 v[16:19], a[136:139], a[8:11], v[16:19]\n"
    "s_add_u32 m0, s77, 80912\n"
    "ds_read_b64_tr_b16 a[148:149], v7 offset:15360\n"
    "ds_read_b64_tr_b16 a[150:151], v7 offset:16896\n"
    "v_mfma_f32_16x16x32_bf16 v[20:23], a[136:139], a[24:27], v[20:23]\n"
    "v_mfma_f32_16x16x32_bf16 v[24:27], a[136:139], a[40:43], v[24:27]\n"
    "v_mfma_f32_16x16x32_bf16 v[28:31], a[136:139], a[56:59], v[28:31]\n"
    "s_waitcnt lgkmcnt(4)\n"
    "s_add_u32 s88, s79, 2752640\n"
    "v_mfma_f32_16x16x32_bf16 v[16:19], a[140:143], a[12:15], v[16:19]\n"
    "ds_read_b64_tr_b16 a[152:153], v7 offset:18432\n"
    "ds_read_b64_tr_b16 a[154:155], v7 offset:19968\n"
    "v_mfma_f32_16x16x32_bf16 v[20:23], a[140:143], a[28:31], v[20:23]\n"
    "v_mfma_f32_16x16x32_bf16 v[24:27], a[140:143], a[44:47], v[24:27]\n"
    "buffer_load_dwordx4 v1, s[44:47], s88 offen offset:0 lds\n"
    "v_mfma_f32_16x16x32_bf16 v[28:31], a[140:143], a[60:63], v[28:31]\n"
    "s_waitcnt lgkmcnt(4)\n"
    "v_mfma_f32_16x16x32_bf16 v[32:35], a[144:147], a[0:3], 0\n"
    "s_add_u32 m0, s77, 85008\n"
    "ds_read_b64_tr_b16 a[156:157], v7 offset:21504\n"
    "ds_read_b64_tr_b16 a[158:159], v7 offset:23040\n"
    "v_mfma_f32_16x16x32_bf16 v[36:39], a[144:147], a[16:19], 0\n"
    "v_mfma_f32_16x16x32_bf16 v[40:43], a[144:147], a[32:35], 0\n"
    "v_mfma_f32_16x16x32_bf16 v[44:47], a[144:147], a[48:51], 0\n"
    "s_waitcnt lgkmcnt(4)\n"
    "s_add_u32 s88, s79, 2752768\n"
    "v_mfma_f32_16x16x32_bf16 v[32:35], a[148:151], a[4:7], v[32:35]\n"
    "ds_read_b64_tr_b16 a[128:129], v7 offset:12320\n"
    "ds_read_b64_tr_b16 a[130:131], v7 offset:13856\n"
    "v_mfma_f32_16x16x32_bf16 v[36:39], a[148:151], a[20:23], v[36:39]\n"
    "v_mfma_f32_16x16x32_bf16 v[40:43], a[148:151], a[36:39], v[40:43]\n"
    "buffer_load_dwordx4 v1, s[44:47], s88 offen offset:0 lds\n"
    "v_mfma_f32_16x16x32_bf16 v[44:47], a[148:151], a[52:55], v[44:47]\n"
    "s_waitcnt lgkmcnt(4)\n"
    "v_mfma_f32_16x16x32_bf16 v[32:35], a[152:155], a[8:11], v[32:35]\n"
    "s_add_u32 s89, s99, 4096\n"
    "ds_read_b64_tr_b16 a[132:133], v7 offset:15392\n"
    "ds_read_b64_tr_b16 a[134:135], v7 offset:16928\n"
    "v_mfma_f32_16x16x32_bf16 v[36:39], a[152:155], a[24:27], v[36:39]\n"
    "v_mfma_f32_16x16x32_bf16 v[40:43], a[152:155], a[40:43], v[40:43]\n"
    "v_mfma_f32_16x16x32_bf16 v[44:47], a[152:155], a[56:59], v[44:47]\n"
    "s_waitcnt lgkmcnt(4)\n"
    "v_mfma_f32_16x16x32_bf16 v[32:35], a[156:159], a[12:15], v[32:35]\n"
    "buffer_load_dwordx4 a[112:115], v0, s[40:43], s89 offen offset:0 sc1\n"
    "v_mfma_f32_16x16x32_bf16 v[36:39], a[156:159], a[28:31], v[36:39]\n"
    "ds_read_b64_tr_b16 a[136:137], v7 offset:18464\n"
    "ds_read_b64_tr_b16 a[138:139], v7 offset:20000\n"
    "v_mfma_f32_16x16x32_bf16 v[40:43], a[156:159], a[44:47], v[40:43]\n"
    "buffer_load_dwordx4 a[116:119], v0, s[40:43], s89 offen offset:1024 sc1\n"
    "v_mfma_f32_16x16x32_bf16 v[44:47], a[156:159], a[60:63], v[44:47]\n"
    "s_waitcnt lgkmcnt(4)\n"
    "v_mfma_f32_16x16x32_bf16 v[48:51], a[128:131], a[0:3], 0\n"
    "ds_read_b64_tr_b16 a[140:141], v7 offset:21536\n"
    "ds_read_b64_tr_b16 a[142:143], v7 offset:23072\n"
    "v_mfma_f32_16x16x32_bf16 v[52:55], a[128:131], a[16:19], 0\n"
    "buffer_load_dwordx4 a[120:123], v0, s[40:43], s89 offen offset:2048 sc1\n"
    "v_mfma_f32_16x16x32_bf16 v[56:59], a[128:131], a[32:35], 0\n"
    "v_mfma_f32_16x16x32_bf16 v[60:63], a[128:131], a[48:51], 0\n"
    "s_waitcnt lgkmcnt(4)\n"
    "v_mfma_f32_16x16x32_bf16 v[48:51], a[132:135], a[4:7], v[48:51]\n"
    "buffer_load_dwordx4 a[124:127], v0, s[40:43], s89 offen offset:3072 sc1\n"
    "v_mfma_f32_16x16x32_bf16 v[52:55], a[132:135], a[20:23], v[52:55]\n"
    "ds_read_b64_tr_b16 a[144:145], v7 offset:12352\n"
    "ds_read_b64_tr_b16 a[146:147], v7 offset:13888\n"
    "v_mfma_f32_16x16x32_bf16 v[56:59], a[132:135], a[36:39], v[56:59]\n"
    "v_mfma_f32_16x16x32_bf16 v[60:63], a[132:135], a[52:55], v[60:63]\n"
    "s_waitcnt lgkmcnt(4)\n"
    "v_mfma_f32_16x16x32_bf16 v[48:51], a[136:139], a[8:11], v[48:51]\n"
    "ds_read_b64_tr_b16 a[148:149], v7 offset:15424\n"
    "ds_read_b64_tr_b16 a[150:151], v7 offset:16960\n"
    "v_mfma_f32_16x16x32_bf16 v[52:55], a[136:139], a[24:27], v[52:55]\n"
    "v_mfma_f32_16x16x32_bf16 v[56:59], a[136:139], a[40:43], v[56:59]\n"
    "v_mfma_f32_16x16x32_bf16 v[60:63], a[136:139], a[56:59], v[60:63]\n"
    "s_waitcnt lgkmcnt(4)\n"
    "v_mfma_f32_16x16x32_bf16 v[48:51], a[140:143], a[12:15], v[48:51]\n"
    "ds_read_b64_tr_b16 a[152:153], v7 offset:18496\n"
    "ds_read_b64_tr_b16 a[154:155], v7 offset:20032\n"
    "v_mfma_f32_16x16x32_bf16 v[52:55], a[140:143], a[28:31], v[52:55]\n"
    "v_mfma_f32_16x16x32_bf16 v[56:59], a[140:143], a[44:47], v[56:59]\n"
    "v_mfma_f32_16x16x32_bf16 v[60:63], a[140:143], a[60:63], v[60:63]\n"
    "s_waitcnt lgkmcnt(4)\n"
    "v_mfma_f32_16x16x32_bf16 v[64:67], a[144:147], a[0:3], 0\n"
    "ds_read_b64_tr_b16 a[156:157], v7 offset:21568\n"
    "ds_read_b64_tr_b16 a[158:159], v7 offset:23104\n"
    "v_mfma_f32_16x16x32_bf16 v[68:71], a[144:147], a[16:19], 0\n"
    "v_mfma_f32_16x16x32_bf16 v[72:75], a[144:147], a[32:35], 0\n"
    "v_mfma_f32_16x16x32_bf16 v[76:79], a[144:147], a[48:51], 0\n"
    "s_waitcnt lgkmcnt(4)\n"
    "v_mfma_f32_16x16x32_bf16 v[64:67], a[148:151], a[4:7], v[64:67]\n"
    "s_waitcnt vmcnt(4) lgkmcnt(0)\n"
    "s_barrier\n"
    "ds_read_b64_tr_b16 a[128:129], v8 offset:0\n"
    "v_mfma_f32_16x16x32_bf16 v[68:71], a[148:151], a[20:23], v[68:71]\n"
    "ds_read_b64_tr_b16 a[130:131], v8 offset:512\n"
    "v_mfma_f32_16x16x32_bf16 v[72:75], a[148:151], a[36:39], v[72:75]\n"
    "ds_read_b64_tr_b16 a[132:133], v8 offset:1024\n"
    "v_mfma_f32_16x16x32_bf16 v[76:79], a[148:151], a[52:55], v[76:79]\n"
    "ds_read_b64_tr_b16 a[134:135], v8 offset:1536\n"
    "v_mfma_f32_16x16x32_bf16 v[64:67], a[152:155], a[8:11], v[64:67]\n"
    "ds_read_b64_tr_b16 a[136:137], v8 offset:2048\n"
    "v_mfma_f32_16x16x32_bf16 v[68:71], a[152:155], a[24:27], v[68:71]\n"
    "ds_read_b64_tr_b16 a[138:139], v8 offset:2560\n"
    "v_mfma_f32_16x16x32_bf16 v[72:75], a[152:155], a[40:43], v[72:75]\n"
    "ds_read_b64_tr_b16 a[140:141], v8 offset:3072\n"
    "v_mfma_f32_16x16x32_bf16 v[76:79], a[152:155], a[56:59], v[76:79]\n"
    "ds_read_b64_tr_b16 a[142:143], v8 offset:3584\n"
    "v_mfma_f32_16x16x32_bf16 v[64:67], a[156:159], a[12:15], v[64:67]\n"
    "v_mfma_f32_16x16x32_bf16 v[68:71], a[156:159], a[28:31], v[68:71]\n"
    "v_mfma_f32_16x16x32_bf16 v[72:75], a[156:159], a[44:47], v[72:75]\n"
    "v_mfma_f32_16x16x32_bf16 v[76:79], a[156:159], a[60:63], v[76:79]\n"
    "s_waitcnt lgkmcnt(4)\n"
    "v_mfma_f32_16x16x32_bf16 v[80:83], a[128:131], a[64:67], v[80:83]\n"
    "s_add_u32 s8, s8, s9\n"
    "v_mfma_f32_16x16x32_bf16 v[80:83], a[132:135], a[68:71], v[80:83]\n"
    "s_waitcnt vmcnt(0) lgkmcnt(0)\n"
    "v_mfma_f32_16x16x32_bf16 v[80:83], a[136:139], a[72:75], v[80:83]\n"
    "buffer_load_dword a248, v14, s[4:7], s8 offen offset:0\n"
    "v_mfma_f32_16x16x32_bf16 v[80:83], a[140:143], a[76:79], v[80:83]\n"
    "ds_read_b64_tr_b16 a[144:145], v8 offset:8\n"
    "ds_read_b64_tr_b16 a[146:147], v8 offset:520\n"
    "v_mfma_f32_16x16x32_bf16 v[84:87], a[128:131], a[80:83], v[84:87]\n"
    "s_add_u32 m0, s78, 73744\n"
    "ds_read_b64_tr_b16 a[148:149], v8 offset:1032\n"
    "v_mfma_f32_16x16x32_bf16 v[84:87], a[132:135], a[84:87], v[84:87]\n"
    "ds_read_b64_tr_b16 a[150:151], v8 offset:1544\n"
    "v_mfma_f32_16x16x32_bf16 v[84:87], a[136:139], a[88:91], v[84:87]\n"
    "s_add_u32 s88, s80, 393216\n"
    "ds_read_b64_tr_b16 a[152:153], v8 offset:2056\n"
    "v_mfma_f32_16x16x32_bf16 v[84:87], a[140:143], a[92:95], v[84:87]\n"
    "ds_read_b64_tr_b16 a[154:155], v8 offset:2568\n"
    "v_mfma_f32_16x16x32_bf16 v[88:91], a[128:131], a[96:99], v[88:91]\n"
    "ds_read_b64_tr_b16 a[156:157], v8 offset:3080\n"
    "v_mfma_f32_16x16x32_bf16 v[88:91], a[132:135], a[100:103], v[88:91]\n"
    "buffer_load_dwordx4 v2, s[48:51], s88 offen offset:0 lds\n"
    "v_mfma_f32_16x16x32_bf16 v[88:91], a[136:139], a[104:107], v[88:91]\n"
    "ds_read_b64_tr_b16 a[158:159], v8 offset:3592\n"
    "v_mfma_f32_16x16x32_bf16 v[88:91], a[140:143], a[108:111], v[88:91]\n"
    "v_mfma_f32_16x16x32_bf16 v[92:95], a[128:131], a[112:115], v[92:95]\n"
    "s_add_u32 m0, s78, 74768\n"
    "v_mfma_f32_16x16x32_bf16 v[92:95], a[132:135], a[116:119], v[92:95]\n"
    "v_mfma_f32_16x16x32_bf16 v[92:95], a[136:139], a[120:123], v[92:95]\n"
    "v_mfma_f32_16x16x32_bf16 v[92:95], a[140:143], a[124:127], v[92:95]\n"
    "s_waitcnt lgkmcnt(4)\n"
    "s_add_u32 s88, s80, 393216\n"
    "v_mfma_f32_16x16x32_bf16 v[96:99], a[144:147], a[64:67], v[96:99]\n"
    "v_mfma_f32_16x16x32_bf16 v[96:99], a[148:151], a[68:71], v[96:99]\n"
    "s_waitcnt lgkmcnt(0)\n"
    "v_mfma_f32_16x16x32_bf16 v[96:99], a[152:155], a[72:75], v[96:99]\n"
    "buffer_load_dwordx4 v3, s[48:51], s88 offen offset:0 lds\n"
    "v_mfma_f32_16x16x32_bf16 v[96:99], a[156:159], a[76:79], v[96:99]\n"
    "ds_read_b64_tr_b16 a[128:129], v9 offset:0\n"
    "ds_read_b64_tr_b16 a[130:131], v9 offset:512\n"
    "v_mfma_f32_16x16x32_bf16 v[100:103], a[144:147], a[80:83], v[100:103]\n"
    "ds_read_b64_tr_b16 a[132:133], v9 offset:1024\n"
    "v_mfma_f32_16x16x32_bf16 v[100:103], a[148:151], a[84:87], v[100:103]\n"
    "s_add_u32 m0, s78, 75792\n"
    "ds_read_b64_tr_b16 a[134:135], v9 offset:1536\n"
    "v_mfma_f32_16x16x32_bf16 v[100:103], a[152:155], a[88:91], v[100:103]\n"
    "ds_read_b64_tr_b16 a[136:137], v9 offset:2048\n"
    "v_mfma_f32_16x16x32_bf16 v[100:103], a[156:159], a[92:95], v[100:103]\n"
    "ds_read_b64_tr_b16 a[138:139], v9 offset:2560\n"
    "v_mfma_f32_16x16x32_bf16 v[104:107], a[144:147], a[96:99], v[104:107]\n"
    "s_add_u32 s88, s80, 393216\n"
    "ds_read_b64_tr_b16 a[140:141], v9 offset:3072\n"
    "v_mfma_f32_16x16x32_bf16 v[104:107], a[148:151], a[100:103], v[104:107]\n"
    "ds_read_b64_tr_b16 a[142:143], v9 offset:3584\n"
    "v_mfma_f32_16x16x32_bf16 v[104:107], a[152:155], a[104:107], v[104:107]\n"
    "v_mfma_f32_16x16x32_bf16 v[104:107], a[156:159], a[108:111], v[104:107]\n"
    "buffer_load_dwordx4 v4, s[48:51], s88 offen offset:0 lds\n"
    "v_mfma_f32_16x16x32_bf16 v[108:111], a[144:147], a[112:115], v[108:111]\n"
    "v_mfma_f32_16x16x32_bf16 v[108:111], a[148:151], a[116:119], v[108:111]\n"
    "v_mfma_f32_16x16x32_bf16 v[108:111], a[152:155], a[120:123], v[108:111]\n"
    "s_add_u32 m0, s78, 86032\n"
    "v_mfma_f32_16x16x32_bf16 v[108:111], a[156:159], a[124:127], v[108:111]\n"
    "s_waitcnt lgkmcnt(4)\n"
    "v_mfma_f32_16x16x32_bf16 a[160:163], a[128:131], a[64:67], a[160:163]\n"
    "s_add_u32 s88, s80, 393216\n"
    "v_mfma_f32_16x16x32_bf16 a[160:163], a[132:135], a[68:71], a[160:163]\n"
    "s_waitcnt lgkmcnt(0)\n"
    "v_mfma_f32_16x16x32_bf16 a[160:163], a[136:139], a[72:75], a[160:163]\n"
    "ds_read_b64_tr_b16 a[144:145], v9 offset:8\n"
    "v_mfma_f32_16x16x32_bf16 a[160:163], a[140:143], a[76:79], a[160:163]\n"
    "buffer_load_dwordx4 v2, s[52:55], s88 offen offset:0 lds\n"
    "v_mfma_f32_16x16x32_bf16 a[164:167], a[128:131], a[80:83], a[164:167]\n"
    "ds_read_b64_tr_b16 a[146:147], v9 offset:520\n"
    "ds_read_b64_tr_b16 a[148:149], v9 offset:1032\n"
    "v_mfma_f32_16x16x32_bf16 a[164:167], a[132:135], a[84:87], a[164:167]\n"
    "ds_read_b64_tr_b16 a[150:151], v9 offset:1544\n"
    "v_mfma_f32_16x16x32_bf16 a[164:167], a[136:139], a[88:91], a[164:167]\n"
    "s_add_u32 m0, s78, 87056\n"
    "ds_read_b64_tr_b16 a[152:153], v9 offset:2056\n"
    "v_mfma_f32_16x16x32_bf16 a[164:167], a[140:143], a[92:95], a[164:167]\n"
    "ds_read_b64_tr_b16 a[154:155], v9 offset:2568\n"
    "v_mfma_f32_16x16x32_bf16 a[168:171], a[128:131], a[96:99], a[168:171]\n"
    "ds_read_b64_tr_b16 a[156:157], v9 offset:3080\n"
    "v_mfma_f32_16x16x32_bf16 a[168:171], a[132:135], a[100:103], a[168:171]\n"
    "s_add_u32 s88, s80, 393216\n"
    "ds_read_b64_tr_b16 a[158:159], v9 offset:3592\n"
    "v_mfma_f32_16x16x32_bf16 a[168:171], a[136:139], a[104:107], a[168:171]\n"
    "v_readlane_b32 s4, v15, 0\n"
    "v_mfma_f32_16x16x32_bf16 a[168:171], a[140:143], a[108:111], a[168:171]\n"
    "v_mfma_f32_16x16x32_bf16 a[172:175], a[128:131], a[112:115], a[172:175]\n"
    "buffer_load_dwordx4 v3, s[52:55], s88 offen offset:0 lds\n"
    "v_mfma_f32_16x16x32_bf16 a[172:175], a[132:135], a[116:119], a[172:175]\n"
    "v_readlane_b32 s5, v15, 1\n"
    "v_mfma_f32_16x16x32_bf16 a[172:175], a[136:139], a[120:123], a[172:175]\n"
    "v_mfma_f32_16x16x32_bf16 a[172:175], a[140:143], a[124:127], a[172:175]\n"
    "s_waitcnt lgkmcnt(4)\n"
    "v_readlane_b32 s6, v15, 2\n"
    "s_add_u32 m0, s78, 88080\n"
    "v_mfma_f32_16x16x32_bf16 a[176:179], a[144:147], a[64:67], a[176:179]\n"
    "v_mfma_f32_16x16x32_bf16 a[176:179], a[148:151], a[68:71], a[176:179]\n"
    "s_waitcnt lgkmcnt(0)\n"
    "v_mfma_f32_16x16x32_bf16 a[176:179], a[152:155], a[72:75], a[176:179]\n"
    "s_add_u32 s88, s80, 393216\n"
    "ds_read_b64_tr_b16 a[128:129], v8 offset:4096\n"
    "v_readlane_b32 s7, v15, 3\n"
    "v_mfma_f32_16x16x32_bf16 a[176:179], a[156:159], a[76:79], a[176:179]\n"
    "ds_read_b64_tr_b16 a[130:131], v8 offset:4608\n"
    "v_mfma_f32_16x16x32_bf16 a[180:183], a[144:147], a[80:83], a[180:183]\n"
    "ds_read_b64_tr_b16 a[132:133], v8 offset:5120\n"
    "v_readlane_b32 s8, v15, 4\n"
    "v_mfma_f32_16x16x32_bf16 a[180:183], a[148:151], a[84:87], a[180:183]\n"
    "buffer_load_dwordx4 v4, s[52:55], s88 offen offset:0 lds\n"
    "v_mfma_f32_16x16x32_bf16 a[180:183], a[152:155], a[88:91], a[180:183]\n"
    "ds_read_b64_tr_b16 a[134:135], v8 offset:5632\n"
    "ds_read_b64_tr_b16 a[136:137], v8 offset:6144\n"
    "v_mfma_f32_16x16x32_bf16 a[180:183], a[156:159], a[92:95], a[180:183]\n"
    "ds_read_b64_tr_b16 a[138:139], v8 offset:6656\n"
    "v_readlane_b32 s9, v15, 5\n"
    "v_mfma_f32_16x16x32_bf16 a[184:187], a[144:147], a[96:99], a[184:187]\n"
    "ds_read_b64_tr_b16 a[140:141], v8 offset:7168\n"
    "v_mfma_f32_16x16x32_bf16 a[184:187], a[148:151], a[100:103], a[184:187]\n"
    "ds_read_b64_tr_b16 a[142:143], v8 offset:7680\n"
    "v_readlane_b32 s10, v15, 6\n"
    "v_mfma_f32_16x16x32_bf16 a[184:187], a[152:155], a[104:107], a[184:187]\n"
    "v_mfma_f32_16x16x32_bf16 a[184:187], a[156:159], a[108:111], a[184:187]\n"
    "v_mfma_f32_16x16x32_bf16 a[188:191], a[144:147], a[112:115], a[188:191]\n"
    "s_add_u32 s89, s81, 8192\n"
    "v_readlane_b32 s11, v15, 7\n"
    "v_mfma_f32_16x16x32_bf16 a[188:191], a[148:151], a[116:119], a[188:191]\n"
    "v_mfma_f32_16x16x32_bf16 a[188:191], a[152:155], a[120:123], a[188:191]\n"
    "buffer_load_dwordx4 a[0:3], v0, s[40:43], s89 offen offset:0 sc1\n"
    "v_mfma_f32_16x16x32_bf16 a[188:191], a[156:159], a[124:127], a[188:191]\n"
    "s_waitcnt lgkmcnt(4)\n"
    "v_readlane_b32 s12, v15, 8\n"
    "v_mfma_f32_16x16x32_bf16 a[192:195], a[128:131], a[64:67], a[192:195]\n"
    "v_mfma_f32_16x16x32_bf16 a[192:195], a[132:135], a[68:71], a[192:195]\n"
    "s_waitcnt lgkmcnt(0)\n"
    "v_readlane_b32 s13, v15, 9\n"
    "v_mfma_f32_16x16x32_bf16 a[192:195], a[136:139], a[72:75], a[192:195]\n"
    "buffer_load_dwordx4 a[4:7], v0, s[40:43], s89 offen offset:1024 sc1\n"
    "v_mfma_f32_16x16x32_bf16 a[192:195], a[140:143], a[76:79], a[192:195]\n"
    "ds_read_b64_tr_b16 a[144:145], v8 offset:4104\n"
    "ds_read_b64_tr_b16 a[146:147], v8 offset:4616\n"
    "v_mfma_f32_16x16x32_bf16 a[196:199], a[128:131], a[80:83], a[196:199]\n"
    "buffer_load_dwordx4 a[8:11], v0, s[40:43], s89 offen offset:2048 sc1\n"
    "v_mfma_f32_16x16x32_bf16 a[196:199], a[132:135], a[84:87], a[196:199]\n"
    "ds_read_b64_tr_b16 a[148:149], v8 offset:5128\n"
    "ds_read_b64_tr_b16 a[150:151], v8 offset:5640\n"
    "v_mfma_f32_16x16x32_bf16 a[196:199], a[136:139], a[88:91], a[196:199]\n"
    "ds_read_b64_tr_b16 a[152:153], v8 offset:6152\n"
    "v_readlane_b32 s14, v15, 10\n"
    "v_mfma_f32_16x16x32_bf16 a[196:199], a[140:143], a[92:95], a[196:199]\n"
    "buffer_load_dwordx4 a[12:15], v0, s[40:43], s89 offen offset:3072 sc1\n"
    "v_mfma_f32_16x16x32_bf16 a[200:203], a[128:131], a[96:99], a[200:203]\n"
    "ds_read_b64_tr_b16 a[154:155], v8 offset:6664\n"
    "ds_read_b64_tr_b16 a[156:157], v8 offset:7176\n"
    "v_mfma_f32_16x16x32_bf16 a[200:203], a[132:135], a[100:103], a[200:203]\n"
    "s_add_u32 m0, s77, 0\n"
    "ds_read_b64_tr_b16 a[158:159], v8 offset:7688\n"
    "v_readlane_b32 s15, v15, 11\n"
    "v_mfma_f32_16x16x32_bf16 a[200:203], a[136:139], a[104:107], a[200:203]\n"
    "v_readlane_b32 s16, v15, 12\n"
    "v_mfma_f32_16x16x32_bf16 a[200:203], a[140:143], a[108:111], a[200:203]\n"
    "s_add_u32 s88, s79, 3145728\n"
    "v_readlane_b32 s17, v15, 13\n"
    "v_mfma_f32_16x16x32_bf16 a[204:207], a[128:131], a[112:115], a[204:207]\n"
    "v_mfma_f32_16x16x32_bf16 a[204:207], a[132:135], a[116:119], a[204:207]\n"
    "v_readlane_b32 s18, v15, 14\n"
    "v_mfma_f32_16x16x32_bf16 a[204:207], a[136:139], a[120:123], a[204:207]\n"
    "buffer_load_dwordx4 v1, s[44:47], s88 offen offset:0 lds\n"
    "v_mfma_f32_16x16x32_bf16 a[204:207], a[140:143], a[124:127], a[204:207]\n"
    "s_waitcnt lgkmcnt(4)\n"
    "v_mfma_f32_16x16x32_bf16 v[112:115], a[144:147], a[64:67], v[112:115]\n"
    "v_readlane_b32 s19, v15, 15\n"
    "v_mfma_f32_16x16x32_bf16 v[112:115], a[148:151], a[68:71], v[112:115]\n"
    "s_waitcnt lgkmcnt(0)\n"
    "s_add_u32 m0, s77, 4096\n"
    "v_mfma_f32_16x16x32_bf16 v[112:115], a[152:155], a[72:75], v[112:115]\n"
    "ds_read_b64_tr_b16 a[128:129], v9 offset:4096\n"
    "v_mfma_f32_16x16x32_bf16 v[112:115], a[156:159], a[76:79], v[112:115]\n"
    "s_add_u32 s88, s79, 3145856\n"
    "ds_read_b64_tr_b16 a[130:131], v9 offset:4608\n"
    "v_readlane_b32 s20, v15, 16\n"
    "v_mfma_f32_16x16x32_bf16 v[116:119], a[144:147], a[80:83], v[116:119]\n"
    "ds_read_b64_tr_b16 a[132:133], v9 offset:5120\n"
    "v_mfma_f32_16x16x32_bf16 v[116:119], a[148:151], a[84:87], v[116:119]\n"
    "ds_read_b64_tr_b16 a[134:135], v9 offset:5632\n"
    "v_readlane_b32 s21, v15, 17\n"
    "v_mfma_f32_16x16x32_bf16 v[116:119], a[152:155], a[88:91], v[116:119]\n"
    "buffer_load_dwordx4 v1, s[44:47], s88 offen offset:0 lds\n"
    "v_mfma_f32_16x16x32_bf16 v[116:119], a[156:159], a[92:95], v[116:119]\n"
    "ds_read_b64_tr_b16 a[136:137], v9 offset:6144\n"
    "ds_read_b64_tr_b16 a[138:139], v9 offset:6656\n"
    "v_mfma_f32_16x16x32_bf16 v[120:123], a[144:147], a[96:99], v[120:123]\n"
    "ds_read_b64_tr_b16 a[140:141], v9 offset:7168\n"
    "v_readlane_b32 s22, v15, 18\n"
    "v_mfma_f32_16x16x32_bf16 v[120:123], a[148:151], a[100:103], v[120:123]\n"
    "s_add_u32 m0, s77, 8192\n"
    "ds_read_b64_tr_b16 a[142:143], v9 offset:7680\n"
    "v_mfma_f32_16x16x32_bf16 v[120:123], a[152:155], a[104:107], v[120:123]\n"
    "v_readlane_b32 s23, v15, 19\n"
    "v_mfma_f32_16x16x32_bf16 v[120:123], a[156:159], a[108:111], v[120:123]\n"
    "s_add_u32 s88, s79, 3145984\n"
    "v_mfma_f32_16x16x32_bf16 v[124:127], a[144:147], a[112:115], v[124:127]\n"
    "v_mfma_f32_16x16x32_bf16 v[124:127], a[148:151], a[116:119], v[124:127]\n"
    "v_readlane_b32 s24, v15, 20\n"
    "v_mfma_f32_16x16x32_bf16 v[124:127], a[152:155], a[120:123], v[124:127]\n"
    "buffer_load_dwordx4 v1, s[44:47], s88 offen offset:0 lds\n"
    "v_mfma_f32_16x16x32_bf16 v[124:127], a[156:159], a[124:127], v[124:127]\n"
    "s_waitcnt lgkmcnt(4)\n"
    "v_readlane_b32 s25, v15, 21\n"
    "v_mfma_f32_16x16x32_bf16 v[128:131], a[128:131], a[64:67], v[128:131]\n"
    "v_mfma_f32_16x16x32_bf16 v[128:131], a[132:135], a[68:71], v[128:131]\n"
    "s_waitcnt lgkmcnt(0)\n"
    "s_add_u32 s89, s3, 8192\n"
    "v_mfma_f32_16x16x32_bf16 v[128:131], a[136:139], a[72:75], v[128:131]\n"
    "ds_read_b64_tr_b16 a[144:145], v9 offset:4104\n"
    "v_readlane_b32 s26, v15, 22\n"
    "v_mfma_f32_16x16x32_bf16 v[128:131], a[140:143], a[76:79], v[128:131]\n"
    "buffer_load_dwordx4 a[16:19], v0, s[40:43], s89 offen offset:0 sc1\n"
    "v_mfma_f32_16x16x32_bf16 v[132:135], a[128:131], a[80:83], v[132:135]\n"
    "ds_read_b64_tr_b16 a[146:147], v9 offset:4616\n"
    "ds_read_b64_tr_b16 a[148:149], v9 offset:5128\n"
    "v_mfma_f32_16x16x32_bf16 v[132:135], a[132:135], a[84:87], v[132:135]\n"
    "ds_read_b64_tr_b16 a[150:151], v9 offset:5640\n"
    "v_readlane_b32 s27, v15, 23\n"
    "v_mfma_f32_16x16x32_bf16 v[132:135], a[136:139], a[88:91], v[132:135]\n"
    "buffer_load_dwordx4 a[20:23], v0, s[40:43], s89 offen offset:1024 sc1\n"
    "v_mfma_f32_16x16x32_bf16 v[132:135], a[140:143], a[92:95], v[132:135]\n"
    "ds_read_b64_tr_b16 a[152:153], v9 offset:6152\n"
    "ds_read_b64_tr_b16 a[154:155], v9 offset:6664\n"
    "v_mfma_f32_16x16x32_bf16 v[136:139], a[128:131], a[96:99], v[136:139]\n"
    "ds_read_b64_tr_b16 a[156:157], v9 offset:7176\n"
    "v_readlane_b32 s28, v15, 24\n"
    "v_mfma_f32_16x16x32_bf16 v[136:139], a[132:135], a[100:103], v[136:139]\n"
    "ds_read_b64_tr_b16 a[158:159], v9 offset:7688\n"
    "v_readlane_b32 s29, v15, 25\n"
    "v_mfma_f32_16x16x32_bf16 v[136:139], a[136:139], a[104:107], v[136:139]\n"
    "buffer_load_dwordx4 a[24:27], v0, s[40:43], s89 offen offset:2048 sc1\n"
    "v_mfma_f32_16x16x32_bf16 v[136:139], a[140:143], a[108:111], v[136:139]\n"
    "buffer_load_dwordx4 a[28:31], v0, s[40:43], s89 offen offset:3072 sc1\n"
    "v_mfma_f32_16x16x32_bf16 v[140:143], a[128:131], a[112:115], v[140:143]\n"
    "v_readlane_b32 s30, v15, 26\n"
    "v_mfma_f32_16x16x32_bf16 v[140:143], a[132:135], a[116:119], v[140:143]\n"
    "v_mfma_f32_16x16x32_bf16 v[140:143], a[136:139], a[120:123], v[140:143]\n"
    "s_add_u32 m0, s77, 1024\n"
    "v_readlane_b32 s31, v15, 27\n"
    "v_mfma_f32_16x16x32_bf16 v[140:143], a[140:143], a[124:127], v[140:143]\n"
    "s_waitcnt lgkmcnt(4)\n"
    "v_mfma_f32_16x16x32_bf16 v[144:147], a[144:147], a[64:67], v[144:147]\n"
    "s_add_u32 s88, s79, 3538944\n"
    "v_mfma_f32_16x16x32_bf16 v[144:147], a[148:151], a[68:71], v[144:147]\n"
    "s_waitcnt lgkmcnt(0)\n"
    "v_readlane_b32 s32, v15, 28\n"
    "v_mfma_f32_16x16x32_bf16 v[144:147], a[152:155], a[72:75], v[144:147]\n"
    "ds_read_b64_tr_b16 a[128:129], v8 offset:8192\n"
    "v_mfma_f32_16x16x32_bf16 v[144:147], a[156:159], a[76:79], v[144:147]\n"
    "ds_read_b64_tr_b16 a[130:131], v8 offset:8704\n"
    "v_readlane_b32 s33, v15, 29\n"
    "v_mfma_f32_16x16x32_bf16 v[148:151], a[144:147], a[80:83], v[148:151]\n"
    "buffer_load_dwordx4 v1, s[44:47], s88 offen offset:0 lds\n"
    "v_mfma_f32_16x16x32_bf16 v[148:151], a[148:151], a[84:87], v[148:151]\n"
    "ds_read_b64_tr_b16 a[132:133], v8 offset:9216\n"
    "ds_read_b64_tr_b16 a[134:135], v8 offset:9728\n"
    "v_mfma_f32_16x16x32_bf16 v[148:151], a[152:155], a[88:91], v[148:151]\n"
    "s_add_u32 m0, s77, 5120\n"
    "ds_read_b64_tr_b16 a[136:137], v8 offset:10240\n"
    "v_readlane_b32 s34, v15, 30\n"
    "v_mfma_f32_16x16x32_bf16 v[148:151], a[156:159], a[92:95], v[148:151]\n"
    "ds_read_b64_tr_b16 a[138:139], v8 offset:10752\n"
    "v_mfma_f32_16x16x32_bf16 v[152:155], a[144:147], a[96:99], v[152:155]\n"
    "s_add_u32 s88, s79, 3539072\n"
    "ds_read_b64_tr_b16 a[140:141], v8 offset:11264\n"
    "v_mfma_f32_16x16x32_bf16 v[152:155], a[148:151], a[100:103], v[152:155]\n"
    "ds_read_b64_tr_b16 a[142:143], v8 offset:11776\n"
    "v_readlane_b32 s35, v15, 31\n"
    "v_mfma_f32_16x16x32_bf16 v[152:155], a[152:155], a[104:107], v[152:155]\n"
    "v_mfma_f32_16x16x32_bf16 v[152:155], a[156:159], a[108:111], v[152:155]\n"
    "buffer_load_dwordx4 v1, s[44:47], s88 offen offset:0 lds\n"
    "v_mfma_f32_16x16x32_bf16 v[156:159], a[144:147], a[112:115], v[156:159]\n"
    "v_readlane_b32 s36, v15, 32\n"
    "v_mfma_f32_16x16x32_bf16 v[156:159], a[148:151], a[116:119], v[156:159]\n"
    "v_mfma_f32_16x16x32_bf16 v[156:159], a[152:155], a[120:123], v[156:159]\n"
    "s_add_u32 m0, s77, 9216\n"
    "v_readlane_b32 s37, v15, 33\n"
    "v_mfma_f32_16x16x32_bf16 v[156:159], a[156:159], a[124:127], v[156:159]\n"
    "s_waitcnt lgkmcnt(4)\n"
    "v_mfma_f32_16x16x32_bf16 v[160:163], a[128:131], a[64:67], v[160:163]\n"
    "s_add_u32 s88, s79, 3539200\n"
    "v_readlane_b32 s38, v15, 34\n"
    "v_mfma_f32_16x16x32_bf16 v[160:163], a[132:135], a[68:71], v[160:163]\n"
    "s_waitcnt lgkmcnt(0)\n"
    "v_mfma_f32_16x16x32_bf16 v[160:163], a[136:139], a[72:75], v[160:163]\n"
    "ds_read_b64_tr_b16 a[144:145], v8 offset:8200\n"
    "v_mfma_f32_16x16x32_bf16 v[160:163], a[140:143], a[76:79], v[160:163]\n"
    "buffer_load_dwordx4 v1, s[44:47], s88 offen offset:0 lds\n"
    "v_mfma_f32_16x16x32_bf16 v[164:167], a[128:131], a[80:83], v[164:167]\n"
    "s_lshl_b32 s86, s76, 3\n"
    "ds_read_b64_tr_b16 a[146:147], v8 offset:8712\n"
    "ds_read_b64_tr_b16 a[148:149], v8 offset:9224\n"
    "v_mfma_f32_16x16x32_bf16 v[164:167], a[132:135], a[84:87], v[164:167]\n"
    "s_and_b32 s87, s76, 1\n"
    "ds_read_b64_tr_b16 a[150:151], v8 offset:9736\n"
    "v_mfma_f32_16x16x32_bf16 v[164:167], a[136:139], a[88:91], v[164:167]\n"
    "s_add_u32 s89, s39, 8192\n"
    "ds_read_b64_tr_b16 a[152:153], v8 offset:10248\n"
    "v_mfma_f32_16x16x32_bf16 v[164:167], a[140:143], a[92:95], v[164:167]\n"
    "ds_read_b64_tr_b16 a[154:155], v8 offset:10760\n"
    "v_mfma_f32_16x16x32_bf16 v[168:171], a[128:131], a[96:99], v[168:171]\n"
    "buffer_load_dwordx4 a[32:35], v0, s[40:43], s89 offen offset:0 sc1\n"
    "v_mfma_f32_16x16x32_bf16 v[168:171], a[132:135], a[100:103], v[168:171]\n"
    "s_lshl_b32 s87, s87, 2\n"
    "ds_read_b64_tr_b16 a[156:157], v8 offset:11272\n"
    "ds_read_b64_tr_b16 a[158:159], v8 offset:11784\n"
    "v_mfma_f32_16x16x32_bf16 v[168:171], a[136:139], a[104:107], v[168:171]\n"
    "v_lshrrev_b32 v11, 7, v0\n"
    "v_mfma_f32_16x16x32_bf16 v[168:171], a[140:143], a[108:111], v[168:171]\n"
    "buffer_load_dwordx4 a[36:39], v0, s[40:43], s89 offen offset:1024 sc1\n"
    "v_mfma_f32_16x16x32_bf16 v[172:175], a[128:131], a[112:115], v[172:175]\n"
    "v_mfma_f32_16x16x32_bf16 v[172:175], a[132:135], a[116:119], v[172:175]\n"
    "v_lshrrev_b32 v12, 8, v0\n"
    "v_mfma_f32_16x16x32_bf16 v[172:175], a[136:139], a[120:123], v[172:175]\n"
    "buffer_load_dwordx4 a[40:43], v0, s[40:43], s89 offen offset:2048 sc1\n"
    "v_mfma_f32_16x16x32_bf16 v[172:175], a[140:143], a[124:127], v[172:175]\n"
    "s_waitcnt lgkmcnt(4)\n"
    "v_add_u32 v12, s87, v12\n"
    "v_mfma_f32_16x16x32_bf16 v[176:179], a[144:147], a[64:67], v[176:179]\n"
    "buffer_load_dwordx4 a[44:47], v0, s[40:43], s89 offen offset:3072 sc1\n"
    "v_mfma_f32_16x16x32_bf16 v[176:179], a[148:151], a[68:71], v[176:179]\n"
    "s_waitcnt lgkmcnt(0)\n"
    "v_mfma_f32_16x16x32_bf16 v[176:179], a[152:155], a[72:75], v[176:179]\n"
    "ds_read_b64_tr_b16 a[128:129], v9 offset:8192\n"
    "v_add_u32 v11, s86, v11\n"
    "v_mfma_f32_16x16x32_bf16 v[176:179], a[156:159], a[76:79], v[176:179]\n"
    "s_add_u32 m0, s77, 2048\n"
    "ds_read_b64_tr_b16 a[130:131], v9 offset:8704\n"
    "v_mfma_f32_16x16x32_bf16 v[180:183], a[144:147], a[80:83], v[180:183]\n"
    "ds_read_b64_tr_b16 a[132:133], v9 offset:9216\n"
    "v_mul_u32_u24 v11, 384, v11\n"
    "v_mfma_f32_16x16x32_bf16 v[180:183], a[148:151], a[84:87], v[180:183]\n"
    "ds_read_b64_tr_b16 a[134:135], v9 offset:9728\n"
    "v_mfma_f32_16x16x32_bf16 v[180:183], a[152:155], a[88:91], v[180:183]\n"
    "s_add_u32 s88, s79, 3932160\n"
    "ds_read_b64_tr_b16 a[136:137], v9 offset:10240\n"
    "v_mfma_f32_16x16x32_bf16 v[180:183], a[156:159], a[92:95], v[180:183]\n"
    "ds_read_b64_tr_b16 a[138:139], v9 offset:10752\n"
    "v_bfe_u32 v13, v0, 4, 3\n"
    "v_mfma_f32_16x16x32_bf16 v[184:187], a[144:147], a[96:99], v[184:187]\n"
    "buffer_load_dwordx4 v1, s[44:47], s88 offen offset:0 lds\n"
    "v_mfma_f32_16x16x32_bf16 v[184:187], a[148:151], a[100:103], v[184:187]\n"
    "ds_read_b64_tr_b16 a[140:141], v9 offset:11264\n"
    "ds_read_b64_tr_b16 a[142:143], v9 offset:11776\n"
    "v_mfma_f32_16x16x32_bf16 v[184:187], a[152:155], a[104:107], v[184:187]\n"
    "v_xor_b32 v12, v12, v13\n"
    "v_mfma_f32_16x16x32_bf16 v[184:187], a[156:159], a[108:111], v[184:187]\n"
    "s_add_u32 m0, s77, 6144\n"
    "v_mfma_f32_16x16x32_bf16 v[188:191], a[144:147], a[112:115], v[188:191]\n"
    "v_lshl_add_u32 v11, v12, 4, v11\n"
    "v_mfma_f32_16x16x32_bf16 v[188:191], a[148:151], a[116:119], v[188:191]\n"
    "v_mfma_f32_16x16x32_bf16 v[188:191], a[152:155], a[120:123], v[188:191]\n"
    "s_add_u32 s88, s79, 3932288\n"
    "v_bfe_u32 v14, v0, 7, 1\n"
    "v_mfma_f32_16x16x32_bf16 v[188:191], a[156:159], a[124:127], v[188:191]\n"
    "s_waitcnt lgkmcnt(4)\n"
    "v_mfma_f32_16x16x32_bf16 v[192:195], a[128:131], a[64:67], v[192:195]\n"
    "buffer_load_dwordx4 v1, s[44:47], s88 offen offset:0 lds\n"
    "v_mfma_f32_16x16x32_bf16 v[192:195], a[132:135], a[68:71], v[192:195]\n"
    "s_waitcnt lgkmcnt(0)\n"
    "v_mul_u32_u24 v14, 3072, v14\n"
    "v_mfma_f32_16x16x32_bf16 v[192:195], a[136:139], a[72:75], v[192:195]\n"
    "ds_read_b64_tr_b16 a[144:145], v9 offset:8200\n"
    "v_mfma_f32_16x16x32_bf16 v[192:195], a[140:143], a[76:79], v[192:195]\n"
    "s_add_u32 m0, s77, 10240\n"
    "ds_read_b64_tr_b16 a[146:147], v9 offset:8712\n"
    "v_mfma_f32_16x16x32_bf16 v[196:199], a[128:131], a[80:83], v[196:199]\n"
    "ds_read_b64_tr_b16 a[148:149], v9 offset:9224\n"
    "v_lshl_add_u32 v14, v13, 7, v14\n"
    "v_mfma_f32_16x16x32_bf16 v[196:199], a[132:135], a[84:87], v[196:199]\n"
    "s_add_u32 s88, s79, 3932416\n"
    "ds_read_b64_tr_b16 a[150:151], v9 offset:9736\n"
    "v_mfma_f32_16x16x32_bf16 v[196:199], a[136:139], a[88:91], v[196:199]\n"
    "ds_read_b64_tr_b16 a[152:153], v9 offset:10248\n"
    "v_bfe_u32 v12, v0, 5, 3\n"
    "v_mfma_f32_16x16x32_bf16 v[196:199], a[140:143], a[92:95], v[196:199]\n"
    "ds_read_b64_tr_b16 a[154:155], v9 offset:10760\n"
    "v_mfma_f32_16x16x32_bf16 v[200:203], a[128:131], a[96:99], v[200:203]\n"
    "buffer_load_dwordx4 v1, s[44:47], s88 offen offset:0 lds\n"
    "v_mfma_f32_16x16x32_bf16 v[200:203], a[132:135], a[100:103], v[200:203]\n"
    "ds_read_b64_tr_b16 a[156:157], v9 offset:11272\n"
    "ds_read_b64_tr_b16 a[158:159], v9 offset:11784\n"
    "v_mfma_f32_16x16x32_bf16 v[200:203], a[136:139], a[104:107], v[200:203]\n"
    "v_lshrrev_b32 v13, 8, v0\n"
    "v_mfma_f32_16x16x32_bf16 v[200:203], a[140:143], a[108:111], v[200:203]\n"
    "s_add_u32 s89, s99, 8192\n"
    "v_xor_b32 v12, v12, v13\n"
    "v_mfma_f32_16x16x32_bf16 v[204:207], a[128:131], a[112:115], v[204:207]\n"
    "v_mfma_f32_16x16x32_bf16 v[204:207], a[132:135], a[116:119], v[204:207]\n"
    "buffer_load_dwordx4 a[48:51], v0, s[40:43], s89 offen offset:0 sc1\n"
    "v_mfma_f32_16x16x32_bf16 v[204:207], a[136:139], a[120:123], v[204:207]\n"
    "v_lshl_add_u32 v14, v12, 4, v14\n"
    "v_mfma_f32_16x16x32_bf16 v[204:207], a[140:143], a[124:127], v[204:207]\n"
    "s_waitcnt vmcnt(22) lgkmcnt(0)\n"
    "s_barrier\n"
    "v_mfma_f32_16x16x32_bf16 v[208:211], a[144:147], a[64:67], v[208:211]\n"
    "ds_read_b64_tr_b16 a[128:129], v10 offset:0\n"
    "v_add_u32 v14, 114688, v14\n"
    "v_mfma_f32_16x16x32_bf16 v[208:211], a[148:151], a[68:71], v[208:211]\n"
    "buffer_load_dwordx4 a[52:55], v0, s[40:43], s89 offen offset:1024 sc1\n"
    "v_mfma_f32_16x16x32_bf16 v[208:211], a[152:155], a[72:75], v[208:211]\n"
    "ds_read_b64_tr_b16 a[130:131], v10 offset:1536\n"
    "ds_read_b64_tr_b16 a[132:133], v10 offset:3072\n"
    "v_mfma_f32_16x16x32_bf16 v[208:211], a[156:159], a[76:79], v[208:211]\n"
    "ds_read_b64_tr_b16 a[134:135], v10 offset:4608\n"
    "v_xor_b32 v15, 64, v14\n"
    "v_mfma_f32_16x16x32_bf16 v[212:215], a[144:147], a[80:83], v[212:215]\n"
    "buffer_load_dwordx4 a[56:59], v0, s[40:43], s89 offen offset:2048 sc1\n"
    "v_mfma_f32_16x16x32_bf16 v[212:215], a[148:151], a[84:87], v[212:215]\n"
    "ds_read_b64_tr_b16 a[136:137], v10 offset:6144\n"
    "ds_read_b64_tr_b16 a[138:139], v10 offset:7680\n"
    "v_mfma_f32_16x16x32_bf16 v[212:215], a[152:155], a[88:91], v[212:215]\n"
    "buffer_load_dwordx4 a[60:63], v0, s[40:43], s89 offen offset:3072 sc1\n"
    "v_mfma_f32_16x16x32_bf16 v[212:215], a[156:159], a[92:95], v[212:215]\n"
    "ds_read_b64_tr_b16 a[140:141], v10 offset:9216\n"
    "ds_read_b64_tr_b16 a[142:143], v10 offset:10752\n"
    "v_mfma_f32_16x16x32_bf16 v[216:219], a[144:147], a[96:99], v[216:219]\n"
    "s_add_u32 m0, s77, 3072\n"
    "v_add_u32 v13, 256, v11\n"
    "v_add_u32 v12, 1152, v11\n"
    "v_mfma_f32_16x16x32_bf16 v[216:219], a[148:151], a[100:103], v[216:219]\n"
    "v_mfma_f32_16x16x32_bf16 v[216:219], a[152:155], a[104:107], v[216:219]\n"
    "v_add_u32 v11, 2048, v11\n"
    "v_mfma_f32_16x16x32_bf16 v[216:219], a[156:159], a[108:111], v[216:219]\n"
    "s_add_u32 s88, s79, 4325376\n"
    "v_mfma_f32_16x16x32_bf16 v[220:223], a[144:147], a[112:115], v[220:223]\n"
    "v_mfma_f32_16x16x32_bf16 v[220:223], a[148:151], a[116:119], v[220:223]\n"
    "buffer_load_dwordx4 v1, s[44:47], s88 offen offset:0 lds\n"
    "v_mfma_f32_16x16x32_bf16 v[220:223], a[152:155], a[120:123], v[220:223]\n"
    "s_and_b32 s57, s57, 0xffff\n"
    "v_mfma_f32_16x16x32_bf16 v[220:223], a[156:159], a[124:127], v[220:223]\n"
    "s_waitcnt lgkmcnt(4)\n"
    "s_mov_b32 s58, 25165824\n"
    "v_mfma_f32_16x16x32_bf16 v[224:227], a[128:131], a[64:67], v[224:227]\n"
    "s_add_u32 m0, s77, 7168\n"
    "v_mfma_f32_16x16x32_bf16 v[224:227], a[132:135], a[68:71], v[224:227]\n"
    "s_waitcnt lgkmcnt(0)\n"
    "v_mfma_f32_16x16x32_bf16 v[224:227], a[136:139], a[72:75], v[224:227]\n"
    "s_mov_b32 s59, 0x20000\n"
    "ds_read_b64_tr_b16 a[144:145], v10 offset:32\n"
    "v_mfma_f32_16x16x32_bf16 v[224:227], a[140:143], a[76:79], v[224:227]\n"
    "s_add_u32 s88, s79, 4325504\n"
    "ds_read_b64_tr_b16 a[146:147], v10 offset:1568\n"
    "v_mfma_f32_16x16x32_bf16 v[228:231], a[128:131], a[80:83], v[228:231]\n"
    "s_and_b32 s61, s61, 0xffff\n"
    "ds_read_b64_tr_b16 a[148:149], v10 offset:3104\n"
    "v_mfma_f32_16x16x32_bf16 v[228:231], a[132:135], a[84:87], v[228:231]\n"
    "buffer_load_dwordx4 v1, s[44:47], s88 offen offset:0 lds\n"
    "v_mfma_f32_16x16x32_bf16 v[228:231], a[136:139], a[88:91], v[228:231]\n"
    "ds_read_b64_tr_b16 a[150:151], v10 offset:4640\n"
    "ds_read_b64_tr_b16 a[152:153], v10 offset:6176\n"
    "v_mfma_f32_16x16x32_bf16 v[228:231], a[140:143], a[92:95], v[228:231]\n"
    "s_mov_b32 s62, 25165824\n"
    "ds_read_b64_tr_b16 a[154:155], v10 offset:7712\n"
    "v_mfma_f32_16x16x32_bf16 v[232:235], a[128:131], a[96:99], v[232:235]\n"
    "s_add_u32 m0, s77, 11264\n"
    "ds_read_b64_tr_b16 a[156:157], v10 offset:9248\n"
    "v_mfma_f32_16x16x32_bf16 v[232:235], a[132:135], a[100:103], v[232:235]\n"
    "ds_read_b64_tr_b16 a[158:159], v10 offset:10784\n"
    "v_mfma_f32_16x16x32_bf16 v[232:235], a[136:139], a[104:107], v[232:235]\n"
    "s_mov_b32 s63, 0x20000\n"
    "v_mfma_f32_16x16x32_bf16 v[232:235], a[140:143], a[108:111], v[232:235]\n"
    "s_add_u32 s88, s79, 4325632\n"
    "v_mfma_f32_16x16x32_bf16 v[236:239], a[128:131], a[112:115], v[236:239]\n"
    "s_and_b32 s73, s73, 0xffff\n"
    "v_mfma_f32_16x16x32_bf16 v[236:239], a[132:135], a[116:119], v[236:239]\n"
    "buffer_load_dwordx4 v1, s[44:47], s88 offen offset:0 lds\n"
    "v_mfma_f32_16x16x32_bf16 v[236:239], a[136:139], a[120:123], v[236:239]\n"
    "v_mfma_f32_16x16x32_bf16 v[236:239], a[140:143], a[124:127], v[236:239]\n"
    "s_waitcnt lgkmcnt(4)\n"
    "s_mov_b32 s74, 2621440\n"
    "v_mfma_f32_16x16x32_bf16 v[240:243], a[144:147], a[64:67], v[240:243]\n"
    "s_add_u32 m0, s78, 0\n"
    "v_mfma_f32_16x16x32_bf16 v[240:243], a[148:151], a[68:71], v[240:243]\n"
    "s_waitcnt lgkmcnt(0)\n"
    "s_mov_b32 s75, 0x20000\n"
    "v_mfma_f32_16x16x32_bf16 v[240:243], a[152:155], a[72:75], v[240:243]\n"
    "s_add_u32 s88, s80, 786432\n"
    "ds_read_b64_tr_b16 a[128:129], v10 offset:64\n"
    "v_mfma_f32_16x16x32_bf16 v[240:243], a[156:159], a[76:79], v[240:243]\n"
    "ds_read_b64_tr_b16 a[130:131], v10 offset:1600\n"
    "v_mfma_f32_16x16x32_bf16 v[244:247], a[144:147], a[80:83], v[244:247]\n"
    "s_and_b32 s65, s65, 0xffff\n"
    "ds_read_b64_tr_b16 a[132:133], v10 offset:3136\n"
    "v_mfma_f32_16x16x32_bf16 v[244:247], a[148:151], a[84:87], v[244:247]\n"
    "buffer_load_dwordx4 v2, s[48:51], s88 offen offset:0 lds\n"
    "v_mfma_f32_16x16x32_bf16 v[244:247], a[152:155], a[88:91], v[244:247]\n"
    "s_mov_b32 s67, 0x20000\n"
    "ds_read_b64_tr_b16 a[134:135], v10 offset:4672\n"
    "ds_read_b64_tr_b16 a[136:137], v10 offset:6208\n"
    "v_mfma_f32_16x16x32_bf16 v[244:247], a[156:159], a[92:95], v[244:247]\n"
    "ds_read_b64_tr_b16 a[138:139], v10 offset:7744\n"
    "v_mfma_f32_16x16x32_bf16 v[248:251], a[144:147], a[96:99], v[248:251]\n"
    "s_add_u32 m0, s78, 1024\n"
    "ds_read_b64_tr_b16 a[140:141], v10 offset:9280\n"
    "v_mfma_f32_16x16x32_bf16 v[248:251], a[148:151], a[100:103], v[248:251]\n"
    "s_mul_i32 s86, s93, 786432\n"
    "ds_read_b64_tr_b16 a[142:143], v10 offset:10816\n"
    "v_mfma_f32_16x16x32_bf16 v[248:251], a[152:155], a[104:107], v[248:251]\n"
    "s_add_u32 s88, s80, 786432\n"
    "v_mfma_f32_16x16x32_bf16 v[248:251], a[156:159], a[108:111], v[248:251]\n"
    "s_add_u32 s82, s86, 587776\n"
    "v_mfma_f32_16x16x32_bf16 v[252:255], a[144:147], a[112:115], v[252:255]\n"
    "v_mfma_f32_16x16x32_bf16 v[252:255], a[148:151], a[116:119], v[252:255]\n"
    "buffer_load_dwordx4 v3, s[48:51], s88 offen offset:0 lds\n"
    "v_mfma_f32_16x16x32_bf16 v[252:255], a[152:155], a[120:123], v[252:255]\n"
    "s_mul_i32 s83, s76, 3072\n"
    "v_mfma_f32_16x16x32_bf16 v[252:255], a[156:159], a[124:127], v[252:255]\n"
    "s_waitcnt lgkmcnt(4)\n"
    "v_mfma_f32_16x16x32_bf16 v[16:19], a[128:131], a[64:67], v[16:19]\n"
    "s_add_u32 m0, s78, 2048\n"
    "s_add_u32 s83, s83, 114688\n"
    "v_mfma_f32_16x16x32_bf16 v[16:19], a[132:135], a[68:71], v[16:19]\n"
    "s_waitcnt lgkmcnt(0)\n"
    "v_mfma_f32_16x16x32_bf16 v[16:19], a[136:139], a[72:75], v[16:19]\n"
    "s_add_u32 s88, s80, 786432\n"
    "ds_read_b64_tr_b16 a[144:145], v10 offset:12288\n"
    "v_mfma_f32_16x16x32_bf16 v[16:19], a[140:143], a[76:79], v[16:19]\n"
    "s_mul_i32 s84, s76, 24576\n"
    "ds_read_b64_tr_b16 a[146:147], v10 offset:13824\n"
    "v_mfma_f32_16x16x32_bf16 v[20:23], a[128:131], a[80:83], v[20:23]\n"
    "ds_read_b64_tr_b16 a[148:149], v10 offset:15360\n"
    "v_mfma_f32_16x16x32_bf16 v[20:23], a[132:135], a[84:87], v[20:23]\n"
    "buffer_load_dwordx4 v4, s[48:51], s88 offen offset:0 lds\n"
    "v_mfma_f32_16x16x32_bf16 v[20:23], a[136:139], a[88:91], v[20:23]\n"
    "s_add_u32 s84, s84, s86\n"
    "ds_read_b64_tr_b16 a[150:151], v10 offset:16896\n"
    "ds_read_b64_tr_b16 a[152:153], v10 offset:18432\n"
    "v_mfma_f32_16x16x32_bf16 v[20:23], a[140:143], a[92:95], v[20:23]\n"
    "ds_read_b64_tr_b16 a[154:155], v10 offset:19968\n"
    "v_mfma_f32_16x16x32_bf16 v[24:27], a[128:131], a[96:99], v[24:27]\n"
    "s_add_u32 m0, s78, 12288\n"
    "ds_read_b64_tr_b16 a[156:157], v10 offset:21504\n"
    "s_add_u32 s84, s84, 589824\n"
    "v_mfma_f32_16x16x32_bf16 v[24:27], a[132:135], a[100:103], v[24:27]\n"
    "ds_read_b64_tr_b16 a[158:159], v10 offset:23040\n"
    "v_mfma_f32_16x16x32_bf16 v[24:27], a[136:139], a[104:107], v[24:27]\n"
    "s_add_u32 s88, s80, 786432\n"
    "s_lshl_b32 s85, s76, 13\n"
    "v_mfma_f32_16x16x32_bf16 v[24:27], a[140:143], a[108:111], v[24:27]\n"
    "v_mfma_f32_16x16x32_bf16 v[28:31], a[128:131], a[112:115], v[28:31]\n"
    "v_mfma_f32_16x16x32_bf16 v[28:31], a[132:135], a[116:119], v[28:31]\n"
    "buffer_load_dwordx4 v2, s[52:55], s88 offen offset:0 lds\n"
    "v_mfma_f32_16x16x32_bf16 v[28:31], a[136:139], a[120:123], v[28:31]\n"
    "s_mov_b32 s91, 0x3f803f80\n"
    "v_mfma_f32_16x16x32_bf16 v[28:31], a[140:143], a[124:127], v[28:31]\n"
    "s_waitcnt lgkmcnt(4)\n"
    "v_mfma_f32_16x16x32_bf16 v[32:35], a[144:147], a[64:67], v[32:35]\n"
    "s_add_u32 m0, s78, 13312\n"
    "v_mfma_f32_16x16x32_bf16 v[32:35], a[148:151], a[68:71], v[32:35]\n"
    "s_waitcnt lgkmcnt(0)\n"
    "v_mfma_f32_16x16x32_bf16 v[32:35], a[152:155], a[72:75], v[32:35]\n"
    "s_add_u32 s88, s80, 786432\n"
    "ds_read_b64_tr_b16 a[128:129], v10 offset:12320\n"
    "v_mfma_f32_16x16x32_bf16 v[32:35], a[156:159], a[76:79], v[32:35]\n"
    "ds_read_b64_tr_b16 a[130:131], v10 offset:13856\n"
    "v_mfma_f32_16x16x32_bf16 v[36:39], a[144:147], a[80:83], v[36:39]\n"
    "ds_read_b64_tr_b16 a[132:133], v10 offset:15392\n"
    "v_mfma_f32_16x16x32_bf16 v[36:39], a[148:151], a[84:87], v[36:39]\n"
    "buffer_load_dwordx4 v3, s[52:55], s88 offen offset:0 lds\n"
    "v_mfma_f32_16x16x32_bf16 v[36:39], a[152:155], a[88:91], v[36:39]\n"
    "ds_read_b64_tr_b16 a[134:135], v10 offset:16928\n"
    "ds_read_b64_tr_b16 a[136:137], v10 offset:18464\n"
    "v_mfma_f32_16x16x32_bf16 v[36:39], a[156:159], a[92:95], v[36:39]\n"
    "ds_read_b64_tr_b16 a[138:139], v10 offset:20000\n"
    "v_mfma_f32_16x16x32_bf16 v[40:43], a[144:147], a[96:99], v[40:43]\n"
    "s_add_u32 m0, s78, 14336\n"
    "ds_read_b64_tr_b16 a[140:141], v10 offset:21536\n"
    "v_mfma_f32_16x16x32_bf16 v[40:43], a[148:151], a[100:103], v[40:43]\n"
    "ds_read_b64_tr_b16 a[142:143], v10 offset:23072\n"
    "v_mfma_f32_16x16x32_bf16 v[40:43], a[152:155], a[104:107], v[40:43]\n"
    "s_add_u32 s88, s80, 786432\n"
    "v_mfma_f32_16x16x32_bf16 v[40:43], a[156:159], a[108:111], v[40:43]\n"
    "v_mfma_f32_16x16x32_bf16 v[44:47], a[144:147], a[112:115], v[44:47]\n"
    "v_mfma_f32_16x16x32_bf16 v[44:47], a[148:151], a[116:119], v[44:47]\n"
    "buffer_load_dwordx4 v4, s[52:55], s88 offen offset:0 lds\n"
    "v_mfma_f32_16x16x32_bf16 v[44:47], a[152:155], a[120:123], v[44:47]\n"
    "v_mfma_f32_16x16x32_bf16 v[44:47], a[156:159], a[124:127], v[44:47]\n"
    "s_waitcnt lgkmcnt(4)\n"
    "v_mfma_f32_16x16x32_bf16 v[48:51], a[128:131], a[64:67], v[48:51]\n"
    "v_mfma_f32_16x16x32_bf16 v[48:51], a[132:135], a[68:71], v[48:51]\n"
    "s_waitcnt lgkmcnt(0)\n"
    "v_mfma_f32_16x16x32_bf16 v[48:51], a[136:139], a[72:75], v[48:51]\n"
    "ds_read_b64_tr_b16 a[144:145], v10 offset:12352\n"
    "v_mfma_f32_16x16x32_bf16 v[48:51], a[140:143], a[76:79], v[48:51]\n"
    "ds_read_b64_tr_b16 a[146:147], v10 offset:13888\n"
    "v_mfma_f32_16x16x32_bf16 v[52:55], a[128:131], a[80:83], v[52:55]\n"
    "ds_read_b64_tr_b16 a[148:149], v10 offset:15424\n"
    "v_mfma_f32_16x16x32_bf16 v[52:55], a[132:135], a[84:87], v[52:55]\n"
    "ds_read_b64_tr_b16 a[150:151], v10 offset:16960\n"
    "v_mfma_f32_16x16x32_bf16 v[52:55], a[136:139], a[88:91], v[52:55]\n"
    "ds_read_b64_tr_b16 a[152:153], v10 offset:18496\n"
    "v_mfma_f32_16x16x32_bf16 v[52:55], a[140:143], a[92:95], v[52:55]\n"
    "ds_read_b64_tr_b16 a[154:155], v10 offset:20032\n"
    "v_mfma_f32_16x16x32_bf16 v[56:59], a[128:131], a[96:99], v[56:59]\n"
    "ds_read_b64_tr_b16 a[156:157], v10 offset:21568\n"
    "v_mfma_f32_16x16x32_bf16 v[56:59], a[132:135], a[100:103], v[56:59]\n"
    "ds_read_b64_tr_b16 a[158:159], v10 offset:23104\n"
    "v_mfma_f32_16x16x32_bf16 v[56:59], a[136:139], a[104:107], v[56:59]\n"
    "v_mfma_f32_16x16x32_bf16 v[56:59], a[140:143], a[108:111], v[56:59]\n"
    "v_mfma_f32_16x16x32_bf16 v[60:63], a[128:131], a[112:115], v[60:63]\n"
    "v_mfma_f32_16x16x32_bf16 v[60:63], a[132:135], a[116:119], v[60:63]\n"
    "v_mfma_f32_16x16x32_bf16 v[60:63], a[136:139], a[120:123], v[60:63]\n"
    "v_mfma_f32_16x16x32_bf16 v[60:63], a[140:143], a[124:127], v[60:63]\n"
    "s_waitcnt vmcnt(0) lgkmcnt(0)\n"
    "s_barrier\n"
    "v_mfma_f32_16x16x32_bf16 v[64:67], a[144:147], a[64:67], v[64:67]\n"
    "ds_read_b64_tr_b16 a[128:129], v7 offset:0\n"
    "v_mfma_f32_16x16x32_bf16 v[64:67], a[148:151], a[68:71], v[64:67]\n"
    "ds_read_b64_tr_b16 a[130:131], v7 offset:1536\n"
    "v_mfma_f32_16x16x32_bf16 v[64:67], a[152:155], a[72:75], v[64:67]\n"
    "ds_read_b64_tr_b16 a[132:133], v7 offset:3072\n"
    "v_mfma_f32_16x16x32_bf16 v[64:67], a[156:159], a[76:79], v[64:67]\n"
    "ds_read_b64_tr_b16 a[134:135], v7 offset:4608\n"
    "v_mfma_f32_16x16x32_bf16 v[68:71], a[144:147], a[80:83], v[68:71]\n"
    "ds_read_b64_tr_b16 a[136:137], v7 offset:6144\n"
    "v_mfma_f32_16x16x32_bf16 v[68:71], a[148:151], a[84:87], v[68:71]\n"
    "ds_read_b64_tr_b16 a[138:139], v7 offset:7680\n"
    "v_mfma_f32_16x16x32_bf16 v[68:71], a[152:155], a[88:91], v[68:71]\n"
    "ds_read_b64_tr_b16 a[140:141], v7 offset:9216\n"
    "v_mfma_f32_16x16x32_bf16 v[68:71], a[156:159], a[92:95], v[68:71]\n"
    "ds_read_b64_tr_b16 a[142:143], v7 offset:10752\n"
    "v_mfma_f32_16x16x32_bf16 v[72:75], a[144:147], a[96:99], v[72:75]\n"
    "v_mfma_f32_16x16x32_bf16 v[72:75], a[148:151], a[100:103], v[72:75]\n"
    "v_mfma_f32_16x16x32_bf16 v[72:75], a[152:155], a[104:107], v[72:75]\n"
    "v_mfma_f32_16x16x32_bf16 v[72:75], a[156:159], a[108:111], v[72:75]\n"
    "v_mfma_f32_16x16x32_bf16 v[76:79], a[144:147], a[112:115], v[76:79]\n"
    "v_mfma_f32_16x16x32_bf16 v[76:79], a[148:151], a[116:119], v[76:79]\n"
    "v_mfma_f32_16x16x32_bf16 v[76:79], a[152:155], a[120:123], v[76:79]\n"
    "v_mfma_f32_16x16x32_bf16 v[76:79], a[156:159], a[124:127], v[76:79]\n"
    ".L_q1_%=:\n"
    "s_waitcnt lgkmcnt(4)\n"
    "v_mfma_f32_16x16x32_bf16 v[224:227], a[128:131], a[0:3], v[224:227]\n"
    "v_mfma_f32_16x16x32_bf16 v[224:227], a[132:135], a[4:7], v[224:227]\n"
    "s_waitcnt lgkmcnt(0)\n"
    "v_mfma_f32_16x16x32_bf16 v[224:227], a[136:139], a[8:11], v[224:227]\n"
    "ds_read_b64_tr_b16 a[144:145], v7 offset:32\n"
    "v_mfma_f32_16x16x32_bf16 v[224:227], a[140:143], a[12:15], v[224:227]\n"
    "ds_read_b64_tr_b16 a[146:147], v7 offset:1568\n"
    "v_mfma_f32_16x16x32_bf16 v[228:231], a[128:131], a[16:19], v[228:231]\n"
    "s_add_u32 m0, s78, 73744\n"
    "ds_read_b64_tr_b16 a[148:149], v7 offset:3104\n"
    "v_mfma_f32_16x16x32_bf16 v[228:231], a[132:135], a[20:23], v[228:231]\n"
    "ds_read_b64_tr_b16 a[150:151], v7 offset:4640\n"
    "v_mfma_f32_16x16x32_bf16 v[228:231], a[136:139], a[24:27], v[228:231]\n"
    "ds_read_b64_tr_b16 a[152:153], v7 offset:6176\n"
    "v_mfma_f32_16x16x32_bf16 v[228:231], a[140:143], a[28:31], v[228:231]\n"
    "s_add_u32 s88, s80, 1179648\n"
    "ds_read_b64_tr_b16 a[154:155], v7 offset:7712\n"
    "v_mfma_f32_16x16x32_bf16 v[232:235], a[128:131], a[32:35], v[232:235]\n"
    "ds_read_b64_tr_b16 a[156:157], v7 offset:9248\n"
    "v_mfma_f32_16x16x32_bf16 v[232:235], a[132:135], a[36:39], v[232:235]\n"
    "ds_read_b64_tr_b16 a[158:159], v7 offset:10784\n"
    "v_mfma_f32_16x16x32_bf16 v[232:235], a[136:139], a[40:43], v[232:235]\n"
    "buffer_load_dwordx4 v2, s[48:51], s88 offen offset:0 lds\n"
    "v_mfma_f32_16x16x32_bf16 v[232:235], a[140:143], a[44:47], v[232:235]\n"
    "v_mfma_f32_16x16x32_bf16 v[236:239], a[128:131], a[48:51], v[236:239]\n"
    "v_mfma_f32_16x16x32_bf16 v[236:239], a[132:135], a[52:55], v[236:239]\n"
    "s_add_u32 m0, s78, 74768\n"
    "v_mfma_f32_16x16x32_bf16 v[236:239], a[136:139], a[56:59], v[236:239]\n"
    "v_mfma_f32_16x16x32_bf16 v[236:239], a[140:143], a[60:63], v[236:239]\n"
    "s_waitcnt lgkmcnt(4)\n"
    "v_mfma_f32_16x16x32_bf16 v[240:243], a[144:147], a[0:3], v[240:243]\n"
    "s_add_u32 s88, s80, 1179648\n"
    "v_mfma_f32_16x16x32_bf16 v[240:243], a[148:151], a[4:7], v[240:243]\n"
    "s_waitcnt lgkmcnt(0)\n"
    "v_mfma_f32_16x16x32_bf16 v[240:243], a[152:155], a[8:11], v[240:243]\n"
    "ds_read_b64_tr_b16 a[128:129], v7 offset:64\n"
    "v_mfma_f32_16x16x32_bf16 v[240:243], a[156:159], a[12:15], v[240:243]\n"
    "buffer_load_dwordx4 v3, s[48:51], s88 offen offset:0 lds\n"
    "v_mfma_f32_16x16x32_bf16 v[244:247], a[144:147], a[16:19], v[244:247]\n"
    "ds_read_b64_tr_b16 a[130:131], v7 offset:1600\n"
    "ds_read_b64_tr_b16 a[132:133], v7 offset:3136\n"
    "v_mfma_f32_16x16x32_bf16 v[244:247], a[148:151], a[20:23], v[244:247]\n"
    "ds_read_b64_tr_b16 a[134:135], v7 offset:4672\n"
    "v_mfma_f32_16x16x32_bf16 v[244:247], a[152:155], a[24:27], v[244:247]\n"
    "s_add_u32 m0, s78, 75792\n"
    "ds_read_b64_tr_b16 a[136:137], v7 offset:6208\n"
    "v_mfma_f32_16x16x32_bf16 v[244:247], a[156:159], a[28:31], v[244:247]\n"
    "ds_read_b64_tr_b16 a[138:139], v7 offset:7744\n"
    "v_mfma_f32_16x16x32_bf16 v[248:251], a[144:147], a[32:35], v[248:251]\n"
    "ds_read_b64_tr_b16 a[140:141], v7 offset:9280\n"
    "v_mfma_f32_16x16x32_bf16 v[248:251], a[148:151], a[36:39], v[248:251]\n"
    "s_add_u32 s88, s80, 1179648\n"
    "ds_read_b64_tr_b16 a[142:143], v7 offset:10816\n"
    "v_mfma_f32_16x16x32_bf16 v[248:251], a[152:155], a[40:43], v[248:251]\n"
    "v_mfma_f32_16x16x32_bf16 v[248:251], a[156:159], a[44:47], v[248:251]\n"
    "v_mfma_f32_16x16x32_bf16 v[252:255], a[144:147], a[48:51], v[252:255]\n"
    "buffer_load_dwordx4 v4, s[48:51], s88 offen offset:0 lds\n"
    "v_mfma_f32_16x16x32_bf16 v[252:255], a[148:151], a[52:55], v[252:255]\n"
    "v_mfma_f32_16x16x32_bf16 v[252:255], a[152:155], a[56:59], v[252:255]\n"
    "v_mfma_f32_16x16x32_bf16 v[252:255], a[156:159], a[60:63], v[252:255]\n"
    "s_waitcnt lgkmcnt(4)\n"
    "s_add_u32 s89, s81, 12288\n"
    "v_mfma_f32_16x16x32_bf16 v[16:19], a[128:131], a[0:3], v[16:19]\n"
    "v_mfma_f32_16x16x32_bf16 v[16:19], a[132:135], a[4:7], v[16:19]\n"
    "s_waitcnt lgkmcnt(0)\n"
    "v_mfma_f32_16x16x32_bf16 v[16:19], a[136:139], a[8:11], v[16:19]\n"
    "buffer_load_dwordx4 a[64:67], v0, s[40:43], s89 offen offset:0 sc1\n"
    "v_mfma_f32_16x16x32_bf16 v[16:19], a[140:143], a[12:15], v[16:19]\n"
    "ds_read_b64_tr_b16 a[144:145], v7 offset:12288\n"
    "ds_read_b64_tr_b16 a[146:147], v7 offset:13824\n"
    "v_mfma_f32_16x16x32_bf16 v[20:23], a[128:131], a[16:19], v[20:23]\n"
    "ds_read_b64_tr_b16 a[148:149], v7 offset:15360\n"
    "v_mfma_f32_16x16x32_bf16 v[20:23], a[132:135], a[20:23], v[20:23]\n"
    "buffer_load_dwordx4 a[68:71], v0, s[40:43], s89 offen offset:1024 sc1\n"
    "v_mfma_f32_16x16x32_bf16 v[20:23], a[136:139], a[24:27], v[20:23]\n"
    "ds_read_b64_tr_b16 a[150:151], v7 offset:16896\n"
    "ds_read_b64_tr_b16 a[152:153], v7 offset:18432\n"
    "v_mfma_f32_16x16x32_bf16 v[20:23], a[140:143], a[28:31], v[20:23]\n"
    "ds_read_b64_tr_b16 a[154:155], v7 offset:19968\n"
    "v_mfma_f32_16x16x32_bf16 v[24:27], a[128:131], a[32:35], v[24:27]\n"
    "buffer_load_dwordx4 a[72:75], v0, s[40:43], s89 offen offset:2048 sc1\n"
    "v_mfma_f32_16x16x32_bf16 v[24:27], a[132:135], a[36:39], v[24:27]\n"
    "ds_read_b64_tr_b16 a[156:157], v7 offset:21504\n"
    "ds_read_b64_tr_b16 a[158:159], v7 offset:23040\n"
    "v_mfma_f32_16x16x32_bf16 v[24:27], a[136:139], a[40:43], v[24:27]\n"
    "v_mfma_f32_16x16x32_bf16 v[24:27], a[140:143], a[44:47], v[24:27]\n"
    "buffer_load_dwordx4 a[76:79], v0, s[40:43], s89 offen offset:3072 sc1\n"
    "v_mfma_f32_16x16x32_bf16 v[28:31], a[128:131], a[48:51], v[28:31]\n"
    "v_mfma_f32_16x16x32_bf16 v[28:31], a[132:135], a[52:55], v[28:31]\n"
    "v_mfma_f32_16x16x32_bf16 v[28:31], a[136:139], a[56:59], v[28:31]\n"
    "s_add_u32 m0, s78, 86032\n"
    "v_mfma_f32_16x16x32_bf16 v[28:31], a[140:143], a[60:63], v[28:31]\n"
    "s_waitcnt lgkmcnt(4)\n"
    "v_mfma_f32_16x16x32_bf16 v[32:35], a[144:147], a[0:3], v[32:35]\n"
    "v_mfma_f32_16x16x32_bf16 v[32:35], a[148:151], a[4:7], v[32:35]\n"
    "s_waitcnt lgkmcnt(0)\n"
    "s_add_u32 s88, s80, 1179648\n"
    "v_mfma_f32_16x16x32_bf16 v[32:35], a[152:155], a[8:11], v[32:35]\n"
    "ds_read_b64_tr_b16 a[128:129], v7 offset:12320\n"
    "v_mfma_f32_16x16x32_bf16 v[32:35], a[156:159], a[12:15], v[32:35]\n"
    "ds_read_b64_tr_b16 a[130:131], v7 offset:13856\n"
    "v_mfma_f32_16x16x32_bf16 v[36:39], a[144:147], a[16:19], v[36:39]\n"
    "buffer_load_dwordx4 v2, s[52:55], s88 offen offset:0 lds\n"
    "v_mfma_f32_16x16x32_bf16 v[36:39], a[148:151], a[20:23], v[36:39]\n"
    "ds_read_b64_tr_b16 a[132:133], v7 offset:15392\n"
    "ds_read_b64_tr_b16 a[134:135], v7 offset:16928\n"
    "v_mfma_f32_16x16x32_bf16 v[36:39], a[152:155], a[24:27], v[36:39]\n"
    "ds_read_b64_tr_b16 a[136:137], v7 offset:18464\n"
    "v_mfma_f32_16x16x32_bf16 v[36:39], a[156:159], a[28:31], v[36:39]\n"
    "s_add_u32 m0, s78, 87056\n"
    "ds_read_b64_tr_b16 a[138:139], v7 offset:20000\n"
    "v_mfma_f32_16x16x32_bf16 v[40:43], a[144:147], a[32:35], v[40:43]\n"
    "ds_read_b64_tr_b16 a[140:141], v7 offset:21536\n"
    "v_mfma_f32_16x16x32_bf16 v[40:43], a[148:151], a[36:39], v[40:43]\n"
    "ds_read_b64_tr_b16 a[142:143], v7 offset:23072\n"
    "v_mfma_f32_16x16x32_bf16 v[40:43], a[152:155], a[40:43], v[40:43]\n"
    "s_add_u32 s88, s80, 1179648\n"
    "v_mfma_f32_16x16x32_bf16 v[40:43], a[156:159], a[44:47], v[40:43]\n"
    "v_mfma_f32_16x16x32_bf16 v[44:47], a[144:147], a[48:51], v[44:47]\n"
    "v_mfma_f32_16x16x32_bf16 v[44:47], a[148:151], a[52:55], v[44:47]\n"
    "v_mfma_f32_16x16x32_bf16 v[44:47], a[152:155], a[56:59], v[44:47]\n"
    "buffer_load_dwordx4 v3, s[52:55], s88 offen offset:0 lds\n"
    "v_mfma_f32_16x16x32_bf16 v[44:47], a[156:159], a[60:63], v[44:47]\n"
    "s_waitcnt lgkmcnt(4)\n"
    "v_mfma_f32_16x16x32_bf16 v[48:51], a[128:131], a[0:3], v[48:51]\n"
    "v_mfma_f32_16x16x32_bf16 v[48:51], a[132:135], a[4:7], v[48:51]\n"
    "s_waitcnt lgkmcnt(0)\n"
    "s_add_u32 m0, s78, 88080\n"
    "v_mfma_f32_16x16x32_bf16 v[48:51], a[136:139], a[8:11], v[48:51]\n"
    "ds_read_b64_tr_b16 a[144:145], v7 offset:12352\n"
    "v_mfma_f32_16x16x32_bf16 v[48:51], a[140:143], a[12:15], v[48:51]\n"
    "ds_read_b64_tr_b16 a[146:147], v7 offset:13888\n"
    "v_mfma_f32_16x16x32_bf16 v[52:55], a[128:131], a[16:19], v[52:55]\n"
    "s_add_u32 s88, s80, 1179648\n"
    "ds_read_b64_tr_b16 a[148:149], v7 offset:15424\n"
    "v_mfma_f32_16x16x32_bf16 v[52:55], a[132:135], a[20:23], v[52:55]\n"
    "ds_read_b64_tr_b16 a[150:151], v7 offset:16960\n"
    "v_mfma_f32_16x16x32_bf16 v[52:55], a[136:139], a[24:27], v[52:55]\n"
    "ds_read_b64_tr_b16 a[152:153], v7 offset:18496\n"
    "v_mfma_f32_16x16x32_bf16 v[52:55], a[140:143], a[28:31], v[52:55]\n"
    "buffer_load_dwordx4 v4, s[52:55], s88 offen offset:0 lds\n"
    "v_mfma_f32_16x16x32_bf16 v[56:59], a[128:131], a[32:35], v[56:59]\n"
    "ds_read_b64_tr_b16 a[154:155], v7 offset:20032\n"
    "ds_read_b64_tr_b16 a[156:157], v7 offset:21568\n"
    "v_mfma_f32_16x16x32_bf16 v[56:59], a[132:135], a[36:39], v[56:59]\n"
    "ds_read_b64_tr_b16 a[158:159], v7 offset:23104\n"
    "v_mfma_f32_16x16x32_bf16 v[56:59], a[136:139], a[40:43], v[56:59]\n"
    "s_add_u32 s89, s3, 12288\n"
    "v_mfma_f32_16x16x32_bf16 v[56:59], a[140:143], a[44:47], v[56:59]\n"
    "v_mfma_f32_16x16x32_bf16 v[60:63], a[128:131], a[48:51], v[60:63]\n"
    "v_mfma_f32_16x16x32_bf16 v[60:63], a[132:135], a[52:55], v[60:63]\n"
    "buffer_load_dwordx4 a[80:83], v0, s[40:43], s89 offen offset:0 sc1\n"
    "v_mfma_f32_16x16x32_bf16 v[60:63], a[136:139], a[56:59], v[60:63]\n"
    "v_mfma_f32_16x16x32_bf16 v[60:63], a[140:143], a[60:63], v[60:63]\n"
    "s_waitcnt lgkmcnt(4)\n"
    "v_mfma_f32_16x16x32_bf16 v[64:67], a[144:147], a[0:3], v[64:67]\n"
    "buffer_load_dwordx4 a[84:87], v0, s[40:43], s89 offen offset:1024 sc1\n"
    "v_mfma_f32_16x16x32_bf16 v[64:67], a[148:151], a[4:7], v[64:67]\n"
    "s_waitcnt lgkmcnt(0)\n"
    "v_mfma_f32_16x16x32_bf16 v[64:67], a[152:155], a[8:11], v[64:67]\n"
    "ds_read_b64_tr_b16 a[128:129], v5 offset:0\n"
    "v_mfma_f32_16x16x32_bf16 v[64:67], a[156:159], a[12:15], v[64:67]\n"
    "buffer_load_dwordx4 a[88:91], v0, s[40:43], s89 offen offset:2048 sc1\n"
    "v_mfma_f32_16x16x32_bf16 v[68:71], a[144:147], a[16:19], v[68:71]\n"
    "ds_read_b64_tr_b16 a[130:131], v5 offset:512\n"
    "ds_read_b64_tr_b16 a[132:133], v5 offset:1024\n"
    "v_mfma_f32_16x16x32_bf16 v[68:71], a[148:151], a[20:23], v[68:71]\n"
    "ds_read_b64_tr_b16 a[134:135], v5 offset:1536\n"
    "v_mfma_f32_16x16x32_bf16 v[68:71], a[152:155], a[24:27], v[68:71]\n"
    "buffer_load_dwordx4 a[92:95], v0, s[40:43], s89 offen offset:3072 sc1\n"
    "v_mfma_f32_16x16x32_bf16 v[68:71], a[156:159], a[28:31], v[68:71]\n"
    "ds_read_b64_tr_b16 a[136:137], v5 offset:2048\n"
    "ds_read_b64_tr_b16 a[138:139], v5 offset:2560\n"
    "v_mfma_f32_16x16x32_bf16 v[72:75], a[144:147], a[32:35], v[72:75]\n"
    "ds_read_b64_tr_b16 a[140:141], v5 offset:3072\n"
    "v_mfma_f32_16x16x32_bf16 v[72:75], a[148:151], a[36:39], v[72:75]\n"
    "s_add_u32 m0, s77, 73744\n"
    "ds_read_b64_tr_b16 a[142:143], v5 offset:3584\n"
    "v_mfma_f32_16x16x32_bf16 v[72:75], a[152:155], a[40:43], v[72:75]\n"
    "v_mfma_f32_16x16x32_bf16 v[72:75], a[156:159], a[44:47], v[72:75]\n"
    "v_mfma_f32_16x16x32_bf16 v[76:79], a[144:147], a[48:51], v[76:79]\n"
    "s_add_u32 s88, s79, 4718592\n"
    "v_mfma_f32_16x16x32_bf16 v[76:79], a[148:151], a[52:55], v[76:79]\n"
    "v_mfma_f32_16x16x32_bf16 v[76:79], a[152:155], a[56:59], v[76:79]\n"
    "v_mfma_f32_16x16x32_bf16 v[76:79], a[156:159], a[60:63], v[76:79]\n"
    "s_waitcnt lgkmcnt(4)\n"
    "v_mfma_f32_16x16x32_bf16 v[80:83], a[128:131], a[0:3], v[80:83]\n"
    "buffer_load_dwordx4 v1, s[44:47], s88 offen offset:0 lds\n"
    "v_mfma_f32_16x16x32_bf16 v[80:83], a[132:135], a[4:7], v[80:83]\n"
    "s_waitcnt lgkmcnt(0)\n"
    "v_mfma_f32_16x16x32_bf16 v[80:83], a[136:139], a[8:11], v[80:83]\n"
    "s_add_u32 m0, s77, 77840\n"
    "ds_read_b64_tr_b16 a[144:145], v5 offset:8\n"
    "v_mfma_f32_16x16x32_bf16 v[80:83], a[140:143], a[12:15], v[80:83]\n"
    "ds_read_b64_tr_b16 a[146:147], v5 offset:520\n"
    "v_mfma_f32_16x16x32_bf16 v[84:87], a[128:131], a[16:19], v[84:87]\n"
    "ds_read_b64_tr_b16 a[148:149], v5 offset:1032\n"
    "v_mfma_f32_16x16x32_bf16 v[84:87], a[132:135], a[20:23], v[84:87]\n"
    "s_add_u32 s88, s79, 4718720\n"
    "ds_read_b64_tr_b16 a[150:151], v5 offset:1544\n"
    "v_mfma_f32_16x16x32_bf16 v[84:87], a[136:139], a[24:27], v[84:87]\n"
    "ds_read_b64_tr_b16 a[152:153], v5 offset:2056\n"
    "v_mfma_f32_16x16x32_bf16 v[84:87], a[140:143], a[28:31], v[84:87]\n"
    "ds_read_b64_tr_b16 a[154:155], v5 offset:2568\n"
    "v_mfma_f32_16x16x32_bf16 v[88:91], a[128:131], a[32:35], v[88:91]\n"
    "buffer_load_dwordx4 v1, s[44:47], s88 offen offset:0 lds\n"
    "v_mfma_f32_16x16x32_bf16 v[88:91], a[132:135], a[36:39], v[88:91]\n"
    "ds_read_b64_tr_b16 a[156:157], v5 offset:3080\n"
    "ds_read_b64_tr_b16 a[158:159], v5 offset:3592\n"
    "v_mfma_f32_16x16x32_bf16 v[88:91], a[136:139], a[40:43], v[88:91]\n"
    "v_mfma_f32_16x16x32_bf16 v[88:91], a[140:143], a[44:47], v[88:91]\n"
    "s_add_u32 m0, s77, 81936\n"
    "v_mfma_f32_16x16x32_bf16 v[92:95], a[128:131], a[48:51], v[92:95]\n"
    "v_mfma_f32_16x16x32_bf16 v[92:95], a[132:135], a[52:55], v[92:95]\n"
    "v_mfma_f32_16x16x32_bf16 v[92:95], a[136:139], a[56:59], v[92:95]\n"
    "s_add_u32 s88, s79, 4718848\n"
    "v_mfma_f32_16x16x32_bf16 v[92:95], a[140:143], a[60:63], v[92:95]\n"
    "s_waitcnt lgkmcnt(4)\n"
    "v_mfma_f32_16x16x32_bf16 v[96:99], a[144:147], a[0:3], v[96:99]\n"
    "v_mfma_f32_16x16x32_bf16 v[96:99], a[148:151], a[4:7], v[96:99]\n"
    "s_waitcnt lgkmcnt(0)\n"
    "v_mfma_f32_16x16x32_bf16 v[96:99], a[152:155], a[8:11], v[96:99]\n"
    "buffer_load_dwordx4 v1, s[44:47], s88 offen offset:0 lds\n"
    "v_mfma_f32_16x16x32_bf16 v[96:99], a[156:159], a[12:15], v[96:99]\n"
    "ds_read_b64_tr_b16 a[128:129], v6 offset:0\n"
    "ds_read_b64_tr_b16 a[130:131], v6 offset:512\n"
    "v_mfma_f32_16x16x32_bf16 v[100:103], a[144:147], a[16:19], v[100:103]\n"
    "ds_read_b64_tr_b16 a[132:133], v6 offset:1024\n"
    "v_mfma_f32_16x16x32_bf16 v[100:103], a[148:151], a[20:23], v[100:103]\n"
    "s_add_u32 m0, s77, 74768\n"
    "ds_read_b64_tr_b16 a[134:135], v6 offset:1536\n"
    "v_mfma_f32_16x16x32_bf16 v[100:103], a[152:155], a[24:27], v[100:103]\n"
    "ds_read_b64_tr_b16 a[136:137], v6 offset:2048\n"
    "v_mfma_f32_16x16x32_bf16 v[100:103], a[156:159], a[28:31], v[100:103]\n"
    "ds_read_b64_tr_b16 a[138:139], v6 offset:2560\n"
    "v_mfma_f32_16x16x32_bf16 v[104:107], a[144:147], a[32:35], v[104:107]\n"
    "s_add_u32 s88, s79, 5111808\n"
    "ds_read_b64_tr_b16 a[140:141], v6 offset:3072\n"
    "v_mfma_f32_16x16x32_bf16 v[104:107], a[148:151], a[36:39], v[104:107]\n"
    "ds_read_b64_tr_b16 a[142:143], v6 offset:3584\n"
    "v_mfma_f32_16x16x32_bf16 v[104:107], a[152:155], a[40:43], v[104:107]\n"
    "v_mfma_f32_16x16x32_bf16 v[104:107], a[156:159], a[44:47], v[104:107]\n"
    "buffer_load_dwordx4 v1, s[44:47], s88 offen offset:0 lds\n"
    "v_mfma_f32_16x16x32_bf16 v[108:111], a[144:147], a[48:51], v[108:111]\n"
    "v_mfma_f32_16x16x32_bf16 v[108:111], a[148:151], a[52:55], v[108:111]\n"
    "v_mfma_f32_16x16x32_bf16 v[108:111], a[152:155], a[56:59], v[108:111]\n"
    "s_add_u32 m0, s77, 78864\n"
    "v_mfma_f32_16x16x32_bf16 v[108:111], a[156:159], a[60:63], v[108:111]\n"
    "s_waitcnt lgkmcnt(4)\n"
    "v_mfma_f32_16x16x32_bf16 a[160:163], a[128:131], a[0:3], a[160:163]\n"
    "v_mfma_f32_16x16x32_bf16 a[160:163], a[132:135], a[4:7], a[160:163]\n"
    "s_waitcnt lgkmcnt(0)\n"
    "s_add_u32 s88, s79, 5111936\n"
    "v_mfma_f32_16x16x32_bf16 a[160:163], a[136:139], a[8:11], a[160:163]\n"
    "ds_read_b64_tr_b16 a[144:145], v6 offset:8\n"
    "v_mfma_f32_16x16x32_bf16 a[160:163], a[140:143], a[12:15], a[160:163]\n"
    "ds_read_b64_tr_b16 a[146:147], v6 offset:520\n"
    "v_mfma_f32_16x16x32_bf16 a[164:167], a[128:131], a[16:19], a[164:167]\n"
    "buffer_load_dwordx4 v1, s[44:47], s88 offen offset:0 lds\n"
    "v_mfma_f32_16x16x32_bf16 a[164:167], a[132:135], a[20:23], a[164:167]\n"
    "ds_read_b64_tr_b16 a[148:149], v6 offset:1032\n"
    "ds_read_b64_tr_b16 a[150:151], v6 offset:1544\n"
    "v_mfma_f32_16x16x32_bf16 a[164:167], a[136:139], a[24:27], a[164:167]\n"
    "ds_read_b64_tr_b16 a[152:153], v6 offset:2056\n"
    "v_mfma_f32_16x16x32_bf16 a[164:167], a[140:143], a[28:31], a[164:167]\n"
    "s_add_u32 m0, s77, 82960\n"
    "ds_read_b64_tr_b16 a[154:155], v6 offset:2568\n"
    "v_mfma_f32_16x16x32_bf16 a[168:171], a[128:131], a[32:35], a[168:171]\n"
    "ds_read_b64_tr_b16 a[156:157], v6 offset:3080\n"
    "v_mfma_f32_16x16x32_bf16 a[168:171], a[132:135], a[36:39], a[168:171]\n"
    "ds_read_b64_tr_b16 a[158:159], v6 offset:3592\n"
    "v_mfma_f32_16x16x32_bf16 a[168:171], a[136:139], a[40:43], a[168:171]\n"
    "s_add_u32 s88, s79, 5112064\n"
    "v_mfma_f32_16x16x32_bf16 a[168:171], a[140:143], a[44:47], a[168:171]\n"
    "v_mfma_f32_16x16x32_bf16 a[172:175], a[128:131], a[48:51], a[172:175]\n"
    "v_mfma_f32_16x16x32_bf16 a[172:175], a[132:135], a[52:55], a[172:175]\n"
    "buffer_load_dwordx4 v1, s[44:47], s88 offen offset:0 lds\n"
    "v_mfma_f32_16x16x32_bf16 a[172:175], a[136:139], a[56:59], a[172:175]\n"
    "v_mfma_f32_16x16x32_bf16 a[172:175], a[140:143], a[60:63], a[172:175]\n"
    "s_waitcnt lgkmcnt(4)\n"
    "v_mfma_f32_16x16x32_bf16 a[176:179], a[144:147], a[0:3], a[176:179]\n"
    "s_add_u32 s89, s39, 12288\n"
    "v_mfma_f32_16x16x32_bf16 a[176:179], a[148:151], a[4:7], a[176:179]\n"
    "s_waitcnt lgkmcnt(0)\n"
    "v_mfma_f32_16x16x32_bf16 a[176:179], a[152:155], a[8:11], a[176:179]\n"
    "ds_read_b64_tr_b16 a[128:129], v5 offset:4096\n"
    "v_mfma_f32_16x16x32_bf16 a[176:179], a[156:159], a[12:15], a[176:179]\n"
    "buffer_load_dwordx4 a[96:99], v0, s[40:43], s89 offen offset:0 sc1\n"
    "v_mfma_f32_16x16x32_bf16 a[180:183], a[144:147], a[16:19], a[180:183]\n"
    "ds_read_b64_tr_b16 a[130:131], v5 offset:4608\n"
    "ds_read_b64_tr_b16 a[132:133], v5 offset:5120\n"
    "v_mfma_f32_16x16x32_bf16 a[180:183], a[148:151], a[20:23], a[180:183]\n"
    "ds_read_b64_tr_b16 a[134:135], v5 offset:5632\n"
    "v_mfma_f32_16x16x32_bf16 a[180:183], a[152:155], a[24:27], a[180:183]\n"
    "buffer_load_dwordx4 a[100:103], v0, s[40:43], s89 offen offset:1024 sc1\n"
    "v_mfma_f32_16x16x32_bf16 a[180:183], a[156:159], a[28:31], a[180:183]\n"
    "ds_read_b64_tr_b16 a[136:137], v5 offset:6144\n"
    "ds_read_b64_tr_b16 a[138:139], v5 offset:6656\n"
    "v_mfma_f32_16x16x32_bf16 a[184:187], a[144:147], a[32:35], a[184:187]\n"
    "ds_read_b64_tr_b16 a[140:141], v5 offset:7168\n"
    "v_mfma_f32_16x16x32_bf16 a[184:187], a[148:151], a[36:39], a[184:187]\n"
    "buffer_load_dwordx4 a[104:107], v0, s[40:43], s89 offen offset:2048 sc1\n"
    "v_mfma_f32_16x16x32_bf16 a[184:187], a[152:155], a[40:43], a[184:187]\n"
    "ds_read_b64_tr_b16 a[142:143], v5 offset:7680\n"
    "v_mfma_f32_16x16x32_bf16 a[184:187], a[156:159], a[44:47], a[184:187]\n"
    "v_mfma_f32_16x16x32_bf16 a[188:191], a[144:147], a[48:51], a[188:191]\n"
    "buffer_load_dwordx4 a[108:111], v0, s[40:43], s89 offen offset:3072 sc1\n"
    "v_mfma_f32_16x16x32_bf16 a[188:191], a[148:151], a[52:55], a[188:191]\n"
    "v_mfma_f32_16x16x32_bf16 a[188:191], a[152:155], a[56:59], a[188:191]\n"
    "v_mfma_f32_16x16x32_bf16 a[188:191], a[156:159], a[60:63], a[188:191]\n"
    "s_waitcnt lgkmcnt(4)\n"
    "s_add_u32 m0, s77, 75792\n"
    "v_mfma_f32_16x16x32_bf16 a[192:195], a[128:131], a[0:3], a[192:195]\n"
    "v_mfma_f32_16x16x32_bf16 a[192:195], a[132:135], a[4:7], a[192:195]\n"
    "s_waitcnt lgkmcnt(0)\n"
    "v_mfma_f32_16x16x32_bf16 a[192:195], a[136:139], a[8:11], a[192:195]\n"
    "s_add_u32 s88, s79, 5505024\n"
    "ds_read_b64_tr_b16 a[144:145], v5 offset:4104\n"
    "v_mfma_f32_16x16x32_bf16 a[192:195], a[140:143], a[12:15], a[192:195]\n"
    "ds_read_b64_tr_b16 a[146:147], v5 offset:4616\n"
    "v_mfma_f32_16x16x32_bf16 a[196:199], a[128:131], a[16:19], a[196:199]\n"
    "ds_read_b64_tr_b16 a[148:149], v5 offset:5128\n"
    "v_mfma_f32_16x16x32_bf16 a[196:199], a[132:135], a[20:23], a[196:199]\n"
    "buffer_load_dwordx4 v1, s[44:47], s88 offen offset:0 lds\n"
    "v_mfma_f32_16x16x32_bf16 a[196:199], a[136:139], a[24:27], a[196:199]\n"
    "ds_read_b64_tr_b16 a[150:151], v5 offset:5640\n"
    "ds_read_b64_tr_b16 a[152:153], v5 offset:6152\n"
    "v_mfma_f32_16x16x32_bf16 a[196:199], a[140:143], a[28:31], a[196:199]\n"
    "ds_read_b64_tr_b16 a[154:155], v5 offset:6664\n"
    "v_mfma_f32_16x16x32_bf16 a[200:203], a[128:131], a[32:35], a[200:203]\n"
    "s_add_u32 m0, s77, 79888\n"
    "ds_read_b64_tr_b16 a[156:157], v5 offset:7176\n"
    "v_mfma_f32_16x16x32_bf16 a[200:203], a[132:135], a[36:39], a[200:203]\n"
    "ds_read_b64_tr_b16 a[158:159], v5 offset:7688\n"
    "v_mfma_f32_16x16x32_bf16 a[200:203], a[136:139], a[40:43], a[200:203]\n"
    "v_mfma_f32_16x16x32_bf16 a[200:203], a[140:143], a[44:47], a[200:203]\n"
    "s_add_u32 s88, s79, 5505152\n"
    "v_mfma_f32_16x16x32_bf16 a[204:207], a[128:131], a[48:51], a[204:207]\n"
    "v_mfma_f32_16x16x32_bf16 a[204:207], a[132:135], a[52:55], a[204:207]\n"
    "v_mfma_f32_16x16x32_bf16 a[204:207], a[136:139], a[56:59], a[204:207]\n"
    "v_mfma_f32_16x16x32_bf16 a[204:207], a[140:143], a[60:63], a[204:207]\n"
    "s_waitcnt lgkmcnt(4)\n"
    "v_mfma_f32_16x16x32_bf16 v[112:115], a[144:147], a[0:3], v[112:115]\n"
    "buffer_load_dwordx4 v1, s[44:47], s88 offen offset:0 lds\n"
    "v_mfma_f32_16x16x32_bf16 v[112:115], a[148:151], a[4:7], v[112:115]\n"
    "s_waitcnt lgkmcnt(0)\n"
    "v_mfma_f32_16x16x32_bf16 v[112:115], a[152:155], a[8:11], v[112:115]\n"
    "s_add_u32 m0, s77, 83984\n"
    "ds_read_b64_tr_b16 a[128:129], v6 offset:4096\n"
    "v_mfma_f32_16x16x32_bf16 v[112:115], a[156:159], a[12:15], v[112:115]\n"
    "ds_read_b64_tr_b16 a[130:131], v6 offset:4608\n"
    "v_mfma_f32_16x16x32_bf16 v[116:119], a[144:147], a[16:19], v[116:119]\n"
    "ds_read_b64_tr_b16 a[132:133], v6 offset:5120\n"
    "v_mfma_f32_16x16x32_bf16 v[116:119], a[148:151], a[20:23], v[116:119]\n"
    "s_add_u32 s88, s79, 5505280\n"
    "ds_read_b64_tr_b16 a[134:135], v6 offset:5632\n"
    "v_mfma_f32_16x16x32_bf16 v[116:119], a[152:155], a[24:27], v[116:119]\n"
    "ds_read_b64_tr_b16 a[136:137], v6 offset:6144\n"
    "v_mfma_f32_16x16x32_bf16 v[116:119], a[156:159], a[28:31], v[116:119]\n"
    "ds_read_b64_tr_b16 a[138:139], v6 offset:6656\n"
    "v_mfma_f32_16x16x32_bf16 v[120:123], a[144:147], a[32:35], v[120:123]\n"
    "buffer_load_dwordx4 v1, s[44:47], s88 offen offset:0 lds\n"
    "v_mfma_f32_16x16x32_bf16 v[120:123], a[148:151], a[36:39], v[120:123]\n"
    "ds_read_b64_tr_b16 a[140:141], v6 offset:7168\n"
    "ds_read_b64_tr_b16 a[142:143], v6 offset:7680\n"
    "v_mfma_f32_16x16x32_bf16 v[120:123], a[152:155], a[40:43], v[120:123]\n"
    "v_mfma_f32_16x16x32_bf16 v[120:123], a[156:159], a[44:47], v[120:123]\n"
    "s_add_u32 s89, s99, 12288\n"
    "v_mfma_f32_16x16x32_bf16 v[124:127], a[144:147], a[48:51], v[124:127]\n"
    "v_mfma_f32_16x16x32_bf16 v[124:127], a[148:151], a[52:55], v[124:127]\n"
    "v_mfma_f32_16x16x32_bf16 v[124:127], a[152:155], a[56:59], v[124:127]\n"
    "buffer_load_dwordx4 a[112:115], v0, s[40:43], s89 offen offset:0 sc1\n"
    "v_mfma_f32_16x16x32_bf16 v[124:127], a[156:159], a[60:63], v[124:127]\n"
    "s_waitcnt lgkmcnt(4)\n"
    "v_mfma_f32_16x16x32_bf16 v[128:131], a[128:131], a[0:3], v[128:131]\n"
    "v_mfma_f32_16x16x32_bf16 v[128:131], a[132:135], a[4:7], v[128:131]\n"
    "s_waitcnt lgkmcnt(0)\n"
    "v_mfma_f32_16x16x32_bf16 v[128:131], a[136:139], a[8:11], v[128:131]\n"
    "buffer_load_dwordx4 a[116:119], v0, s[40:43], s89 offen offset:1024 sc1\n"
    "v_mfma_f32_16x16x32_bf16 v[128:131], a[140:143], a[12:15], v[128:131]\n"
    "ds_read_b64_tr_b16 a[144:145], v6 offset:4104\n"
    "ds_read_b64_tr_b16 a[146:147], v6 offset:4616\n"
    "v_mfma_f32_16x16x32_bf16 v[132:135], a[128:131], a[16:19], v[132:135]\n"
    "buffer_load_dwordx4 a[120:123], v0, s[40:43], s89 offen offset:2048 sc1\n"
    "v_mfma_f32_16x16x32_bf16 v[132:135], a[132:135], a[20:23], v[132:135]\n"
    "ds_read_b64_tr_b16 a[148:149], v6 offset:5128\n"
    "ds_read_b64_tr_b16 a[150:151], v6 offset:5640\n"
    "v_mfma_f32_16x16x32_bf16 v[132:135], a[136:139], a[24:27], v[132:135]\n"
    "ds_read_b64_tr_b16 a[152:153], v6 offset:6152\n"
    "v_mfma_f32_16x16x32_bf16 v[132:135], a[140:143], a[28:31], v[132:135]\n"
    "buffer_load_dwordx4 a[124:127], v0, s[40:43], s89 offen offset:3072 sc1\n"
    "v_mfma_f32_16x16x32_bf16 v[136:139], a[128:131], a[32:35], v[136:139]\n"
    "ds_read_b64_tr_b16 a[154:155], v6 offset:6664\n"
    "ds_read_b64_tr_b16 a[156:157], v6 offset:7176\n"
    "v_mfma_f32_16x16x32_bf16 v[136:139], a[132:135], a[36:39], v[136:139]\n"
    "ds_read_b64_tr_b16 a[158:159], v6 offset:7688\n"
    "v_min_u32 v15, 752, v0\n"
    "v_mfma_f32_16x16x32_bf16 v[136:139], a[136:139], a[40:43], v[136:139]\n"
    "s_add_u32 m0, s77, 76816\n"
    "v_lshlrev_b32 v15, 3, v15\n"
    "s_add_u32 s94, s30, 0\n"
    "v_mfma_f32_16x16x32_bf16 v[136:139], a[140:143], a[44:47], v[136:139]\n"
    "buffer_load_dword a250, v15, s[56:59], s94 offen offset:0\n"
    "v_mfma_f32_16x16x32_bf16 v[140:143], a[128:131], a[48:51], v[140:143]\n"
    "s_lshr_b32 s94, %[bid], 3\n"
    "s_lshl2_add_u32 s94, s94, s76\n"
    "s_lshl_b32 s94, s94, 8\n"
    "v_mfma_f32_16x16x32_bf16 v[140:143], a[132:135], a[52:55], v[140:143]\n"
    "s_add_u32 s88, s79, 5898240\n"
    "v_bfe_u32 v15, v0, 4, 1\n"
    "v_lshl_add_u32 v15, v15, 7, s94\n"
    "v_mfma_f32_16x16x32_bf16 v[140:143], a[136:139], a[56:59], v[140:143]\n"
    "v_min_u32 v15, .L_pv_%=-.L_sgo_%=-4, v15\n"
    "s_getpc_b64 s[86:87]\n"
    ".L_cw1_%=:\n"
    "s_add_u32 s86, s86, .L_sgo_%=-.L_cw1_%=\n"
    "s_addc_u32 s87, s87, 0\n"
    "v_mfma_f32_16x16x32_bf16 v[140:143], a[140:143], a[60:63], v[140:143]\n"
    "s_waitcnt lgkmcnt(4)\n"
    "v_mfma_f32_16x16x32_bf16 v[144:147], a[144:147], a[0:3], v[144:147]\n"
    "buffer_load_dwordx4 v1, s[44:47], s88 offen offset:0 lds\n"
    "v_mfma_f32_16x16x32_bf16 v[144:147], a[148:151], a[4:7], v[144:147]\n"
    "s_waitcnt lgkmcnt(0)\n"
    "v_mfma_f32_16x16x32_bf16 v[144:147], a[152:155], a[8:11], v[144:147]\n"
    "global_load_dword a251, v15, s[86:87]\n"
    "v_mfma_f32_16x16x32_bf16 v[144:147], a[156:159], a[12:15], v[144:147]\n"
    "s_add_u32 m0, s77, 80912\n"
    "ds_read_b64_tr_b16 a[128:129], v5 offset:8192\n"
    "ds_read_b64_tr_b16 a[130:131], v5 offset:8704\n"
    "v_mfma_f32_16x16x32_bf16 v[148:151], a[144:147], a[16:19], v[148:151]\n"
    "ds_read_b64_tr_b16 a[132:133], v5 offset:9216\n"
    "v_xor_b32 v15, 64, v14\n"
    "v_mfma_f32_16x16x32_bf16 v[148:151], a[148:151], a[20:23], v[148:151]\n"
    "ds_read_b64_tr_b16 a[134:135], v5 offset:9728\n"
    "v_mfma_f32_16x16x32_bf16 v[148:151], a[152:155], a[24:27], v[148:151]\n"
    "s_add_u32 s88, s79, 5898368\n"
    "ds_read_b64_tr_b16 a[136:137], v5 offset:10240\n"
    "v_mfma_f32_16x16x32_bf16 v[148:151], a[156:159], a[28:31], v[148:151]\n"
    "ds_read_b64_tr_b16 a[138:139], v5 offset:10752\n"
    "v_mfma_f32_16x16x32_bf16 v[152:155], a[144:147], a[32:35], v[152:155]\n"
    "ds_read_b64_tr_b16 a[140:141], v5 offset:11264\n"
    "v_mfma_f32_16x16x32_bf16 v[152:155], a[148:151], a[36:39], v[152:155]\n"
    "buffer_load_dwordx4 v1, s[44:47], s88 offen offset:0 lds\n"
    "v_mfma_f32_16x16x32_bf16 v[152:155], a[152:155], a[40:43], v[152:155]\n"
    "ds_read_b64_tr_b16 a[142:143], v5 offset:11776\n"
    "v_mfma_f32_16x16x32_bf16 v[152:155], a[156:159], a[44:47], v[152:155]\n"
    "v_mfma_f32_16x16x32_bf16 v[156:159], a[144:147], a[48:51], v[156:159]\n"
    "s_add_u32 m0, s77, 85008\n"
    "v_mfma_f32_16x16x32_bf16 v[156:159], a[148:151], a[52:55], v[156:159]\n"
    "v_mfma_f32_16x16x32_bf16 v[156:159], a[152:155], a[56:59], v[156:159]\n"
    "v_mfma_f32_16x16x32_bf16 v[156:159], a[156:159], a[60:63], v[156:159]\n"
    "s_waitcnt lgkmcnt(4)\n"
    "s_add_u32 s88, s79, 5898496\n"
    "v_mfma_f32_16x16x32_bf16 v[160:163], a[128:131], a[0:3], v[160:163]\n"
    "v_mfma_f32_16x16x32_bf16 v[160:163], a[132:135], a[4:7], v[160:163]\n"
    "s_waitcnt lgkmcnt(0)\n"
    "v_mfma_f32_16x16x32_bf16 v[160:163], a[136:139], a[8:11], v[160:163]\n"
    "buffer_load_dwordx4 v1, s[44:47], s88 offen offset:0 lds\n"
    "v_mfma_f32_16x16x32_bf16 v[160:163], a[140:143], a[12:15], v[160:163]\n"
    "ds_read_b64_tr_b16 a[144:145], v5 offset:8200\n"
    "ds_read_b64_tr_b16 a[146:147], v5 offset:8712\n"
    "v_mfma_f32_16x16x32_bf16 v[164:167], a[128:131], a[16:19], v[164:167]\n"
    "ds_read_b64_tr_b16 a[148:149], v5 offset:9224\n"
    "v_mfma_f32_16x16x32_bf16 v[164:167], a[132:135], a[20:23], v[164:167]\n"
    "ds_read_b64_tr_b16 a[150:151], v5 offset:9736\n"
    "v_mfma_f32_16x16x32_bf16 v[164:167], a[136:139], a[24:27], v[164:167]\n"
    "ds_read_b64_tr_b16 a[152:153], v5 offset:10248\n"
    "v_mfma_f32_16x16x32_bf16 v[164:167], a[140:143], a[28:31], v[164:167]\n"
    "ds_read_b64_tr_b16 a[154:155], v5 offset:10760\n"
    "v_mfma_f32_16x16x32_bf16 v[168:171], a[128:131], a[32:35], v[168:171]\n"
    "ds_read_b64_tr_b16 a[156:157], v5 offset:11272\n"
    "v_mfma_f32_16x16x32_bf16 v[168:171], a[132:135], a[36:39], v[168:171]\n"
    "ds_read_b64_tr_b16 a[158:159], v5 offset:11784\n"
    "v_mfma_f32_16x16x32_bf16 v[168:171], a[136:139], a[40:43], v[168:171]\n"
    "v_mfma_f32_16x16x32_bf16 v[168:171], a[140:143], a[44:47], v[168:171]\n"
    "v_mfma_f32_16x16x32_bf16 v[172:175], a[128:131], a[48:51], v[172:175]\n"
    "v_mfma_f32_16x16x32_bf16 v[172:175], a[132:135], a[52:55], v[172:175]\n"
    "v_mfma_f32_16x16x32_bf16 v[172:175], a[136:139], a[56:59], v[172:175]\n"
    "v_mfma_f32_16x16x32_bf16 v[172:175], a[140:143], a[60:63], v[172:175]\n"
    "s_waitcnt lgkmcnt(4)\n"
    "v_mfma_f32_16x16x32_bf16 v[176:179], a[144:147], a[0:3], v[176:179]\n"
    "v_mfma_f32_16x16x32_bf16 v[176:179], a[148:151], a[4:7], v[176:179]\n"
    "s_waitcnt lgkmcnt(0)\n"
    "v_mfma_f32_16x16x32_bf16 v[176:179], a[152:155], a[8:11], v[176:179]\n"
    "ds_read_b64_tr_b16 a[128:129], v6 offset:8192\n"
    "v_mfma_f32_16x16x32_bf16 v[176:179], a[156:159], a[12:15], v[176:179]\n"
    "ds_read_b64_tr_b16 a[130:131], v6 offset:8704\n"
    "v_mfma_f32_16x16x32_bf16 v[180:183], a[144:147], a[16:19], v[180:183]\n"
    "ds_read_b64_tr_b16 a[132:133], v6 offset:9216\n"
    "v_mfma_f32_16x16x32_bf16 v[180:183], a[148:151], a[20:23], v[180:183]\n"
    "ds_read_b64_tr_b16 a[134:135], v6 offset:9728\n"
    "v_mfma_f32_16x16x32_bf16 v[180:183], a[152:155], a[24:27], v[180:183]\n"
    "ds_read_b64_tr_b16 a[136:137], v6 offset:10240\n"
    "v_mfma_f32_16x16x32_bf16 v[180:183], a[156:159], a[28:31], v[180:183]\n"
    "ds_read_b64_tr_b16 a[138:139], v6 offset:10752\n"
    "v_mfma_f32_16x16x32_bf16 v[184:187], a[144:147], a[32:35], v[184:187]\n"
    "ds_read_b64_tr_b16 a[140:141], v6 offset:11264\n"
    "v_mfma_f32_16x16x32_bf16 v[184:187], a[148:151], a[36:39], v[184:187]\n"
    "ds_read_b64_tr_b16 a[142:143], v6 offset:11776\n"
    "v_mfma_f32_16x16x32_bf16 v[184:187], a[152:155], a[40:43], v[184:187]\n"
    "v_mfma_f32_16x16x32_bf16 v[184:187], a[156:159], a[44:47], v[184:187]\n"
    "v_mfma_f32_16x16x32_bf16 v[188:191], a[144:147], a[48:51], v[188:191]\n"
    "v_mfma_f32_16x16x32_bf16 v[188:191], a[148:151], a[52:55], v[188:191]\n"
    "v_mfma_f32_16x16x32_bf16 v[188:191], a[152:155], a[56:59], v[188:191]\n"
    "v_mfma_f32_16x16x32_bf16 v[188:191], a[156:159], a[60:63], v[188:191]\n"
    "s_waitcnt lgkmcnt(4)\n"
    "v_mfma_f32_16x16x32_bf16 v[192:195], a[128:131], a[0:3], v[192:195]\n"
    "v_mfma_f32_16x16x32_bf16 v[192:195], a[132:135], a[4:7], v[192:195]\n"
    "s_waitcnt lgkmcnt(0)\n"
    "v_mfma_f32_16x16x32_bf16 v[192:195], a[136:139], a[8:11], v[192:195]\n"
    "ds_read_b64_tr_b16 a[144:145], v6 offset:8200\n"
    "v_mfma_f32_16x16x32_bf16 v[192:195], a[140:143], a[12:15], v[192:195]\n"
    "ds_read_b64_tr_b16 a[146:147], v6 offset:8712\n"
    "v_mfma_f32_16x16x32_bf16 v[196:199], a[128:131], a[16:19], v[196:199]\n"
    "ds_read_b64_tr_b16 a[148:149], v6 offset:9224\n"
    "v_mfma_f32_16x16x32_bf16 v[196:199], a[132:135], a[20:23], v[196:199]\n"
    "ds_read_b64_tr_b16 a[150:151], v6 offset:9736\n"
    "v_mfma_f32_16x16x32_bf16 v[196:199], a[136:139], a[24:27], v[196:199]\n"
    "ds_read_b64_tr_b16 a[152:153], v6 offset:10248\n"
    "v_mfma_f32_16x16x32_bf16 v[196:199], a[140:143], a[28:31], v[196:199]\n"
    "ds_read_b64_tr_b16 a[154:155], v6 offset:10760\n"
    "v_mfma_f32_16x16x32_bf16 v[200:203], a[128:131], a[32:35], v[200:203]\n"
    "ds_read_b64_tr_b16 a[156:157], v6 offset:11272\n"
    "v_mfma_f32_16x16x32_bf16 v[200:203], a[132:135], a[36:39], v[200:203]\n"
    "ds_read_b64_tr_b16 a[158:159], v6 offset:11784\n"
    "v_mfma_f32_16x16x32_bf16 v[200:203], a[136:139], a[40:43], v[200:203]\n"
    "v_mfma_f32_16x16x32_bf16 v[200:203], a[140:143], a[44:47], v[200:203]\n"
    "v_mfma_f32_16x16x32_bf16 v[204:207], a[128:131], a[48:51], v[204:207]\n"
    "v_mfma_f32_16x16x32_bf16 v[204:207], a[132:135], a[52:55], v[204:207]\n"
    "v_mfma_f32_16x16x32_bf16 v[204:207], a[136:139], a[56:59], v[204:207]\n"
    "v_mfma_f32_16x16x32_bf16 v[204:207], a[140:143], a[60:63], v[204:207]\n"
    "s_waitcnt vmcnt(0) lgkmcnt(0)\n"
    "s_barrier\n"
    "v_mfma_f32_16x16x32_bf16 v[208:211], a[144:147], a[0:3], v[208:211]\n"
    "ds_read_b64_tr_b16 a[128:129], v10 offset:0\n"
    "v_mfma_f32_16x16x32_bf16 v[208:211], a[148:151], a[4:7], v[208:211]\n"
    "ds_read_b64_tr_b16 a[130:131], v10 offset:1536\n"
    "v_mfma_f32_16x16x32_bf16 v[208:211], a[152:155], a[8:11], v[208:211]\n"
    "ds_read_b64_tr_b16 a[132:133], v10 offset:3072\n"
    "v_mfma_f32_16x16x32_bf16 v[208:211], a[156:159], a[12:15], v[208:211]\n"
    "ds_read_b64_tr_b16 a[134:135], v10 offset:4608\n"
    "v_mfma_f32_16x16x32_bf16 v[212:215], a[144:147], a[16:19], v[212:215]\n"
    "ds_read_b64_tr_b16 a[136:137], v10 offset:6144\n"
    "v_mfma_f32_16x16x32_bf16 v[212:215], a[148:151], a[20:23], v[212:215]\n"
    "ds_read_b64_tr_b16 a[138:139], v10 offset:7680\n"
    "v_mfma_f32_16x16x32_bf16 v[212:215], a[152:155], a[24:27], v[212:215]\n"
    "ds_read_b64_tr_b16 a[140:141], v10 offset:9216\n"
    "v_mfma_f32_16x16x32_bf16 v[212:215], a[156:159], a[28:31], v[212:215]\n"
    "ds_read_b64_tr_b16 a[142:143], v10 offset:10752\n"
    "v_mfma_f32_16x16x32_bf16 v[216:219], a[144:147], a[32:35], v[216:219]\n"
    "v_mfma_f32_16x16x32_bf16 v[216:219], a[148:151], a[36:39], v[216:219]\n"
    "v_mfma_f32_16x16x32_bf16 v[216:219], a[152:155], a[40:43], v[216:219]\n"
    "v_mfma_f32_16x16x32_bf16 v[216:219], a[156:159], a[44:47], v[216:219]\n"
    "v_mfma_f32_16x16x32_bf16 v[220:223], a[144:147], a[48:51], v[220:223]\n"
    "v_mfma_f32_16x16x32_bf16 v[220:223], a[148:151], a[52:55], v[220:223]\n"
    "v_mfma_f32_16x16x32_bf16 v[220:223], a[152:155], a[56:59], v[220:223]\n"
    "v_mfma_f32_16x16x32_bf16 v[220:223], a[156:159], a[60:63], v[220:223]\n"
    "s_waitcnt lgkmcnt(4)\n"
    "v_mfma_f32_16x16x32_bf16 v[224:227], a[128:131], a[64:67], v[224:227]\n"
    "v_mfma_f32_16x16x32_bf16 v[224:227], a[132:135], a[68:71], v[224:227]\n"
    "s_waitcnt lgkmcnt(0)\n"
    "v_mfma_f32_16x16x32_bf16 v[224:227], a[136:139], a[72:75], v[224:227]\n"
    "ds_read_b64_tr_b16 a[144:145], v10 offset:32\n"
    "v_mfma_f32_16x16x32_bf16 v[224:227], a[140:143], a[76:79], v[224:227]\n"
    "ds_read_b64_tr_b16 a[146:147], v10 offset:1568\n"
    "v_mfma_f32_16x16x32_bf16 v[228:231], a[128:131], a[80:83], v[228:231]\n"
    "s_add_u32 m0, s78, 0\n"
    "ds_read_b64_tr_b16 a[148:149], v10 offset:3104\n"
    "v_mfma_f32_16x16x32_bf16 v[228:231], a[132:135], a[84:87], v[228:231]\n"
    "ds_read_b64_tr_b16 a[150:151], v10 offset:4640\n"
    "v_mfma_f32_16x16x32_bf16 v[228:231], a[136:139], a[88:91], v[228:231]\n"
    "ds_read_b64_tr_b16 a[152:153], v10 offset:6176\n"
    "v_mfma_f32_16x16x32_bf16 v[228:231], a[140:143], a[92:95], v[228:231]\n"
    "s_add_u32 s88, s80, 1572864\n"
    "ds_read_b64_tr_b16 a[154:155], v10 offset:7712\n"
    "v_mfma_f32_16x16x32_bf16 v[232:235], a[128:131], a[96:99], v[232:235]\n"
    "ds_read_b64_tr_b16 a[156:157], v10 offset:9248\n"
    "v_mfma_f32_16x16x32_bf16 v[232:235], a[132:135], a[100:103], v[232:235]\n"
    "ds_read_b64_tr_b16 a[158:159], v10 offset:10784\n"
    "v_mfma_f32_16x16x32_bf16 v[232:235], a[136:139], a[104:107], v[232:235]\n"
    "buffer_load_dwordx4 v2, s[48:51], s88 offen offset:0 lds\n"
    "v_mfma_f32_16x16x32_bf16 v[232:235], a[140:143], a[108:111], v[232:235]\n"
    "v_mfma_f32_16x16x32_bf16 v[236:239], a[128:131], a[112:115], v[236:239]\n"
    "v_mfma_f32_16x16x32_bf16 v[236:239], a[132:135], a[116:119], v[236:239]\n"
    "s_add_u32 m0, s78, 1024\n"
    "v_mfma_f32_16x16x32_bf16 v[236:239], a[136:139], a[120:123], v[236:239]\n"
    "v_mfma_f32_16x16x32_bf16 v[236:239], a[140:143], a[124:127], v[236:239]\n"
    "s_waitcnt lgkmcnt(4)\n"
    "v_mfma_f32_16x16x32_bf16 v[240:243], a[144:147], a[64:67], v[240:243]\n"
    "s_add_u32 s88, s80, 1572864\n"
    "v_mfma_f32_16x16x32_bf16 v[240:243], a[148:151], a[68:71], v[240:243]\n"
    "s_waitcnt lgkmcnt(0)\n"
    "v_mfma_f32_16x16x32_bf16 v[240:243], a[152:155], a[72:75], v[240:243]\n"
    "ds_read_b64_tr_b16 a[128:129], v10 offset:64\n"
    "v_mfma_f32_16x16x32_bf16 v[240:243], a[156:159], a[76:79], v[240:243]\n"
    "buffer_load_dwordx4 v3, s[48:51], s88 offen offset:0 lds\n"
    "v_mfma_f32_16x16x32_bf16 v[244:247], a[144:147], a[80:83], v[244:247]\n"
    "ds_read_b64_tr_b16 a[130:131], v10 offset:1600\n"
    "ds_read_b64_tr_b16 a[132:133], v10 offset:3136\n"
    "v_mfma_f32_16x16x32_bf16 v[244:247], a[148:151], a[84:87], v[244:247]\n"
    "ds_read_b64_tr_b16 a[134:135], v10 offset:4672\n"
    "v_mfma_f32_16x16x32_bf16 v[244:247], a[152:155], a[88:91], v[244:247]\n"
    "s_add_u32 m0, s78, 2048\n"
    "ds_read_b64_tr_b16 a[136:137], v10 offset:6208\n"
    "v_mfma_f32_16x16x32_bf16 v[244:247], a[156:159], a[92:95], v[244:247]\n"
    "ds_read_b64_tr_b16 a[138:139], v10 offset:7744\n"
    "v_mfma_f32_16x16x32_bf16 v[248:251], a[144:147], a[96:99], v[248:251]\n"
    "ds_read_b64_tr_b16 a[140:141], v10 offset:9280\n"
    "v_mfma_f32_16x16x32_bf16 v[248:251], a[148:151], a[100:103], v[248:251]\n"
    "s_add_u32 s88, s80, 1572864\n"
    "ds_read_b64_tr_b16 a[142:143], v10 offset:10816\n"
    "v_mfma_f32_16x16x32_bf16 v[248:251], a[152:155], a[104:107], v[248:251]\n"
    "v_mfma_f32_16x16x32_bf16 v[248:251], a[156:159], a[108:111], v[248:251]\n"
    "v_mfma_f32_16x16x32_bf16 v[252:255], a[144:147], a[112:115], v[252:255]\n"
    "buffer_load_dwordx4 v4, s[48:51], s88 offen offset:0 lds\n"
    "v_mfma_f32_16x16x32_bf16 v[252:255], a[148:151], a[116:119], v[252:255]\n"
    "v_mfma_f32_16x16x32_bf16 v[252:255], a[152:155], a[120:123], v[252:255]\n"
    "v_mfma_f32_16x16x32_bf16 v[252:255], a[156:159], a[124:127], v[252:255]\n"
    "s_waitcnt lgkmcnt(4)\n"
    "s_add_u32 s89, s81, 16384\n"
    "v_mfma_f32_16x16x32_bf16 v[16:19], a[128:131], a[64:67], v[16:19]\n"
    "v_mfma_f32_16x16x32_bf16 v[16:19], a[132:135], a[68:71], v[16:19]\n"
    "s_waitcnt lgkmcnt(0)\n"
    "v_mfma_f32_16x16x32_bf16 v[16:19], a[136:139], a[72:75], v[16:19]\n"
    "buffer_load_dwordx4 a[0:3], v0, s[40:43], s89 offen offset:0 sc1\n"
    "v_mfma_f32_16x16x32_bf16 v[16:19], a[140:143], a[76:79], v[16:19]\n"
    "ds_read_b64_tr_b16 a[144:145], v10 offset:12288\n"
    "ds_read_b64_tr_b16 a[146:147], v10 offset:13824\n"
    "v_mfma_f32_16x16x32_bf16 v[20:23], a[128:131], a[80:83], v[20:23]\n"
    "ds_read_b64_tr_b16 a[148:149], v10 offset:15360\n"
    "v_mfma_f32_16x16x32_bf16 v[20:23], a[132:135], a[84:87], v[20:23]\n"
    "buffer_load_dwordx4 a[4:7], v0, s[40:43], s89 offen offset:1024 sc1\n"
    "v_mfma_f32_16x16x32_bf16 v[20:23], a[136:139], a[88:91], v[20:23]\n"
    "ds_read_b64_tr_b16 a[150:151], v10 offset:16896\n"
    "ds_read_b64_tr_b16 a[152:153], v10 offset:18432\n"
    "v_mfma_f32_16x16x32_bf16 v[20:23], a[140:143], a[92:95], v[20:23]\n"
    "ds_read_b64_tr_b16 a[154:155], v10 offset:19968\n"
    "v_mfma_f32_16x16x32_bf16 v[24:27], a[128:131], a[96:99], v[24:27]\n"
    "buffer_load_dwordx4 a[8:11], v0, s[40:43], s89 offen offset:2048 sc1\n"
    "v_mfma_f32_16x16x32_bf16 v[24:27], a[132:135], a[100:103], v[24:27]\n"
    "ds_read_b64_tr_b16 a[156:157], v10 offset:21504\n"
    "ds_read_b64_tr_b16 a[158:159], v10 offset:23040\n"
    "v_mfma_f32_16x16x32_bf16 v[24:27], a[136:139], a[104:107], v[24:27]\n"
    "v_mfma_f32_16x16x32_bf16 v[24:27], a[140:143], a[108:111], v[24:27]\n"
    "buffer_load_dwordx4 a[12:15], v0, s[40:43], s89 offen offset:3072 sc1\n"
    "v_mfma_f32_16x16x32_bf16 v[28:31], a[128:131], a[112:115], v[28:31]\n"
    "v_mfma_f32_16x16x32_bf16 v[28:31], a[132:135], a[116:119], v[28:31]\n"
    "v_mfma_f32_16x16x32_bf16 v[28:31], a[136:139], a[120:123], v[28:31]\n"
    "s_add_u32 m0, s78, 12288\n"
    "v_mfma_f32_16x16x32_bf16 v[28:31], a[140:143], a[124:127], v[28:31]\n"
    "s_waitcnt lgkmcnt(4)\n"
    "v_mfma_f32_16x16x32_bf16 v[32:35], a[144:147], a[64:67], v[32:35]\n"
    "v_mfma_f32_16x16x32_bf16 v[32:35], a[148:151], a[68:71], v[32:35]\n"
    "s_waitcnt lgkmcnt(0)\n"
    "s_add_u32 s88, s80, 1572864\n"
    "v_mfma_f32_16x16x32_bf16 v[32:35], a[152:155], a[72:75], v[32:35]\n"
    "ds_read_b64_tr_b16 a[128:129], v10 offset:12320\n"
    "v_mfma_f32_16x16x32_bf16 v[32:35], a[156:159], a[76:79], v[32:35]\n"
    "ds_read_b64_tr_b16 a[130:131], v10 offset:13856\n"
    "v_mfma_f32_16x16x32_bf16 v[36:39], a[144:147], a[80:83], v[36:39]\n"
    "buffer_load_dwordx4 v2, s[52:55], s88 offen offset:0 lds\n"
    "v_mfma_f32_16x16x32_bf16 v[36:39], a[148:151], a[84:87], v[36:39]\n"
    "ds_read_b64_tr_b16 a[132:133], v10 offset:15392\n"
    "ds_read_b64_tr_b16 a[134:135], v10 offset:16928\n"
    "v_mfma_f32_16x16x32_bf16 v[36:39], a[152:155], a[88:91], v[36:39]\n"
    "ds_read_b64_tr_b16 a[136:137], v10 offset:18464\n"
    "v_mfma_f32_16x16x32_bf16 v[36:39], a[156:159], a[92:95], v[36:39]\n"
    "s_add_u32 m0, s78, 13312\n"
    "ds_read_b64_tr_b16 a[138:139], v10 offset:20000\n"
    "v_mfma_f32_16x16x32_bf16 v[40:43], a[144:147], a[96:99], v[40:43]\n"
    "ds_read_b64_tr_b16 a[140:141], v10 offset:21536\n"
    "v_mfma_f32_16x16x32_bf16 v[40:43], a[148:151], a[100:103], v[40:43]\n"
    "ds_read_b64_tr_b16 a[142:143], v10 offset:23072\n"
    "v_mfma_f32_16x16x32_bf16 v[40:43], a[152:155], a[104:107], v[40:43]\n"
    "s_add_u32 s88, s80, 1572864\n"
    "v_mfma_f32_16x16x32_bf16 v[40:43], a[156:159], a[108:111], v[40:43]\n"
    "v_mfma_f32_16x16x32_bf16 v[44:47], a[144:147], a[112:115], v[44:47]\n"
    "v_mfma_f32_16x16x32_bf16 v[44:47], a[148:151], a[116:119], v[44:47]\n"
    "v_mfma_f32_16x16x32_bf16 v[44:47], a[152:155], a[120:123], v[44:47]\n"
    "buffer_load_dwordx4 v3, s[52:55], s88 offen offset:0 lds\n"
    "v_mfma_f32_16x16x32_bf16 v[44:47], a[156:159], a[124:127], v[44:47]\n"
    "s_waitcnt lgkmcnt(4)\n"
    "v_mfma_f32_16x16x32_bf16 v[48:51], a[128:131], a[64:67], v[48:51]\n"
    "v_mfma_f32_16x16x32_bf16 v[48:51], a[132:135], a[68:71], v[48:51]\n"
    "s_waitcnt lgkmcnt(0)\n"
    "s_add_u32 m0, s78, 14336\n"
    "v_mfma_f32_16x16x32_bf16 v[48:51], a[136:139], a[72:75], v[48:51]\n"
    "ds_read_b64_tr_b16 a[144:145], v10 offset:12352\n"
    "v_mfma_f32_16x16x32_bf16 v[48:51], a[140:143], a[76:79], v[48:51]\n"
    "ds_read_b64_tr_b16 a[146:147], v10 offset:13888\n"
    "v_mfma_f32_16x16x32_bf16 v[52:55], a[128:131], a[80:83], v[52:55]\n"
    "s_add_u32 s88, s80, 1572864\n"
    "ds_read_b64_tr_b16 a[148:149], v10 offset:15424\n"
    "v_mfma_f32_16x16x32_bf16 v[52:55], a[132:135], a[84:87], v[52:55]\n"
    "ds_read_b64_tr_b16 a[150:151], v10 offset:16960\n"
    "v_mfma_f32_16x16x32_bf16 v[52:55], a[136:139], a[88:91], v[52:55]\n"
    "ds_read_b64_tr_b16 a[152:153], v10 offset:18496\n"
    "v_mfma_f32_16x16x32_bf16 v[52:55], a[140:143], a[92:95], v[52:55]\n"
    "buffer_load_dwordx4 v4, s[52:55], s88 offen offset:0 lds\n"
    "v_mfma_f32_16x16x32_bf16 v[56:59], a[128:131], a[96:99], v[56:59]\n"
    "ds_read_b64_tr_b16 a[154:155], v10 offset:20032\n"
    "ds_read_b64_tr_b16 a[156:157], v10 offset:21568\n"
    "v_mfma_f32_16x16x32_bf16 v[56:59], a[132:135], a[100:103], v[56:59]\n"
    "ds_read_b64_tr_b16 a[158:159], v10 offset:23104\n"
    "v_mfma_f32_16x16x32_bf16 v[56:59], a[136:139], a[104:107], v[56:59]\n"
    "s_add_u32 s89, s3, 16384\n"
    "v_mfma_f32_16x16x32_bf16 v[56:59], a[140:143], a[108:111], v[56:59]\n"
    "v_mfma_f32_16x16x32_bf16 v[60:63], a[128:131], a[112:115], v[60:63]\n"
    "v_mfma_f32_16x16x32_bf16 v[60:63], a[132:135], a[116:119], v[60:63]\n"
    "buffer_load_dwordx4 a[16:19], v0, s[40:43], s89 offen offset:0 sc1\n"
    "v_mfma_f32_16x16x32_bf16 v[60:63], a[136:139], a[120:123], v[60:63]\n"
    "v_mfma_f32_16x16x32_bf16 v[60:63], a[140:143], a[124:127], v[60:63]\n"
    "s_waitcnt lgkmcnt(4)\n"
    "v_mfma_f32_16x16x32_bf16 v[64:67], a[144:147], a[64:67], v[64:67]\n"
    "buffer_load_dwordx4 a[20:23], v0, s[40:43], s89 offen offset:1024 sc1\n"
    "v_mfma_f32_16x16x32_bf16 v[64:67], a[148:151], a[68:71], v[64:67]\n"
    "s_waitcnt lgkmcnt(0)\n"
    "v_mfma_f32_16x16x32_bf16 v[64:67], a[152:155], a[72:75], v[64:67]\n"
    "ds_read_b64_tr_b16 a[128:129], v8 offset:0\n"
    "v_mfma_f32_16x16x32_bf16 v[64:67], a[156:159], a[76:79], v[64:67]\n"
    "buffer_load_dwordx4 a[24:27], v0, s[40:43], s89 offen offset:2048 sc1\n"
    "v_mfma_f32_16x16x32_bf16 v[68:71], a[144:147], a[80:83], v[68:71]\n"
    "ds_read_b64_tr_b16 a[130:131], v8 offset:512\n"
    "ds_read_b64_tr_b16 a[132:133], v8 offset:1024\n"
    "v_mfma_f32_16x16x32_bf16 v[68:71], a[148:151], a[84:87], v[68:71]\n"
    "ds_read_b64_tr_b16 a[134:135], v8 offset:1536\n"
    "v_mfma_f32_16x16x32_bf16 v[68:71], a[152:155], a[88:91], v[68:71]\n"
    "buffer_load_dwordx4 a[28:31], v0, s[40:43], s89 offen offset:3072 sc1\n"
    "v_mfma_f32_16x16x32_bf16 v[68:71], a[156:159], a[92:95], v[68:71]\n"
    "ds_read_b64_tr_b16 a[136:137], v8 offset:2048\n"
    "ds_read_b64_tr_b16 a[138:139], v8 offset:2560\n"
    "v_mfma_f32_16x16x32_bf16 v[72:75], a[144:147], a[96:99], v[72:75]\n"
    "ds_read_b64_tr_b16 a[140:141], v8 offset:3072\n"
    "v_mfma_f32_16x16x32_bf16 v[72:75], a[148:151], a[100:103], v[72:75]\n"
    "s_add_u32 m0, s77, 0\n"
    "ds_read_b64_tr_b16 a[142:143], v8 offset:3584\n"
    "v_mfma_f32_16x16x32_bf16 v[72:75], a[152:155], a[104:107], v[72:75]\n"
    "v_mfma_f32_16x16x32_bf16 v[72:75], a[156:159], a[108:111], v[72:75]\n"
    "v_mfma_f32_16x16x32_bf16 v[76:79], a[144:147], a[112:115], v[76:79]\n"
    "s_add_u32 s88, s79, 6291456\n"
    "v_mfma_f32_16x16x32_bf16 v[76:79], a[148:151], a[116:119], v[76:79]\n"
    "v_mfma_f32_16x16x32_bf16 v[76:79], a[152:155], a[120:123], v[76:79]\n"
    "v_mfma_f32_16x16x32_bf16 v[76:79], a[156:159], a[124:127], v[76:79]\n"
    "s_waitcnt lgkmcnt(4)\n"
    "v_mfma_f32_16x16x32_bf16 v[80:83], a[128:131], a[64:67], v[80:83]\n"
    "buffer_load_dwordx4 v1, s[44:47], s88 offen offset:0 lds\n"
    "v_mfma_f32_16x16x32_bf16 v[80:83], a[132:135], a[68:71], v[80:83]\n"
    "s_waitcnt lgkmcnt(0)\n"
    "v_mfma_f32_16x16x32_bf16 v[80:83], a[136:139], a[72:75], v[80:83]\n"
    "s_add_u32 m0, s77, 4096\n"
    "ds_read_b64_tr_b16 a[144:145], v8 offset:8\n"
    "v_mfma_f32_16x16x32_bf16 v[80:83], a[140:143], a[76:79], v[80:83]\n"
    "ds_read_b64_tr_b16 a[146:147], v8 offset:520\n"
    "v_mfma_f32_16x16x32_bf16 v[84:87], a[128:131], a[80:83], v[84:87]\n"
    "ds_read_b64_tr_b16 a[148:149], v8 offset:1032\n"
    "v_mfma_f32_16x16x32_bf16 v[84:87], a[132:135], a[84:87], v[84:87]\n"
    "s_add_u32 s88, s79, 6291584\n"
    "ds_read_b64_tr_b16 a[150:151], v8 offset:1544\n"
    "v_mfma_f32_16x16x32_bf16 v[84:87], a[136:139], a[88:91], v[84:87]\n"
    "ds_read_b64_tr_b16 a[152:153], v8 offset:2056\n"
    "v_mfma_f32_16x16x32_bf16 v[84:87], a[140:143], a[92:95], v[84:87]\n"
    "ds_read_b64_tr_b16 a[154:155], v8 offset:2568\n"
    "v_mfma_f32_16x16x32_bf16 v[88:91], a[128:131], a[96:99], v[88:91]\n"
    "buffer_load_dwordx4 v1, s[44:47], s88 offen offset:0 lds\n"
    "v_mfma_f32_16x16x32_bf16 v[88:91], a[132:135], a[100:103], v[88:91]\n"
    "ds_read_b64_tr_b16 a[156:157], v8 offset:3080\n"
    "ds_read_b64_tr_b16 a[158:159], v8 offset:3592\n"
    "v_mfma_f32_16x16x32_bf16 v[88:91], a[136:139], a[104:107], v[88:91]\n"
    "v_mfma_f32_16x16x32_bf16 v[88:91], a[140:143], a[108:111], v[88:91]\n"
    "s_add_u32 m0, s77, 8192\n"
    "v_mfma_f32_16x16x32_bf16 v[92:95], a[128:131], a[112:115], v[92:95]\n"
    "v_mfma_f32_16x16x32_bf16 v[92:95], a[132:135], a[116:119], v[92:95]\n"
    "v_mfma_f32_16x16x32_bf16 v[92:95], a[136:139], a[120:123], v[92:95]\n"
    "s_add_u32 s88, s79, 6291712\n"
    "v_mfma_f32_16x16x32_bf16 v[92:95], a[140:143], a[124:127], v[92:95]\n"
    "s_waitcnt lgkmcnt(4)\n"
    "v_mfma_f32_16x16x32_bf16 v[96:99], a[144:147], a[64:67], v[96:99]\n"
    "v_mfma_f32_16x16x32_bf16 v[96:99], a[148:151], a[68:71], v[96:99]\n"
    "s_waitcnt lgkmcnt(0)\n"
    "v_mfma_f32_16x16x32_bf16 v[96:99], a[152:155], a[72:75], v[96:99]\n"
    "buffer_load_dwordx4 v1, s[44:47], s88 offen offset:0 lds\n"
    "v_mfma_f32_16x16x32_bf16 v[96:99], a[156:159], a[76:79], v[96:99]\n"
    "ds_read_b64_tr_b16 a[128:129], v9 offset:0\n"
    "ds_read_b64_tr_b16 a[130:131], v9 offset:512\n"
    "v_mfma_f32_16x16x32_bf16 v[100:103], a[144:147], a[80:83], v[100:103]\n"
    "ds_read_b64_tr_b16 a[132:133], v9 offset:1024\n"
    "v_mfma_f32_16x16x32_bf16 v[100:103], a[148:151], a[84:87], v[100:103]\n"
    "s_add_u32 m0, s77, 1024\n"
    "ds_read_b64_tr_b16 a[134:135], v9 offset:1536\n"
    "v_mfma_f32_16x16x32_bf16 v[100:103], a[152:155], a[88:91], v[100:103]\n"
    "ds_read_b64_tr_b16 a[136:137], v9 offset:2048\n"
    "v_mfma_f32_16x16x32_bf16 v[100:103], a[156:159], a[92:95], v[100:103]\n"
    "ds_read_b64_tr_b16 a[138:139], v9 offset:2560\n"
    "v_mfma_f32_16x16x32_bf16 v[104:107], a[144:147], a[96:99], v[104:107]\n"
    "s_add_u32 s88, s79, 6684672\n"
    "ds_read_b64_tr_b16 a[140:141], v9 offset:3072\n"
    "v_mfma_f32_16x16x32_bf16 v[104:107], a[148:151], a[100:103], v[104:107]\n"
    "ds_read_b64_tr_b16 a[142:143], v9 offset:3584\n"
    "v_mfma_f32_16x16x32_bf16 v[104:107], a[152:155], a[104:107], v[104:107]\n"
    "v_mfma_f32_16x16x32_bf16 v[104:107], a[156:159], a[108:111], v[104:107]\n"
    "buffer_load_dwordx4 v1, s[44:47], s88 offen offset:0 lds\n"
    "v_mfma_f32_16x16x32_bf16 v[108:111], a[144:147], a[112:115], v[108:111]\n"
    "v_mfma_f32_16x16x32_bf16 v[108:111], a[148:151], a[116:119], v[108:111]\n"
    "v_mfma_f32_16x16x32_bf16 v[108:111], a[152:155], a[120:123], v[108:111]\n"
    "s_add_u32 m0, s77, 5120\n"
    "v_mfma_f32_16x16x32_bf16 v[108:111], a[156:159], a[124:127], v[108:111]\n"
    "s_waitcnt lgkmcnt(4)\n"
    "v_mfma_f32_16x16x32_bf16 a[160:163], a[128:131], a[64:67], a[160:163]\n"
    "v_mfma_f32_16x16x32_bf16 a[160:163], a[132:135], a[68:71], a[160:163]\n"
    "s_waitcnt lgkmcnt(0)\n"
    "s_add_u32 s88, s79, 6684800\n"
    "v_mfma_f32_16x16x32_bf16 a[160:163], a[136:139], a[72:75], a[160:163]\n"
    "ds_read_b64_tr_b16 a[144:145], v9 offset:8\n"
    "v_mfma_f32_16x16x32_bf16 a[160:163], a[140:143], a[76:79], a[160:163]\n"
    "ds_read_b64_tr_b16 a[146:147], v9 offset:520\n"
    "v_mfma_f32_16x16x32_bf16 a[164:167], a[128:131], a[80:83], a[164:167]\n"
    "buffer_load_dwordx4 v1, s[44:47], s88 offen offset:0 lds\n"
    "v_mfma_f32_16x16x32_bf16 a[164:167], a[132:135], a[84:87], a[164:167]\n"
    "ds_read_b64_tr_b16 a[148:149], v9 offset:1032\n"
    "ds_read_b64_tr_b16 a[150:151], v9 offset:1544\n"
    "v_mfma_f32_16x16x32_bf16 a[164:167], a[136:139], a[88:91], a[164:167]\n"
    "ds_read_b64_tr_b16 a[152:153], v9 offset:2056\n"
    "v_mfma_f32_16x16x32_bf16 a[164:167], a[140:143], a[92:95], a[164:167]\n"
    "s_add_u32 m0, s77, 9216\n"
    "ds_read_b64_tr_b16 a[154:155], v9 offset:2568\n"
    "v_mfma_f32_16x16x32_bf16 a[168:171], a[128:131], a[96:99], a[168:171]\n"
    "ds_read_b64_tr_b16 a[156:157], v9 offset:3080\n"
    "v_mfma_f32_16x16x32_bf16 a[168:171], a[132:135], a[100:103], a[168:171]\n"
    "ds_read_b64_tr_b16 a[158:159], v9 offset:3592\n"
    "v_mfma_f32_16x16x32_bf16 a[168:171], a[136:139], a[104:107], a[168:171]\n"
    "s_add_u32 s88, s79, 6684928\n"
    "v_mfma_f32_16x16x32_bf16 a[168:171], a[140:143], a[108:111], a[168:171]\n"
    "v_mfma_f32_16x16x32_bf16 a[172:175], a[128:131], a[112:115], a[172:175]\n"
    "v_mfma_f32_16x16x32_bf16 a[172:175], a[132:135], a[116:119], a[172:175]\n"
    "buffer_load_dwordx4 v1, s[44:47], s88 offen offset:0 lds\n"
    "v_mfma_f32_16x16x32_bf16 a[172:175], a[136:139], a[120:123], a[172:175]\n"
    "v_mfma_f32_16x16x32_bf16 a[172:175], a[140:143], a[124:127], a[172:175]\n"
    "s_waitcnt lgkmcnt(4)\n"
    "v_mfma_f32_16x16x32_bf16 a[176:179], a[144:147], a[64:67], a[176:179]\n"
    "s_add_u32 s89, s39, 16384\n"
    "v_mfma_f32_16x16x32_bf16 a[176:179], a[148:151], a[68:71], a[176:179]\n"
    "s_waitcnt lgkmcnt(0)\n"
    "v_mfma_f32_16x16x32_bf16 a[176:179], a[152:155], a[72:75], a[176:179]\n"
    "ds_read_b64_tr_b16 a[128:129], v8 offset:4096\n"
    "v_mfma_f32_16x16x32_bf16 a[176:179], a[156:159], a[76:79], a[176:179]\n"
    "buffer_load_dwordx4 a[32:35], v0, s[40:43], s89 offen offset:0 sc1\n"
    "v_mfma_f32_16x16x32_bf16 a[180:183], a[144:147], a[80:83], a[180:183]\n"
    "ds_read_b64_tr_b16 a[130:131], v8 offset:4608\n"
    "ds_read_b64_tr_b16 a[132:133], v8 offset:5120\n"
    "v_mfma_f32_16x16x32_bf16 a[180:183], a[148:151], a[84:87], a[180:183]\n"
    "ds_read_b64_tr_b16 a[134:135], v8 offset:5632\n"
    "v_mfma_f32_16x16x32_bf16 a[180:183], a[152:155], a[88:91], a[180:183]\n"
    "buffer_load_dwordx4 a[36:39], v0, s[40:43], s89 offen offset:1024 sc1\n"
    "v_mfma_f32_16x16x32_bf16 a[180:183], a[156:159], a[92:95], a[180:183]\n"
    "ds_read_b64_tr_b16 a[136:137], v8 offset:6144\n"
    "ds_read_b64_tr_b16 a[138:139], v8 offset:6656\n"
    "v_mfma_f32_16x16x32_bf16 a[184:187], a[144:147], a[96:99], a[184:187]\n"
    "ds_read_b64_tr_b16 a[140:141], v8 offset:7168\n"
    "v_mfma_f32_16x16x32_bf16 a[184:187], a[148:151], a[100:103], a[184:187]\n"
    "buffer_load_dwordx4 a[40:43], v0, s[40:43], s89 offen offset:2048 sc1\n"
    "v_mfma_f32_16x16x32_bf16 a[184:187], a[152:155], a[104:107], a[184:187]\n"
    "ds_read_b64_tr_b16 a[142:143], v8 offset:7680\n"
    "v_mfma_f32_16x16x32_bf16 a[184:187], a[156:159], a[108:111], a[184:187]\n"
    "v_mfma_f32_16x16x32_bf16 a[188:191], a[144:147], a[112:115], a[188:191]\n"
    "buffer_load_dwordx4 a[44:47], v0, s[40:43], s89 offen offset:3072 sc1\n"
    "v_mfma_f32_16x16x32_bf16 a[188:191], a[148:151], a[116:119], a[188:191]\n"
    "v_mfma_f32_16x16x32_bf16 a[188:191], a[152:155], a[120:123], a[188:191]\n"
    "v_mfma_f32_16x16x32_bf16 a[188:191], a[156:159], a[124:127], a[188:191]\n"
    "s_waitcnt lgkmcnt(4)\n"
    "s_add_u32 m0, s77, 2048\n"
    "v_mfma_f32_16x16x32_bf16 a[192:195], a[128:131], a[64:67], a[192:195]\n"
    "v_mfma_f32_16x16x32_bf16 a[192:195], a[132:135], a[68:71], a[192:195]\n"
    "s_waitcnt lgkmcnt(0)\n"
    "v_mfma_f32_16x16x32_bf16 a[192:195], a[136:139], a[72:75], a[192:195]\n"
    "s_add_u32 s88, s79, 7077888\n"
    "ds_read_b64_tr_b16 a[144:145], v8 offset:4104\n"
    "v_mfma_f32_16x16x32_bf16 a[192:195], a[140:143], a[76:79], a[192:195]\n"
    "ds_read_b64_tr_b16 a[146:147], v8 offset:4616\n"
    "v_mfma_f32_16x16x32_bf16 a[196:199], a[128:131], a[80:83], a[196:199]\n"
    "ds_read_b64_tr_b16 a[148:149], v8 offset:5128\n"
    "v_mfma_f32_16x16x32_bf16 a[196:199], a[132:135], a[84:87], a[196:199]\n"
    "buffer_load_dwordx4 v1, s[44:47], s88 offen offset:0 lds\n"
    "v_mfma_f32_16x16x32_bf16 a[196:199], a[136:139], a[88:91], a[196:199]\n"
    "ds_read_b64_tr_b16 a[150:151], v8 offset:5640\n"
    "ds_read_b64_tr_b16 a[152:153], v8 offset:6152\n"
    "v_mfma_f32_16x16x32_bf16 a[196:199], a[140:143], a[92:95], a[196:199]\n"
    "ds_read_b64_tr_b16 a[154:155], v8 offset:6664\n"
    "v_mfma_f32_16x16x32_bf16 a[200:203], a[128:131], a[96:99], a[200:203]\n"
    "s_add_u32 m0, s77, 6144\n"
    "ds_read_b64_tr_b16 a[156:157], v8 offset:7176\n"
    "v_mfma_f32_16x16x32_bf16 a[200:203], a[132:135], a[100:103], a[200:203]\n"
    "ds_read_b64_tr_b16 a[158:159], v8 offset:7688\n"
    "v_mfma_f32_16x16x32_bf16 a[200:203], a[136:139], a[104:107], a[200:203]\n"
    "v_mfma_f32_16x16x32_bf16 a[200:203], a[140:143], a[108:111], a[200:203]\n"
    "s_add_u32 s88, s79, 7078016\n"
    "v_mfma_f32_16x16x32_bf16 a[204:207], a[128:131], a[112:115], a[204:207]\n"
    "v_mfma_f32_16x16x32_bf16 a[204:207], a[132:135], a[116:119], a[204:207]\n"
    "v_mfma_f32_16x16x32_bf16 a[204:207], a[136:139], a[120:123], a[204:207]\n"
    "v_mfma_f32_16x16x32_bf16 a[204:207], a[140:143], a[124:127], a[204:207]\n"
    "s_waitcnt lgkmcnt(4)\n"
    "v_mfma_f32_16x16x32_bf16 v[112:115], a[144:147], a[64:67], v[112:115]\n"
    "buffer_load_dwordx4 v1, s[44:47], s88 offen offset:0 lds\n"
    "v_mfma_f32_16x16x32_bf16 v[112:115], a[148:151], a[68:71], v[112:115]\n"
    "s_waitcnt lgkmcnt(0)\n"
    "v_mfma_f32_16x16x32_bf16 v[112:115], a[152:155], a[72:75], v[112:115]\n"
    "s_add_u32 m0, s77, 10240\n"
    "ds_read_b64_tr_b16 a[128:129], v9 offset:4096\n"
    "v_mfma_f32_16x16x32_bf16 v[112:115], a[156:159], a[76:79], v[112:115]\n"
    "ds_read_b64_tr_b16 a[130:131], v9 offset:4608\n"
    "v_mfma_f32_16x16x32_bf16 v[116:119], a[144:147], a[80:83], v[116:119]\n"
    "ds_read_b64_tr_b16 a[132:133], v9 offset:5120\n"
    "v_mfma_f32_16x16x32_bf16 v[116:119], a[148:151], a[84:87], v[116:119]\n"
    "s_add_u32 s88, s79, 7078144\n"
    "ds_read_b64_tr_b16 a[134:135], v9 offset:5632\n"
    "v_mfma_f32_16x16x32_bf16 v[116:119], a[152:155], a[88:91], v[116:119]\n"
    "ds_read_b64_tr_b16 a[136:137], v9 offset:6144\n"
    "v_mfma_f32_16x16x32_bf16 v[116:119], a[156:159], a[92:95], v[116:119]\n"
    "ds_read_b64_tr_b16 a[138:139], v9 offset:6656\n"
    "v_mfma_f32_16x16x32_bf16 v[120:123], a[144:147], a[96:99], v[120:123]\n"
    "buffer_load_dwordx4 v1, s[44:47], s88 offen offset:0 lds\n"
    "v_mfma_f32_16x16x32_bf16 v[120:123], a[148:151], a[100:103], v[120:123]\n"
    "ds_read_b64_tr_b16 a[140:141], v9 offset:7168\n"
    "ds_read_b64_tr_b16 a[142:143], v9 offset:7680\n"
    "v_mfma_f32_16x16x32_bf16 v[120:123], a[152:155], a[104:107], v[120:123]\n"
    "v_mfma_f32_16x16x32_bf16 v[120:123], a[156:159], a[108:111], v[120:123]\n"
    "s_add_u32 s89, s99, 16384\n"
    "v_mfma_f32_16x16x32_bf16 v[124:127], a[144:147], a[112:115], v[124:127]\n"
    "v_mfma_f32_16x16x32_bf16 v[124:127], a[148:151], a[116:119], v[124:127]\n"
    "v_mfma_f32_16x16x32_bf16 v[124:127], a[152:155], a[120:123], v[124:127]\n"
    "buffer_load_dwordx4 a[48:51], v0, s[40:43], s89 offen offset:0 sc1\n"
    "v_mfma_f32_16x16x32_bf16 v[124:127], a[156:159], a[124:127], v[124:127]\n"
    "s_waitcnt lgkmcnt(4)\n"
    "v_mfma_f32_16x16x32_bf16 v[128:131], a[128:131], a[64:67], v[128:131]\n"
    "v_mfma_f32_16x16x32_bf16 v[128:131], a[132:135], a[68:71], v[128:131]\n"
    "s_waitcnt lgkmcnt(0)\n"
    "v_mfma_f32_16x16x32_bf16 v[128:131], a[136:139], a[72:75], v[128:131]\n"
    "buffer_load_dwordx4 a[52:55], v0, s[40:43], s89 offen offset:1024 sc1\n"
    "v_mfma_f32_16x16x32_bf16 v[128:131], a[140:143], a[76:79], v[128:131]\n"
    "ds_read_b64_tr_b16 a[144:145], v9 offset:4104\n"
    "ds_read_b64_tr_b16 a[146:147], v9 offset:4616\n"
    "v_mfma_f32_16x16x32_bf16 v[132:135], a[128:131], a[80:83], v[132:135]\n"
    "buffer_load_dwordx4 a[56:59], v0, s[40:43], s89 offen offset:2048 sc1\n"
    "v_mfma_f32_16x16x32_bf16 v[132:135], a[132:135], a[84:87], v[132:135]\n"
    "ds_read_b64_tr_b16 a[148:149], v9 offset:5128\n"
    "ds_read_b64_tr_b16 a[150:151], v9 offset:5640\n"
    "v_mfma_f32_16x16x32_bf16 v[132:135], a[136:139], a[88:91], v[132:135]\n"
    "ds_read_b64_tr_b16 a[152:153], v9 offset:6152\n"
    "v_mfma_f32_16x16x32_bf16 v[132:135], a[140:143], a[92:95], v[132:135]\n"
    "buffer_load_dwordx4 a[60:63], v0, s[40:43], s89 offen offset:3072 sc1\n"
    "v_mfma_f32_16x16x32_bf16 v[136:139], a[128:131], a[96:99], v[136:139]\n"
    "ds_read_b64_tr_b16 a[154:155], v9 offset:6664\n"
    "ds_read_b64_tr_b16 a[156:157], v9 offset:7176\n"
    "v_mfma_f32_16x16x32_bf16 v[136:139], a[132:135], a[100:103], v[136:139]\n"
    "ds_read_b64_tr_b16 a[158:159], v9 offset:7688\n"
    "v_mfma_f32_16x16x32_bf16 v[136:139], a[136:139], a[104:107], v[136:139]\n"
    "s_add_u32 m0, s77, 3072\n"
    "v_mfma_f32_16x16x32_bf16 v[136:139], a[140:143], a[108:111], v[136:139]\n"
    "v_mfma_f32_16x16x32_bf16 v[140:143], a[128:131], a[112:115], v[140:143]\n"
    "v_mfma_f32_16x16x32_bf16 v[140:143], a[132:135], a[116:119], v[140:143]\n"
    "s_add_u32 s88, s79, 7471104\n"
    "v_mfma_f32_16x16x32_bf16 v[140:143], a[136:139], a[120:123], v[140:143]\n"
    "v_mfma_f32_16x16x32_bf16 v[140:143], a[140:143], a[124:127], v[140:143]\n"
    "s_waitcnt lgkmcnt(4)\n"
    "v_mfma_f32_16x16x32_bf16 v[144:147], a[144:147], a[64:67], v[144:147]\n"
    "buffer_load_dwordx4 v1, s[44:47], s88 offen offset:0 lds\n"
    "v_mfma_f32_16x16x32_bf16 v[144:147], a[148:151], a[68:71], v[144:147]\n"
    "s_waitcnt lgkmcnt(0)\n"
    "v_mfma_f32_16x16x32_bf16 v[144:147], a[152:155], a[72:75], v[144:147]\n"
    "ds_read_b64_tr_b16 a[128:129], v8 offset:8192\n"
    "v_mfma_f32_16x16x32_bf16 v[144:147], a[156:159], a[76:79], v[144:147]\n"
    "s_add_u32 m0, s77, 7168\n"
    "ds_read_b64_tr_b16 a[130:131], v8 offset:8704\n"
    "v_mfma_f32_16x16x32_bf16 v[148:151], a[144:147], a[80:83], v[148:151]\n"
    "ds_read_b64_tr_b16 a[132:133], v8 offset:9216\n"
    "v_mfma_f32_16x16x32_bf16 v[148:151], a[148:151], a[84:87], v[148:151]\n"
    "ds_read_b64_tr_b16 a[134:135], v8 offset:9728\n"
    "v_mfma_f32_16x16x32_bf16 v[148:151], a[152:155], a[88:91], v[148:151]\n"
    "s_add_u32 s88, s79, 7471232\n"
    "ds_read_b64_tr_b16 a[136:137], v8 offset:10240\n"
    "v_mfma_f32_16x16x32_bf16 v[148:151], a[156:159], a[92:95], v[148:151]\n"
    "ds_read_b64_tr_b16 a[138:139], v8 offset:10752\n"
    "v_mfma_f32_16x16x32_bf16 v[152:155], a[144:147], a[96:99], v[152:155]\n"
    "ds_read_b64_tr_b16 a[140:141], v8 offset:11264\n"
    "v_mfma_f32_16x16x32_bf16 v[152:155], a[148:151], a[100:103], v[152:155]\n"
    "buffer_load_dwordx4 v1, s[44:47], s88 offen offset:0 lds\n"
    "v_mfma_f32_16x16x32_bf16 v[152:155], a[152:155], a[104:107], v[152:155]\n"
    "ds_read_b64_tr_b16 a[142:143], v8 offset:11776\n"
    "v_mfma_f32_16x16x32_bf16 v[152:155], a[156:159], a[108:111], v[152:155]\n"
    "v_mfma_f32_16x16x32_bf16 v[156:159], a[144:147], a[112:115], v[156:159]\n"
    "s_add_u32 m0, s77, 11264\n"
    "v_mfma_f32_16x16x32_bf16 v[156:159], a[148:151], a[116:119], v[156:159]\n"
    "v_mfma_f32_16x16x32_bf16 v[156:159], a[152:155], a[120:123], v[156:159]\n"
    "v_mfma_f32_16x16x32_bf16 v[156:159], a[156:159], a[124:127], v[156:159]\n"
    "s_waitcnt lgkmcnt(4)\n"
    "s_add_u32 s88, s79, 7471360\n"
    "v_mfma_f32_16x16x32_bf16 v[160:163], a[128:131], a[64:67], v[160:163]\n"
    "v_mfma_f32_16x16x32_bf16 v[160:163], a[132:135], a[68:71], v[160:163]\n"
    "s_waitcnt lgkmcnt(0)\n"
    "v_mfma_f32_16x16x32_bf16 v[160:163], a[136:139], a[72:75], v[160:163]\n"
    "buffer_load_dwordx4 v1, s[44:47], s88 offen offset:0 lds\n"
    "v_mfma_f32_16x16x32_bf16 v[160:163], a[140:143], a[76:79], v[160:163]\n"
    "ds_read_b64_tr_b16 a[144:145], v8 offset:8200\n"
    "ds_read_b64_tr_b16 a[146:147], v8 offset:8712\n"
    "v_mfma_f32_16x16x32_bf16 v[164:167], a[128:131], a[80:83], v[164:167]\n"
    "ds_read_b64_tr_b16 a[148:149], v8 offset:9224\n"
    "v_mfma_f32_16x16x32_bf16 v[164:167], a[132:135], a[84:87], v[164:167]\n"
    "ds_read_b64_tr_b16 a[150:151], v8 offset:9736\n"
    "v_mfma_f32_16x16x32_bf16 v[164:167], a[136:139], a[88:91], v[164:167]\n"
    "ds_read_b64_tr_b16 a[152:153], v8 offset:10248\n"
    "v_mfma_f32_16x16x32_bf16 v[164:167], a[140:143], a[92:95], v[164:167]\n"
    "ds_read_b64_tr_b16 a[154:155], v8 offset:10760\n"
    "v_mfma_f32_16x16x32_bf16 v[168:171], a[128:131], a[96:99], v[168:171]\n"
    "ds_read_b64_tr_b16 a[156:157], v8 offset:11272\n"
    "v_mfma_f32_16x16x32_bf16 v[168:171], a[132:135], a[100:103], v[168:171]\n"
    "ds_read_b64_tr_b16 a[158:159], v8 offset:11784\n"
    "v_mfma_f32_16x16x32_bf16 v[168:171], a[136:139], a[104:107], v[168:171]\n"
    "v_mfma_f32_16x16x32_bf16 v[168:171], a[140:143], a[108:111], v[168:171]\n"
    "v_mfma_f32_16x16x32_bf16 v[172:175], a[128:131], a[112:115], v[172:175]\n"
    "v_mfma_f32_16x16x32_bf16 v[172:175], a[132:135], a[116:119], v[172:175]\n"
    "v_mfma_f32_16x16x32_bf16 v[172:175], a[136:139], a[120:123], v[172:175]\n"
    "v_mfma_f32_16x16x32_bf16 v[172:175], a[140:143], a[124:127], v[172:175]\n"
    "s_waitcnt lgkmcnt(4)\n"
    "v_mfma_f32_16x16x32_bf16 v[176:179], a[144:147], a[64:67], v[176:179]\n"
    "v_mfma_f32_16x16x32_bf16 v[176:179], a[148:151], a[68:71], v[176:179]\n"
    "s_waitcnt lgkmcnt(0)\n"
    "v_mfma_f32_16x16x32_bf16 v[176:179], a[152:155], a[72:75], v[176:179]\n"
    "ds_read_b64_tr_b16 a[128:129], v9 offset:8192\n"
    "v_mfma_f32_16x16x32_bf16 v[176:179], a[156:159], a[76:79], v[176:179]\n"
    "ds_read_b64_tr_b16 a[130:131], v9 offset:8704\n"
    "v_mfma_f32_16x16x32_bf16 v[180:183], a[144:147], a[80:83], v[180:183]\n"
    "ds_read_b64_tr_b16 a[132:133], v9 offset:9216\n"
    "v_mfma_f32_16x16x32_bf16 v[180:183], a[148:151], a[84:87], v[180:183]\n"
    "ds_read_b64_tr_b16 a[134:135], v9 offset:9728\n"
    "v_mfma_f32_16x16x32_bf16 v[180:183], a[152:155], a[88:91], v[180:183]\n"
    "ds_read_b64_tr_b16 a[136:137], v9 offset:10240\n"
    "v_mfma_f32_16x16x32_bf16 v[180:183], a[156:159], a[92:95], v[180:183]\n"
    "ds_read_b64_tr_b16 a[138:139], v9 offset:10752\n"
    "v_mfma_f32_16x16x32_bf16 v[184:187], a[144:147], a[96:99], v[184:187]\n"
    "ds_read_b64_tr_b16 a[140:141], v9 offset:11264\n"
    "v_mfma_f32_16x16x32_bf16 v[184:187], a[148:151], a[100:103], v[184:187]\n"
    "ds_read_b64_tr_b16 a[142:143], v9 offset:11776\n"
    "v_mfma_f32_16x16x32_bf16 v[184:187], a[152:155], a[104:107], v[184:187]\n"
    "v_mfma_f32_16x16x32_bf16 v[184:187], a[156:159], a[108:111], v[184:187]\n"
    "v_mfma_f32_16x16x32_bf16 v[188:191], a[144:147], a[112:115], v[188:191]\n"
    "v_mfma_f32_16x16x32_bf16 v[188:191], a[148:151], a[116:119], v[188:191]\n"
    "v_mfma_f32_16x16x32_bf16 v[188:191], a[152:155], a[120:123], v[188:191]\n"
    "v_mfma_f32_16x16x32_bf16 v[188:191], a[156:159], a[124:127], v[188:191]\n"
    "s_waitcnt lgkmcnt(4)\n"
    "v_mfma_f32_16x16x32_bf16 v[192:195], a[128:131], a[64:67], v[192:195]\n"
    "v_mfma_f32_16x16x32_bf16 v[192:195], a[132:135], a[68:71], v[192:195]\n"
    "s_waitcnt lgkmcnt(0)\n"
    "v_mfma_f32_16x16x32_bf16 v[192:195], a[136:139], a[72:75], v[192:195]\n"
    "ds_read_b64_tr_b16 a[144:145], v9 offset:8200\n"
    "v_mfma_f32_16x16x32_bf16 v[192:195], a[140:143], a[76:79], v[192:195]\n"
    "ds_read_b64_tr_b16 a[146:147], v9 offset:8712\n"
    "v_mfma_f32_16x16x32_bf16 v[196:199], a[128:131], a[80:83], v[196:199]\n"
    "ds_read_b64_tr_b16 a[148:149], v9 offset:9224\n"
    "v_mfma_f32_16x16x32_bf16 v[196:199], a[132:135], a[84:87], v[196:199]\n"
    "ds_read_b64_tr_b16 a[150:151], v9 offset:9736\n"
    "v_mfma_f32_16x16x32_bf16 v[196:199], a[136:139], a[88:91], v[196:199]\n"
    "ds_read_b64_tr_b16 a[152:153], v9 offset:10248\n"
    "v_mfma_f32_16x16x32_bf16 v[196:199], a[140:143], a[92:95], v[196:199]\n"
    "ds_read_b64_tr_b16 a[154:155], v9 offset:10760\n"
    "v_mfma_f32_16x16x32_bf16 v[200:203], a[128:131], a[96:99], v[200:203]\n"
    "ds_read_b64_tr_b16 a[156:157], v9 offset:11272\n"
    "v_mfma_f32_16x16x32_bf16 v[200:203], a[132:135], a[100:103], v[200:203]\n"
    "ds_read_b64_tr_b16 a[158:159], v9 offset:11784\n"
    "v_mfma_f32_16x16x32_bf16 v[200:203], a[136:139], a[104:107], v[200:203]\n"
    "v_mfma_f32_16x16x32_bf16 v[200:203], a[140:143], a[108:111], v[200:203]\n"
    "v_mfma_f32_16x16x32_bf16 v[204:207], a[128:131], a[112:115], v[204:207]\n"
    "v_mfma_f32_16x16x32_bf16 v[204:207], a[132:135], a[116:119], v[204:207]\n"
    "v_mfma_f32_16x16x32_bf16 v[204:207], a[136:139], a[120:123], v[204:207]\n"
    "v_mfma_f32_16x16x32_bf16 v[204:207], a[140:143], a[124:127], v[204:207]\n"
    "s_waitcnt vmcnt(0) lgkmcnt(0)\n"
    "s_barrier\n"
    "v_mfma_f32_16x16x32_bf16 v[208:211], a[144:147], a[64:67], v[208:211]\n"
    "ds_read_b64_tr_b16 a[128:129], v7 offset:0\n"
    "v_mfma_f32_16x16x32_bf16 v[208:211], a[148:151], a[68:71], v[208:211]\n"
    "ds_read_b64_tr_b16 a[130:131], v7 offset:1536\n"
    "v_mfma_f32_16x16x32_bf16 v[208:211], a[152:155], a[72:75], v[208:211]\n"
    "ds_read_b64_tr_b16 a[132:133], v7 offset:3072\n"
    "v_mfma_f32_16x16x32_bf16 v[208:211], a[156:159], a[76:79], v[208:211]\n"
    "ds_read_b64_tr_b16 a[134:135], v7 offset:4608\n"
    "v_mfma_f32_16x16x32_bf16 v[212:215], a[144:147], a[80:83], v[212:215]\n"
    "ds_read_b64_tr_b16 a[136:137], v7 offset:6144\n"
    "v_mfma_f32_16x16x32_bf16 v[212:215], a[148:151], a[84:87], v[212:215]\n"
    "ds_read_b64_tr_b16 a[138:139], v7 offset:7680\n"
    "v_mfma_f32_16x16x32_bf16 v[212:215], a[152:155], a[88:91], v[212:215]\n"
    "ds_read_b64_tr_b16 a[140:141], v7 offset:9216\n"
    "v_mfma_f32_16x16x32_bf16 v[212:215], a[156:159], a[92:95], v[212:215]\n"
    "ds_read_b64_tr_b16 a[142:143], v7 offset:10752\n"
    "v_mfma_f32_16x16x32_bf16 v[216:219], a[144:147], a[96:99], v[216:219]\n"
    "v_mfma_f32_16x16x32_bf16 v[216:219], a[148:151], a[100:103], v[216:219]\n"
    "v_mfma_f32_16x16x32_bf16 v[216:219], a[152:155], a[104:107], v[216:219]\n"
    "v_mfma_f32_16x16x32_bf16 v[216:219], a[156:159], a[108:111], v[216:219]\n"
    "v_mfma_f32_16x16x32_bf16 v[220:223], a[144:147], a[112:115], v[220:223]\n"
    "v_mfma_f32_16x16x32_bf16 v[220:223], a[148:151], a[116:119], v[220:223]\n"
    "v_mfma_f32_16x16x32_bf16 v[220:223], a[152:155], a[120:123], v[220:223]\n"
    "v_mfma_f32_16x16x32_bf16 v[220:223], a[156:159], a[124:127], v[220:223]\n"
    "s_waitcnt lgkmcnt(4)\n"
    "v_mfma_f32_16x16x32_bf16 v[224:227], a[128:131], a[0:3], v[224:227]\n"
    "s_add_u32 m0, s83, 0\n"
    "v_mfma_f32_16x16x32_bf16 v[224:227], a[132:135], a[4:7], v[224:227]\n"
    "s_waitcnt lgkmcnt(0)\n"
    "v_mfma_f32_16x16x32_bf16 v[224:227], a[136:139], a[8:11], v[224:227]\n"
    "ds_read_b64_tr_b16 a[144:145], v7 offset:32\n"
    "v_mfma_f32_16x16x32_bf16 v[224:227], a[140:143], a[12:15], v[224:227]\n"
    "s_add_u32 s88, s82, 0\n"
    "ds_read_b64_tr_b16 a[146:147], v7 offset:1568\n"
    "v_mfma_f32_16x16x32_bf16 v[228:231], a[128:131], a[16:19], v[228:231]\n"
    "s_mov_b32 s81, 0\n"
    "ds_read_b64_tr_b16 a[148:149], v7 offset:3104\n"
    "v_mfma_f32_16x16x32_bf16 v[228:231], a[132:135], a[20:23], v[228:231]\n"
    "ds_read_b64_tr_b16 a[150:151], v7 offset:4640\n"
    "v_mfma_f32_16x16x32_bf16 v[228:231], a[136:139], a[24:27], v[228:231]\n"
    "buffer_load_dwordx4 v11, s[56:59], s88 offen offset:0 lds\n"
    "v_mfma_f32_16x16x32_bf16 v[228:231], a[140:143], a[28:31], v[228:231]\n"
    "ds_read_b64_tr_b16 a[152:153], v7 offset:6176\n"
    "ds_read_b64_tr_b16 a[154:155], v7 offset:7712\n"
    "v_mfma_f32_16x16x32_bf16 v[232:235], a[128:131], a[32:35], v[232:235]\n"
    "ds_read_b64_tr_b16 a[156:157], v7 offset:9248\n"
    "v_mfma_f32_16x16x32_bf16 v[232:235], a[132:135], a[36:39], v[232:235]\n"
    "buffer_load_dwordx4 v12, s[56:59], s88 offen offset:1024 lds\n"
    "v_mfma_f32_16x16x32_bf16 v[232:235], a[136:139], a[40:43], v[232:235]\n"
    "ds_read_b64_tr_b16 a[158:159], v7 offset:10784\n"
    "v_mfma_f32_16x16x32_bf16 v[232:235], a[140:143], a[44:47], v[232:235]\n"
    "s_mul_i32 s81, s81, 249856\n"
    "v_mfma_f32_16x16x32_bf16 v[236:239], a[128:131], a[48:51], v[236:239]\n"
    "buffer_load_dwordx4 v13, s[56:59], s88 offen offset:2048 lds\n"
    "v_mfma_f32_16x16x32_bf16 v[236:239], a[132:135], a[52:55], v[236:239]\n"
    "v_mfma_f32_16x16x32_bf16 v[236:239], a[136:139], a[56:59], v[236:239]\n"
    "v_mfma_f32_16x16x32_bf16 v[236:239], a[140:143], a[60:63], v[236:239]\n"
    "s_waitcnt lgkmcnt(4)\n"
    "s_add_u32 m0, s83, 12288\n"
    "v_mfma_f32_16x16x32_bf16 v[240:243], a[144:147], a[0:3], v[240:243]\n"
    "v_mfma_f32_16x16x32_bf16 v[240:243], a[148:151], a[4:7], v[240:243]\n"
    "s_waitcnt lgkmcnt(0)\n"
    "v_mfma_f32_16x16x32_bf16 v[240:243], a[152:155], a[8:11], v[240:243]\n"
    "s_add_u32 s88, s82, 12288\n"
    "ds_read_b64_tr_b16 a[128:129], v7 offset:64\n"
    "s_mov_b32 s3, 1\n"
    "v_mfma_f32_16x16x32_bf16 v[240:243], a[156:159], a[12:15], v[240:243]\n"
    "ds_read_b64_tr_b16 a[130:131], v7 offset:1600\n"
    "v_mfma_f32_16x16x32_bf16 v[244:247], a[144:147], a[16:19], v[244:247]\n"
    "ds_read_b64_tr_b16 a[132:133], v7 offset:3136\n"
    "v_mfma_f32_16x16x32_bf16 v[244:247], a[148:151], a[20:23], v[244:247]\n"
    "buffer_load_dwordx4 v11, s[56:59], s88 offen offset:0 lds\n"
    "v_mfma_f32_16x16x32_bf16 v[244:247], a[152:155], a[24:27], v[244:247]\n"
    "ds_read_b64_tr_b16 a[134:135], v7 offset:4672\n"
    "ds_read_b64_tr_b16 a[136:137], v7 offset:6208\n"
    "v_mfma_f32_16x16x32_bf16 v[244:247], a[156:159], a[28:31], v[244:247]\n"
    "ds_read_b64_tr_b16 a[138:139], v7 offset:7744\n"
    "v_mfma_f32_16x16x32_bf16 v[248:251], a[144:147], a[32:35], v[248:251]\n"
    "buffer_load_dwordx4 v12, s[56:59], s88 offen offset:1024 lds\n"
    "v_mfma_f32_16x16x32_bf16 v[248:251], a[148:151], a[36:39], v[248:251]\n"
    "s_mul_i32 s3, s3, 249856\n"
    "ds_read_b64_tr_b16 a[140:141], v7 offset:9280\n"
    "ds_read_b64_tr_b16 a[142:143], v7 offset:10816\n"
    "v_mfma_f32_16x16x32_bf16 v[248:251], a[152:155], a[40:43], v[248:251]\n"
    "v_mfma_f32_16x16x32_bf16 v[248:251], a[156:159], a[44:47], v[248:251]\n"
    "buffer_load_dwordx4 v13, s[56:59], s88 offen offset:2048 lds\n"
    "v_mfma_f32_16x16x32_bf16 v[252:255], a[144:147], a[48:51], v[252:255]\n"
    "v_mfma_f32_16x16x32_bf16 v[252:255], a[148:151], a[52:55], v[252:255]\n"
    "v_mfma_f32_16x16x32_bf16 v[252:255], a[152:155], a[56:59], v[252:255]\n"
    "s_add_u32 m0, s83, 24576\n"
    "v_mfma_f32_16x16x32_bf16 v[252:255], a[156:159], a[60:63], v[252:255]\n"
    "s_waitcnt lgkmcnt(4)\n"
    "v_mfma_f32_16x16x32_bf16 v[16:19], a[128:131], a[0:3], v[16:19]\n"
    "s_mov_b32 s39, 2\n"
    "v_mfma_f32_16x16x32_bf16 v[16:19], a[132:135], a[4:7], v[16:19]\n"
    "s_waitcnt lgkmcnt(0)\n"
    "s_add_u32 s88, s82, 24576\n"
    "v_mfma_f32_16x16x32_bf16 v[16:19], a[136:139], a[8:11], v[16:19]\n"
    "ds_read_b64_tr_b16 a[144:145], v7 offset:12288\n"
    "v_mfma_f32_16x16x32_bf16 v[16:19], a[140:143], a[12:15], v[16:19]\n"
    "ds_read_b64_tr_b16 a[146:147], v7 offset:13824\n"
    "v_mfma_f32_16x16x32_bf16 v[20:23], a[128:131], a[16:19], v[20:23]\n"
    "buffer_load_dwordx4 v11, s[56:59], s88 offen offset:0 lds\n"
    "v_mfma_f32_16x16x32_bf16 v[20:23], a[132:135], a[20:23], v[20:23]\n"
    "ds_read_b64_tr_b16 a[148:149], v7 offset:15360\n"
    "ds_read_b64_tr_b16 a[150:151], v7 offset:16896\n"
    "v_mfma_f32_16x16x32_bf16 v[20:23], a[136:139], a[24:27], v[20:23]\n"
    "ds_read_b64_tr_b16 a[152:153], v7 offset:18432\n"
    "v_mfma_f32_16x16x32_bf16 v[20:23], a[140:143], a[28:31], v[20:23]\n"
    "buffer_load_dwordx4 v12, s[56:59], s88 offen offset:1024 lds\n"
    "v_mfma_f32_16x16x32_bf16 v[24:27], a[128:131], a[32:35], v[24:27]\n"
    "s_mul_i32 s39, s39, 249856\n"
    "ds_read_b64_tr_b16 a[154:155], v7 offset:19968\n"
    "ds_read_b64_tr_b16 a[156:157], v7 offset:21504\n"
    "v_mfma_f32_16x16x32_bf16 v[24:27], a[132:135], a[36:39], v[24:27]\n"
    "ds_read_b64_tr_b16 a[158:159], v7 offset:23040\n"
    "v_mfma_f32_16x16x32_bf16 v[24:27], a[136:139], a[40:43], v[24:27]\n"
    "buffer_load_dwordx4 v13, s[56:59], s88 offen offset:2048 lds\n"
    "v_mfma_f32_16x16x32_bf16 v[24:27], a[140:143], a[44:47], v[24:27]\n"
    "v_mfma_f32_16x16x32_bf16 v[28:31], a[128:131], a[48:51], v[28:31]\n"
    "v_mfma_f32_16x16x32_bf16 v[28:31], a[132:135], a[52:55], v[28:31]\n"
    "s_add_u32 m0, s83, 36864\n"
    "v_mfma_f32_16x16x32_bf16 v[28:31], a[136:139], a[56:59], v[28:31]\n"
    "s_mov_b32 s99, 3\n"
    "v_mfma_f32_16x16x32_bf16 v[28:31], a[140:143], a[60:63], v[28:31]\n"
    "s_waitcnt lgkmcnt(4)\n"
    "v_mfma_f32_16x16x32_bf16 v[32:35], a[144:147], a[0:3], v[32:35]\n"
    "s_add_u32 s88, s82, 36864\n"
    "v_mfma_f32_16x16x32_bf16 v[32:35], a[148:151], a[4:7], v[32:35]\n"
    "s_waitcnt lgkmcnt(0)\n"
    "v_mfma_f32_16x16x32_bf16 v[32:35], a[152:155], a[8:11], v[32:35]\n"
    "ds_read_b64_tr_b16 a[128:129], v7 offset:12320\n"
    "v_mfma_f32_16x16x32_bf16 v[32:35], a[156:159], a[12:15], v[32:35]\n"
    "buffer_load_dwordx4 v11, s[56:59], s88 offen offset:0 lds\n"
    "v_mfma_f32_16x16x32_bf16 v[36:39], a[144:147], a[16:19], v[36:39]\n"
    "ds_read_b64_tr_b16 a[130:131], v7 offset:13856\n"
    "ds_read_b64_tr_b16 a[132:133], v7 offset:15392\n"
    "v_mfma_f32_16x16x32_bf16 v[36:39], a[148:151], a[20:23], v[36:39]\n"
    "s_mul_i32 s99, s99, 249856\n"
    "ds_read_b64_tr_b16 a[134:135], v7 offset:16928\n"
    "v_mfma_f32_16x16x32_bf16 v[36:39], a[152:155], a[24:27], v[36:39]\n"
    "ds_read_b64_tr_b16 a[136:137], v7 offset:18464\n"
    "v_mul_f32 v32, v224, v32\n"
    "v_mfma_f32_16x16x32_bf16 v[36:39], a[156:159], a[28:31], v[36:39]\n"
    "ds_read_b64_tr_b16 a[138:139], v7 offset:20000\n"
    "v_mul_f32 v33, v225, v33\n"
    "v_mfma_f32_16x16x32_bf16 v[40:43], a[144:147], a[32:35], v[40:43]\n"
    "ds_read_b64_tr_b16 a[140:141], v7 offset:21536\n"
    "v_mul_f32 v34, v226, v34\n"
    "v_mfma_f32_16x16x32_bf16 v[40:43], a[148:151], a[36:39], v[40:43]\n"
    "ds_read_b64_tr_b16 a[142:143], v7 offset:23072\n"
    "v_mul_f32 v35, v227, v35\n"
    "v_mfma_f32_16x16x32_bf16 v[40:43], a[152:155], a[40:43], v[40:43]\n"
    "buffer_load_dwordx4 v12, s[56:59], s88 offen offset:1024 lds\n"
    "v_mfma_f32_16x16x32_bf16 v[40:43], a[156:159], a[44:47], v[40:43]\n"
    "buffer_load_dwordx4 v13, s[56:59], s88 offen offset:2048 lds\n"
    "v_mfma_f32_16x16x32_bf16 v[44:47], a[144:147], a[48:51], v[44:47]\n"
    "v_cvt_pk_bf16_f32 v224, v32, v33\n"
    "v_cvt_pk_bf16_f32 v225, v34, v35\n"
    "v_mfma_f32_16x16x32_bf16 v[44:47], a[148:151], a[52:55], v[44:47]\n"
    "v_mul_f32 v36, v228, v36\n"
    "v_mul_f32 v37, v229, v37\n"
    "v_mfma_f32_16x16x32_bf16 v[44:47], a[152:155], a[56:59], v[44:47]\n"
    "v_mul_f32 v38, v230, v38\n"
    "v_mul_f32 v39, v231, v39\n"
    "v_mfma_f32_16x16x32_bf16 v[44:47], a[156:159], a[60:63], v[44:47]\n"
    "s_waitcnt lgkmcnt(4)\n"
    "v_cvt_pk_bf16_f32 v228, v36, v37\n"
    "v_cvt_pk_bf16_f32 v229, v38, v39\n"
    "v_mfma_f32_16x16x32_bf16 v[48:51], a[128:131], a[0:3], v[48:51]\n"
    "v_mul_f32 v40, v232, v40\n"
    "v_mul_f32 v41, v233, v41\n"
    "v_mfma_f32_16x16x32_bf16 v[48:51], a[132:135], a[4:7], v[48:51]\n"
    "s_waitcnt lgkmcnt(0)\n"
    "v_mul_f32 v42, v234, v42\n"
    "v_mul_f32 v43, v235, v43\n"
    "v_mfma_f32_16x16x32_bf16 v[48:51], a[136:139], a[8:11], v[48:51]\n"
    "ds_read_b64_tr_b16 a[144:145], v7 offset:12352\n"
    "v_cvt_pk_bf16_f32 v232, v40, v41\n"
    "v_mfma_f32_16x16x32_bf16 v[48:51], a[140:143], a[12:15], v[48:51]\n"
    "ds_read_b64_tr_b16 a[146:147], v7 offset:13888\n"
    "v_cvt_pk_bf16_f32 v233, v42, v43\n"
    "v_mfma_f32_16x16x32_bf16 v[52:55], a[128:131], a[16:19], v[52:55]\n"
    "ds_read_b64_tr_b16 a[148:149], v7 offset:15424\n"
    "v_mul_f32 v44, v236, v44\n"
    "v_mfma_f32_16x16x32_bf16 v[52:55], a[132:135], a[20:23], v[52:55]\n"
    "ds_read_b64_tr_b16 a[150:151], v7 offset:16960\n"
    "v_mul_f32 v45, v237, v45\n"
    "v_mfma_f32_16x16x32_bf16 v[52:55], a[136:139], a[24:27], v[52:55]\n"
    "ds_read_b64_tr_b16 a[152:153], v7 offset:18496\n"
    "v_mul_f32 v46, v238, v46\n"
    "v_mfma_f32_16x16x32_bf16 v[52:55], a[140:143], a[28:31], v[52:55]\n"
    "ds_read_b64_tr_b16 a[154:155], v7 offset:20032\n"
    "v_mul_f32 v47, v239, v47\n"
    "v_mfma_f32_16x16x32_bf16 v[56:59], a[128:131], a[32:35], v[56:59]\n"
    "ds_read_b64_tr_b16 a[156:157], v7 offset:21568\n"
    "v_cvt_pk_bf16_f32 v236, v44, v45\n"
    "v_mfma_f32_16x16x32_bf16 v[56:59], a[132:135], a[36:39], v[56:59]\n"
    "ds_read_b64_tr_b16 a[158:159], v7 offset:23104\n"
    "v_cvt_pk_bf16_f32 v237, v46, v47\n"
    "v_mfma_f32_16x16x32_bf16 v[56:59], a[136:139], a[40:43], v[56:59]\n"
    "v_mul_f32 v48, v240, v48\n"
    "v_mul_f32 v49, v241, v49\n"
    "v_mfma_f32_16x16x32_bf16 v[56:59], a[140:143], a[44:47], v[56:59]\n"
    "v_mul_f32 v50, v242, v50\n"
    "v_mul_f32 v51, v243, v51\n"
    "v_mfma_f32_16x16x32_bf16 v[60:63], a[128:131], a[48:51], v[60:63]\n"
    "v_cvt_pk_bf16_f32 v226, v48, v49\n"
    "v_cvt_pk_bf16_f32 v227, v50, v51\n"
    "v_mfma_f32_16x16x32_bf16 v[60:63], a[132:135], a[52:55], v[60:63]\n"
    "v_mul_f32 v52, v244, v52\n"
    "v_mul_f32 v53, v245, v53\n"
    "v_mfma_f32_16x16x32_bf16 v[60:63], a[136:139], a[56:59], v[60:63]\n"
    "v_mul_f32 v54, v246, v54\n"
    "v_mul_f32 v55, v247, v55\n"
    "v_mfma_f32_16x16x32_bf16 v[60:63], a[140:143], a[60:63], v[60:63]\n"
    "s_waitcnt lgkmcnt(4)\n"
    "v_cvt_pk_bf16_f32 v230, v52, v53\n"
    "v_cvt_pk_bf16_f32 v231, v54, v55\n"
    "v_mfma_f32_16x16x32_bf16 v[64:67], a[144:147], a[0:3], v[64:67]\n"
    "v_mul_f32 v56, v248, v56\n"
    "v_mul_f32 v57, v249, v57\n"
    "v_mfma_f32_16x16x32_bf16 v[64:67], a[148:151], a[4:7], v[64:67]\n"
    "s_waitcnt lgkmcnt(0)\n"
    "v_mul_f32 v58, v250, v58\n"
    "v_mul_f32 v59, v251, v59\n"
    "v_mfma_f32_16x16x32_bf16 v[64:67], a[152:155], a[8:11], v[64:67]\n"
    "ds_read_b64_tr_b16 a[128:129], v5 offset:0\n"
    "v_cvt_pk_bf16_f32 v234, v56, v57\n"
    "v_mfma_f32_16x16x32_bf16 v[64:67], a[156:159], a[12:15], v[64:67]\n"
    "ds_read_b64_tr_b16 a[130:131], v5 offset:512\n"
    "v_cvt_pk_bf16_f32 v235, v58, v59\n"
    "v_mfma_f32_16x16x32_bf16 v[68:71], a[144:147], a[16:19], v[68:71]\n"
    "ds_read_b64_tr_b16 a[132:133], v5 offset:1024\n"
    "v_mul_f32 v60, v252, v60\n"
    "v_mfma_f32_16x16x32_bf16 v[68:71], a[148:151], a[20:23], v[68:71]\n"
    "ds_read_b64_tr_b16 a[134:135], v5 offset:1536\n"
    "v_mul_f32 v61, v253, v61\n"
    "v_mfma_f32_16x16x32_bf16 v[68:71], a[152:155], a[24:27], v[68:71]\n"
    "ds_read_b64_tr_b16 a[136:137], v5 offset:2048\n"
    "v_mul_f32 v62, v254, v62\n"
    "v_mfma_f32_16x16x32_bf16 v[68:71], a[156:159], a[28:31], v[68:71]\n"
    "ds_read_b64_tr_b16 a[138:139], v5 offset:2560\n"
    "v_mul_f32 v63, v255, v63\n"
    "v_mfma_f32_16x16x32_bf16 v[72:75], a[144:147], a[32:35], v[72:75]\n"
    "ds_read_b64_tr_b16 a[140:141], v5 offset:3072\n"
    "v_cvt_pk_bf16_f32 v238, v60, v61\n"
    "v_mfma_f32_16x16x32_bf16 v[72:75], a[148:151], a[36:39], v[72:75]\n"
    "ds_read_b64_tr_b16 a[142:143], v5 offset:3584\n"
    "v_cvt_pk_bf16_f32 v239, v62, v63\n"
    "v_mfma_f32_16x16x32_bf16 v[72:75], a[152:155], a[40:43], v[72:75]\n"
    "v_mul_f32 v64, v16, v64\n"
    "v_mul_f32 v65, v17, v65\n"
    "v_mfma_f32_16x16x32_bf16 v[72:75], a[156:159], a[44:47], v[72:75]\n"
    "v_mul_f32 v66, v18, v66\n"
    "v_mul_f32 v67, v19, v67\n"
    "v_mfma_f32_16x16x32_bf16 v[76:79], a[144:147], a[48:51], v[76:79]\n"
    "v_cvt_pk_bf16_f32 v16, v64, v65\n"
    "v_cvt_pk_bf16_f32 v17, v66, v67\n"
    "v_mfma_f32_16x16x32_bf16 v[76:79], a[148:151], a[52:55], v[76:79]\n"
    "v_mul_f32 v68, v20, v68\n"
    "v_mul_f32 v69, v21, v69\n"
    "v_mfma_f32_16x16x32_bf16 v[76:79], a[152:155], a[56:59], v[76:79]\n"
    "v_mul_f32 v70, v22, v70\n"
    "v_mul_f32 v71, v23, v71\n"
    "v_mfma_f32_16x16x32_bf16 v[76:79], a[156:159], a[60:63], v[76:79]\n"
    "s_waitcnt lgkmcnt(4)\n"
    "v_cvt_pk_bf16_f32 v20, v68, v69\n"
    "v_cvt_pk_bf16_f32 v21, v70, v71\n"
    "v_mfma_f32_16x16x32_bf16 v[80:83], a[128:131], a[0:3], v[80:83]\n"
    "v_mul_f32 v72, v24, v72\n"
    "v_mul_f32 v73, v25, v73\n"
    "v_mfma_f32_16x16x32_bf16 v[80:83], a[132:135], a[4:7], v[80:83]\n"
    "s_waitcnt lgkmcnt(0)\n"
    "v_mul_f32 v74, v26, v74\n"
    "v_mul_f32 v75, v27, v75\n"
    "v_mfma_f32_16x16x32_bf16 v[80:83], a[136:139], a[8:11], v[80:83]\n"
    "ds_read_b64_tr_b16 a[144:145], v5 offset:8\n"
    "v_cvt_pk_bf16_f32 v24, v72, v73\n"
    "v_mfma_f32_16x16x32_bf16 v[80:83], a[140:143], a[12:15], v[80:83]\n"
    "ds_read_b64_tr_b16 a[146:147], v5 offset:520\n"
    "v_cvt_pk_bf16_f32 v25, v74, v75\n"
    "v_mfma_f32_16x16x32_bf16 v[84:87], a[128:131], a[16:19], v[84:87]\n"
    "s_add_u32 s90, s10, s81\n"
    "ds_read_b64_tr_b16 a[148:149], v5 offset:1032\n"
    "v_mul_f32 v76, v28, v76\n"
    "v_mfma_f32_16x16x32_bf16 v[84:87], a[132:135], a[20:23], v[84:87]\n"
    "ds_read_b64_tr_b16 a[150:151], v5 offset:1544\n"
    "v_mul_f32 v77, v29, v77\n"
    "v_mfma_f32_16x16x32_bf16 v[84:87], a[136:139], a[24:27], v[84:87]\n"
    "ds_read_b64_tr_b16 a[152:153], v5 offset:2056\n"
    "v_mul_f32 v78, v30, v78\n"
    "v_mfma_f32_16x16x32_bf16 v[84:87], a[140:143], a[28:31], v[84:87]\n"
    "ds_read_b64_tr_b16 a[154:155], v5 offset:2568\n"
    "v_mul_f32 v79, v31, v79\n"
    "v_mfma_f32_16x16x32_bf16 v[88:91], a[128:131], a[32:35], v[88:91]\n"
    "buffer_store_dwordx4 v[224:227], v0, s[40:43], s90 offen offset:0 sc1\n"
    "v_mfma_f32_16x16x32_bf16 v[88:91], a[132:135], a[36:39], v[88:91]\n"
    "ds_read_b64_tr_b16 a[156:157], v5 offset:3080\n"
    "ds_read_b64_tr_b16 a[158:159], v5 offset:3592\n"
    "v_mfma_f32_16x16x32_bf16 v[88:91], a[136:139], a[40:43], v[88:91]\n"
    "v_cvt_pk_bf16_f32 v28, v76, v77\n"
    "v_cvt_pk_bf16_f32 v29, v78, v79\n"
    "v_mfma_f32_16x16x32_bf16 v[88:91], a[140:143], a[44:47], v[88:91]\n"
    "v_mfma_f32_16x16x32_bf16 v[92:95], a[128:131], a[48:51], v[92:95]\n"
    "v_mfma_f32_16x16x32_bf16 v[92:95], a[132:135], a[52:55], v[92:95]\n"
    "v_mfma_f32_16x16x32_bf16 v[92:95], a[136:139], a[56:59], v[92:95]\n"
    "v_mfma_f32_16x16x32_bf16 v[92:95], a[140:143], a[60:63], v[92:95]\n"
    "s_waitcnt lgkmcnt(4)\n"
    "v_mfma_f32_16x16x32_bf16 v[96:99], a[144:147], a[0:3], v[96:99]\n"
    "v_lshrrev_b32 v240, 7, v0\n"
    "v_mfma_f32_16x16x32_bf16 v[96:99], a[148:151], a[4:7], v[96:99]\n"
    "s_waitcnt lgkmcnt(0)\n"
    "v_mfma_f32_16x16x32_bf16 v[96:99], a[152:155], a[8:11], v[96:99]\n"
    "ds_read_b64_tr_b16 a[128:129], v6 offset:0\n"
    "v_bfe_u32 v241, v0, 8, 2\n"
    "v_mfma_f32_16x16x32_bf16 v[96:99], a[156:159], a[12:15], v[96:99]\n"
    "ds_read_b64_tr_b16 a[130:131], v6 offset:512\n"
    "v_mfma_f32_16x16x32_bf16 v[100:103], a[144:147], a[16:19], v[100:103]\n"
    "ds_read_b64_tr_b16 a[132:133], v6 offset:1024\n"
    "v_mfma_f32_16x16x32_bf16 v[100:103], a[148:151], a[20:23], v[100:103]\n"
    "ds_read_b64_tr_b16 a[134:135], v6 offset:1536\n"
    "v_lshlrev_b32 v241, 1, v241\n"
    "v_mfma_f32_16x16x32_bf16 v[100:103], a[152:155], a[24:27], v[100:103]\n"
    "ds_read_b64_tr_b16 a[136:137], v6 offset:2048\n"
    "v_cvt_pk_bf16_f32 v80, v80, v81\n"
    "v_mfma_f32_16x16x32_bf16 v[100:103], a[156:159], a[28:31], v[100:103]\n"
    "ds_read_b64_tr_b16 a[138:139], v6 offset:2560\n"
    "v_cvt_pk_bf16_f32 v82, v82, v83\n"
    "v_mfma_f32_16x16x32_bf16 v[104:107], a[144:147], a[32:35], v[104:107]\n"
    "ds_read_b64_tr_b16 a[140:141], v6 offset:3072\n"
    "v_cvt_pk_bf16_f32 v96, v96, v97\n"
    "v_mfma_f32_16x16x32_bf16 v[104:107], a[148:151], a[36:39], v[104:107]\n"
    "ds_read_b64_tr_b16 a[142:143], v6 offset:3584\n"
    "v_cvt_pk_bf16_f32 v98, v98, v99\n"
    "v_mfma_f32_16x16x32_bf16 v[104:107], a[152:155], a[40:43], v[104:107]\n"
    "s_add_u32 s90, s10, s3\n"
    "v_accvgpr_write_b32 a64, v80\n"
    "v_accvgpr_write_b32 a65, v82\n"
    "v_mfma_f32_16x16x32_bf16 v[104:107], a[156:159], a[44:47], v[104:107]\n"
    "buffer_store_dwordx4 v[228:231], v0, s[40:43], s90 offen offset:0 sc1\n"
    "v_mfma_f32_16x16x32_bf16 v[108:111], a[144:147], a[48:51], v[108:111]\n"
    "v_accvgpr_write_b32 a66, v96\n"
    "v_accvgpr_write_b32 a67, v98\n"
    "v_mfma_f32_16x16x32_bf16 v[108:111], a[148:151], a[52:55], v[108:111]\n"
    "v_cvt_pk_bf16_f32 v84, v84, v85\n"
    "v_cvt_pk_bf16_f32 v86, v86, v87\n"
    "v_mfma_f32_16x16x32_bf16 v[108:111], a[152:155], a[56:59], v[108:111]\n"
    "v_cvt_pk_bf16_f32 v100, v100, v101\n"
    "v_cvt_pk_bf16_f32 v102, v102, v103\n"
    "v_mfma_f32_16x16x32_bf16 v[108:111], a[156:159], a[60:63], v[108:111]\n"
    "s_waitcnt lgkmcnt(4)\n"
    "v_accvgpr_write_b32 a68, v84\n"
    "v_accvgpr_write_b32 a69, v86\n"
    "v_mfma_f32_16x16x32_bf16 a[160:163], a[128:131], a[0:3], a[160:163]\n"
    "v_accvgpr_write_b32 a70, v100\n"
    "v_accvgpr_write_b32 a71, v102\n"
    "v_mfma_f32_16x16x32_bf16 a[160:163], a[132:135], a[4:7], a[160:163]\n"
    "s_waitcnt lgkmcnt(0)\n"
    "v_cvt_pk_bf16_f32 v88, v88, v89\n"
    "v_cvt_pk_bf16_f32 v90, v90, v91\n"
    "v_mfma_f32_16x16x32_bf16 a[160:163], a[136:139], a[8:11], a[160:163]\n"
    "ds_read_b64_tr_b16 a[144:145], v6 offset:8\n"
    "v_cvt_pk_bf16_f32 v104, v104, v105\n"
    "v_mfma_f32_16x16x32_bf16 a[160:163], a[140:143], a[12:15], a[160:163]\n"
    "ds_read_b64_tr_b16 a[146:147], v6 offset:520\n"
    "v_cvt_pk_bf16_f32 v106, v106, v107\n"
    "v_mfma_f32_16x16x32_bf16 a[164:167], a[128:131], a[16:19], a[164:167]\n"
    "ds_read_b64_tr_b16 a[148:149], v6 offset:1032\n"
    "v_accvgpr_write_b32 a72, v88\n"
    "v_mfma_f32_16x16x32_bf16 a[164:167], a[132:135], a[20:23], a[164:167]\n"
    "ds_read_b64_tr_b16 a[150:151], v6 offset:1544\n"
    "v_accvgpr_write_b32 a73, v90\n"
    "v_mfma_f32_16x16x32_bf16 a[164:167], a[136:139], a[24:27], a[164:167]\n"
    "ds_read_b64_tr_b16 a[152:153], v6 offset:2056\n"
    "v_accvgpr_write_b32 a74, v104\n"
    "v_mfma_f32_16x16x32_bf16 a[164:167], a[140:143], a[28:31], a[164:167]\n"
    "ds_read_b64_tr_b16 a[154:155], v6 offset:2568\n"
    "v_accvgpr_write_b32 a75, v106\n"
    "v_mfma_f32_16x16x32_bf16 a[168:171], a[128:131], a[32:35], a[168:171]\n"
    "ds_read_b64_tr_b16 a[156:157], v6 offset:3080\n"
    "v_cvt_pk_bf16_f32 v92, v92, v93\n"
    "v_mfma_f32_16x16x32_bf16 a[168:171], a[132:135], a[36:39], a[168:171]\n"
    "ds_read_b64_tr_b16 a[158:159], v6 offset:3592\n"
    "v_cvt_pk_bf16_f32 v94, v94, v95\n"
    "v_mfma_f32_16x16x32_bf16 a[168:171], a[136:139], a[40:43], a[168:171]\n"
    "v_cvt_pk_bf16_f32 v108, v108, v109\n"
    "v_cvt_pk_bf16_f32 v110, v110, v111\n"
    "v_mfma_f32_16x16x32_bf16 a[168:171], a[140:143], a[44:47], a[168:171]\n"
    "v_accvgpr_write_b32 a76, v92\n"
    "v_accvgpr_write_b32 a77, v94\n"
    "v_mfma_f32_16x16x32_bf16 a[172:175], a[128:131], a[48:51], a[172:175]\n"
    "v_accvgpr_write_b32 a78, v108\n"
    "v_accvgpr_write_b32 a79, v110\n"
    "v_mfma_f32_16x16x32_bf16 a[172:175], a[132:135], a[52:55], a[172:175]\n"
    "v_bfe_u32 v242, v0, 4, 3\n"
    "v_xor_b32 v241, v241, v242\n"
    "v_mfma_f32_16x16x32_bf16 a[172:175], a[136:139], a[56:59], a[172:175]\n"
    "v_mul_u32_u24 v240, 384, v240\n"
    "v_lshl_add_u32 v1, v241, 4, v240\n"
    "v_mfma_f32_16x16x32_bf16 a[172:175], a[140:143], a[60:63], a[172:175]\n"
    "s_waitcnt lgkmcnt(4)\n"
    "v_add_u32 v2, 2048, v1\n"
    "v_add_u32 v3, 4096, v1\n"
    "v_mfma_f32_16x16x32_bf16 a[176:179], a[144:147], a[0:3], a[176:179]\n"
    "s_add_u32 s90, s10, s39\n"
    "v_add_u32 v4, 6144, v1\n"
    "v_lshrrev_b32 v240, 6, v0\n"
    "v_mfma_f32_16x16x32_bf16 a[176:179], a[148:151], a[4:7], a[176:179]\n"
    "s_waitcnt lgkmcnt(0)\n"
    "v_bfe_u32 v241, v0, 7, 2\n"
    "v_lshlrev_b32 v240, 7, v240\n"
    "v_mfma_f32_16x16x32_bf16 a[176:179], a[152:155], a[8:11], a[176:179]\n"
    "ds_read_b64_tr_b16 a[128:129], v5 offset:4096\n"
    "v_lshl_add_u32 v240, v241, 5, v240\n"
    "v_mfma_f32_16x16x32_bf16 a[176:179], a[156:159], a[12:15], a[176:179]\n"
    "buffer_store_dwordx4 v[232:235], v0, s[40:43], s90 offen offset:0 sc1\n"
    "v_mfma_f32_16x16x32_bf16 a[180:183], a[144:147], a[16:19], a[180:183]\n"
    "ds_read_b64_tr_b16 a[130:131], v5 offset:4608\n"
    "ds_read_b64_tr_b16 a[132:133], v5 offset:5120\n"
    "v_mfma_f32_16x16x32_bf16 a[180:183], a[148:151], a[20:23], a[180:183]\n"
    "ds_read_b64_tr_b16 a[134:135], v5 offset:5632\n"
    "v_bfe_u32 v241, v0, 4, 2\n"
    "v_mfma_f32_16x16x32_bf16 a[180:183], a[152:155], a[24:27], a[180:183]\n"
    "ds_read_b64_tr_b16 a[136:137], v5 offset:6144\n"
    "v_accvgpr_read_b32 v32, a160\n"
    "v_mfma_f32_16x16x32_bf16 a[180:183], a[156:159], a[28:31], a[180:183]\n"
    "ds_read_b64_tr_b16 a[138:139], v5 offset:6656\n"
    "v_accvgpr_read_b32 v33, a161\n"
    "v_mfma_f32_16x16x32_bf16 a[184:187], a[144:147], a[32:35], a[184:187]\n"
    "ds_read_b64_tr_b16 a[140:141], v5 offset:7168\n"
    "v_accvgpr_read_b32 v34, a162\n"
    "v_mfma_f32_16x16x32_bf16 a[184:187], a[148:151], a[36:39], a[184:187]\n"
    "ds_read_b64_tr_b16 a[142:143], v5 offset:7680\n"
    "v_accvgpr_read_b32 v35, a163\n"
    "v_mfma_f32_16x16x32_bf16 a[184:187], a[152:155], a[40:43], a[184:187]\n"
    "v_accvgpr_read_b32 v36, a176\n"
    "v_accvgpr_read_b32 v37, a177\n"
    "v_mfma_f32_16x16x32_bf16 a[184:187], a[156:159], a[44:47], a[184:187]\n"
    "v_accvgpr_read_b32 v38, a178\n"
    "v_accvgpr_read_b32 v39, a179\n"
    "v_mfma_f32_16x16x32_bf16 a[188:191], a[144:147], a[48:51], a[188:191]\n"
    "v_cvt_pk_bf16_f32 v32, v32, v33\n"
    "v_cvt_pk_bf16_f32 v34, v34, v35\n"
    "v_mfma_f32_16x16x32_bf16 a[188:191], a[148:151], a[52:55], a[188:191]\n"
    "v_cvt_pk_bf16_f32 v36, v36, v37\n"
    "v_cvt_pk_bf16_f32 v38, v38, v39\n"
    "v_mfma_f32_16x16x32_bf16 a[188:191], a[152:155], a[56:59], a[188:191]\n"
    "v_accvgpr_write_b32 a80, v32\n"
    "v_accvgpr_write_b32 a81, v34\n"
    "v_mfma_f32_16x16x32_bf16 a[188:191], a[156:159], a[60:63], a[188:191]\n"
    "s_waitcnt lgkmcnt(4)\n"
    "v_accvgpr_write_b32 a82, v36\n"
    "v_accvgpr_write_b32 a83, v38\n"
    "v_mfma_f32_16x16x32_bf16 a[192:195], a[128:131], a[0:3], a[192:195]\n"
    "v_accvgpr_read_b32 v40, a164\n"
    "v_accvgpr_read_b32 v41, a165\n"
    "v_mfma_f32_16x16x32_bf16 a[192:195], a[132:135], a[4:7], a[192:195]\n"
    "s_waitcnt lgkmcnt(0)\n"
    "v_accvgpr_read_b32 v42, a166\n"
    "v_accvgpr_read_b32 v43, a167\n"
    "v_mfma_f32_16x16x32_bf16 a[192:195], a[136:139], a[8:11], a[192:195]\n"
    "ds_read_b64_tr_b16 a[144:145], v5 offset:4104\n"
    "v_accvgpr_read_b32 v44, a180\n"
    "v_mfma_f32_16x16x32_bf16 a[192:195], a[140:143], a[12:15], a[192:195]\n"
    "ds_read_b64_tr_b16 a[146:147], v5 offset:4616\n"
    "v_accvgpr_read_b32 v45, a181\n"
    "v_mfma_f32_16x16x32_bf16 a[196:199], a[128:131], a[16:19], a[196:199]\n"
    "ds_read_b64_tr_b16 a[148:149], v5 offset:5128\n"
    "v_accvgpr_read_b32 v46, a182\n"
    "v_mfma_f32_16x16x32_bf16 a[196:199], a[132:135], a[20:23], a[196:199]\n"
    "ds_read_b64_tr_b16 a[150:151], v5 offset:5640\n"
    "v_accvgpr_read_b32 v47, a183\n"
    "v_mfma_f32_16x16x32_bf16 a[196:199], a[136:139], a[24:27], a[196:199]\n"
    "s_add_u32 s90, s10, s99\n"
    "ds_read_b64_tr_b16 a[152:153], v5 offset:6152\n"
    "v_cvt_pk_bf16_f32 v40, v40, v41\n"
    "v_mfma_f32_16x16x32_bf16 a[196:199], a[140:143], a[28:31], a[196:199]\n"
    "ds_read_b64_tr_b16 a[154:155], v5 offset:6664\n"
    "v_cvt_pk_bf16_f32 v42, v42, v43\n"
    "v_mfma_f32_16x16x32_bf16 a[200:203], a[128:131], a[32:35], a[200:203]\n"
    "ds_read_b64_tr_b16 a[156:157], v5 offset:7176\n"
    "v_cvt_pk_bf16_f32 v44, v44, v45\n"
    "v_mfma_f32_16x16x32_bf16 a[200:203], a[132:135], a[36:39], a[200:203]\n"
    "ds_read_b64_tr_b16 a[158:159], v5 offset:7688\n"
    "v_cvt_pk_bf16_f32 v46, v46, v47\n"
    "v_mfma_f32_16x16x32_bf16 a[200:203], a[136:139], a[40:43], a[200:203]\n"
    "buffer_store_dwordx4 v[236:239], v0, s[40:43], s90 offen offset:0 sc1\n"
    "v_mfma_f32_16x16x32_bf16 a[200:203], a[140:143], a[44:47], a[200:203]\n"
    "v_accvgpr_write_b32 a84, v40\n"
    "v_accvgpr_write_b32 a85, v42\n"
    "v_mfma_f32_16x16x32_bf16 a[204:207], a[128:131], a[48:51], a[204:207]\n"
    "v_accvgpr_write_b32 a86, v44\n"
    "v_accvgpr_write_b32 a87, v46\n"
    "v_mfma_f32_16x16x32_bf16 a[204:207], a[132:135], a[52:55], a[204:207]\n"
    "v_accvgpr_read_b32 v48, a168\n"
    "v_accvgpr_read_b32 v49, a169\n"
    "v_mfma_f32_16x16x32_bf16 a[204:207], a[136:139], a[56:59], a[204:207]\n"
    "v_accvgpr_read_b32 v50, a170\n"
    "v_accvgpr_read_b32 v51, a171\n"
    "v_mfma_f32_16x16x32_bf16 a[204:207], a[140:143], a[60:63], a[204:207]\n"
    "s_waitcnt lgkmcnt(4)\n"
    "v_accvgpr_read_b32 v52, a184\n"
    "v_accvgpr_read_b32 v53, a185\n"
    "v_mfma_f32_16x16x32_bf16 v[112:115], a[144:147], a[0:3], v[112:115]\n"
    "v_accvgpr_read_b32 v54, a186\n"
    "v_accvgpr_read_b32 v55, a187\n"
    "v_mfma_f32_16x16x32_bf16 v[112:115], a[148:151], a[4:7], v[112:115]\n"
    "s_waitcnt lgkmcnt(0)\n"
    "v_cvt_pk_bf16_f32 v48, v48, v49\n"
    "v_cvt_pk_bf16_f32 v50, v50, v51\n"
    "v_mfma_f32_16x16x32_bf16 v[112:115], a[152:155], a[8:11], v[112:115]\n"
    "ds_read_b64_tr_b16 a[128:129], v6 offset:4096\n"
    "v_cvt_pk_bf16_f32 v52, v52, v53\n"
    "v_mfma_f32_16x16x32_bf16 v[112:115], a[156:159], a[12:15], v[112:115]\n"
    "ds_read_b64_tr_b16 a[130:131], v6 offset:4608\n"
    "v_cvt_pk_bf16_f32 v54, v54, v55\n"
    "v_mfma_f32_16x16x32_bf16 v[116:119], a[144:147], a[16:19], v[116:119]\n"
    "ds_read_b64_tr_b16 a[132:133], v6 offset:5120\n"
    "v_accvgpr_write_b32 a88, v48\n"
    "v_mfma_f32_16x16x32_bf16 v[116:119], a[148:151], a[20:23], v[116:119]\n"
    "v_accvgpr_write_b32 a89, v50\n"
    "v_accvgpr_write_b32 a90, v52\n"
    "v_mfma_f32_16x16x32_bf16 v[116:119], a[152:155], a[24:27], v[116:119]\n"
    "ds_read_b64_tr_b16 a[134:135], v6 offset:5632\n"
    "v_accvgpr_write_b32 a91, v54\n"
    "v_mfma_f32_16x16x32_bf16 v[116:119], a[156:159], a[28:31], v[116:119]\n"
    "ds_read_b64_tr_b16 a[136:137], v6 offset:6144\n"
    "ds_read_b64_tr_b16 a[138:139], v6 offset:6656\n"
    "v_mfma_f32_16x16x32_bf16 v[120:123], a[144:147], a[32:35], v[120:123]\n"
    "ds_read_b64_tr_b16 a[140:141], v6 offset:7168\n"
    "v_accvgpr_read_b32 v56, a172\n"
    "v_mfma_f32_16x16x32_bf16 v[120:123], a[148:151], a[36:39], v[120:123]\n"
    "v_accvgpr_read_b32 v57, a173\n"
    "v_accvgpr_read_b32 v58, a174\n"
    "v_mfma_f32_16x16x32_bf16 v[120:123], a[152:155], a[40:43], v[120:123]\n"
    "v_accvgpr_read_b32 v59, a175\n"
    "v_accvgpr_read_b32 v60, a188\n"
    "v_accvgpr_read_b32 v61, a189\n"
    "v_accvgpr_read_b32 v62, a190\n"
    "v_accvgpr_read_b32 v63, a191\n"
    "v_cvt_pk_bf16_f32 v56, v56, v57\n"
    "v_cvt_pk_bf16_f32 v58, v58, v59\n"
    "v_cvt_pk_bf16_f32 v60, v60, v61\n"
    "v_cvt_pk_bf16_f32 v62, v62, v63\n"
    "v_accvgpr_write_b32 a92, v56\n"
    "v_accvgpr_write_b32 a93, v58\n"
    "v_accvgpr_write_b32 a94, v60\n"
    "v_accvgpr_write_b32 a95, v62\n"
    "v_mfma_f32_16x16x32_bf16 v[120:123], a[156:159], a[44:47], v[120:123]\n"
    "ds_read_b64_tr_b16 a[142:143], v6 offset:7680\n"
    "v_lshl_add_u32 v8, v241, 3, v240\n"
    "v_mfma_f32_16x16x32_bf16 v[124:127], a[144:147], a[48:51], v[124:127]\n"
    "s_add_u32 s90, s11, s81\n"
    "v_xor_b32 v9, 32, v8\n"
    "v_xor_b32 v10, 64, v8\n"
    "v_mfma_f32_16x16x32_bf16 v[124:127], a[148:151], a[52:55], v[124:127]\n"
    "buffer_store_dwordx2 v[16:17], v0, s[40:43], s90 offen offset:0 sc1\n"
    "v_mfma_f32_16x16x32_bf16 v[124:127], a[152:155], a[56:59], v[124:127]\n"
    "v_mov_b32 v240, 0\n"
    "v_mov_b32 v241, 0\n"
    "v_mfma_f32_16x16x32_bf16 v[124:127], a[156:159], a[60:63], v[124:127]\n"
    "s_waitcnt lgkmcnt(4)\n"
    "v_mov_b32 v242, 0\n"
    "v_mov_b32 v243, 0\n"
    "v_mfma_f32_16x16x32_bf16 v[128:131], a[128:131], a[0:3], v[128:131]\n"
    "v_accvgpr_read_b32 v32, a192\n"
    "v_accvgpr_read_b32 v33, a193\n"
    "v_mfma_f32_16x16x32_bf16 v[128:131], a[132:135], a[4:7], v[128:131]\n"
    "s_waitcnt lgkmcnt(0)\n"
    "v_accvgpr_read_b32 v34, a194\n"
    "v_accvgpr_read_b32 v35, a195\n"
    "v_mfma_f32_16x16x32_bf16 v[128:131], a[136:139], a[8:11], v[128:131]\n"
    "ds_read_b64_tr_b16 a[144:145], v6 offset:4104\n"
    "v_cvt_pk_bf16_f32 v32, v32, v33\n"
    "v_mfma_f32_16x16x32_bf16 v[128:131], a[140:143], a[12:15], v[128:131]\n"
    "ds_read_b64_tr_b16 a[146:147], v6 offset:4616\n"
    "v_cvt_pk_bf16_f32 v34, v34, v35\n"
    "v_mfma_f32_16x16x32_bf16 v[132:135], a[128:131], a[16:19], v[132:135]\n"
    "ds_read_b64_tr_b16 a[148:149], v6 offset:5128\n"
    "v_cvt_pk_bf16_f32 v112, v112, v113\n"
    "v_mfma_f32_16x16x32_bf16 v[132:135], a[132:135], a[20:23], v[132:135]\n"
    "ds_read_b64_tr_b16 a[150:151], v6 offset:5640\n"
    "v_cvt_pk_bf16_f32 v114, v114, v115\n"
    "v_mfma_f32_16x16x32_bf16 v[132:135], a[136:139], a[24:27], v[132:135]\n"
    "ds_read_b64_tr_b16 a[152:153], v6 offset:6152\n"
    "v_accvgpr_write_b32 a96, v32\n"
    "v_mfma_f32_16x16x32_bf16 v[132:135], a[140:143], a[28:31], v[132:135]\n"
    "ds_read_b64_tr_b16 a[154:155], v6 offset:6664\n"
    "v_accvgpr_write_b32 a97, v34\n"
    "v_mfma_f32_16x16x32_bf16 v[136:139], a[128:131], a[32:35], v[136:139]\n"
    "ds_read_b64_tr_b16 a[156:157], v6 offset:7176\n"
    "v_accvgpr_write_b32 a98, v112\n"
    "v_mfma_f32_16x16x32_bf16 v[136:139], a[132:135], a[36:39], v[136:139]\n"
    "ds_read_b64_tr_b16 a[158:159], v6 offset:7688\n"
    "v_accvgpr_write_b32 a99, v114\n"
    "v_mfma_f32_16x16x32_bf16 v[136:139], a[136:139], a[40:43], v[136:139]\n"
    "v_mov_b32 v244, 0\n"
    "v_accvgpr_read_b32 v40, a196\n"
    "v_mfma_f32_16x16x32_bf16 v[136:139], a[140:143], a[44:47], v[136:139]\n"
    "v_accvgpr_read_b32 v41, a197\n"
    "v_accvgpr_read_b32 v42, a198\n"
    "v_mfma_f32_16x16x32_bf16 v[140:143], a[128:131], a[48:51], v[140:143]\n"
    "v_accvgpr_read_b32 v43, a199\n"
    "v_cvt_pk_bf16_f32 v40, v40, v41\n"
    "v_mfma_f32_16x16x32_bf16 v[140:143], a[132:135], a[52:55], v[140:143]\n"
    "v_cvt_pk_bf16_f32 v42, v42, v43\n"
    "v_cvt_pk_bf16_f32 v116, v116, v117\n"
    "v_mfma_f32_16x16x32_bf16 v[140:143], a[136:139], a[56:59], v[140:143]\n"
    "v_cvt_pk_bf16_f32 v118, v118, v119\n"
    "v_accvgpr_write_b32 a100, v40\n"
    "v_mfma_f32_16x16x32_bf16 v[140:143], a[140:143], a[60:63], v[140:143]\n"
    "s_waitcnt lgkmcnt(4)\n"
    "v_accvgpr_write_b32 a101, v42\n"
    "v_accvgpr_write_b32 a102, v116\n"
    "v_mfma_f32_16x16x32_bf16 v[144:147], a[144:147], a[0:3], v[144:147]\n"
    "v_accvgpr_write_b32 a103, v118\n"
    "v_mov_b32 v245, 0\n"
    "v_mfma_f32_16x16x32_bf16 v[144:147], a[148:151], a[4:7], v[144:147]\n"
    "s_waitcnt lgkmcnt(0)\n"
    "v_mov_b32 v246, 0\n"
    "v_accvgpr_read_b32 v48, a200\n"
    "v_mfma_f32_16x16x32_bf16 v[144:147], a[152:155], a[8:11], v[144:147]\n"
    "s_add_u32 s90, s11, s3\n"
    "ds_read_b64_tr_b16 a[128:129], v5 offset:8192\n"
    "v_accvgpr_read_b32 v49, a201\n"
    "v_mfma_f32_16x16x32_bf16 v[144:147], a[156:159], a[12:15], v[144:147]\n"
    "ds_read_b64_tr_b16 a[130:131], v5 offset:8704\n"
    "v_accvgpr_read_b32 v50, a202\n"
    "v_mfma_f32_16x16x32_bf16 v[148:151], a[144:147], a[16:19], v[148:151]\n"
    "ds_read_b64_tr_b16 a[132:133], v5 offset:9216\n"
    "v_accvgpr_read_b32 v51, a203\n"
    "v_mfma_f32_16x16x32_bf16 v[148:151], a[148:151], a[20:23], v[148:151]\n"
    "v_cvt_pk_bf16_f32 v48, v48, v49\n"
    "v_cvt_pk_bf16_f32 v50, v50, v51\n"
    "v_mfma_f32_16x16x32_bf16 v[148:151], a[152:155], a[24:27], v[148:151]\n"
    "v_cvt_pk_bf16_f32 v120, v120, v121\n"
    "v_cvt_pk_bf16_f32 v122, v122, v123\n"
    "v_accvgpr_write_b32 a104, v48\n"
    "v_accvgpr_write_b32 a105, v50\n"
    "v_accvgpr_write_b32 a106, v120\n"
    "v_accvgpr_write_b32 a107, v122\n"
    "v_mfma_f32_16x16x32_bf16 v[148:151], a[156:159], a[28:31], v[148:151]\n"
    "ds_read_b64_tr_b16 a[134:135], v5 offset:9728\n"
    "ds_read_b64_tr_b16 a[136:137], v5 offset:10240\n"
    "v_mfma_f32_16x16x32_bf16 v[152:155], a[144:147], a[32:35], v[152:155]\n"
    "ds_read_b64_tr_b16 a[138:139], v5 offset:10752\n"
    "v_mov_b32 v247, 0\n"
    "buffer_store_dwordx2 v[20:21], v0, s[40:43], s90 offen offset:0 sc1\n"
    "v_mfma_f32_16x16x32_bf16 v[152:155], a[148:151], a[36:39], v[152:155]\n"
    "ds_read_b64_tr_b16 a[140:141], v5 offset:11264\n"
    "v_accvgpr_read_b32 v56, a204\n"
    "v_mfma_f32_16x16x32_bf16 v[152:155], a[152:155], a[40:43], v[152:155]\n"
    "v_accvgpr_read_b32 v57, a205\n"
    "v_accvgpr_read_b32 v58, a206\n"
    "v_accvgpr_read_b32 v59, a207\n"
    "v_cvt_pk_bf16_f32 v56, v56, v57\n"
    "v_cvt_pk_bf16_f32 v58, v58, v59\n"
    "v_cvt_pk_bf16_f32 v124, v124, v125\n"
    "v_cvt_pk_bf16_f32 v126, v126, v127\n"
    "v_accvgpr_write_b32 a108, v56\n"
    "v_accvgpr_write_b32 a109, v58\n"
    "v_accvgpr_write_b32 a110, v124\n"
    "v_accvgpr_write_b32 a111, v126\n"
    "v_mfma_f32_16x16x32_bf16 v[152:155], a[156:159], a[44:47], v[152:155]\n"
    "ds_read_b64_tr_b16 a[142:143], v5 offset:11776\n"
    "v_mov_b32 v248, 0\n"
    "v_mfma_f32_16x16x32_bf16 v[156:159], a[144:147], a[48:51], v[156:159]\n"
    "v_mov_b32 v249, 0\n"
    "v_mov_b32 v250, 0\n"
    "v_mfma_f32_16x16x32_bf16 v[156:159], a[148:151], a[52:55], v[156:159]\n"
    "v_mov_b32 v251, 0\n"
    "v_mov_b32 v252, 0\n"
    "v_mfma_f32_16x16x32_bf16 v[156:159], a[152:155], a[56:59], v[156:159]\n"
    "v_mov_b32 v253, 0\n"
    "v_mov_b32 v254, 0\n"
    "v_mfma_f32_16x16x32_bf16 v[156:159], a[156:159], a[60:63], v[156:159]\n"
    "s_waitcnt lgkmcnt(4)\n"
    "v_mov_b32 v255, 0\n"
    "v_cvt_pk_bf16_f32 v128, v128, v129\n"
    "v_mfma_f32_16x16x32_bf16 v[160:163], a[128:131], a[0:3], v[160:163]\n"
    "v_cvt_pk_bf16_f32 v130, v130, v131\n"
    "v_cvt_pk_bf16_f32 v144, v144, v145\n"
    "v_mfma_f32_16x16x32_bf16 v[160:163], a[132:135], a[4:7], v[160:163]\n"
    "s_waitcnt lgkmcnt(0)\n"
    "v_cvt_pk_bf16_f32 v146, v146, v147\n"
    "v_accvgpr_write_b32 a112, v128\n"
    "v_mfma_f32_16x16x32_bf16 v[160:163], a[136:139], a[8:11], v[160:163]\n"
    "ds_read_b64_tr_b16 a[144:145], v5 offset:8200\n"
    "v_accvgpr_write_b32 a113, v130\n"
    "v_mfma_f32_16x16x32_bf16 v[160:163], a[140:143], a[12:15], v[160:163]\n"
    "ds_read_b64_tr_b16 a[146:147], v5 offset:8712\n"
    "v_accvgpr_write_b32 a114, v144\n"
    "v_mfma_f32_16x16x32_bf16 v[164:167], a[128:131], a[16:19], v[164:167]\n"
    "ds_read_b64_tr_b16 a[148:149], v5 offset:9224\n"
    "v_accvgpr_write_b32 a115, v146\n"
    "v_mfma_f32_16x16x32_bf16 v[164:167], a[132:135], a[20:23], v[164:167]\n"
    "ds_read_b64_tr_b16 a[150:151], v5 offset:9736\n"
    "v_cvt_pk_bf16_f32 v132, v132, v133\n"
    "v_mfma_f32_16x16x32_bf16 v[164:167], a[136:139], a[24:27], v[164:167]\n"
    "ds_read_b64_tr_b16 a[152:153], v5 offset:10248\n"
    "v_cvt_pk_bf16_f32 v134, v134, v135\n"
    "v_mfma_f32_16x16x32_bf16 v[164:167], a[140:143], a[28:31], v[164:167]\n"
    "ds_read_b64_tr_b16 a[154:155], v5 offset:10760\n"
    "v_cvt_pk_bf16_f32 v148, v148, v149\n"
    "v_mfma_f32_16x16x32_bf16 v[168:171], a[128:131], a[32:35], v[168:171]\n"
    "s_add_u32 s90, s11, s39\n"
    "ds_read_b64_tr_b16 a[156:157], v5 offset:11272\n"
    "v_cvt_pk_bf16_f32 v150, v150, v151\n"
    "v_mfma_f32_16x16x32_bf16 v[168:171], a[132:135], a[36:39], v[168:171]\n"
    "ds_read_b64_tr_b16 a[158:159], v5 offset:11784\n"
    "v_accvgpr_write_b32 a116, v132\n"
    "v_mfma_f32_16x16x32_bf16 v[168:171], a[136:139], a[40:43], v[168:171]\n"
    "buffer_store_dwordx2 v[24:25], v0, s[40:43], s90 offen offset:0 sc1\n"
    "v_mfma_f32_16x16x32_bf16 v[168:171], a[140:143], a[44:47], v[168:171]\n"
    "v_accvgpr_write_b32 a117, v134\n"
    "v_accvgpr_write_b32 a118, v148\n"
    "v_mfma_f32_16x16x32_bf16 v[172:175], a[128:131], a[48:51], v[172:175]\n"
    "v_accvgpr_write_b32 a119, v150\n"
    "v_cvt_pk_bf16_f32 v136, v136, v137\n"
    "v_mfma_f32_16x16x32_bf16 v[172:175], a[132:135], a[52:55], v[172:175]\n"
    "v_cvt_pk_bf16_f32 v138, v138, v139\n"
    "v_cvt_pk_bf16_f32 v152, v152, v153\n"
    "v_mfma_f32_16x16x32_bf16 v[172:175], a[136:139], a[56:59], v[172:175]\n"
    "v_cvt_pk_bf16_f32 v154, v154, v155\n"
    "v_accvgpr_write_b32 a120, v136\n"
    "v_mfma_f32_16x16x32_bf16 v[172:175], a[140:143], a[60:63], v[172:175]\n"
    "s_waitcnt lgkmcnt(4)\n"
    "v_accvgpr_write_b32 a121, v138\n"
    "v_accvgpr_write_b32 a122, v152\n"
    "v_mfma_f32_16x16x32_bf16 v[176:179], a[144:147], a[0:3], v[176:179]\n"
    "v_accvgpr_write_b32 a123, v154\n"
    "v_cvt_pk_bf16_f32 v140, v140, v141\n"
    "v_mfma_f32_16x16x32_bf16 v[176:179], a[148:151], a[4:7], v[176:179]\n"
    "s_waitcnt lgkmcnt(0)\n"
    "v_cvt_pk_bf16_f32 v142, v142, v143\n"
    "v_cvt_pk_bf16_f32 v156, v156, v157\n"
    "v_mfma_f32_16x16x32_bf16 v[176:179], a[152:155], a[8:11], v[176:179]\n"
    "ds_read_b64_tr_b16 a[128:129], v6 offset:8192\n"
    "v_cvt_pk_bf16_f32 v158, v158, v159\n"
    "v_mfma_f32_16x16x32_bf16 v[176:179], a[156:159], a[12:15], v[176:179]\n"
    "ds_read_b64_tr_b16 a[130:131], v6 offset:8704\n"
    "v_accvgpr_write_b32 a124, v140\n"
    "v_mfma_f32_16x16x32_bf16 v[180:183], a[144:147], a[16:19], v[180:183]\n"
    "ds_read_b64_tr_b16 a[132:133], v6 offset:9216\n"
    "v_accvgpr_write_b32 a125, v142\n"
    "v_mfma_f32_16x16x32_bf16 v[180:183], a[148:151], a[20:23], v[180:183]\n"
    "ds_read_b64_tr_b16 a[134:135], v6 offset:9728\n"
    "v_accvgpr_write_b32 a126, v156\n"
    "v_mfma_f32_16x16x32_bf16 v[180:183], a[152:155], a[24:27], v[180:183]\n"
    "ds_read_b64_tr_b16 a[136:137], v6 offset:10240\n"
    "v_accvgpr_write_b32 a127, v158\n"
    "v_mfma_f32_16x16x32_bf16 v[180:183], a[156:159], a[28:31], v[180:183]\n"
    "ds_read_b64_tr_b16 a[138:139], v6 offset:10752\n"
    "v_cvt_pk_bf16_f32 v160, v160, v161\n"
    "v_mfma_f32_16x16x32_bf16 v[184:187], a[144:147], a[32:35], v[184:187]\n"
    "ds_read_b64_tr_b16 a[140:141], v6 offset:11264\n"
    "v_cvt_pk_bf16_f32 v162, v162, v163\n"
    "v_mfma_f32_16x16x32_bf16 v[184:187], a[148:151], a[36:39], v[184:187]\n"
    "ds_read_b64_tr_b16 a[142:143], v6 offset:11776\n"
    "v_cvt_pk_bf16_f32 v176, v176, v177\n"
    "v_mfma_f32_16x16x32_bf16 v[184:187], a[152:155], a[40:43], v[184:187]\n"
    "v_cvt_pk_bf16_f32 v178, v178, v179\n"
    "v_accvgpr_write_b32 a208, v160\n"
    "v_mfma_f32_16x16x32_bf16 v[184:187], a[156:159], a[44:47], v[184:187]\n"
    "v_accvgpr_write_b32 a209, v162\n"
    "v_accvgpr_write_b32 a210, v176\n"
    "v_mfma_f32_16x16x32_bf16 v[188:191], a[144:147], a[48:51], v[188:191]\n"
    "v_accvgpr_write_b32 a211, v178\n"
    "v_cvt_pk_bf16_f32 v164, v164, v165\n"
    "v_mfma_f32_16x16x32_bf16 v[188:191], a[148:151], a[52:55], v[188:191]\n"
    "v_cvt_pk_bf16_f32 v166, v166, v167\n"
    "v_cvt_pk_bf16_f32 v180, v180, v181\n"
    "v_mfma_f32_16x16x32_bf16 v[188:191], a[152:155], a[56:59], v[188:191]\n"
    "s_add_u32 s90, s11, s99\n"
    "v_cvt_pk_bf16_f32 v182, v182, v183\n"
    "v_accvgpr_write_b32 a212, v164\n"
    "v_mfma_f32_16x16x32_bf16 v[188:191], a[156:159], a[60:63], v[188:191]\n"
    "s_waitcnt lgkmcnt(4)\n"
    "v_accvgpr_write_b32 a213, v166\n"
    "v_accvgpr_write_b32 a214, v180\n"
    "v_mfma_f32_16x16x32_bf16 v[192:195], a[128:131], a[0:3], v[192:195]\n"
    "buffer_store_dwordx2 v[28:29], v0, s[40:43], s90 offen offset:0 sc1\n"
    "v_mfma_f32_16x16x32_bf16 v[192:195], a[132:135], a[4:7], v[192:195]\n"
    "s_waitcnt lgkmcnt(0)\n"
    "v_accvgpr_write_b32 a215, v182\n"
    "v_cvt_pk_bf16_f32 v168, v168, v169\n"
    "v_mfma_f32_16x16x32_bf16 v[192:195], a[136:139], a[8:11], v[192:195]\n"
    "ds_read_b64_tr_b16 a[144:145], v6 offset:8200\n"
    "v_cvt_pk_bf16_f32 v170, v170, v171\n"
    "v_mfma_f32_16x16x32_bf16 v[192:195], a[140:143], a[12:15], v[192:195]\n"
    "ds_read_b64_tr_b16 a[146:147], v6 offset:8712\n"
    "v_cvt_pk_bf16_f32 v184, v184, v185\n"
    "v_mfma_f32_16x16x32_bf16 v[196:199], a[128:131], a[16:19], v[196:199]\n"
    "ds_read_b64_tr_b16 a[148:149], v6 offset:9224\n"
    "v_cvt_pk_bf16_f32 v186, v186, v187\n"
    "v_mfma_f32_16x16x32_bf16 v[196:199], a[132:135], a[20:23], v[196:199]\n"
    "ds_read_b64_tr_b16 a[150:151], v6 offset:9736\n"
    "v_accvgpr_write_b32 a216, v168\n"
    "v_mfma_f32_16x16x32_bf16 v[196:199], a[136:139], a[24:27], v[196:199]\n"
    "ds_read_b64_tr_b16 a[152:153], v6 offset:10248\n"
    "v_accvgpr_write_b32 a217, v170\n"
    "v_mfma_f32_16x16x32_bf16 v[196:199], a[140:143], a[28:31], v[196:199]\n"
    "ds_read_b64_tr_b16 a[154:155], v6 offset:10760\n"
    "v_accvgpr_write_b32 a218, v184\n"
    "v_mfma_f32_16x16x32_bf16 v[200:203], a[128:131], a[32:35], v[200:203]\n"
    "ds_read_b64_tr_b16 a[156:157], v6 offset:11272\n"
    "v_accvgpr_write_b32 a219, v186\n"
    "v_mfma_f32_16x16x32_bf16 v[200:203], a[132:135], a[36:39], v[200:203]\n"
    "ds_read_b64_tr_b16 a[158:159], v6 offset:11784\n"
    "v_cvt_pk_bf16_f32 v172, v172, v173\n"
    "v_mfma_f32_16x16x32_bf16 v[200:203], a[136:139], a[40:43], v[200:203]\n"
    "v_cvt_pk_bf16_f32 v174, v174, v175\n"
    "v_cvt_pk_bf16_f32 v188, v188, v189\n"
    "v_mfma_f32_16x16x32_bf16 v[200:203], a[140:143], a[44:47], v[200:203]\n"
    "v_cvt_pk_bf16_f32 v190, v190, v191\n"
    "v_accvgpr_write_b32 a220, v172\n"
    "v_mfma_f32_16x16x32_bf16 v[204:207], a[128:131], a[48:51], v[204:207]\n"
    "v_accvgpr_write_b32 a221, v174\n"
    "v_accvgpr_write_b32 a222, v188\n"
    "v_mfma_f32_16x16x32_bf16 v[204:207], a[132:135], a[52:55], v[204:207]\n"
    "v_accvgpr_write_b32 a223, v190\n"
    "v_mfma_f32_16x16x32_bf16 v[204:207], a[136:139], a[56:59], v[204:207]\n"
    "v_mfma_f32_16x16x32_bf16 v[204:207], a[140:143], a[60:63], v[204:207]\n"
    "s_waitcnt vmcnt(17) lgkmcnt(0)\n"
    "s_barrier\n"
    "v_mfma_f32_16x16x32_bf16 v[208:211], a[144:147], a[0:3], v[208:211]\n"
    "ds_read_b128 a[160:163], v14 offset:0\n"
    "v_mfma_f32_16x16x32_bf16 v[208:211], a[148:151], a[4:7], v[208:211]\n"
    "ds_read_b128 a[164:167], v15 offset:0\n"
    "v_mfma_f32_16x16x32_bf16 v[208:211], a[152:155], a[8:11], v[208:211]\n"
    "ds_read_b128 a[168:171], v14 offset:1024\n"
    "v_mfma_f32_16x16x32_bf16 v[208:211], a[156:159], a[12:15], v[208:211]\n"
    "ds_read_b128 a[172:175], v15 offset:1024\n"
    "v_mfma_f32_16x16x32_bf16 v[212:215], a[144:147], a[16:19], v[212:215]\n"
    "ds_read_b128 a[176:179], v14 offset:2048\n"
    "v_mfma_f32_16x16x32_bf16 v[212:215], a[148:151], a[20:23], v[212:215]\n"
    "ds_read_b128 a[180:183], v15 offset:2048\n"
    "v_mfma_f32_16x16x32_bf16 v[212:215], a[152:155], a[24:27], v[212:215]\n"
    "v_cvt_pk_bf16_f32 v192, v192, v193\n"
    "v_cvt_pk_bf16_f32 v194, v194, v195\n"
    "v_mfma_f32_16x16x32_bf16 v[212:215], a[156:159], a[28:31], v[212:215]\n"
    "v_cvt_pk_bf16_f32 v208, v208, v209\n"
    "v_cvt_pk_bf16_f32 v210, v210, v211\n"
    "v_mfma_f32_16x16x32_bf16 v[216:219], a[144:147], a[32:35], v[216:219]\n"
    "v_accvgpr_write_b32 a224, v192\n"
    "v_accvgpr_write_b32 a225, v194\n"
    "v_mfma_f32_16x16x32_bf16 v[216:219], a[148:151], a[36:39], v[216:219]\n"
    "v_accvgpr_write_b32 a226, v208\n"
    "v_accvgpr_write_b32 a227, v210\n"
    "v_mfma_f32_16x16x32_bf16 v[216:219], a[152:155], a[40:43], v[216:219]\n"
    "v_cvt_pk_bf16_f32 v196, v196, v197\n"
    "v_cvt_pk_bf16_f32 v198, v198, v199\n"
    "v_mfma_f32_16x16x32_bf16 v[216:219], a[156:159], a[44:47], v[216:219]\n"
    "v_cvt_pk_bf16_f32 v212, v212, v213\n"
    "v_cvt_pk_bf16_f32 v214, v214, v215\n"
    "v_mfma_f32_16x16x32_bf16 v[220:223], a[144:147], a[48:51], v[220:223]\n"
    "v_accvgpr_write_b32 a228, v196\n"
    "v_accvgpr_write_b32 a229, v198\n"
    "v_mfma_f32_16x16x32_bf16 v[220:223], a[148:151], a[52:55], v[220:223]\n"
    "v_accvgpr_write_b32 a230, v212\n"
    "v_accvgpr_write_b32 a231, v214\n"
    "v_mfma_f32_16x16x32_bf16 v[220:223], a[152:155], a[56:59], v[220:223]\n"
    "v_cvt_pk_bf16_f32 v200, v200, v201\n"
    "v_cvt_pk_bf16_f32 v202, v202, v203\n"
    "v_mfma_f32_16x16x32_bf16 v[220:223], a[156:159], a[60:63], v[220:223]\n"
    "v_cvt_pk_bf16_f32 v216, v216, v217\n"
    "v_cvt_pk_bf16_f32 v218, v218, v219\n"
    "v_accvgpr_write_b32 a232, v200\n"
    "v_accvgpr_write_b32 a233, v202\n"
    "v_accvgpr_write_b32 a234, v216\n"
    "v_accvgpr_write_b32 a235, v218\n"
    "v_cvt_pk_bf16_f32 v204, v204, v205\n"
    "v_cvt_pk_bf16_f32 v206, v206, v207\n"
    "v_cvt_pk_bf16_f32 v220, v220, v221\n"
    "v_cvt_pk_bf16_f32 v222, v222, v223\n"
    "v_accvgpr_write_b32 a236, v204\n"
    "v_accvgpr_write_b32 a237, v206\n"
    "v_accvgpr_write_b32 a238, v220\n"
    "v_accvgpr_write_b32 a239, v222\n"
    "s_cmp_lg_u32 s92, 0\n"
    "s_cbranch_scc1 .L_max_%=\n"
    ".L_sgo_%=:\n"
    "s_waitcnt lgkmcnt(0)\n"
    "v_mfma_f32_16x16x32_bf16 v[224:227], a[160:163], a[64:67], v[240:243]\n"
    "ds_read_b128 a[184:187], v14 offset:6144\n"
    "v_accvgpr_write_b32 a48, s91\n"
    "v_mfma_f32_16x16x32_bf16 v[224:227], a[164:167], a[80:83], v[224:227]\n"
    "v_accvgpr_write_b32 a49, s91\n"
    "v_accvgpr_write_b32 a50, s91\n"
    "v_mfma_f32_16x16x32_bf16 v[224:227], a[168:171], a[96:99], v[224:227]\n"
    "v_accvgpr_write_b32 a51, s91\n"
    "v_xor_b32 v6, 96, v8\n"
    "v_mfma_f32_16x16x32_bf16 v[224:227], a[172:175], a[112:115], v[224:227]\n"
    "ds_read_b128 a[188:191], v15 offset:6144\n"
    "v_min_u32 v7, 1, v0\n"
    "v_mfma_f32_16x16x32_bf16 v[224:227], a[176:179], a[208:211], v[224:227]\n"
    "v_lshlrev_b32 v7, 31, v7\n"
    "v_accvgpr_write_b32 a52, s96\n"
    "v_mfma_f32_16x16x32_bf16 v[224:227], a[180:183], a[224:227], v[224:227]\n"
    "v_mfma_f32_16x16x32_bf16 v[228:231], a[160:163], a[68:71], v[244:247]\n"
    "ds_read_b128 a[192:195], v14 offset:7168\n"
    "v_mfma_f32_16x16x32_bf16 v[228:231], a[164:167], a[84:87], v[228:231]\n"
    "v_mfma_f32_16x16x32_bf16 v[228:231], a[168:171], a[100:103], v[228:231]\n"
    "v_exp_f32 v224, v224\n"
    "v_mfma_f32_16x16x32_bf16 v[228:231], a[172:175], a[116:119], v[228:231]\n"
    "v_exp_f32 v225, v225\n"
    "v_mfma_f32_16x16x32_bf16 v[228:231], a[176:179], a[212:215], v[228:231]\n"
    "v_exp_f32 v226, v226\n"
    "v_mfma_f32_16x16x32_bf16 v[228:231], a[180:183], a[228:231], v[228:231]\n"
    "v_exp_f32 v227, v227\n"
    "v_mfma_f32_16x16x32_bf16 v[232:235], a[160:163], a[72:75], v[248:251]\n"
    "ds_read_b128 a[196:199], v15 offset:7168\n"
    "ds_read_b128 a[200:203], v14 offset:8192\n"
    "v_mfma_f32_16x16x32_bf16 v[232:235], a[164:167], a[88:91], v[232:235]\n"
    "v_cvt_pk_bf16_f32 v16, v224, v225\n"
    "v_cvt_pk_bf16_f32 v17, v226, v227\n"
    "v_mfma_f32_16x16x32_bf16 v[232:235], a[168:171], a[104:107], v[232:235]\n"
    "v_exp_f32 v228, v228\n"
    "v_mfma_f32_16x16x32_bf16 v[232:235], a[172:175], a[120:123], v[232:235]\n"
    "v_exp_f32 v229, v229\n"
    "v_mfma_f32_16x16x32_bf16 v[232:235], a[176:179], a[216:219], v[232:235]\n"
    "v_exp_f32 v230, v230\n"
    "v_mfma_f32_16x16x32_bf16 v[232:235], a[180:183], a[232:235], v[232:235]\n"
    "v_exp_f32 v231, v231\n"
    "v_mfma_f32_16x16x32_bf16 v[236:239], a[160:163], a[76:79], v[252:255]\n"
    "ds_read_b128 a[204:207], v15 offset:8192\n"
    "v_cvt_pk_bf16_f32 v20, v228, v229\n"
    "v_mfma_f32_16x16x32_bf16 v[236:239], a[164:167], a[92:95], v[236:239]\n"
    "v_cvt_pk_bf16_f32 v21, v230, v231\n"
    "v_mfma_f32_16x16x32_bf16 v[236:239], a[168:171], a[108:111], v[236:239]\n"
    "v_exp_f32 v232, v232\n"
    "v_mfma_f32_16x16x32_bf16 v[236:239], a[172:175], a[124:127], v[236:239]\n"
    "v_exp_f32 v233, v233\n"
    "v_mfma_f32_16x16x32_bf16 v[236:239], a[176:179], a[220:223], v[236:239]\n"
    "v_exp_f32 v234, v234\n"
    "v_mfma_f32_16x16x32_bf16 v[236:239], a[180:183], a[236:239], v[236:239]\n"
    "s_waitcnt vmcnt(14) lgkmcnt(0)\n"
    "v_exp_f32 v235, v235\n"
    "s_barrier\n"
    "v_mfma_f32_16x16x32_bf16 v[224:227], a[184:187], a[64:67], v[240:243]\n"
    "ds_read_b128 a[160:163], v14 offset:12288\n"
    "v_cvt_pk_bf16_f32 v24, v232, v233\n"
    "v_mfma_f32_16x16x32_bf16 v[224:227], a[188:191], a[80:83], v[224:227]\n"
    "s_add_u32 m0, s83, 0\n"
    "v_cvt_pk_bf16_f32 v25, v234, v235\n"
    "v_mfma_f32_16x16x32_bf16 v[224:227], a[192:195], a[96:99], v[224:227]\n"
    "v_exp_f32 v236, v236\n"
    "v_mfma_f32_16x16x32_bf16 v[224:227], a[196:199], a[112:115], v[224:227]\n"
    "v_exp_f32 v237, v237\n"
    "v_mfma_f32_16x16x32_bf16 v[224:227], a[200:203], a[208:211], v[224:227]\n"
    "s_add_u32 s88, s82, 49152\n"
    "v_exp_f32 v238, v238\n"
    "v_mfma_f32_16x16x32_bf16 v[224:227], a[204:207], a[224:227], v[224:227]\n"
    "v_exp_f32 v239, v239\n"
    "v_mfma_f32_16x16x32_bf16 v[228:231], a[184:187], a[68:71], v[244:247]\n"
    "ds_read_b128 a[164:167], v15 offset:12288\n"
    "ds_read_b128 a[168:171], v14 offset:13312\n"
    "v_mfma_f32_16x16x32_bf16 v[228:231], a[188:191], a[84:87], v[228:231]\n"
    "v_cvt_pk_bf16_f32 v28, v236, v237\n"
    "v_cvt_pk_bf16_f32 v29, v238, v239\n"
    "v_mfma_f32_16x16x32_bf16 v[228:231], a[192:195], a[100:103], v[228:231]\n"
    "v_exp_f32 v224, v224\n"
    "v_mfma_f32_16x16x32_bf16 v[228:231], a[196:199], a[116:119], v[228:231]\n"
    "v_exp_f32 v225, v225\n"
    "v_mfma_f32_16x16x32_bf16 v[228:231], a[200:203], a[212:215], v[228:231]\n"
    "v_exp_f32 v226, v226\n"
    "v_mfma_f32_16x16x32_bf16 v[228:231], a[204:207], a[228:231], v[228:231]\n"
    "v_exp_f32 v227, v227\n"
    "v_mfma_f32_16x16x32_bf16 v[232:235], a[184:187], a[72:75], v[248:251]\n"
    "ds_read_b128 a[172:175], v15 offset:13312\n"
    "ds_read_b128 a[176:179], v14 offset:14336\n"
    "v_mfma_f32_16x16x32_bf16 v[232:235], a[188:191], a[88:91], v[232:235]\n"
    "v_cvt_pk_bf16_f32 v18, v224, v225\n"
    "v_cvt_pk_bf16_f32 v19, v226, v227\n"
    "v_mfma_f32_16x16x32_bf16 v[232:235], a[192:195], a[104:107], v[232:235]\n"
    "v_exp_f32 v228, v228\n"
    "v_mfma_f32_16x16x32_bf16 v[232:235], a[196:199], a[120:123], v[232:235]\n"
    "v_exp_f32 v229, v229\n"
    "v_mfma_f32_16x16x32_bf16 v[232:235], a[200:203], a[216:219], v[232:235]\n"
    "v_exp_f32 v230, v230\n"
    "v_mfma_f32_16x16x32_bf16 v[232:235], a[204:207], a[232:235], v[232:235]\n"
    "v_exp_f32 v231, v231\n"
    "v_mfma_f32_16x16x32_bf16 v[236:239], a[184:187], a[76:79], v[252:255]\n"
    "ds_read_b128 a[180:183], v15 offset:14336\n"
    "v_cvt_pk_bf16_f32 v22, v228, v229\n"
    "v_mfma_f32_16x16x32_bf16 v[236:239], a[188:191], a[92:95], v[236:239]\n"
    "buffer_load_dwordx4 v11, s[56:59], s88 offen offset:0 lds\n"
    "v_mfma_f32_16x16x32_bf16 v[236:239], a[192:195], a[108:111], v[236:239]\n"
    "v_exp_f32 v232, v232\n"
    "v_mfma_f32_16x16x32_bf16 v[236:239], a[196:199], a[124:127], v[236:239]\n"
    "v_exp_f32 v233, v233\n"
    "v_mfma_f32_16x16x32_bf16 v[236:239], a[200:203], a[220:223], v[236:239]\n"
    "v_exp_f32 v234, v234\n"
    "v_mfma_f32_16x16x32_bf16 v[236:239], a[204:207], a[236:239], v[236:239]\n"
    "s_waitcnt lgkmcnt(0)\n"
    "v_exp_f32 v235, v235\n"
    "v_mfma_f32_16x16x32_bf16 v[224:227], a[160:163], a[64:67], v[240:243]\n"
    "ds_read_b128 a[184:187], v14 offset:18432\n"
    "v_cvt_pk_bf16_f32 v23, v230, v231\n"
    "v_mfma_f32_16x16x32_bf16 v[224:227], a[164:167], a[80:83], v[224:227]\n"
    "v_cvt_pk_bf16_f32 v26, v232, v233\n"
    "v_cvt_pk_bf16_f32 v27, v234, v235\n"
    "v_mfma_f32_16x16x32_bf16 v[224:227], a[168:171], a[96:99], v[224:227]\n"
    "v_exp_f32 v236, v236\n"
    "v_mfma_f32_16x16x32_bf16 v[224:227], a[172:175], a[112:115], v[224:227]\n"
    "v_exp_f32 v237, v237\n"
    "v_mfma_f32_16x16x32_bf16 v[224:227], a[176:179], a[208:211], v[224:227]\n"
    "v_exp_f32 v238, v238\n"
    "v_mfma_f32_16x16x32_bf16 v[224:227], a[180:183], a[224:227], v[224:227]\n"
    "v_exp_f32 v239, v239\n"
    "v_mfma_f32_16x16x32_bf16 v[228:231], a[160:163], a[68:71], v[244:247]\n"
    "ds_read_b128 a[188:191], v15 offset:18432\n"
    "ds_read_b128 a[192:195], v14 offset:19456\n"
    "v_mfma_f32_16x16x32_bf16 v[228:231], a[164:167], a[84:87], v[228:231]\n"
    "buffer_load_dwordx4 v12, s[56:59], s88 offen offset:1024 lds\n"
    "v_mfma_f32_16x16x32_bf16 v[228:231], a[168:171], a[100:103], v[228:231]\n"
    "v_exp_f32 v224, v224\n"
    "v_mfma_f32_16x16x32_bf16 v[228:231], a[172:175], a[116:119], v[228:231]\n"
    "buffer_load_dwordx4 v13, s[56:59], s88 offen offset:2048 lds\n"
    "v_mfma_f32_16x16x32_bf16 v[228:231], a[176:179], a[212:215], v[228:231]\n"
    "s_add_u32 m0, s85, 0\n"
    "v_exp_f32 v225, v225\n"
    "v_mfma_f32_16x16x32_bf16 v[228:231], a[180:183], a[228:231], v[228:231]\n"
    "s_add_u32 s88, s84, 0\n"
    "v_cvt_pk_bf16_f32 v30, v236, v237\n"
    "v_cvt_pk_bf16_f32 v31, v238, v239\n"
    "v_mfma_f32_16x16x32_bf16 a[240:243], a[48:51], v[16:19], 0\n"
    "ds_read_b128 a[196:199], v15 offset:19456\n"
    "ds_read_b128 a[200:203], v14 offset:20480\n"
    "v_mfma_f32_16x16x32_bf16 v[232:235], a[160:163], a[72:75], v[248:251]\n"
    "v_exp_f32 v226, v226\n"
    "v_mfma_f32_16x16x32_bf16 v[232:235], a[164:167], a[88:91], v[232:235]\n"
    "v_exp_f32 v227, v227\n"
    "v_mfma_f32_16x16x32_bf16 v[232:235], a[168:171], a[104:107], v[232:235]\n"
    "v_exp_f32 v228, v228\n"
    "v_mfma_f32_16x16x32_bf16 v[232:235], a[172:175], a[120:123], v[232:235]\n"
    "v_exp_f32 v229, v229\n"
    "v_mfma_f32_16x16x32_bf16 v[232:235], a[176:179], a[216:219], v[232:235]\n"
    "v_cvt_pk_bf16_f32 v32, v224, v225\n"
    "v_cvt_pk_bf16_f32 v33, v226, v227\n"
    "v_mfma_f32_16x16x32_bf16 v[232:235], a[180:183], a[232:235], v[232:235]\n"
    "v_exp_f32 v230, v230\n"
    "v_mfma_f32_16x16x32_bf16 v[236:239], a[160:163], a[76:79], v[252:255]\n"
    "ds_read_b128 a[204:207], v15 offset:20480\n"
    "v_mfma_f32_16x16x32_bf16 v[236:239], a[164:167], a[92:95], v[236:239]\n"
    "v_exp_f32 v231, v231\n"
    "v_mfma_f32_16x16x32_bf16 v[236:239], a[168:171], a[108:111], v[236:239]\n"
    "buffer_load_dwordx4 v1, s[60:63], s88 offen offset:0 lds\n"
    "v_mfma_f32_16x16x32_bf16 v[236:239], a[172:175], a[124:127], v[236:239]\n"
    "buffer_load_dwordx4 v2, s[60:63], s88 offen offset:1024 lds\n"
    "v_mfma_f32_16x16x32_bf16 v[236:239], a[176:179], a[220:223], v[236:239]\n"
    "v_exp_f32 v232, v232\n"
    "v_mfma_f32_16x16x32_bf16 v[236:239], a[180:183], a[236:239], v[236:239]\n"
    "v_cvt_pk_bf16_f32 v36, v228, v229\n"
    "v_cvt_pk_bf16_f32 v37, v230, v231\n"
    "v_mfma_f32_16x16x32_bf16 a[244:247], a[48:51], v[20:23], 0\n"
    "s_waitcnt vmcnt(16) lgkmcnt(0)\n"
    "v_exp_f32 v233, v233\n"
    "s_barrier\n"
    "v_mfma_f32_16x16x32_bf16 v[224:227], a[184:187], a[64:67], v[240:243]\n"
    "v_exp_f32 v234, v234\n"
    "v_mfma_f32_16x16x32_bf16 v[224:227], a[188:191], a[80:83], v[224:227]\n"
    "buffer_load_dwordx4 v3, s[60:63], s88 offen offset:2048 lds\n"
    "v_mfma_f32_16x16x32_bf16 v[224:227], a[192:195], a[96:99], v[224:227]\n"
    "v_exp_f32 v235, v235\n"
    "v_mfma_f32_16x16x32_bf16 v[224:227], a[196:199], a[112:115], v[224:227]\n"
    "buffer_load_dwordx4 v4, s[60:63], s88 offen offset:3072 lds\n"
    "v_mfma_f32_16x16x32_bf16 v[224:227], a[200:203], a[208:211], v[224:227]\n"
    "ds_read_b128 a[160:163], v14 offset:24576\n"
    "v_cvt_pk_bf16_f32 v40, v232, v233\n"
    "v_mfma_f32_16x16x32_bf16 v[224:227], a[204:207], a[224:227], v[224:227]\n"
    "v_exp_f32 v236, v236\n"
    "v_mfma_f32_16x16x32_bf16 v[228:231], a[184:187], a[68:71], v[244:247]\n"
    "v_exp_f32 v237, v237\n"
    "v_cvt_pk_bf16_f32 v41, v234, v235\n"
    "v_mfma_f32_16x16x32_bf16 v[228:231], a[188:191], a[84:87], v[228:231]\n"
    "v_exp_f32 v238, v238\n"
    "ds_read_b128 a[164:167], v15 offset:24576\n"
    "v_min_u32 v5, 752, v0\n"
    "v_mfma_f32_16x16x32_bf16 v[228:231], a[192:195], a[100:103], v[228:231]\n"
    "v_exp_f32 v239, v239\n"
    "v_mfma_f32_16x16x32_bf16 v[228:231], a[196:199], a[116:119], v[228:231]\n"
    "ds_read_b128 a[168:171], v14 offset:25600\n"
    "v_lshlrev_b32 v5, 3, v5\n"
    "v_mfma_f32_16x16x32_bf16 v[228:231], a[200:203], a[212:215], v[228:231]\n"
    "s_add_u32 s94, s30, 0\n"
    "v_exp_f32 v224, v224\n"
    "v_mfma_f32_16x16x32_bf16 v[228:231], a[204:207], a[228:231], v[228:231]\n"
    "v_cvt_pk_bf16_f32 v44, v236, v237\n"
    "v_cvt_pk_bf16_f32 v45, v238, v239\n"
    "v_mfma_f32_16x16x32_bf16 a[248:251], a[48:51], v[24:27], 0\n"
    "v_exp_f32 v225, v225\n"
    "v_mfma_f32_16x16x32_bf16 v[232:235], a[184:187], a[72:75], v[248:251]\n"
    "ds_read_b128 a[172:175], v15 offset:25600\n"
    "ds_read_b128 a[176:179], v14 offset:26624\n"
    "v_mfma_f32_16x16x32_bf16 v[232:235], a[188:191], a[88:91], v[232:235]\n"
    "v_exp_f32 v226, v226\n"
    "v_mfma_f32_16x16x32_bf16 v[232:235], a[192:195], a[104:107], v[232:235]\n"
    "v_exp_f32 v227, v227\n"
    "v_mfma_f32_16x16x32_bf16 v[232:235], a[196:199], a[120:123], v[232:235]\n"
    "buffer_load_dword a58, v5, s[60:63], s94 offen offset:0\n"
    "v_mfma_f32_16x16x32_bf16 v[232:235], a[200:203], a[216:219], v[232:235]\n"
    "s_add_u32 m0, s83, 12288\n"
    "v_exp_f32 v228, v228\n"
    "v_mfma_f32_16x16x32_bf16 v[232:235], a[204:207], a[232:235], v[232:235]\n"
    "s_add_u32 s88, s82, 61440\n"
    "v_exp_f32 v229, v229\n"
    "v_mfma_f32_16x16x32_bf16 v[236:239], a[184:187], a[76:79], v[252:255]\n"
    "v_cvt_pk_bf16_f32 v34, v224, v225\n"
    "v_cvt_pk_bf16_f32 v35, v226, v227\n"
    "ds_read_b128 a[180:183], v15 offset:26624\n"
    "v_mfma_f32_16x16x32_bf16 v[236:239], a[188:191], a[92:95], v[236:239]\n"
    "v_exp_f32 v230, v230\n"
    "v_mfma_f32_16x16x32_bf16 v[236:239], a[192:195], a[108:111], v[236:239]\n"
    "v_exp_f32 v231, v231\n"
    "v_mfma_f32_16x16x32_bf16 v[236:239], a[196:199], a[124:127], v[236:239]\n"
    "v_exp_f32 v232, v232\n"
    "v_mfma_f32_16x16x32_bf16 v[236:239], a[200:203], a[220:223], v[236:239]\n"
    "v_cvt_pk_bf16_f32 v38, v228, v229\n"
    "v_cvt_pk_bf16_f32 v39, v230, v231\n"
    "v_mfma_f32_16x16x32_bf16 v[236:239], a[204:207], a[236:239], v[236:239]\n"
    "v_exp_f32 v233, v233\n"
    "v_mfma_f32_16x16x32_bf16 a[252:255], a[48:51], v[28:31], 0\n"
    "s_waitcnt lgkmcnt(0)\n"
    "v_exp_f32 v234, v234\n"
    "v_mfma_f32_16x16x32_bf16 v[224:227], a[160:163], a[64:67], v[240:243]\n"
    "v_exp_f32 v235, v235\n"
    "v_mfma_f32_16x16x32_bf16 v[224:227], a[164:167], a[80:83], v[224:227]\n"
    "buffer_load_dwordx4 v11, s[56:59], s88 offen offset:0 lds\n"
    "v_mfma_f32_16x16x32_bf16 v[224:227], a[168:171], a[96:99], v[224:227]\n"
    "ds_read_b128 a[184:187], v14 offset:30720\n"
    "v_cvt_pk_bf16_f32 v42, v232, v233\n"
    "v_mfma_f32_16x16x32_bf16 v[224:227], a[172:175], a[112:115], v[224:227]\n"
    "v_exp_f32 v236, v236\n"
    "v_mfma_f32_16x16x32_bf16 v[224:227], a[176:179], a[208:211], v[224:227]\n"
    "v_exp_f32 v237, v237\n"
    "v_mfma_f32_16x16x32_bf16 v[224:227], a[180:183], a[224:227], v[224:227]\n"
    "ds_read_b128 a[188:191], v15 offset:30720\n"
    "v_cvt_pk_bf16_f32 v43, v234, v235\n"
    "v_mfma_f32_16x16x32_bf16 v[228:231], a[160:163], a[68:71], v[244:247]\n"
    "v_exp_f32 v238, v238\n"
    "v_mfma_f32_16x16x32_bf16 v[228:231], a[164:167], a[84:87], v[228:231]\n"
    "buffer_load_dwordx4 v12, s[56:59], s88 offen offset:1024 lds\n"
    "v_mfma_f32_16x16x32_bf16 v[228:231], a[168:171], a[100:103], v[228:231]\n"
    "v_exp_f32 v239, v239\n"
    "v_mfma_f32_16x16x32_bf16 v[228:231], a[172:175], a[116:119], v[228:231]\n"
    "buffer_load_dwordx4 v13, s[56:59], s88 offen offset:2048 lds\n"
    "v_mfma_f32_16x16x32_bf16 v[228:231], a[176:179], a[212:215], v[228:231]\n"
    "ds_read_b128 a[192:195], v14 offset:31744\n"
    "v_cvt_pk_bf16_f32 v46, v236, v237\n"
    "v_mfma_f32_16x16x32_bf16 v[228:231], a[180:183], a[228:231], v[228:231]\n"
    "v_exp_f32 v224, v224\n"
    "v_mfma_f32_16x16x32_bf16 a[240:243], a[48:51], v[32:35], a[240:243]\n"
    "ds_read_b128 a[196:199], v15 offset:31744\n"
    "v_cvt_pk_bf16_f32 v47, v238, v239\n"
    "v_mfma_f32_16x16x32_bf16 v[232:235], a[160:163], a[72:75], v[248:251]\n"
    "v_exp_f32 v225, v225\n"
    "v_mfma_f32_16x16x32_bf16 v[232:235], a[164:167], a[88:91], v[232:235]\n"
    "v_exp_f32 v226, v226\n"
    "v_mfma_f32_16x16x32_bf16 v[232:235], a[168:171], a[104:107], v[232:235]\n"
    "v_exp_f32 v227, v227\n"
    "v_mfma_f32_16x16x32_bf16 v[232:235], a[172:175], a[120:123], v[232:235]\n"
    "ds_read_b128 a[200:203], v14 offset:32768\n"
    "v_cvt_pk_bf16_f32 v48, v224, v225\n"
    "v_mfma_f32_16x16x32_bf16 v[232:235], a[176:179], a[216:219], v[232:235]\n"
    "v_exp_f32 v228, v228\n"
    "v_mfma_f32_16x16x32_bf16 v[232:235], a[180:183], a[232:235], v[232:235]\n"
    "v_exp_f32 v229, v229\n"
    "v_mfma_f32_16x16x32_bf16 v[236:239], a[160:163], a[76:79], v[252:255]\n"
    "ds_read_b128 a[204:207], v15 offset:32768\n"
    "v_cvt_pk_bf16_f32 v49, v226, v227\n"
    "v_mfma_f32_16x16x32_bf16 v[236:239], a[164:167], a[92:95], v[236:239]\n"
    "v_exp_f32 v230, v230\n"
    "v_mfma_f32_16x16x32_bf16 v[236:239], a[168:171], a[108:111], v[236:239]\n"
    "v_exp_f32 v231, v231\n"
    "v_mfma_f32_16x16x32_bf16 v[236:239], a[172:175], a[124:127], v[236:239]\n"
    "v_exp_f32 v232, v232\n"
    "v_mfma_f32_16x16x32_bf16 v[236:239], a[176:179], a[220:223], v[236:239]\n"
    "v_cvt_pk_bf16_f32 v52, v228, v229\n"
    "v_cvt_pk_bf16_f32 v53, v230, v231\n"
    "v_mfma_f32_16x16x32_bf16 v[236:239], a[180:183], a[236:239], v[236:239]\n"
    "v_exp_f32 v233, v233\n"
    "v_mfma_f32_16x16x32_bf16 a[244:247], a[48:51], v[36:39], a[244:247]\n"
    "s_waitcnt vmcnt(19) lgkmcnt(0)\n"
    "v_exp_f32 v234, v234\n"
    "s_barrier\n"
    "v_mfma_f32_16x16x32_bf16 v[224:227], a[184:187], a[64:67], v[240:243]\n"
    "v_exp_f32 v235, v235\n"
    "v_mfma_f32_16x16x32_bf16 v[224:227], a[188:191], a[80:83], v[224:227]\n"
    "s_add_u32 m0, s83, 24576\n"
    "ds_read_b128 a[160:163], v14 offset:36864\n"
    "v_cvt_pk_bf16_f32 v56, v232, v233\n"
    "v_mfma_f32_16x16x32_bf16 v[224:227], a[192:195], a[96:99], v[224:227]\n"
    "v_exp_f32 v236, v236\n"
    "v_mfma_f32_16x16x32_bf16 v[224:227], a[196:199], a[112:115], v[224:227]\n"
    "v_exp_f32 v237, v237\n"
    "v_mfma_f32_16x16x32_bf16 v[224:227], a[200:203], a[208:211], v[224:227]\n"
    "s_add_u32 s88, s82, 73728\n"
    "ds_read_b128 a[164:167], v15 offset:36864\n"
    "v_cvt_pk_bf16_f32 v57, v234, v235\n"
    "v_mfma_f32_16x16x32_bf16 v[224:227], a[204:207], a[224:227], v[224:227]\n"
    "v_exp_f32 v238, v238\n"
    "v_mfma_f32_16x16x32_bf16 v[228:231], a[184:187], a[68:71], v[244:247]\n"
    "v_exp_f32 v239, v239\n"
    "v_mfma_f32_16x16x32_bf16 v[228:231], a[188:191], a[84:87], v[228:231]\n"
    "ds_read_b128 a[168:171], v14 offset:37888\n"
    "v_cvt_pk_bf16_f32 v60, v236, v237\n"
    "v_mfma_f32_16x16x32_bf16 v[228:231], a[192:195], a[100:103], v[228:231]\n"
    "v_exp_f32 v224, v224\n"
    "v_mfma_f32_16x16x32_bf16 v[228:231], a[196:199], a[116:119], v[228:231]\n"
    "ds_read_b128 a[172:175], v15 offset:37888\n"
    "v_cvt_pk_bf16_f32 v61, v238, v239\n"
    "v_mfma_f32_16x16x32_bf16 v[228:231], a[200:203], a[212:215], v[228:231]\n"
    "v_exp_f32 v225, v225\n"
    "v_mfma_f32_16x16x32_bf16 v[228:231], a[204:207], a[228:231], v[228:231]\n"
    "v_exp_f32 v226, v226\n"
    "v_mfma_f32_16x16x32_bf16 a[248:251], a[48:51], v[40:43], a[248:251]\n"
    "v_exp_f32 v227, v227\n"
    "v_mfma_f32_16x16x32_bf16 v[232:235], a[184:187], a[72:75], v[248:251]\n"
    "ds_read_b128 a[176:179], v14 offset:38912\n"
    "v_cvt_pk_bf16_f32 v50, v224, v225\n"
    "v_mfma_f32_16x16x32_bf16 v[232:235], a[188:191], a[88:91], v[232:235]\n"
    "v_exp_f32 v228, v228\n"
    "v_mfma_f32_16x16x32_bf16 v[232:235], a[192:195], a[104:107], v[232:235]\n"
    "v_exp_f32 v229, v229\n"
    "v_mfma_f32_16x16x32_bf16 v[232:235], a[196:199], a[120:123], v[232:235]\n"
    "ds_read_b128 a[180:183], v15 offset:38912\n"
    "v_cvt_pk_bf16_f32 v51, v226, v227\n"
    "v_mfma_f32_16x16x32_bf16 v[232:235], a[200:203], a[216:219], v[232:235]\n"
    "v_exp_f32 v230, v230\n"
    "v_mfma_f32_16x16x32_bf16 v[232:235], a[204:207], a[232:235], v[232:235]\n"
    "v_exp_f32 v231, v231\n"
    "v_mfma_f32_16x16x32_bf16 v[236:239], a[184:187], a[76:79], v[252:255]\n"
    "v_cvt_pk_bf16_f32 v54, v228, v229\n"
    "v_cvt_pk_bf16_f32 v55, v230, v231\n"
    "v_mfma_f32_16x16x32_bf16 v[236:239], a[188:191], a[92:95], v[236:239]\n"
    "buffer_load_dwordx4 v11, s[56:59], s88 offen offset:0 lds\n"
    "v_mfma_f32_16x16x32_bf16 v[236:239], a[192:195], a[108:111], v[236:239]\n"
    "v_exp_f32 v232, v232\n"
    "v_mfma_f32_16x16x32_bf16 v[236:239], a[196:199], a[124:127], v[236:239]\n"
    "v_exp_f32 v233, v233\n"
    "v_mfma_f32_16x16x32_bf16 v[236:239], a[200:203], a[220:223], v[236:239]\n"
    "v_exp_f32 v234, v234\n"
    "v_mfma_f32_16x16x32_bf16 v[236:239], a[204:207], a[236:239], v[236:239]\n"
    "v_exp_f32 v235, v235\n"
    "v_mfma_f32_16x16x32_bf16 a[252:255], a[48:51], v[44:47], a[252:255]\n"
    "s_waitcnt lgkmcnt(0)\n"
    "v_cvt_pk_bf16_f32 v58, v232, v233\n"
    "v_cvt_pk_bf16_f32 v59, v234, v235\n"
    "v_mfma_f32_16x16x32_bf16 v[224:227], a[160:163], a[64:67], v[240:243]\n"
    "buffer_load_dwordx4 v12, s[56:59], s88 offen offset:1024 lds\n"
    "v_mfma_f32_16x16x32_bf16 v[224:227], a[164:167], a[80:83], v[224:227]\n"
    "v_exp_f32 v236, v236\n"
    "v_mfma_f32_16x16x32_bf16 v[224:227], a[168:171], a[96:99], v[224:227]\n"
    "v_exp_f32 v237, v237\n"
    "v_mfma_f32_16x16x32_bf16 v[224:227], a[172:175], a[112:115], v[224:227]\n"
    "ds_read_b128 a[184:187], v14 offset:43008\n"
    "ds_read_b128 a[188:191], v15 offset:43008\n"
    "v_mfma_f32_16x16x32_bf16 v[224:227], a[176:179], a[208:211], v[224:227]\n"
    "v_exp_f32 v238, v238\n"
    "v_mfma_f32_16x16x32_bf16 v[224:227], a[180:183], a[224:227], v[224:227]\n"
    "v_exp_f32 v239, v239\n"
    "v_mfma_f32_16x16x32_bf16 v[228:231], a[160:163], a[68:71], v[244:247]\n"
    "buffer_load_dwordx4 v13, s[56:59], s88 offen offset:2048 lds\n"
    "v_mfma_f32_16x16x32_bf16 v[228:231], a[164:167], a[84:87], v[228:231]\n"
    "ds_read_b128 a[192:195], v14 offset:44032\n"
    "v_cvt_pk_bf16_f32 v62, v236, v237\n"
    "v_mfma_f32_16x16x32_bf16 v[228:231], a[168:171], a[100:103], v[228:231]\n"
    "v_exp_f32 v224, v224\n"
    "v_mfma_f32_16x16x32_bf16 v[228:231], a[172:175], a[116:119], v[228:231]\n"
    "ds_read_b128 a[196:199], v15 offset:44032\n"
    "v_cvt_pk_bf16_f32 v63, v238, v239\n"
    "v_mfma_f32_16x16x32_bf16 v[228:231], a[176:179], a[212:215], v[228:231]\n"
    "v_exp_f32 v225, v225\n"
    "v_mfma_f32_16x16x32_bf16 v[228:231], a[180:183], a[228:231], v[228:231]\n"
    "v_exp_f32 v226, v226\n"
    "v_mfma_f32_16x16x32_bf16 a[240:243], a[48:51], v[48:51], a[240:243]\n"
    "v_exp_f32 v227, v227\n"
    "v_mfma_f32_16x16x32_bf16 v[232:235], a[160:163], a[72:75], v[248:251]\n"
    "ds_read_b128 a[200:203], v14 offset:45056\n"
    "v_cvt_pk_bf16_f32 v64, v224, v225\n"
    "v_mfma_f32_16x16x32_bf16 v[232:235], a[164:167], a[88:91], v[232:235]\n"
    "v_exp_f32 v228, v228\n"
    "v_mfma_f32_16x16x32_bf16 v[232:235], a[168:171], a[104:107], v[232:235]\n"
    "v_exp_f32 v229, v229\n"
    "v_mfma_f32_16x16x32_bf16 v[232:235], a[172:175], a[120:123], v[232:235]\n"
    "ds_read_b128 a[204:207], v15 offset:45056\n"
    "v_cvt_pk_bf16_f32 v65, v226, v227\n"
    "v_mfma_f32_16x16x32_bf16 v[232:235], a[176:179], a[216:219], v[232:235]\n"
    "v_exp_f32 v230, v230\n"
    "v_mfma_f32_16x16x32_bf16 v[232:235], a[180:183], a[232:235], v[232:235]\n"
    "v_exp_f32 v231, v231\n"
    "v_mfma_f32_16x16x32_bf16 v[236:239], a[160:163], a[76:79], v[252:255]\n"
    "v_cvt_pk_bf16_f32 v68, v228, v229\n"
    "v_cvt_pk_bf16_f32 v69, v230, v231\n"
    "v_mfma_f32_16x16x32_bf16 v[236:239], a[164:167], a[92:95], v[236:239]\n"
    "v_mfma_f32_16x16x32_bf16 v[236:239], a[168:171], a[108:111], v[236:239]\n"
    "v_exp_f32 v232, v232\n"
    "v_mfma_f32_16x16x32_bf16 v[236:239], a[172:175], a[124:127], v[236:239]\n"
    "v_exp_f32 v233, v233\n"
    "v_mfma_f32_16x16x32_bf16 v[236:239], a[176:179], a[220:223], v[236:239]\n"
    "v_exp_f32 v234, v234\n"
    "v_mfma_f32_16x16x32_bf16 v[236:239], a[180:183], a[236:239], v[236:239]\n"
    "v_exp_f32 v235, v235\n"
    "v_mfma_f32_16x16x32_bf16 a[244:247], a[48:51], v[52:55], a[244:247]\n"
    "s_waitcnt vmcnt(11) lgkmcnt(0)\n"
    "s_barrier\n"
    "v_cvt_pk_bf16_f32 v72, v232, v233\n"
    "v_mfma_f32_16x16x32_bf16 v[224:227], a[184:187], a[64:67], v[240:243]\n"
    "ds_read_b128 a[160:163], v14 offset:0\n"
    "v_cvt_pk_bf16_f32 v73, v234, v235\n"
    "v_mfma_f32_16x16x32_bf16 v[224:227], a[188:191], a[80:83], v[224:227]\n"
    "s_add_u32 m0, s83, 36864\n"
    "v_exp_f32 v236, v236\n"
    "v_mfma_f32_16x16x32_bf16 v[224:227], a[192:195], a[96:99], v[224:227]\n"
    "v_exp_f32 v237, v237\n"
    "v_mfma_f32_16x16x32_bf16 v[224:227], a[196:199], a[112:115], v[224:227]\n"
    "v_exp_f32 v238, v238\n"
    "v_mfma_f32_16x16x32_bf16 v[224:227], a[200:203], a[208:211], v[224:227]\n"
    "s_add_u32 s88, s82, 86016\n"
    "v_exp_f32 v239, v239\n"
    "v_mfma_f32_16x16x32_bf16 v[224:227], a[204:207], a[224:227], v[224:227]\n"
    "ds_read_b128 a[164:167], v15 offset:0\n"
    "v_cvt_pk_bf16_f32 v76, v236, v237\n"
    "v_mfma_f32_16x16x32_bf16 v[228:231], a[184:187], a[68:71], v[244:247]\n"
    "ds_read_b128 a[168:171], v14 offset:1024\n"
    "v_cvt_pk_bf16_f32 v77, v238, v239\n"
    "v_mfma_f32_16x16x32_bf16 v[228:231], a[188:191], a[84:87], v[228:231]\n"
    "buffer_load_dwordx4 v11, s[56:59], s88 offen offset:0 lds\n"
    "v_mfma_f32_16x16x32_bf16 v[228:231], a[192:195], a[100:103], v[228:231]\n"
    "v_exp_f32 v224, v224\n"
    "v_mfma_f32_16x16x32_bf16 v[228:231], a[196:199], a[116:119], v[228:231]\n"
    "v_exp_f32 v225, v225\n"
    "v_mfma_f32_16x16x32_bf16 v[228:231], a[200:203], a[212:215], v[228:231]\n"
    "v_exp_f32 v226, v226\n"
    "v_mfma_f32_16x16x32_bf16 v[228:231], a[204:207], a[228:231], v[228:231]\n"
    "v_exp_f32 v227, v227\n"
    "v_mfma_f32_16x16x32_bf16 a[248:251], a[48:51], v[56:59], a[248:251]\n"
    "ds_read_b128 a[172:175], v15 offset:1024\n"
    "ds_read_b128 a[176:179], v14 offset:2048\n"
    "v_mfma_f32_16x16x32_bf16 v[232:235], a[184:187], a[72:75], v[248:251]\n"
    "v_cvt_pk_bf16_f32 v66, v224, v225\n"
    "v_cvt_pk_bf16_f32 v67, v226, v227\n"
    "v_mfma_f32_16x16x32_bf16 v[232:235], a[188:191], a[88:91], v[232:235]\n"
    "v_exp_f32 v228, v228\n"
    "v_mfma_f32_16x16x32_bf16 v[232:235], a[192:195], a[104:107], v[232:235]\n"
    "v_exp_f32 v229, v229\n"
    "v_mfma_f32_16x16x32_bf16 v[232:235], a[196:199], a[120:123], v[232:235]\n"
    "v_exp_f32 v230, v230\n"
    "v_mfma_f32_16x16x32_bf16 v[232:235], a[200:203], a[216:219], v[232:235]\n"
    "v_exp_f32 v231, v231\n"
    "v_mfma_f32_16x16x32_bf16 v[232:235], a[204:207], a[232:235], v[232:235]\n"
    "ds_read_b128 a[180:183], v15 offset:2048\n"
    "v_cvt_pk_bf16_f32 v70, v228, v229\n"
    "v_mfma_f32_16x16x32_bf16 v[236:239], a[184:187], a[76:79], v[252:255]\n"
    "buffer_load_dwordx4 v12, s[56:59], s88 offen offset:1024 lds\n"
    "v_mfma_f32_16x16x32_bf16 v[236:239], a[188:191], a[92:95], v[236:239]\n"
    "buffer_load_dwordx4 v13, s[56:59], s88 offen offset:2048 lds\n"
    "v_mfma_f32_16x16x32_bf16 v[236:239], a[192:195], a[108:111], v[236:239]\n"
    "s_add_u32 m0, s85, 4096\n"
    "v_exp_f32 v232, v232\n"
    "v_mfma_f32_16x16x32_bf16 v[236:239], a[196:199], a[124:127], v[236:239]\n"
    "s_add_u32 s88, s84, 12288\n"
    "v_exp_f32 v233, v233\n"
    "v_mfma_f32_16x16x32_bf16 v[236:239], a[200:203], a[220:223], v[236:239]\n"
    "v_exp_f32 v234, v234\n"
    "v_mfma_f32_16x16x32_bf16 v[236:239], a[204:207], a[236:239], v[236:239]\n"
    "v_exp_f32 v235, v235\n"
    "v_mfma_f32_16x16x32_bf16 a[252:255], a[48:51], v[60:63], a[252:255]\n"
    "s_waitcnt lgkmcnt(0)\n"
    "v_cvt_pk_bf16_f32 v71, v230, v231\n"
    "v_cvt_pk_bf16_f32 v74, v232, v233\n"
    "v_mfma_f32_16x16x32_bf16 v[224:227], a[160:163], a[64:67], v[240:243]\n"
    "ds_read_b128 a[184:187], v14 offset:6144\n"
    "v_cvt_pk_bf16_f32 v75, v234, v235\n"
    "v_mfma_f32_16x16x32_bf16 v[224:227], a[164:167], a[80:83], v[224:227]\n"
    "v_exp_f32 v236, v236\n"
    "v_mfma_f32_16x16x32_bf16 v[224:227], a[168:171], a[96:99], v[224:227]\n"
    "v_exp_f32 v237, v237\n"
    "v_mfma_f32_16x16x32_bf16 v[224:227], a[172:175], a[112:115], v[224:227]\n"
    "v_exp_f32 v238, v238\n"
    "v_mfma_f32_16x16x32_bf16 v[224:227], a[176:179], a[208:211], v[224:227]\n"
    "v_exp_f32 v239, v239\n"
    "v_mfma_f32_16x16x32_bf16 v[224:227], a[180:183], a[224:227], v[224:227]\n"
    "ds_read_b128 a[188:191], v15 offset:6144\n"
    "v_cvt_pk_bf16_f32 v78, v236, v237\n"
    "v_mfma_f32_16x16x32_bf16 v[228:231], a[160:163], a[68:71], v[244:247]\n"
    "ds_read_b128 a[192:195], v14 offset:7168\n"
    "v_cvt_pk_bf16_f32 v79, v238, v239\n"
    "v_mfma_f32_16x16x32_bf16 v[228:231], a[164:167], a[84:87], v[228:231]\n"
    "buffer_load_dwordx4 v1, s[60:63], s88 offen offset:0 lds\n"
    "v_mfma_f32_16x16x32_bf16 v[228:231], a[168:171], a[100:103], v[228:231]\n"
    "v_exp_f32 v224, v224\n"
    "v_mfma_f32_16x16x32_bf16 v[228:231], a[172:175], a[116:119], v[228:231]\n"
    "v_exp_f32 v225, v225\n"
    "v_mfma_f32_16x16x32_bf16 v[228:231], a[176:179], a[212:215], v[228:231]\n"
    "v_exp_f32 v226, v226\n"
    "v_mfma_f32_16x16x32_bf16 v[228:231], a[180:183], a[228:231], v[228:231]\n"
    "v_exp_f32 v227, v227\n"
    "v_mfma_f32_16x16x32_bf16 a[240:243], a[48:51], v[64:67], a[240:243]\n"
    "ds_read_b128 a[196:199], v15 offset:7168\n"
    "ds_read_b128 a[200:203], v14 offset:8192\n"
    "v_mfma_f32_16x16x32_bf16 v[232:235], a[160:163], a[72:75], v[248:251]\n"
    "v_cvt_pk_bf16_f32 v80, v224, v225\n"
    "v_cvt_pk_bf16_f32 v81, v226, v227\n"
    "v_mfma_f32_16x16x32_bf16 v[232:235], a[164:167], a[88:91], v[232:235]\n"
    "v_exp_f32 v228, v228\n"
    "v_mfma_f32_16x16x32_bf16 v[232:235], a[168:171], a[104:107], v[232:235]\n"
    "v_exp_f32 v229, v229\n"
    "v_mfma_f32_16x16x32_bf16 v[232:235], a[172:175], a[120:123], v[232:235]\n"
    "v_exp_f32 v230, v230\n"
    "v_mfma_f32_16x16x32_bf16 v[232:235], a[176:179], a[216:219], v[232:235]\n"
    "v_exp_f32 v231, v231\n"
    "v_mfma_f32_16x16x32_bf16 v[232:235], a[180:183], a[232:235], v[232:235]\n"
    "ds_read_b128 a[204:207], v15 offset:8192\n"
    "v_cvt_pk_bf16_f32 v84, v228, v229\n"
    "v_mfma_f32_16x16x32_bf16 v[236:239], a[160:163], a[76:79], v[252:255]\n"
    "buffer_load_dwordx4 v2, s[60:63], s88 offen offset:1024 lds\n"
    "v_mfma_f32_16x16x32_bf16 v[236:239], a[164:167], a[92:95], v[236:239]\n"
    "buffer_load_dwordx4 v3, s[60:63], s88 offen offset:2048 lds\n"
    "v_mfma_f32_16x16x32_bf16 v[236:239], a[168:171], a[108:111], v[236:239]\n"
    "v_exp_f32 v232, v232\n"
    "v_mfma_f32_16x16x32_bf16 v[236:239], a[172:175], a[124:127], v[236:239]\n"
    "v_exp_f32 v233, v233\n"
    "v_mfma_f32_16x16x32_bf16 v[236:239], a[176:179], a[220:223], v[236:239]\n"
    "v_exp_f32 v234, v234\n"
    "v_mfma_f32_16x16x32_bf16 v[236:239], a[180:183], a[236:239], v[236:239]\n"
    "v_exp_f32 v235, v235\n"
    "v_mfma_f32_16x16x32_bf16 a[244:247], a[48:51], v[68:71], a[244:247]\n"
    "s_waitcnt vmcnt(9) lgkmcnt(0)\n"
    "v_cvt_pk_bf16_f32 v85, v230, v231\n"
    "s_barrier\n"
    "v_mfma_f32_16x16x32_bf16 v[224:227], a[184:187], a[64:67], v[240:243]\n"
    "buffer_load_dwordx4 v4, s[60:63], s88 offen offset:3072 lds\n"
    "v_mfma_f32_16x16x32_bf16 v[224:227], a[188:191], a[80:83], v[224:227]\n"
    "s_add_u32 m0, s83, 0\n"
    "ds_read_b128 a[160:163], v14 offset:12288\n"
    "v_cvt_pk_bf16_f32 v88, v232, v233\n"
    "v_mfma_f32_16x16x32_bf16 v[224:227], a[192:195], a[96:99], v[224:227]\n"
    "v_exp_f32 v236, v236\n"
    "v_mfma_f32_16x16x32_bf16 v[224:227], a[196:199], a[112:115], v[224:227]\n"
    "v_exp_f32 v237, v237\n"
    "v_mfma_f32_16x16x32_bf16 v[224:227], a[200:203], a[208:211], v[224:227]\n"
    "s_add_u32 s88, s82, 98304\n"
    "ds_read_b128 a[164:167], v15 offset:12288\n"
    "v_cvt_pk_bf16_f32 v89, v234, v235\n"
    "v_mfma_f32_16x16x32_bf16 v[224:227], a[204:207], a[224:227], v[224:227]\n"
    "v_exp_f32 v238, v238\n"
    "v_mfma_f32_16x16x32_bf16 v[228:231], a[184:187], a[68:71], v[244:247]\n"
    "v_exp_f32 v239, v239\n"
    "v_mfma_f32_16x16x32_bf16 v[228:231], a[188:191], a[84:87], v[228:231]\n"
    "ds_read_b128 a[168:171], v14 offset:13312\n"
    "v_cvt_pk_bf16_f32 v92, v236, v237\n"
    "v_mfma_f32_16x16x32_bf16 v[228:231], a[192:195], a[100:103], v[228:231]\n"
    "v_exp_f32 v224, v224\n"
    "v_mfma_f32_16x16x32_bf16 v[228:231], a[196:199], a[116:119], v[228:231]\n"
    "ds_read_b128 a[172:175], v15 offset:13312\n"
    "v_cvt_pk_bf16_f32 v93, v238, v239\n"
    "v_mfma_f32_16x16x32_bf16 v[228:231], a[200:203], a[212:215], v[228:231]\n"
    "v_exp_f32 v225, v225\n"
    "v_mfma_f32_16x16x32_bf16 v[228:231], a[204:207], a[228:231], v[228:231]\n"
    "v_exp_f32 v226, v226\n"
    "v_mfma_f32_16x16x32_bf16 a[248:251], a[48:51], v[72:75], a[248:251]\n"
    "v_exp_f32 v227, v227\n"
    "v_mfma_f32_16x16x32_bf16 v[232:235], a[184:187], a[72:75], v[248:251]\n"
    "ds_read_b128 a[176:179], v14 offset:14336\n"
    "v_cvt_pk_bf16_f32 v82, v224, v225\n"
    "v_mfma_f32_16x16x32_bf16 v[232:235], a[188:191], a[88:91], v[232:235]\n"
    "v_exp_f32 v228, v228\n"
    "v_mfma_f32_16x16x32_bf16 v[232:235], a[192:195], a[104:107], v[232:235]\n"
    "v_exp_f32 v229, v229\n"
    "v_mfma_f32_16x16x32_bf16 v[232:235], a[196:199], a[120:123], v[232:235]\n"
    "ds_read_b128 a[180:183], v15 offset:14336\n"
    "v_cvt_pk_bf16_f32 v83, v226, v227\n"
    "v_mfma_f32_16x16x32_bf16 v[232:235], a[200:203], a[216:219], v[232:235]\n"
    "v_exp_f32 v230, v230\n"
    "v_mfma_f32_16x16x32_bf16 v[232:235], a[204:207], a[232:235], v[232:235]\n"
    "v_exp_f32 v231, v231\n"
    "v_mfma_f32_16x16x32_bf16 v[236:239], a[184:187], a[76:79], v[252:255]\n"
    "v_cvt_pk_bf16_f32 v86, v228, v229\n"
    "v_cvt_pk_bf16_f32 v87, v230, v231\n"
    "v_mfma_f32_16x16x32_bf16 v[236:239], a[188:191], a[92:95], v[236:239]\n"
    "buffer_load_dwordx4 v11, s[56:59], s88 offen offset:0 lds\n"
    "v_mfma_f32_16x16x32_bf16 v[236:239], a[192:195], a[108:111], v[236:239]\n"
    "v_exp_f32 v232, v232\n"
    "v_mfma_f32_16x16x32_bf16 v[236:239], a[196:199], a[124:127], v[236:239]\n"
    "v_exp_f32 v233, v233\n"
    "v_mfma_f32_16x16x32_bf16 v[236:239], a[200:203], a[220:223], v[236:239]\n"
    "v_exp_f32 v234, v234\n"
    "v_mfma_f32_16x16x32_bf16 v[236:239], a[204:207], a[236:239], v[236:239]\n"
    "v_exp_f32 v235, v235\n"
    "v_mfma_f32_16x16x32_bf16 a[252:255], a[48:51], v[76:79], a[252:255]\n"
    "s_waitcnt lgkmcnt(0)\n"
    "v_cvt_pk_bf16_f32 v90, v232, v233\n"
    "v_cvt_pk_bf16_f32 v91, v234, v235\n"
    "v_mfma_f32_16x16x32_bf16 v[224:227], a[160:163], a[64:67], v[240:243]\n"
    "buffer_load_dwordx4 v12, s[56:59], s88 offen offset:1024 lds\n"
    "v_mfma_f32_16x16x32_bf16 v[224:227], a[164:167], a[80:83], v[224:227]\n"
    "v_exp_f32 v236, v236\n"
    "v_mfma_f32_16x16x32_bf16 v[224:227], a[168:171], a[96:99], v[224:227]\n"
    "v_exp_f32 v237, v237\n"
    "v_mfma_f32_16x16x32_bf16 v[224:227], a[172:175], a[112:115], v[224:227]\n"
    "ds_read_b128 a[184:187], v14 offset:18432\n"
    "ds_read_b128 a[188:191], v15 offset:18432\n"
    "v_mfma_f32_16x16x32_bf16 v[224:227], a[176:179], a[208:211], v[224:227]\n"
    "v_exp_f32 v238, v238\n"
    "v_mfma_f32_16x16x32_bf16 v[224:227], a[180:183], a[224:227], v[224:227]\n"
    "v_exp_f32 v239, v239\n"
    "v_mfma_f32_16x16x32_bf16 v[228:231], a[160:163], a[68:71], v[244:247]\n"
    "buffer_load_dwordx4 v13, s[56:59], s88 offen offset:2048 lds\n"
    "v_mfma_f32_16x16x32_bf16 v[228:231], a[164:167], a[84:87], v[228:231]\n"
    "s_add_u32 s94, s97, 2\n"
    "ds_read_b128 a[192:195], v14 offset:19456\n"
    "v_cvt_pk_bf16_f32 v94, v236, v237\n"
    "v_mfma_f32_16x16x32_bf16 v[228:231], a[168:171], a[100:103], v[228:231]\n"
    "v_exp_f32 v224, v224\n"
    "v_mfma_f32_16x16x32_bf16 v[228:231], a[172:175], a[116:119], v[228:231]\n"
    "ds_read_b128 a[196:199], v15 offset:19456\n"
    "v_cvt_pk_bf16_f32 v95, v238, v239\n"
    "v_mfma_f32_16x16x32_bf16 v[228:231], a[176:179], a[212:215], v[228:231]\n"
    "v_exp_f32 v225, v225\n"
    "v_mfma_f32_16x16x32_bf16 v[228:231], a[180:183], a[228:231], v[228:231]\n"
    "v_exp_f32 v226, v226\n"
    "v_mfma_f32_16x16x32_bf16 a[240:243], a[48:51], v[80:83], a[240:243]\n"
    "v_exp_f32 v227, v227\n"
    "v_mfma_f32_16x16x32_bf16 v[232:235], a[160:163], a[72:75], v[248:251]\n"
    "ds_read_b128 a[200:203], v14 offset:20480\n"
    "v_accvgpr_write_b32 a53, s94\n"
    "v_mfma_f32_16x16x32_bf16 v[232:235], a[164:167], a[88:91], v[232:235]\n"
    "v_exp_f32 v228, v228\n"
    "v_mfma_f32_16x16x32_bf16 v[232:235], a[168:171], a[104:107], v[232:235]\n"
    "v_exp_f32 v229, v229\n"
    "v_mfma_f32_16x16x32_bf16 v[232:235], a[172:175], a[120:123], v[232:235]\n"
    "v_cvt_pk_bf16_f32 v96, v224, v225\n"
    "v_cvt_pk_bf16_f32 v97, v226, v227\n"
    "v_mfma_f32_16x16x32_bf16 v[232:235], a[176:179], a[216:219], v[232:235]\n"
    "v_exp_f32 v230, v230\n"
    "v_mfma_f32_16x16x32_bf16 v[232:235], a[180:183], a[232:235], v[232:235]\n"
    "v_exp_f32 v231, v231\n"
    "v_mfma_f32_16x16x32_bf16 v[236:239], a[160:163], a[76:79], v[252:255]\n"
    "ds_read_b128 a[204:207], v15 offset:20480\n"
    "v_cvt_pk_bf16_f32 v100, v228, v229\n"
    "v_mfma_f32_16x16x32_bf16 v[236:239], a[164:167], a[92:95], v[236:239]\n"
    "buffer_store_dwordx2 a[52:53], v7, s[40:43], s23 offen offset:0 sc1\n"
    "v_mfma_f32_16x16x32_bf16 v[236:239], a[168:171], a[108:111], v[236:239]\n"
    "v_exp_f32 v232, v232\n"
    "v_mfma_f32_16x16x32_bf16 v[236:239], a[172:175], a[124:127], v[236:239]\n"
    "v_exp_f32 v233, v233\n"
    "v_mfma_f32_16x16x32_bf16 v[236:239], a[176:179], a[220:223], v[236:239]\n"
    "v_exp_f32 v234, v234\n"
    "v_mfma_f32_16x16x32_bf16 v[236:239], a[180:183], a[236:239], v[236:239]\n"
    "v_exp_f32 v235, v235\n"
    "v_mfma_f32_16x16x32_bf16 a[244:247], a[48:51], v[84:87], a[244:247]\n"
    "s_waitcnt vmcnt(11) lgkmcnt(0)\n"
    "v_cvt_pk_bf16_f32 v101, v230, v231\n"
    "s_barrier\n"
    "v_mfma_f32_16x16x32_bf16 v[224:227], a[184:187], a[64:67], v[240:243]\n"
    "ds_read_b128 a[160:163], v14 offset:24576\n"
    "v_cvt_pk_bf16_f32 v104, v232, v233\n"
    "v_mfma_f32_16x16x32_bf16 v[224:227], a[188:191], a[80:83], v[224:227]\n"
    "s_add_u32 m0, s83, 12288\n"
    "v_exp_f32 v236, v236\n"
    "v_mfma_f32_16x16x32_bf16 v[224:227], a[192:195], a[96:99], v[224:227]\n"
    "v_exp_f32 v237, v237\n"
    "v_mfma_f32_16x16x32_bf16 v[224:227], a[196:199], a[112:115], v[224:227]\n"
    "ds_read_b128 a[164:167], v15 offset:24576\n"
    "v_cvt_pk_bf16_f32 v105, v234, v235\n"
    "v_mfma_f32_16x16x32_bf16 v[224:227], a[200:203], a[208:211], v[224:227]\n"
    "s_add_u32 s88, s82, 110592\n"
    "v_exp_f32 v238, v238\n"
    "v_mfma_f32_16x16x32_bf16 v[224:227], a[204:207], a[224:227], v[224:227]\n"
    "v_exp_f32 v239, v239\n"
    "v_mfma_f32_16x16x32_bf16 v[228:231], a[184:187], a[68:71], v[244:247]\n"
    "ds_read_b128 a[168:171], v14 offset:25600\n"
    "v_cvt_pk_bf16_f32 v108, v236, v237\n"
    "v_mfma_f32_16x16x32_bf16 v[228:231], a[188:191], a[84:87], v[228:231]\n"
    "buffer_load_dwordx4 v11, s[56:59], s88 offen offset:0 lds\n"
    "v_mfma_f32_16x16x32_bf16 v[228:231], a[192:195], a[100:103], v[228:231]\n"
    "v_exp_f32 v224, v224\n"
    "v_mfma_f32_16x16x32_bf16 v[228:231], a[196:199], a[116:119], v[228:231]\n"
    "ds_read_b128 a[172:175], v15 offset:25600\n"
    "v_cvt_pk_bf16_f32 v109, v238, v239\n"
    "v_mfma_f32_16x16x32_bf16 v[228:231], a[200:203], a[212:215], v[228:231]\n"
    "v_exp_f32 v225, v225\n"
    "v_mfma_f32_16x16x32_bf16 v[228:231], a[204:207], a[228:231], v[228:231]\n"
    "v_exp_f32 v226, v226\n"
    "v_mfma_f32_16x16x32_bf16 a[248:251], a[48:51], v[88:91], a[248:251]\n"
    "v_exp_f32 v227, v227\n"
    "v_mfma_f32_16x16x32_bf16 v[232:235], a[184:187], a[72:75], v[248:251]\n"
    "ds_read_b128 a[176:179], v14 offset:26624\n"
    "v_cvt_pk_bf16_f32 v98, v224, v225\n"
    "v_mfma_f32_16x16x32_bf16 v[232:235], a[188:191], a[88:91], v[232:235]\n"
    "v_exp_f32 v228, v228\n"
    "v_mfma_f32_16x16x32_bf16 v[232:235], a[192:195], a[104:107], v[232:235]\n"
    "v_exp_f32 v229, v229\n"
    "v_mfma_f32_16x16x32_bf16 v[232:235], a[196:199], a[120:123], v[232:235]\n"
    "ds_read_b128 a[180:183], v15 offset:26624\n"
    "v_cvt_pk_bf16_f32 v99, v226, v227\n"
    "v_mfma_f32_16x16x32_bf16 v[232:235], a[200:203], a[216:219], v[232:235]\n"
    "v_exp_f32 v230, v230\n"
    "v_mfma_f32_16x16x32_bf16 v[232:235], a[204:207], a[232:235], v[232:235]\n"
    "v_exp_f32 v231, v231\n"
    "v_mfma_f32_16x16x32_bf16 v[236:239], a[184:187], a[76:79], v[252:255]\n"
    "v_cvt_pk_bf16_f32 v102, v228, v229\n"
    "v_cvt_pk_bf16_f32 v103, v230, v231\n"
    "v_mfma_f32_16x16x32_bf16 v[236:239], a[188:191], a[92:95], v[236:239]\n"
    "buffer_load_dwordx4 v12, s[56:59], s88 offen offset:1024 lds\n"
    "v_mfma_f32_16x16x32_bf16 v[236:239], a[192:195], a[108:111], v[236:239]\n"
    "v_exp_f32 v232, v232\n"
    "v_mfma_f32_16x16x32_bf16 v[236:239], a[196:199], a[124:127], v[236:239]\n"
    "v_exp_f32 v233, v233\n"
    "v_mfma_f32_16x16x32_bf16 v[236:239], a[200:203], a[220:223], v[236:239]\n"
    "v_exp_f32 v234, v234\n"
    "v_mfma_f32_16x16x32_bf16 v[236:239], a[204:207], a[236:239], v[236:239]\n"
    "v_exp_f32 v235, v235\n"
    "v_mfma_f32_16x16x32_bf16 a[252:255], a[48:51], v[92:95], a[252:255]\n"
    "s_waitcnt lgkmcnt(0)\n"
    "v_cvt_pk_bf16_f32 v106, v232, v233\n"
    "v_cvt_pk_bf16_f32 v107, v234, v235\n"
    "v_mfma_f32_16x16x32_bf16 v[224:227], a[160:163], a[64:67], v[240:243]\n"
    "buffer_load_dwordx4 v13, s[56:59], s88 offen offset:2048 lds\n"
    "v_mfma_f32_16x16x32_bf16 v[224:227], a[164:167], a[80:83], v[224:227]\n"
    "v_exp_f32 v236, v236\n"
    "v_mfma_f32_16x16x32_bf16 v[224:227], a[168:171], a[96:99], v[224:227]\n"
    "v_exp_f32 v237, v237\n"
    "v_mfma_f32_16x16x32_bf16 v[224:227], a[172:175], a[112:115], v[224:227]\n"
    "ds_read_b128 a[184:187], v14 offset:30720\n"
    "ds_read_b128 a[188:191], v15 offset:30720\n"
    "v_mfma_f32_16x16x32_bf16 v[224:227], a[176:179], a[208:211], v[224:227]\n"
    "v_exp_f32 v238, v238\n"
    "v_mfma_f32_16x16x32_bf16 v[224:227], a[180:183], a[224:227], v[224:227]\n"
    "v_exp_f32 v239, v239\n"
    "v_mfma_f32_16x16x32_bf16 v[228:231], a[160:163], a[68:71], v[244:247]\n"
    "ds_read_b128 a[192:195], v14 offset:31744\n"
    "v_cvt_pk_bf16_f32 v110, v236, v237\n"
    "v_mfma_f32_16x16x32_bf16 v[228:231], a[164:167], a[84:87], v[228:231]\n"
    "v_cvt_pk_bf16_f32 v111, v238, v239\n"
    "v_mfma_f32_16x16x32_bf16 v[228:231], a[168:171], a[100:103], v[228:231]\n"
    "v_exp_f32 v224, v224\n"
    "v_mfma_f32_16x16x32_bf16 v[228:231], a[172:175], a[116:119], v[228:231]\n"
    "v_exp_f32 v225, v225\n"
    "v_mfma_f32_16x16x32_bf16 v[228:231], a[176:179], a[212:215], v[228:231]\n"
    "v_exp_f32 v226, v226\n"
    "v_mfma_f32_16x16x32_bf16 v[228:231], a[180:183], a[228:231], v[228:231]\n"
    "v_exp_f32 v227, v227\n"
    "v_mfma_f32_16x16x32_bf16 a[240:243], a[48:51], v[96:99], a[240:243]\n"
    "ds_read_b128 a[196:199], v15 offset:31744\n"
    "ds_read_b128 a[200:203], v14 offset:32768\n"
    "v_mfma_f32_16x16x32_bf16 v[232:235], a[160:163], a[72:75], v[248:251]\n"
    "v_cvt_pk_bf16_f32 v112, v224, v225\n"
    "v_cvt_pk_bf16_f32 v113, v226, v227\n"
    "v_mfma_f32_16x16x32_bf16 v[232:235], a[164:167], a[88:91], v[232:235]\n"
    "v_exp_f32 v228, v228\n"
    "v_mfma_f32_16x16x32_bf16 v[232:235], a[168:171], a[104:107], v[232:235]\n"
    "v_exp_f32 v229, v229\n"
    "v_mfma_f32_16x16x32_bf16 v[232:235], a[172:175], a[120:123], v[232:235]\n"
    "v_exp_f32 v230, v230\n"
    "v_mfma_f32_16x16x32_bf16 v[232:235], a[176:179], a[216:219], v[232:235]\n"
    "v_exp_f32 v231, v231\n"
    "v_mfma_f32_16x16x32_bf16 v[232:235], a[180:183], a[232:235], v[232:235]\n"
    "ds_read_b128 a[204:207], v15 offset:32768\n"
    "v_cvt_pk_bf16_f32 v116, v228, v229\n"
    "v_mfma_f32_16x16x32_bf16 v[236:239], a[160:163], a[76:79], v[252:255]\n"
    "v_cvt_pk_bf16_f32 v117, v230, v231\n"
    "v_mfma_f32_16x16x32_bf16 v[236:239], a[164:167], a[92:95], v[236:239]\n"
    "v_mfma_f32_16x16x32_bf16 v[236:239], a[168:171], a[108:111], v[236:239]\n"
    "v_exp_f32 v232, v232\n"
    "v_mfma_f32_16x16x32_bf16 v[236:239], a[172:175], a[124:127], v[236:239]\n"
    "v_exp_f32 v233, v233\n"
    "v_mfma_f32_16x16x32_bf16 v[236:239], a[176:179], a[220:223], v[236:239]\n"
    "v_exp_f32 v234, v234\n"
    "v_mfma_f32_16x16x32_bf16 v[236:239], a[180:183], a[236:239], v[236:239]\n"
    "v_exp_f32 v235, v235\n"
    "v_mfma_f32_16x16x32_bf16 a[244:247], a[48:51], v[100:103], a[244:247]\n"
    "s_waitcnt vmcnt(11) lgkmcnt(0)\n"
    "s_barrier\n"
    "v_cvt_pk_bf16_f32 v120, v232, v233\n"
    "v_mfma_f32_16x16x32_bf16 v[224:227], a[184:187], a[64:67], v[240:243]\n"
    "ds_read_b128 a[160:163], v14 offset:36864\n"
    "v_cvt_pk_bf16_f32 v121, v234, v235\n"
    "v_mfma_f32_16x16x32_bf16 v[224:227], a[188:191], a[80:83], v[224:227]\n"
    "s_add_u32 m0, s83, 24576\n"
    "v_exp_f32 v236, v236\n"
    "v_mfma_f32_16x16x32_bf16 v[224:227], a[192:195], a[96:99], v[224:227]\n"
    "v_exp_f32 v237, v237\n"
    "v_mfma_f32_16x16x32_bf16 v[224:227], a[196:199], a[112:115], v[224:227]\n"
    "v_exp_f32 v238, v238\n"
    "v_mfma_f32_16x16x32_bf16 v[224:227], a[200:203], a[208:211], v[224:227]\n"
    "s_add_u32 s88, s82, 122880\n"
    "v_exp_f32 v239, v239\n"
    "v_mfma_f32_16x16x32_bf16 v[224:227], a[204:207], a[224:227], v[224:227]\n"
    "ds_read_b128 a[164:167], v15 offset:36864\n"
    "v_cvt_pk_bf16_f32 v124, v236, v237\n"
    "v_mfma_f32_16x16x32_bf16 v[228:231], a[184:187], a[68:71], v[244:247]\n"
    "ds_read_b128 a[168:171], v14 offset:37888\n"
    "v_cvt_pk_bf16_f32 v125, v238, v239\n"
    "v_mfma_f32_16x16x32_bf16 v[228:231], a[188:191], a[84:87], v[228:231]\n"
    "buffer_load_dwordx4 v11, s[56:59], s88 offen offset:0 lds\n"
    "v_mfma_f32_16x16x32_bf16 v[228:231], a[192:195], a[100:103], v[228:231]\n"
    "v_exp_f32 v224, v224\n"
    "v_mfma_f32_16x16x32_bf16 v[228:231], a[196:199], a[116:119], v[228:231]\n"
    "v_exp_f32 v225, v225\n"
    "v_mfma_f32_16x16x32_bf16 v[228:231], a[200:203], a[212:215], v[228:231]\n"
    "v_exp_f32 v226, v226\n"
    "v_mfma_f32_16x16x32_bf16 v[228:231], a[204:207], a[228:231], v[228:231]\n"
    "v_exp_f32 v227, v227\n"
    "v_mfma_f32_16x16x32_bf16 a[248:251], a[48:51], v[104:107], a[248:251]\n"
    "ds_read_b128 a[172:175], v15 offset:37888\n"
    "ds_read_b128 a[176:179], v14 offset:38912\n"
    "v_mfma_f32_16x16x32_bf16 v[232:235], a[184:187], a[72:75], v[248:251]\n"
    "v_cvt_pk_bf16_f32 v114, v224, v225\n"
    "v_cvt_pk_bf16_f32 v115, v226, v227\n"
    "v_mfma_f32_16x16x32_bf16 v[232:235], a[188:191], a[88:91], v[232:235]\n"
    "v_exp_f32 v228, v228\n"
    "v_mfma_f32_16x16x32_bf16 v[232:235], a[192:195], a[104:107], v[232:235]\n"
    "v_exp_f32 v229, v229\n"
    "v_mfma_f32_16x16x32_bf16 v[232:235], a[196:199], a[120:123], v[232:235]\n"
    "v_exp_f32 v230, v230\n"
    "v_mfma_f32_16x16x32_bf16 v[232:235], a[200:203], a[216:219], v[232:235]\n"
    "v_exp_f32 v231, v231\n"
    "v_mfma_f32_16x16x32_bf16 v[232:235], a[204:207], a[232:235], v[232:235]\n"
    "ds_read_b128 a[180:183], v15 offset:38912\n"
    "v_cvt_pk_bf16_f32 v118, v228, v229\n"
    "v_mfma_f32_16x16x32_bf16 v[236:239], a[184:187], a[76:79], v[252:255]\n"
    "buffer_load_dwordx4 v12, s[56:59], s88 offen offset:1024 lds\n"
    "v_mfma_f32_16x16x32_bf16 v[236:239], a[188:191], a[92:95], v[236:239]\n"
    "buffer_load_dwordx4 v13, s[56:59], s88 offen offset:2048 lds\n"
    "v_mfma_f32_16x16x32_bf16 v[236:239], a[192:195], a[108:111], v[236:239]\n"
    "s_add_u32 m0, s85, 32768\n"
    "v_exp_f32 v232, v232\n"
    "v_mfma_f32_16x16x32_bf16 v[236:239], a[196:199], a[124:127], v[236:239]\n"
    "s_add_u32 s88, s84, 98304\n"
    "v_exp_f32 v233, v233\n"
    "v_mfma_f32_16x16x32_bf16 v[236:239], a[200:203], a[220:223], v[236:239]\n"
    "v_exp_f32 v234, v234\n"
    "v_mfma_f32_16x16x32_bf16 v[236:239], a[204:207], a[236:239], v[236:239]\n"
    "v_exp_f32 v235, v235\n"
    "v_mfma_f32_16x16x32_bf16 a[252:255], a[48:51], v[108:111], a[252:255]\n"
    "s_waitcnt lgkmcnt(0)\n"
    "v_cvt_pk_bf16_f32 v119, v230, v231\n"
    "v_cvt_pk_bf16_f32 v122, v232, v233\n"
    "v_mfma_f32_16x16x32_bf16 v[224:227], a[160:163], a[64:67], v[240:243]\n"
    "ds_read_b128 a[184:187], v14 offset:43008\n"
    "v_cvt_pk_bf16_f32 v123, v234, v235\n"
    "v_mfma_f32_16x16x32_bf16 v[224:227], a[164:167], a[80:83], v[224:227]\n"
    "v_exp_f32 v236, v236\n"
    "v_mfma_f32_16x16x32_bf16 v[224:227], a[168:171], a[96:99], v[224:227]\n"
    "v_exp_f32 v237, v237\n"
    "v_mfma_f32_16x16x32_bf16 v[224:227], a[172:175], a[112:115], v[224:227]\n"
    "v_exp_f32 v238, v238\n"
    "v_mfma_f32_16x16x32_bf16 v[224:227], a[176:179], a[208:211], v[224:227]\n"
    "v_exp_f32 v239, v239\n"
    "v_mfma_f32_16x16x32_bf16 v[224:227], a[180:183], a[224:227], v[224:227]\n"
    "ds_read_b128 a[188:191], v15 offset:43008\n"
    "v_cvt_pk_bf16_f32 v126, v236, v237\n"
    "v_mfma_f32_16x16x32_bf16 v[228:231], a[160:163], a[68:71], v[244:247]\n"
    "ds_read_b128 a[192:195], v14 offset:44032\n"
    "v_cvt_pk_bf16_f32 v127, v238, v239\n"
    "v_mfma_f32_16x16x32_bf16 v[228:231], a[164:167], a[84:87], v[228:231]\n"
    "buffer_load_dwordx4 v1, s[60:63], s88 offen offset:0 lds\n"
    "v_mfma_f32_16x16x32_bf16 v[228:231], a[168:171], a[100:103], v[228:231]\n"
    "v_exp_f32 v224, v224\n"
    "v_mfma_f32_16x16x32_bf16 v[228:231], a[172:175], a[116:119], v[228:231]\n"
    "v_exp_f32 v225, v225\n"
    "v_mfma_f32_16x16x32_bf16 v[228:231], a[176:179], a[212:215], v[228:231]\n"
    "v_exp_f32 v226, v226\n"
    "v_mfma_f32_16x16x32_bf16 v[228:231], a[180:183], a[228:231], v[228:231]\n"
    "v_exp_f32 v227, v227\n"
    "v_mfma_f32_16x16x32_bf16 a[240:243], a[48:51], v[112:115], a[240:243]\n"
    "ds_read_b128 a[196:199], v15 offset:44032\n"
    "ds_read_b128 a[200:203], v14 offset:45056\n"
    "v_mfma_f32_16x16x32_bf16 v[232:235], a[160:163], a[72:75], v[248:251]\n"
    "v_cvt_pk_bf16_f32 v128, v224, v225\n"
    "v_cvt_pk_bf16_f32 v129, v226, v227\n"
    "v_mfma_f32_16x16x32_bf16 v[232:235], a[164:167], a[88:91], v[232:235]\n"
    "v_exp_f32 v228, v228\n"
    "v_mfma_f32_16x16x32_bf16 v[232:235], a[168:171], a[104:107], v[232:235]\n"
    "v_exp_f32 v229, v229\n"
    "v_mfma_f32_16x16x32_bf16 v[232:235], a[172:175], a[120:123], v[232:235]\n"
    "v_exp_f32 v230, v230\n"
    "v_mfma_f32_16x16x32_bf16 v[232:235], a[176:179], a[216:219], v[232:235]\n"
    "v_exp_f32 v231, v231\n"
    "v_mfma_f32_16x16x32_bf16 v[232:235], a[180:183], a[232:235], v[232:235]\n"
    "ds_read_b128 a[204:207], v15 offset:45056\n"
    "v_cvt_pk_bf16_f32 v132, v228, v229\n"
    "v_mfma_f32_16x16x32_bf16 v[236:239], a[160:163], a[76:79], v[252:255]\n"
    "buffer_load_dwordx4 v2, s[60:63], s88 offen offset:1024 lds\n"
    "v_mfma_f32_16x16x32_bf16 v[236:239], a[164:167], a[92:95], v[236:239]\n"
    "buffer_load_dwordx4 v3, s[60:63], s88 offen offset:2048 lds\n"
    "v_mfma_f32_16x16x32_bf16 v[236:239], a[168:171], a[108:111], v[236:239]\n"
    "v_exp_f32 v232, v232\n"
    "v_mfma_f32_16x16x32_bf16 v[236:239], a[172:175], a[124:127], v[236:239]\n"
    "v_exp_f32 v233, v233\n"
    "v_mfma_f32_16x16x32_bf16 v[236:239], a[176:179], a[220:223], v[236:239]\n"
    "v_exp_f32 v234, v234\n"
    "v_mfma_f32_16x16x32_bf16 v[236:239], a[180:183], a[236:239], v[236:239]\n"
    "v_exp_f32 v235, v235\n"
    "v_mfma_f32_16x16x32_bf16 a[244:247], a[48:51], v[116:119], a[244:247]\n"
    "s_waitcnt vmcnt(10) lgkmcnt(0)\n"
    "v_cvt_pk_bf16_f32 v133, v230, v231\n"
    "s_barrier\n"
    "v_mfma_f32_16x16x32_bf16 v[224:227], a[184:187], a[64:67], v[240:243]\n"
    "buffer_load_dwordx4 v4, s[60:63], s88 offen offset:3072 lds\n"
    "v_mfma_f32_16x16x32_bf16 v[224:227], a[188:191], a[80:83], v[224:227]\n"
    "s_add_u32 m0, s83, 36864\n"
    "ds_read_b128 a[160:163], v14 offset:0\n"
    "v_cvt_pk_bf16_f32 v136, v232, v233\n"
    "v_mfma_f32_16x16x32_bf16 v[224:227], a[192:195], a[96:99], v[224:227]\n"
    "v_exp_f32 v236, v236\n"
    "v_mfma_f32_16x16x32_bf16 v[224:227], a[196:199], a[112:115], v[224:227]\n"
    "v_exp_f32 v237, v237\n"
    "v_mfma_f32_16x16x32_bf16 v[224:227], a[200:203], a[208:211], v[224:227]\n"
    "s_add_u32 s88, s82, 135168\n"
    "ds_read_b128 a[164:167], v15 offset:0\n"
    "v_cvt_pk_bf16_f32 v137, v234, v235\n"
    "v_mfma_f32_16x16x32_bf16 v[224:227], a[204:207], a[224:227], v[224:227]\n"
    "v_exp_f32 v238, v238\n"
    "v_mfma_f32_16x16x32_bf16 v[228:231], a[184:187], a[68:71], v[244:247]\n"
    "v_exp_f32 v239, v239\n"
    "v_mfma_f32_16x16x32_bf16 v[228:231], a[188:191], a[84:87], v[228:231]\n"
    "ds_read_b128 a[168:171], v14 offset:1024\n"
    "v_cvt_pk_bf16_f32 v140, v236, v237\n"
    "v_mfma_f32_16x16x32_bf16 v[228:231], a[192:195], a[100:103], v[228:231]\n"
    "v_exp_f32 v224, v224\n"
    "v_mfma_f32_16x16x32_bf16 v[228:231], a[196:199], a[116:119], v[228:231]\n"
    "ds_read_b128 a[172:175], v15 offset:1024\n"
    "v_cvt_pk_bf16_f32 v141, v238, v239\n"
    "v_mfma_f32_16x16x32_bf16 v[228:231], a[200:203], a[212:215], v[228:231]\n"
    "v_exp_f32 v225, v225\n"
    "v_mfma_f32_16x16x32_bf16 v[228:231], a[204:207], a[228:231], v[228:231]\n"
    "v_exp_f32 v226, v226\n"
    "v_mfma_f32_16x16x32_bf16 a[248:251], a[48:51], v[120:123], a[248:251]\n"
    "v_exp_f32 v227, v227\n"
    "v_mfma_f32_16x16x32_bf16 v[232:235], a[184:187], a[72:75], v[248:251]\n"
    "ds_read_b128 a[176:179], v14 offset:2048\n"
    "v_cvt_pk_bf16_f32 v130, v224, v225\n"
    "v_mfma_f32_16x16x32_bf16 v[232:235], a[188:191], a[88:91], v[232:235]\n"
    "v_exp_f32 v228, v228\n"
    "v_mfma_f32_16x16x32_bf16 v[232:235], a[192:195], a[104:107], v[232:235]\n"
    "v_exp_f32 v229, v229\n"
    "v_mfma_f32_16x16x32_bf16 v[232:235], a[196:199], a[120:123], v[232:235]\n"
    "ds_read_b128 a[180:183], v15 offset:2048\n"
    "v_cvt_pk_bf16_f32 v131, v226, v227\n"
    "v_mfma_f32_16x16x32_bf16 v[232:235], a[200:203], a[216:219], v[232:235]\n"
    "v_exp_f32 v230, v230\n"
    "v_mfma_f32_16x16x32_bf16 v[232:235], a[204:207], a[232:235], v[232:235]\n"
    "v_exp_f32 v231, v231\n"
    "v_mfma_f32_16x16x32_bf16 v[236:239], a[184:187], a[76:79], v[252:255]\n"
    "v_cvt_pk_bf16_f32 v134, v228, v229\n"
    "v_cvt_pk_bf16_f32 v135, v230, v231\n"
    "v_mfma_f32_16x16x32_bf16 v[236:239], a[188:191], a[92:95], v[236:239]\n"
    "buffer_load_dwordx4 v11, s[56:59], s88 offen offset:0 lds\n"
    "v_mfma_f32_16x16x32_bf16 v[236:239], a[192:195], a[108:111], v[236:239]\n"
    "v_exp_f32 v232, v232\n"
    "v_mfma_f32_16x16x32_bf16 v[236:239], a[196:199], a[124:127], v[236:239]\n"
    "v_exp_f32 v233, v233\n"
    "v_mfma_f32_16x16x32_bf16 v[236:239], a[200:203], a[220:223], v[236:239]\n"
    "v_exp_f32 v234, v234\n"
    "v_mfma_f32_16x16x32_bf16 v[236:239], a[204:207], a[236:239], v[236:239]\n"
    "v_exp_f32 v235, v235\n"
    "v_mfma_f32_16x16x32_bf16 a[252:255], a[48:51], v[124:127], a[252:255]\n"
    "s_waitcnt lgkmcnt(0)\n"
    "v_cvt_pk_bf16_f32 v138, v232, v233\n"
    "v_cvt_pk_bf16_f32 v139, v234, v235\n"
    "v_mfma_f32_16x16x32_bf16 v[224:227], a[160:163], a[64:67], v[240:243]\n"
    "buffer_load_dwordx4 v12, s[56:59], s88 offen offset:1024 lds\n"
    "v_mfma_f32_16x16x32_bf16 v[224:227], a[164:167], a[80:83], v[224:227]\n"
    "v_exp_f32 v236, v236\n"
    "v_mfma_f32_16x16x32_bf16 v[224:227], a[168:171], a[96:99], v[224:227]\n"
    "v_exp_f32 v237, v237\n"
    "v_mfma_f32_16x16x32_bf16 v[224:227], a[172:175], a[112:115], v[224:227]\n"
    "ds_read_b128 a[184:187], v14 offset:6144\n"
    "ds_read_b128 a[188:191], v15 offset:6144\n"
    "v_mfma_f32_16x16x32_bf16 v[224:227], a[176:179], a[208:211], v[224:227]\n"
    "v_exp_f32 v238, v238\n"
    "v_mfma_f32_16x16x32_bf16 v[224:227], a[180:183], a[224:227], v[224:227]\n"
    "v_exp_f32 v239, v239\n"
    "v_mfma_f32_16x16x32_bf16 v[228:231], a[160:163], a[68:71], v[244:247]\n"
    "buffer_load_dwordx4 v13, s[56:59], s88 offen offset:2048 lds\n"
    "v_mfma_f32_16x16x32_bf16 v[228:231], a[164:167], a[84:87], v[228:231]\n"
    "ds_read_b128 a[192:195], v14 offset:7168\n"
    "v_cvt_pk_bf16_f32 v142, v236, v237\n"
    "v_mfma_f32_16x16x32_bf16 v[228:231], a[168:171], a[100:103], v[228:231]\n"
    "v_exp_f32 v224, v224\n"
    "v_mfma_f32_16x16x32_bf16 v[228:231], a[172:175], a[116:119], v[228:231]\n"
    "ds_read_b128 a[196:199], v15 offset:7168\n"
    "v_cvt_pk_bf16_f32 v143, v238, v239\n"
    "v_mfma_f32_16x16x32_bf16 v[228:231], a[176:179], a[212:215], v[228:231]\n"
    "v_exp_f32 v225, v225\n"
    "v_mfma_f32_16x16x32_bf16 v[228:231], a[180:183], a[228:231], v[228:231]\n"
    "v_exp_f32 v226, v226\n"
    "v_mfma_f32_16x16x32_bf16 a[240:243], a[48:51], v[128:131], a[240:243]\n"
    "v_exp_f32 v227, v227\n"
    "v_mfma_f32_16x16x32_bf16 v[232:235], a[160:163], a[72:75], v[248:251]\n"
    "ds_read_b128 a[200:203], v14 offset:8192\n"
    "v_cvt_pk_bf16_f32 v144, v224, v225\n"
    "v_mfma_f32_16x16x32_bf16 v[232:235], a[164:167], a[88:91], v[232:235]\n"
    "v_exp_f32 v228, v228\n"
    "v_mfma_f32_16x16x32_bf16 v[232:235], a[168:171], a[104:107], v[232:235]\n"
    "v_exp_f32 v229, v229\n"
    "v_mfma_f32_16x16x32_bf16 v[232:235], a[172:175], a[120:123], v[232:235]\n"
    "ds_read_b128 a[204:207], v15 offset:8192\n"
    "v_cvt_pk_bf16_f32 v145, v226, v227\n"
    "v_mfma_f32_16x16x32_bf16 v[232:235], a[176:179], a[216:219], v[232:235]\n"
    "v_exp_f32 v230, v230\n"
    "v_mfma_f32_16x16x32_bf16 v[232:235], a[180:183], a[232:235], v[232:235]\n"
    "v_exp_f32 v231, v231\n"
    "v_mfma_f32_16x16x32_bf16 v[236:239], a[160:163], a[76:79], v[252:255]\n"
    "v_cvt_pk_bf16_f32 v148, v228, v229\n"
    "v_cvt_pk_bf16_f32 v149, v230, v231\n"
    "v_mfma_f32_16x16x32_bf16 v[236:239], a[164:167], a[92:95], v[236:239]\n"
    "v_mfma_f32_16x16x32_bf16 v[236:239], a[168:171], a[108:111], v[236:239]\n"
    "v_exp_f32 v232, v232\n"
    "v_mfma_f32_16x16x32_bf16 v[236:239], a[172:175], a[124:127], v[236:239]\n"
    "v_exp_f32 v233, v233\n"
    "v_mfma_f32_16x16x32_bf16 v[236:239], a[176:179], a[220:223], v[236:239]\n"
    "v_exp_f32 v234, v234\n"
    "v_mfma_f32_16x16x32_bf16 v[236:239], a[180:183], a[236:239], v[236:239]\n"
    "v_exp_f32 v235, v235\n"
    "v_mfma_f32_16x16x32_bf16 a[244:247], a[48:51], v[132:135], a[244:247]\n"
    "s_waitcnt vmcnt(10) lgkmcnt(0)\n"
    "s_barrier\n"
    "v_cvt_pk_bf16_f32 v152, v232, v233\n"
    "v_mfma_f32_16x16x32_bf16 v[224:227], a[184:187], a[64:67], v[240:243]\n"
    "ds_read_b128 a[160:163], v14 offset:12288\n"
    "v_cvt_pk_bf16_f32 v153, v234, v235\n"
    "v_mfma_f32_16x16x32_bf16 v[224:227], a[188:191], a[80:83], v[224:227]\n"
    "s_add_u32 m0, s83, 0\n"
    "v_exp_f32 v236, v236\n"
    "v_mfma_f32_16x16x32_bf16 v[224:227], a[192:195], a[96:99], v[224:227]\n"
    "v_exp_f32 v237, v237\n"
    "v_mfma_f32_16x16x32_bf16 v[224:227], a[196:199], a[112:115], v[224:227]\n"
    "v_exp_f32 v238, v238\n"
    "v_mfma_f32_16x16x32_bf16 v[224:227], a[200:203], a[208:211], v[224:227]\n"
    "s_add_u32 s88, s82, 147456\n"
    "v_exp_f32 v239, v239\n"
    "v_mfma_f32_16x16x32_bf16 v[224:227], a[204:207], a[224:227], v[224:227]\n"
    "ds_read_b128 a[164:167], v15 offset:12288\n"
    "v_cvt_pk_bf16_f32 v156, v236, v237\n"
    "v_mfma_f32_16x16x32_bf16 v[228:231], a[184:187], a[68:71], v[244:247]\n"
    "ds_read_b128 a[168:171], v14 offset:13312\n"
    "v_cvt_pk_bf16_f32 v157, v238, v239\n"
    "v_mfma_f32_16x16x32_bf16 v[228:231], a[188:191], a[84:87], v[228:231]\n"
    "buffer_load_dwordx4 v11, s[56:59], s88 offen offset:0 lds\n"
    "v_mfma_f32_16x16x32_bf16 v[228:231], a[192:195], a[100:103], v[228:231]\n"
    "v_exp_f32 v224, v224\n"
    "v_mfma_f32_16x16x32_bf16 v[228:231], a[196:199], a[116:119], v[228:231]\n"
    "v_exp_f32 v225, v225\n"
    "v_mfma_f32_16x16x32_bf16 v[228:231], a[200:203], a[212:215], v[228:231]\n"
    "v_exp_f32 v226, v226\n"
    "v_mfma_f32_16x16x32_bf16 v[228:231], a[204:207], a[228:231], v[228:231]\n"
    "v_exp_f32 v227, v227\n"
    "v_mfma_f32_16x16x32_bf16 a[248:251], a[48:51], v[136:139], a[248:251]\n"
    "ds_read_b128 a[172:175], v15 offset:13312\n"
    "ds_read_b128 a[176:179], v14 offset:14336\n"
    "v_mfma_f32_16x16x32_bf16 v[232:235], a[184:187], a[72:75], v[248:251]\n"
    "v_cvt_pk_bf16_f32 v146, v224, v225\n"
    "v_cvt_pk_bf16_f32 v147, v226, v227\n"
    "v_mfma_f32_16x16x32_bf16 v[232:235], a[188:191], a[88:91], v[232:235]\n"
    "v_exp_f32 v228, v228\n"
    "v_mfma_f32_16x16x32_bf16 v[232:235], a[192:195], a[104:107], v[232:235]\n"
    "v_exp_f32 v229, v229\n"
    "v_mfma_f32_16x16x32_bf16 v[232:235], a[196:199], a[120:123], v[232:235]\n"
    "v_exp_f32 v230, v230\n"
    "v_mfma_f32_16x16x32_bf16 v[232:235], a[200:203], a[216:219], v[232:235]\n"
    "v_exp_f32 v231, v231\n"
    "v_mfma_f32_16x16x32_bf16 v[232:235], a[204:207], a[232:235], v[232:235]\n"
    "ds_read_b128 a[180:183], v15 offset:14336\n"
    "v_cvt_pk_bf16_f32 v150, v228, v229\n"
    "v_mfma_f32_16x16x32_bf16 v[236:239], a[184:187], a[76:79], v[252:255]\n"
    "buffer_load_dwordx4 v12, s[56:59], s88 offen offset:1024 lds\n"
    "v_mfma_f32_16x16x32_bf16 v[236:239], a[188:191], a[92:95], v[236:239]\n"
    "buffer_load_dwordx4 v13, s[56:59], s88 offen offset:2048 lds\n"
    "v_mfma_f32_16x16x32_bf16 v[236:239], a[192:195], a[108:111], v[236:239]\n"
    "s_lshr_b32 s94, %[bid], 3\n"
    "v_exp_f32 v232, v232\n"
    "v_mfma_f32_16x16x32_bf16 v[236:239], a[196:199], a[124:127], v[236:239]\n"
    "s_lshl2_add_u32 s94, s94, s76\n"
    "v_exp_f32 v233, v233\n"
    "v_mfma_f32_16x16x32_bf16 v[236:239], a[200:203], a[220:223], v[236:239]\n"
    "s_lshl_b32 s94, s94, 8\n"
    "v_exp_f32 v234, v234\n"
    "v_mfma_f32_16x16x32_bf16 v[236:239], a[204:207], a[236:239], v[236:239]\n"
    "v_exp_f32 v235, v235\n"
    "v_mfma_f32_16x16x32_bf16 a[252:255], a[48:51], v[140:143], a[252:255]\n"
    "s_waitcnt lgkmcnt(0)\n"
    "v_cvt_pk_bf16_f32 v151, v230, v231\n"
    "v_cvt_pk_bf16_f32 v154, v232, v233\n"
    "v_mfma_f32_16x16x32_bf16 v[224:227], a[160:163], a[64:67], v[240:243]\n"
    "ds_read_b128 a[184:187], v14 offset:18432\n"
    "v_cvt_pk_bf16_f32 v155, v234, v235\n"
    "v_mfma_f32_16x16x32_bf16 v[224:227], a[164:167], a[80:83], v[224:227]\n"
    "v_exp_f32 v236, v236\n"
    "v_mfma_f32_16x16x32_bf16 v[224:227], a[168:171], a[96:99], v[224:227]\n"
    "v_exp_f32 v237, v237\n"
    "v_mfma_f32_16x16x32_bf16 v[224:227], a[172:175], a[112:115], v[224:227]\n"
    "v_exp_f32 v238, v238\n"
    "v_mfma_f32_16x16x32_bf16 v[224:227], a[176:179], a[208:211], v[224:227]\n"
    "ds_read_b128 a[188:191], v15 offset:18432\n"
    "v_bfe_u32 v5, v0, 4, 1\n"
    "v_mfma_f32_16x16x32_bf16 v[224:227], a[180:183], a[224:227], v[224:227]\n"
    "v_exp_f32 v239, v239\n"
    "v_mfma_f32_16x16x32_bf16 v[228:231], a[160:163], a[68:71], v[244:247]\n"
    "ds_read_b128 a[192:195], v14 offset:19456\n"
    "v_cvt_pk_bf16_f32 v158, v236, v237\n"
    "v_mfma_f32_16x16x32_bf16 v[228:231], a[164:167], a[84:87], v[228:231]\n"
    "v_cvt_pk_bf16_f32 v159, v238, v239\n"
    "v_lshl_add_u32 v5, v5, 7, s94\n"
    "v_mfma_f32_16x16x32_bf16 v[228:231], a[168:171], a[100:103], v[228:231]\n"
    "v_exp_f32 v224, v224\n"
    "v_mfma_f32_16x16x32_bf16 v[228:231], a[172:175], a[116:119], v[228:231]\n"
    "v_exp_f32 v225, v225\n"
    "v_mfma_f32_16x16x32_bf16 v[228:231], a[176:179], a[212:215], v[228:231]\n"
    "ds_read_b128 a[196:199], v15 offset:19456\n"
    "v_min_u32 v5, .L_c_%=-.L_pv_%=-4, v5\n"
    "v_mfma_f32_16x16x32_bf16 v[228:231], a[180:183], a[228:231], v[228:231]\n"
    "s_getpc_b64 s[86:87]\n"
    ".L_cw2_%=:\n"
    "s_add_u32 s86, s86, .L_pv_%=-.L_cw2_%=\n"
    "s_addc_u32 s87, s87, 0\n"
    "v_exp_f32 v226, v226\n"
    "v_mfma_f32_16x16x32_bf16 a[240:243], a[48:51], v[144:147], a[240:243]\n"
    "v_exp_f32 v227, v227\n"
    "v_mfma_f32_16x16x32_bf16 v[232:235], a[160:163], a[72:75], v[248:251]\n"
    "ds_read_b128 a[200:203], v14 offset:20480\n"
    "v_cvt_pk_bf16_f32 v160, v224, v225\n"
    "v_mfma_f32_16x16x32_bf16 v[232:235], a[164:167], a[88:91], v[232:235]\n"
    "v_exp_f32 v228, v228\n"
    "v_mfma_f32_16x16x32_bf16 v[232:235], a[168:171], a[104:107], v[232:235]\n"
    "v_exp_f32 v229, v229\n"
    "v_mfma_f32_16x16x32_bf16 v[232:235], a[172:175], a[120:123], v[232:235]\n"
    "ds_read_b128 a[204:207], v15 offset:20480\n"
    "v_cvt_pk_bf16_f32 v161, v226, v227\n"
    "v_mfma_f32_16x16x32_bf16 v[232:235], a[176:179], a[216:219], v[232:235]\n"
    "v_exp_f32 v230, v230\n"
    "v_mfma_f32_16x16x32_bf16 v[232:235], a[180:183], a[232:235], v[232:235]\n"
    "v_exp_f32 v231, v231\n"
    "v_mfma_f32_16x16x32_bf16 v[236:239], a[160:163], a[76:79], v[252:255]\n"
    "v_cvt_pk_bf16_f32 v164, v228, v229\n"
    "v_cvt_pk_bf16_f32 v165, v230, v231\n"
    "v_mfma_f32_16x16x32_bf16 v[236:239], a[164:167], a[92:95], v[236:239]\n"
    "global_load_dword a56, v5, s[86:87]\n"
    "v_mfma_f32_16x16x32_bf16 v[236:239], a[168:171], a[108:111], v[236:239]\n"
    "v_exp_f32 v232, v232\n"
    "v_mfma_f32_16x16x32_bf16 v[236:239], a[172:175], a[124:127], v[236:239]\n"
    "v_exp_f32 v233, v233\n"
    "v_mfma_f32_16x16x32_bf16 v[236:239], a[176:179], a[220:223], v[236:239]\n"
    "v_exp_f32 v234, v234\n"
    "v_mfma_f32_16x16x32_bf16 v[236:239], a[180:183], a[236:239], v[236:239]\n"
    "v_exp_f32 v235, v235\n"
    "v_mfma_f32_16x16x32_bf16 a[244:247], a[48:51], v[148:151], a[244:247]\n"
    "s_waitcnt vmcnt(11) lgkmcnt(0)\n"
    "s_barrier\n"
    "v_cvt_pk_bf16_f32 v168, v232, v233\n"
    "v_mfma_f32_16x16x32_bf16 v[224:227], a[184:187], a[64:67], v[240:243]\n"
    "ds_read_b128 a[160:163], v14 offset:24576\n"
    "v_cvt_pk_bf16_f32 v169, v234, v235\n"
    "v_mfma_f32_16x16x32_bf16 v[224:227], a[188:191], a[80:83], v[224:227]\n"
    "s_add_u32 m0, s83, 12288\n"
    "v_exp_f32 v236, v236\n"
    "v_mfma_f32_16x16x32_bf16 v[224:227], a[192:195], a[96:99], v[224:227]\n"
    "v_exp_f32 v237, v237\n"
    "v_mfma_f32_16x16x32_bf16 v[224:227], a[196:199], a[112:115], v[224:227]\n"
    "v_exp_f32 v238, v238\n"
    "v_mfma_f32_16x16x32_bf16 v[224:227], a[200:203], a[208:211], v[224:227]\n"
    "s_add_u32 s88, s82, 159744\n"
    "v_exp_f32 v239, v239\n"
    "v_mfma_f32_16x16x32_bf16 v[224:227], a[204:207], a[224:227], v[224:227]\n"
    "ds_read_b128 a[164:167], v15 offset:24576\n"
    "v_cvt_pk_bf16_f32 v172, v236, v237\n"
    "v_mfma_f32_16x16x32_bf16 v[228:231], a[184:187], a[68:71], v[244:247]\n"
    "ds_read_b128 a[168:171], v14 offset:25600\n"
    "v_cvt_pk_bf16_f32 v173, v238, v239\n"
    "v_mfma_f32_16x16x32_bf16 v[228:231], a[188:191], a[84:87], v[228:231]\n"
    "buffer_load_dwordx4 v11, s[56:59], s88 offen offset:0 lds\n"
    "v_mfma_f32_16x16x32_bf16 v[228:231], a[192:195], a[100:103], v[228:231]\n"
    "v_exp_f32 v224, v224\n"
    "v_mfma_f32_16x16x32_bf16 v[228:231], a[196:199], a[116:119], v[228:231]\n"
    "v_exp_f32 v225, v225\n"
    "v_mfma_f32_16x16x32_bf16 v[228:231], a[200:203], a[212:215], v[228:231]\n"
    "v_exp_f32 v226, v226\n"
    "v_mfma_f32_16x16x32_bf16 v[228:231], a[204:207], a[228:231], v[228:231]\n"
    "v_exp_f32 v227, v227\n"
    "v_mfma_f32_16x16x32_bf16 a[248:251], a[48:51], v[152:155], a[248:251]\n"
    "ds_read_b128 a[172:175], v15 offset:25600\n"
    "ds_read_b128 a[176:179], v14 offset:26624\n"
    "v_mfma_f32_16x16x32_bf16 v[232:235], a[184:187], a[72:75], v[248:251]\n"
    "v_cvt_pk_bf16_f32 v162, v224, v225\n"
    "v_cvt_pk_bf16_f32 v163, v226, v227\n"
    "v_mfma_f32_16x16x32_bf16 v[232:235], a[188:191], a[88:91], v[232:235]\n"
    "v_exp_f32 v228, v228\n"
    "v_mfma_f32_16x16x32_bf16 v[232:235], a[192:195], a[104:107], v[232:235]\n"
    "v_exp_f32 v229, v229\n"
    "v_mfma_f32_16x16x32_bf16 v[232:235], a[196:199], a[120:123], v[232:235]\n"
    "v_exp_f32 v230, v230\n"
    "v_mfma_f32_16x16x32_bf16 v[232:235], a[200:203], a[216:219], v[232:235]\n"
    "v_exp_f32 v231, v231\n"
    "v_mfma_f32_16x16x32_bf16 v[232:235], a[204:207], a[232:235], v[232:235]\n"
    "ds_read_b128 a[180:183], v15 offset:26624\n"
    "v_cvt_pk_bf16_f32 v166, v228, v229\n"
    "v_mfma_f32_16x16x32_bf16 v[236:239], a[184:187], a[76:79], v[252:255]\n"
    "buffer_load_dwordx4 v12, s[56:59], s88 offen offset:1024 lds\n"
    "v_mfma_f32_16x16x32_bf16 v[236:239], a[188:191], a[92:95], v[236:239]\n"
    "buffer_load_dwordx4 v13, s[56:59], s88 offen offset:2048 lds\n"
    "v_mfma_f32_16x16x32_bf16 v[236:239], a[192:195], a[108:111], v[236:239]\n"
    "s_add_u32 m0, s85, 36864\n"
    "v_exp_f32 v232, v232\n"
    "v_mfma_f32_16x16x32_bf16 v[236:239], a[196:199], a[124:127], v[236:239]\n"
    "s_add_u32 s88, s84, 110592\n"
    "v_exp_f32 v233, v233\n"
    "v_mfma_f32_16x16x32_bf16 v[236:239], a[200:203], a[220:223], v[236:239]\n"
    "v_exp_f32 v234, v234\n"
    "v_mfma_f32_16x16x32_bf16 v[236:239], a[204:207], a[236:239], v[236:239]\n"
    "v_exp_f32 v235, v235\n"
    "v_mfma_f32_16x16x32_bf16 a[252:255], a[48:51], v[156:159], a[252:255]\n"
    "s_waitcnt lgkmcnt(0)\n"
    "v_cvt_pk_bf16_f32 v167, v230, v231\n"
    "v_cvt_pk_bf16_f32 v170, v232, v233\n"
    "v_mfma_f32_16x16x32_bf16 v[224:227], a[160:163], a[64:67], v[240:243]\n"
    "ds_read_b128 a[184:187], v14 offset:30720\n"
    "v_cvt_pk_bf16_f32 v171, v234, v235\n"
    "v_mfma_f32_16x16x32_bf16 v[224:227], a[164:167], a[80:83], v[224:227]\n"
    "v_exp_f32 v236, v236\n"
    "v_mfma_f32_16x16x32_bf16 v[224:227], a[168:171], a[96:99], v[224:227]\n"
    "v_exp_f32 v237, v237\n"
    "v_mfma_f32_16x16x32_bf16 v[224:227], a[172:175], a[112:115], v[224:227]\n"
    "v_exp_f32 v238, v238\n"
    "v_mfma_f32_16x16x32_bf16 v[224:227], a[176:179], a[208:211], v[224:227]\n"
    "v_exp_f32 v239, v239\n"
    "v_mfma_f32_16x16x32_bf16 v[224:227], a[180:183], a[224:227], v[224:227]\n"
    "ds_read_b128 a[188:191], v15 offset:30720\n"
    "v_cvt_pk_bf16_f32 v174, v236, v237\n"
    "v_mfma_f32_16x16x32_bf16 v[228:231], a[160:163], a[68:71], v[244:247]\n"
    "ds_read_b128 a[192:195], v14 offset:31744\n"
    "v_cvt_pk_bf16_f32 v175, v238, v239\n"
    "v_mfma_f32_16x16x32_bf16 v[228:231], a[164:167], a[84:87], v[228:231]\n"
    "buffer_load_dwordx4 v1, s[60:63], s88 offen offset:0 lds\n"
    "v_mfma_f32_16x16x32_bf16 v[228:231], a[168:171], a[100:103], v[228:231]\n"
    "v_exp_f32 v224, v224\n"
    "v_mfma_f32_16x16x32_bf16 v[228:231], a[172:175], a[116:119], v[228:231]\n"
    "v_exp_f32 v225, v225\n"
    "v_mfma_f32_16x16x32_bf16 v[228:231], a[176:179], a[212:215], v[228:231]\n"
    "v_exp_f32 v226, v226\n"
    "v_mfma_f32_16x16x32_bf16 v[228:231], a[180:183], a[228:231], v[228:231]\n"
    "v_exp_f32 v227, v227\n"
    "v_mfma_f32_16x16x32_bf16 a[240:243], a[48:51], v[160:163], a[240:243]\n"
    "ds_read_b128 a[196:199], v15 offset:31744\n"
    "ds_read_b128 a[200:203], v14 offset:32768\n"
    "v_mfma_f32_16x16x32_bf16 v[232:235], a[160:163], a[72:75], v[248:251]\n"
    "v_cvt_pk_bf16_f32 v176, v224, v225\n"
    "v_cvt_pk_bf16_f32 v177, v226, v227\n"
    "v_mfma_f32_16x16x32_bf16 v[232:235], a[164:167], a[88:91], v[232:235]\n"
    "v_exp_f32 v228, v228\n"
    "v_mfma_f32_16x16x32_bf16 v[232:235], a[168:171], a[104:107], v[232:235]\n"
    "v_exp_f32 v229, v229\n"
    "v_mfma_f32_16x16x32_bf16 v[232:235], a[172:175], a[120:123], v[232:235]\n"
    "v_exp_f32 v230, v230\n"
    "v_mfma_f32_16x16x32_bf16 v[232:235], a[176:179], a[216:219], v[232:235]\n"
    "v_exp_f32 v231, v231\n"
    "v_mfma_f32_16x16x32_bf16 v[232:235], a[180:183], a[232:235], v[232:235]\n"
    "ds_read_b128 a[204:207], v15 offset:32768\n"
    "v_cvt_pk_bf16_f32 v180, v228, v229\n"
    "v_mfma_f32_16x16x32_bf16 v[236:239], a[160:163], a[76:79], v[252:255]\n"
    "buffer_load_dwordx4 v2, s[60:63], s88 offen offset:1024 lds\n"
    "v_mfma_f32_16x16x32_bf16 v[236:239], a[164:167], a[92:95], v[236:239]\n"
    "buffer_load_dwordx4 v3, s[60:63], s88 offen offset:2048 lds\n"
    "v_mfma_f32_16x16x32_bf16 v[236:239], a[168:171], a[108:111], v[236:239]\n"
    "v_exp_f32 v232, v232\n"
    "v_mfma_f32_16x16x32_bf16 v[236:239], a[172:175], a[124:127], v[236:239]\n"
    "v_exp_f32 v233, v233\n"
    "v_mfma_f32_16x16x32_bf16 v[236:239], a[176:179], a[220:223], v[236:239]\n"
    "v_exp_f32 v234, v234\n"
    "v_mfma_f32_16x16x32_bf16 v[236:239], a[180:183], a[236:239], v[236:239]\n"
    "v_exp_f32 v235, v235\n"
    "v_mfma_f32_16x16x32_bf16 a[244:247], a[48:51], v[164:167], a[244:247]\n"
    "s_waitcnt vmcnt(10) lgkmcnt(0)\n"
    "v_cvt_pk_bf16_f32 v181, v230, v231\n"
    "s_barrier\n"
    "v_mfma_f32_16x16x32_bf16 v[224:227], a[184:187], a[64:67], v[240:243]\n"
    "buffer_load_dwordx4 v4, s[60:63], s88 offen offset:3072 lds\n"
    "v_mfma_f32_16x16x32_bf16 v[224:227], a[188:191], a[80:83], v[224:227]\n"
    "ds_read_b128 a[160:163], v14 offset:36864\n"
    "v_min_u32 v5, 624, v0\n"
    "v_mfma_f32_16x16x32_bf16 v[224:227], a[192:195], a[96:99], v[224:227]\n"
    "v_exp_f32 v236, v236\n"
    "v_mfma_f32_16x16x32_bf16 v[224:227], a[196:199], a[112:115], v[224:227]\n"
    "v_exp_f32 v237, v237\n"
    "v_mfma_f32_16x16x32_bf16 v[224:227], a[200:203], a[208:211], v[224:227]\n"
    "v_cvt_pk_bf16_f32 v184, v232, v233\n"
    "v_cvt_pk_bf16_f32 v185, v234, v235\n"
    "v_mfma_f32_16x16x32_bf16 v[224:227], a[204:207], a[224:227], v[224:227]\n"
    "v_exp_f32 v238, v238\n"
    "v_mfma_f32_16x16x32_bf16 v[228:231], a[184:187], a[68:71], v[244:247]\n"
    "ds_read_b128 a[164:167], v15 offset:36864\n"
    "v_lshlrev_b32 v5, 3, v5\n"
    "v_mfma_f32_16x16x32_bf16 v[228:231], a[188:191], a[84:87], v[228:231]\n"
    "s_add_u32 s94, s31, 0\n"
    "v_exp_f32 v239, v239\n"
    "v_mfma_f32_16x16x32_bf16 v[228:231], a[192:195], a[100:103], v[228:231]\n"
    "ds_read_b128 a[168:171], v14 offset:37888\n"
    "v_cvt_pk_bf16_f32 v188, v236, v237\n"
    "v_mfma_f32_16x16x32_bf16 v[228:231], a[196:199], a[116:119], v[228:231]\n"
    "v_exp_f32 v224, v224\n"
    "v_mfma_f32_16x16x32_bf16 v[228:231], a[200:203], a[212:215], v[228:231]\n"
    "ds_read_b128 a[172:175], v15 offset:37888\n"
    "v_cvt_pk_bf16_f32 v189, v238, v239\n"
    "v_mfma_f32_16x16x32_bf16 v[228:231], a[204:207], a[228:231], v[228:231]\n"
    "v_exp_f32 v225, v225\n"
    "v_mfma_f32_16x16x32_bf16 a[248:251], a[48:51], v[168:171], a[248:251]\n"
    "v_exp_f32 v226, v226\n"
    "v_mfma_f32_16x16x32_bf16 v[232:235], a[184:187], a[72:75], v[248:251]\n"
    "buffer_load_dword a54, v5, s[64:67], s94 offen offset:0\n"
    "v_mfma_f32_16x16x32_bf16 v[232:235], a[188:191], a[88:91], v[232:235]\n"
    "s_add_u32 s94, s31, 5120\n"
    "v_exp_f32 v227, v227\n"
    "v_mfma_f32_16x16x32_bf16 v[232:235], a[192:195], a[104:107], v[232:235]\n"
    "ds_read_b128 a[176:179], v14 offset:38912\n"
    "v_cvt_pk_bf16_f32 v178, v224, v225\n"
    "v_mfma_f32_16x16x32_bf16 v[232:235], a[196:199], a[120:123], v[232:235]\n"
    "v_exp_f32 v228, v228\n"
    "v_mfma_f32_16x16x32_bf16 v[232:235], a[200:203], a[216:219], v[232:235]\n"
    "v_exp_f32 v229, v229\n"
    "v_mfma_f32_16x16x32_bf16 v[232:235], a[204:207], a[232:235], v[232:235]\n"
    "ds_read_b128 a[180:183], v15 offset:38912\n"
    "v_cvt_pk_bf16_f32 v179, v226, v227\n"
    "v_mfma_f32_16x16x32_bf16 v[236:239], a[184:187], a[76:79], v[252:255]\n"
    "v_exp_f32 v230, v230\n"
    "v_mfma_f32_16x16x32_bf16 v[236:239], a[188:191], a[92:95], v[236:239]\n"
    "buffer_load_dword a55, v5, s[64:67], s94 offen offset:0\n"
    "v_mfma_f32_16x16x32_bf16 v[236:239], a[192:195], a[108:111], v[236:239]\n"
    "s_add_u32 m0, s83, 24576\n"
    "v_exp_f32 v231, v231\n"
    "v_mfma_f32_16x16x32_bf16 v[236:239], a[196:199], a[124:127], v[236:239]\n"
    "s_add_u32 s88, s82, 172032\n"
    "v_exp_f32 v232, v232\n"
    "v_mfma_f32_16x16x32_bf16 v[236:239], a[200:203], a[220:223], v[236:239]\n"
    "v_cvt_pk_bf16_f32 v182, v228, v229\n"
    "v_cvt_pk_bf16_f32 v183, v230, v231\n"
    "v_mfma_f32_16x16x32_bf16 v[236:239], a[204:207], a[236:239], v[236:239]\n"
    "v_exp_f32 v233, v233\n"
    "v_mfma_f32_16x16x32_bf16 a[252:255], a[48:51], v[172:175], a[252:255]\n"
    "s_waitcnt lgkmcnt(0)\n"
    "v_exp_f32 v234, v234\n"
    "v_mfma_f32_16x16x32_bf16 v[224:227], a[160:163], a[64:67], v[240:243]\n"
    "v_exp_f32 v235, v235\n"
    "v_mfma_f32_16x16x32_bf16 v[224:227], a[164:167], a[80:83], v[224:227]\n"
    "buffer_load_dwordx4 v11, s[56:59], s88 offen offset:0 lds\n"
    "v_mfma_f32_16x16x32_bf16 v[224:227], a[168:171], a[96:99], v[224:227]\n"
    "ds_read_b128 a[184:187], v14 offset:43008\n"
    "v_cvt_pk_bf16_f32 v186, v232, v233\n"
    "v_mfma_f32_16x16x32_bf16 v[224:227], a[172:175], a[112:115], v[224:227]\n"
    "v_exp_f32 v236, v236\n"
    "v_mfma_f32_16x16x32_bf16 v[224:227], a[176:179], a[208:211], v[224:227]\n"
    "v_exp_f32 v237, v237\n"
    "v_mfma_f32_16x16x32_bf16 v[224:227], a[180:183], a[224:227], v[224:227]\n"
    "ds_read_b128 a[188:191], v15 offset:43008\n"
    "v_cvt_pk_bf16_f32 v187, v234, v235\n"
    "v_mfma_f32_16x16x32_bf16 v[228:231], a[160:163], a[68:71], v[244:247]\n"
    "v_exp_f32 v238, v238\n"
    "v_mfma_f32_16x16x32_bf16 v[228:231], a[164:167], a[84:87], v[228:231]\n"
    "buffer_load_dwordx4 v12, s[56:59], s88 offen offset:1024 lds\n"
    "v_mfma_f32_16x16x32_bf16 v[228:231], a[168:171], a[100:103], v[228:231]\n"
    "v_exp_f32 v239, v239\n"
    "v_mfma_f32_16x16x32_bf16 v[228:231], a[172:175], a[116:119], v[228:231]\n"
    "buffer_load_dwordx4 v13, s[56:59], s88 offen offset:2048 lds\n"
    "v_mfma_f32_16x16x32_bf16 v[228:231], a[176:179], a[212:215], v[228:231]\n"
    "ds_read_b128 a[192:195], v14 offset:44032\n"
    "v_cvt_pk_bf16_f32 v190, v236, v237\n"
    "v_mfma_f32_16x16x32_bf16 v[228:231], a[180:183], a[228:231], v[228:231]\n"
    "v_exp_f32 v224, v224\n"
    "v_mfma_f32_16x16x32_bf16 a[240:243], a[48:51], v[176:179], a[240:243]\n"
    "ds_read_b128 a[196:199], v15 offset:44032\n"
    "v_cvt_pk_bf16_f32 v191, v238, v239\n"
    "v_mfma_f32_16x16x32_bf16 v[232:235], a[160:163], a[72:75], v[248:251]\n"
    "v_exp_f32 v225, v225\n"
    "v_mfma_f32_16x16x32_bf16 v[232:235], a[164:167], a[88:91], v[232:235]\n"
    "v_exp_f32 v226, v226\n"
    "v_mfma_f32_16x16x32_bf16 v[232:235], a[168:171], a[104:107], v[232:235]\n"
    "v_exp_f32 v227, v227\n"
    "v_mfma_f32_16x16x32_bf16 v[232:235], a[172:175], a[120:123], v[232:235]\n"
    "ds_read_b128 a[200:203], v14 offset:45056\n"
    "v_cvt_pk_bf16_f32 v192, v224, v225\n"
    "v_mfma_f32_16x16x32_bf16 v[232:235], a[176:179], a[216:219], v[232:235]\n"
    "v_exp_f32 v228, v228\n"
    "v_mfma_f32_16x16x32_bf16 v[232:235], a[180:183], a[232:235], v[232:235]\n"
    "v_exp_f32 v229, v229\n"
    "v_mfma_f32_16x16x32_bf16 v[236:239], a[160:163], a[76:79], v[252:255]\n"
    "ds_read_b128 a[204:207], v15 offset:45056\n"
    "v_cvt_pk_bf16_f32 v193, v226, v227\n"
    "v_mfma_f32_16x16x32_bf16 v[236:239], a[164:167], a[92:95], v[236:239]\n"
    "v_exp_f32 v230, v230\n"
    "v_mfma_f32_16x16x32_bf16 v[236:239], a[168:171], a[108:111], v[236:239]\n"
    "v_exp_f32 v231, v231\n"
    "v_mfma_f32_16x16x32_bf16 v[236:239], a[172:175], a[124:127], v[236:239]\n"
    "v_exp_f32 v232, v232\n"
    "v_mfma_f32_16x16x32_bf16 v[236:239], a[176:179], a[220:223], v[236:239]\n"
    "v_cvt_pk_bf16_f32 v196, v228, v229\n"
    "v_cvt_pk_bf16_f32 v197, v230, v231\n"
    "v_mfma_f32_16x16x32_bf16 v[236:239], a[180:183], a[236:239], v[236:239]\n"
    "v_exp_f32 v233, v233\n"
    "v_mfma_f32_16x16x32_bf16 a[244:247], a[48:51], v[180:183], a[244:247]\n"
    "s_waitcnt vmcnt(13) lgkmcnt(0)\n"
    "v_exp_f32 v234, v234\n"
    "s_barrier\n"
    "v_mfma_f32_16x16x32_bf16 v[224:227], a[184:187], a[64:67], v[240:243]\n"
    "v_exp_f32 v235, v235\n"
    "v_mfma_f32_16x16x32_bf16 v[224:227], a[188:191], a[80:83], v[224:227]\n"
    "s_add_u32 m0, s83, 36864\n"
    "ds_read_b128 a[160:163], v14 offset:0\n"
    "v_cvt_pk_bf16_f32 v200, v232, v233\n"
    "v_mfma_f32_16x16x32_bf16 v[224:227], a[192:195], a[96:99], v[224:227]\n"
    "v_exp_f32 v236, v236\n"
    "v_mfma_f32_16x16x32_bf16 v[224:227], a[196:199], a[112:115], v[224:227]\n"
    "v_exp_f32 v237, v237\n"
    "v_mfma_f32_16x16x32_bf16 v[224:227], a[200:203], a[208:211], v[224:227]\n"
    "s_add_u32 s88, s82, 184320\n"
    "ds_read_b128 a[164:167], v15 offset:0\n"
    "v_cvt_pk_bf16_f32 v201, v234, v235\n"
    "v_mfma_f32_16x16x32_bf16 v[224:227], a[204:207], a[224:227], v[224:227]\n"
    "v_exp_f32 v238, v238\n"
    "v_mfma_f32_16x16x32_bf16 v[228:231], a[184:187], a[68:71], v[244:247]\n"
    "v_exp_f32 v239, v239\n"
    "v_mfma_f32_16x16x32_bf16 v[228:231], a[188:191], a[84:87], v[228:231]\n"
    "ds_read_b128 a[168:171], v14 offset:1024\n"
    "v_cvt_pk_bf16_f32 v204, v236, v237\n"
    "v_mfma_f32_16x16x32_bf16 v[228:231], a[192:195], a[100:103], v[228:231]\n"
    "v_exp_f32 v224, v224\n"
    "v_mfma_f32_16x16x32_bf16 v[228:231], a[196:199], a[116:119], v[228:231]\n"
    "ds_read_b128 a[172:175], v15 offset:1024\n"
    "v_cvt_pk_bf16_f32 v205, v238, v239\n"
    "v_mfma_f32_16x16x32_bf16 v[228:231], a[200:203], a[212:215], v[228:231]\n"
    "v_exp_f32 v225, v225\n"
    "v_mfma_f32_16x16x32_bf16 v[228:231], a[204:207], a[228:231], v[228:231]\n"
    "v_exp_f32 v226, v226\n"
    "v_mfma_f32_16x16x32_bf16 a[248:251], a[48:51], v[184:187], a[248:251]\n"
    "v_exp_f32 v227, v227\n"
    "v_mfma_f32_16x16x32_bf16 v[232:235], a[184:187], a[72:75], v[248:251]\n"
    "ds_read_b128 a[176:179], v14 offset:2048\n"
    "v_cvt_pk_bf16_f32 v194, v224, v225\n"
    "v_mfma_f32_16x16x32_bf16 v[232:235], a[188:191], a[88:91], v[232:235]\n"
    "v_exp_f32 v228, v228\n"
    "v_mfma_f32_16x16x32_bf16 v[232:235], a[192:195], a[104:107], v[232:235]\n"
    "v_exp_f32 v229, v229\n"
    "v_mfma_f32_16x16x32_bf16 v[232:235], a[196:199], a[120:123], v[232:235]\n"
    "ds_read_b128 a[180:183], v15 offset:2048\n"
    "v_cvt_pk_bf16_f32 v195, v226, v227\n"
    "v_mfma_f32_16x16x32_bf16 v[232:235], a[200:203], a[216:219], v[232:235]\n"
    "v_exp_f32 v230, v230\n"
    "v_mfma_f32_16x16x32_bf16 v[232:235], a[204:207], a[232:235], v[232:235]\n"
    "v_exp_f32 v231, v231\n"
    "v_mfma_f32_16x16x32_bf16 v[236:239], a[184:187], a[76:79], v[252:255]\n"
    "v_cvt_pk_bf16_f32 v198, v228, v229\n"
    "v_cvt_pk_bf16_f32 v199, v230, v231\n"
    "v_mfma_f32_16x16x32_bf16 v[236:239], a[188:191], a[92:95], v[236:239]\n"
    "buffer_load_dwordx4 v11, s[56:59], s88 offen offset:0 lds\n"
    "v_mfma_f32_16x16x32_bf16 v[236:239], a[192:195], a[108:111], v[236:239]\n"
    "v_exp_f32 v232, v232\n"
    "v_mfma_f32_16x16x32_bf16 v[236:239], a[196:199], a[124:127], v[236:239]\n"
    "v_exp_f32 v233, v233\n"
    "v_mfma_f32_16x16x32_bf16 v[236:239], a[200:203], a[220:223], v[236:239]\n"
    "v_exp_f32 v234, v234\n"
    "v_mfma_f32_16x16x32_bf16 v[236:239], a[204:207], a[236:239], v[236:239]\n"
    "v_exp_f32 v235, v235\n"
    "v_mfma_f32_16x16x32_bf16 a[252:255], a[48:51], v[188:191], a[252:255]\n"
    "s_waitcnt lgkmcnt(0)\n"
    "v_cvt_pk_bf16_f32 v202, v232, v233\n"
    "v_cvt_pk_bf16_f32 v203, v234, v235\n"
    "v_mfma_f32_16x16x32_bf16 v[224:227], a[160:163], a[64:67], v[240:243]\n"
    "buffer_load_dwordx4 v12, s[56:59], s88 offen offset:1024 lds\n"
    "v_mfma_f32_16x16x32_bf16 v[224:227], a[164:167], a[80:83], v[224:227]\n"
    "v_exp_f32 v236, v236\n"
    "v_mfma_f32_16x16x32_bf16 v[224:227], a[168:171], a[96:99], v[224:227]\n"
    "v_exp_f32 v237, v237\n"
    "v_mfma_f32_16x16x32_bf16 v[224:227], a[172:175], a[112:115], v[224:227]\n"
    "ds_read_b128 a[184:187], v14 offset:6144\n"
    "ds_read_b128 a[188:191], v15 offset:6144\n"
    "v_mfma_f32_16x16x32_bf16 v[224:227], a[176:179], a[208:211], v[224:227]\n"
    "v_exp_f32 v238, v238\n"
    "v_mfma_f32_16x16x32_bf16 v[224:227], a[180:183], a[224:227], v[224:227]\n"
    "v_exp_f32 v239, v239\n"
    "v_mfma_f32_16x16x32_bf16 v[228:231], a[160:163], a[68:71], v[244:247]\n"
    "buffer_load_dwordx4 v13, s[56:59], s88 offen offset:2048 lds\n"
    "v_mfma_f32_16x16x32_bf16 v[228:231], a[164:167], a[84:87], v[228:231]\n"
    "ds_read_b128 a[192:195], v14 offset:7168\n"
    "v_cvt_pk_bf16_f32 v206, v236, v237\n"
    "v_mfma_f32_16x16x32_bf16 v[228:231], a[168:171], a[100:103], v[228:231]\n"
    "v_exp_f32 v224, v224\n"
    "v_mfma_f32_16x16x32_bf16 v[228:231], a[172:175], a[116:119], v[228:231]\n"
    "ds_read_b128 a[196:199], v15 offset:7168\n"
    "v_cvt_pk_bf16_f32 v207, v238, v239\n"
    "v_mfma_f32_16x16x32_bf16 v[228:231], a[176:179], a[212:215], v[228:231]\n"
    "v_exp_f32 v225, v225\n"
    "v_mfma_f32_16x16x32_bf16 v[228:231], a[180:183], a[228:231], v[228:231]\n"
    "v_exp_f32 v226, v226\n"
    "v_mfma_f32_16x16x32_bf16 a[240:243], a[48:51], v[192:195], a[240:243]\n"
    "v_exp_f32 v227, v227\n"
    "v_mfma_f32_16x16x32_bf16 v[232:235], a[160:163], a[72:75], v[248:251]\n"
    "ds_read_b128 a[200:203], v14 offset:8192\n"
    "v_cvt_pk_bf16_f32 v208, v224, v225\n"
    "v_mfma_f32_16x16x32_bf16 v[232:235], a[164:167], a[88:91], v[232:235]\n"
    "v_exp_f32 v228, v228\n"
    "v_mfma_f32_16x16x32_bf16 v[232:235], a[168:171], a[104:107], v[232:235]\n"
    "v_exp_f32 v229, v229\n"
    "v_mfma_f32_16x16x32_bf16 v[232:235], a[172:175], a[120:123], v[232:235]\n"
    "ds_read_b128 a[204:207], v15 offset:8192\n"
    "v_cvt_pk_bf16_f32 v209, v226, v227\n"
    "v_mfma_f32_16x16x32_bf16 v[232:235], a[176:179], a[216:219], v[232:235]\n"
    "v_exp_f32 v230, v230\n"
    "v_mfma_f32_16x16x32_bf16 v[232:235], a[180:183], a[232:235], v[232:235]\n"
    "v_exp_f32 v231, v231\n"
    "v_mfma_f32_16x16x32_bf16 v[236:239], a[160:163], a[76:79], v[252:255]\n"
    "v_cvt_pk_bf16_f32 v212, v228, v229\n"
    "v_cvt_pk_bf16_f32 v213, v230, v231\n"
    "v_mfma_f32_16x16x32_bf16 v[236:239], a[164:167], a[92:95], v[236:239]\n"
    "v_mfma_f32_16x16x32_bf16 v[236:239], a[168:171], a[108:111], v[236:239]\n"
    "v_exp_f32 v232, v232\n"
    "v_mfma_f32_16x16x32_bf16 v[236:239], a[172:175], a[124:127], v[236:239]\n"
    "v_exp_f32 v233, v233\n"
    "v_mfma_f32_16x16x32_bf16 v[236:239], a[176:179], a[220:223], v[236:239]\n"
    "v_exp_f32 v234, v234\n"
    "v_mfma_f32_16x16x32_bf16 v[236:239], a[180:183], a[236:239], v[236:239]\n"
    "v_exp_f32 v235, v235\n"
    "v_mfma_f32_16x16x32_bf16 a[244:247], a[48:51], v[196:199], a[244:247]\n"
    "s_waitcnt vmcnt(12) lgkmcnt(0)\n"
    "s_barrier\n"
    "v_cvt_pk_bf16_f32 v216, v232, v233\n"
    "v_mfma_f32_16x16x32_bf16 v[224:227], a[184:187], a[64:67], v[240:243]\n"
    "ds_read_b128 a[160:163], v14 offset:12288\n"
    "v_cvt_pk_bf16_f32 v217, v234, v235\n"
    "v_mfma_f32_16x16x32_bf16 v[224:227], a[188:191], a[80:83], v[224:227]\n"
    "v_exp_f32 v236, v236\n"
    "v_mfma_f32_16x16x32_bf16 v[224:227], a[192:195], a[96:99], v[224:227]\n"
    "v_exp_f32 v237, v237\n"
    "v_mfma_f32_16x16x32_bf16 v[224:227], a[196:199], a[112:115], v[224:227]\n"
    "v_exp_f32 v238, v238\n"
    "v_mfma_f32_16x16x32_bf16 v[224:227], a[200:203], a[208:211], v[224:227]\n"
    "v_exp_f32 v239, v239\n"
    "v_mfma_f32_16x16x32_bf16 v[224:227], a[204:207], a[224:227], v[224:227]\n"
    "ds_read_b128 a[164:167], v15 offset:12288\n"
    "v_cvt_pk_bf16_f32 v220, v236, v237\n"
    "v_mfma_f32_16x16x32_bf16 v[228:231], a[184:187], a[68:71], v[244:247]\n"
    "ds_read_b128 a[168:171], v14 offset:13312\n"
    "v_cvt_pk_bf16_f32 v221, v238, v239\n"
    "v_mfma_f32_16x16x32_bf16 v[228:231], a[188:191], a[84:87], v[228:231]\n"
    "v_mfma_f32_16x16x32_bf16 v[228:231], a[192:195], a[100:103], v[228:231]\n"
    "v_exp_f32 v224, v224\n"
    "v_mfma_f32_16x16x32_bf16 v[228:231], a[196:199], a[116:119], v[228:231]\n"
    "v_exp_f32 v225, v225\n"
    "v_mfma_f32_16x16x32_bf16 v[228:231], a[200:203], a[212:215], v[228:231]\n"
    "v_exp_f32 v226, v226\n"
    "v_mfma_f32_16x16x32_bf16 v[228:231], a[204:207], a[228:231], v[228:231]\n"
    "v_exp_f32 v227, v227\n"
    "v_mfma_f32_16x16x32_bf16 a[248:251], a[48:51], v[200:203], a[248:251]\n"
    "ds_read_b128 a[172:175], v15 offset:13312\n"
    "ds_read_b128 a[176:179], v14 offset:14336\n"
    "v_mfma_f32_16x16x32_bf16 v[232:235], a[184:187], a[72:75], v[248:251]\n"
    "v_cvt_pk_bf16_f32 v210, v224, v225\n"
    "v_cvt_pk_bf16_f32 v211, v226, v227\n"
    "v_mfma_f32_16x16x32_bf16 v[232:235], a[188:191], a[88:91], v[232:235]\n"
    "v_exp_f32 v228, v228\n"
    "v_mfma_f32_16x16x32_bf16 v[232:235], a[192:195], a[104:107], v[232:235]\n"
    "v_exp_f32 v229, v229\n"
    "v_mfma_f32_16x16x32_bf16 v[232:235], a[196:199], a[120:123], v[232:235]\n"
    "v_exp_f32 v230, v230\n"
    "v_mfma_f32_16x16x32_bf16 v[232:235], a[200:203], a[216:219], v[232:235]\n"
    "v_exp_f32 v231, v231\n"
    "v_mfma_f32_16x16x32_bf16 v[232:235], a[204:207], a[232:235], v[232:235]\n"
    "ds_read_b128 a[180:183], v15 offset:14336\n"
    "v_cvt_pk_bf16_f32 v214, v228, v229\n"
    "v_mfma_f32_16x16x32_bf16 v[236:239], a[184:187], a[76:79], v[252:255]\n"
    "v_cvt_pk_bf16_f32 v215, v230, v231\n"
    "v_mfma_f32_16x16x32_bf16 v[236:239], a[188:191], a[92:95], v[236:239]\n"
    "v_mfma_f32_16x16x32_bf16 v[236:239], a[192:195], a[108:111], v[236:239]\n"
    "v_exp_f32 v232, v232\n"
    "v_mfma_f32_16x16x32_bf16 v[236:239], a[196:199], a[124:127], v[236:239]\n"
    "v_exp_f32 v233, v233\n"
    "v_mfma_f32_16x16x32_bf16 v[236:239], a[200:203], a[220:223], v[236:239]\n"
    "v_exp_f32 v234, v234\n"
    "v_mfma_f32_16x16x32_bf16 v[236:239], a[204:207], a[236:239], v[236:239]\n"
    "v_exp_f32 v235, v235\n"
    "v_mfma_f32_16x16x32_bf16 a[252:255], a[48:51], v[204:207], a[252:255]\n"
    "s_waitcnt lgkmcnt(0)\n"
    "v_cvt_pk_bf16_f32 v218, v232, v233\n"
    "v_cvt_pk_bf16_f32 v219, v234, v235\n"
    "v_mfma_f32_16x16x32_bf16 v[224:227], a[160:163], a[64:67], v[240:243]\n"
    "ds_read_b128 a[184:187], v14 offset:18432\n"
    "v_mfma_f32_16x16x32_bf16 v[224:227], a[164:167], a[80:83], v[224:227]\n"
    "v_exp_f32 v236, v236\n"
    "v_mfma_f32_16x16x32_bf16 v[224:227], a[168:171], a[96:99], v[224:227]\n"
    "v_exp_f32 v237, v237\n"
    "v_mfma_f32_16x16x32_bf16 v[224:227], a[172:175], a[112:115], v[224:227]\n"
    "v_exp_f32 v238, v238\n"
    "v_mfma_f32_16x16x32_bf16 v[224:227], a[176:179], a[208:211], v[224:227]\n"
    "v_exp_f32 v239, v239\n"
    "v_mfma_f32_16x16x32_bf16 v[224:227], a[180:183], a[224:227], v[224:227]\n"
    "ds_read_b128 a[188:191], v15 offset:18432\n"
    "v_cvt_pk_bf16_f32 v222, v236, v237\n"
    "v_mfma_f32_16x16x32_bf16 v[228:231], a[160:163], a[68:71], v[244:247]\n"
    "ds_read_b128 a[192:195], v14 offset:19456\n"
    "v_cvt_pk_bf16_f32 v223, v238, v239\n"
    "v_mfma_f32_16x16x32_bf16 v[228:231], a[164:167], a[84:87], v[228:231]\n"
    "v_mfma_f32_16x16x32_bf16 v[228:231], a[168:171], a[100:103], v[228:231]\n"
    "v_exp_f32 v224, v224\n"
    "v_mfma_f32_16x16x32_bf16 v[228:231], a[172:175], a[116:119], v[228:231]\n"
    "v_exp_f32 v225, v225\n"
    "v_mfma_f32_16x16x32_bf16 v[228:231], a[176:179], a[212:215], v[228:231]\n"
    "v_exp_f32 v226, v226\n"
    "v_mfma_f32_16x16x32_bf16 v[228:231], a[180:183], a[228:231], v[228:231]\n"
    "v_exp_f32 v227, v227\n"
    "v_mfma_f32_16x16x32_bf16 a[240:243], a[48:51], v[208:211], a[240:243]\n"
    "ds_read_b128 a[196:199], v15 offset:19456\n"
    "ds_read_b128 a[200:203], v14 offset:20480\n"
    "v_mfma_f32_16x16x32_bf16 v[232:235], a[160:163], a[72:75], v[248:251]\n"
    "v_cvt_pk_bf16_f32 v224, v224, v225\n"
    "v_cvt_pk_bf16_f32 v226, v226, v227\n"
    "v_mfma_f32_16x16x32_bf16 v[232:235], a[164:167], a[88:91], v[232:235]\n"
    "v_exp_f32 v228, v228\n"
    "v_mfma_f32_16x16x32_bf16 v[232:235], a[168:171], a[104:107], v[232:235]\n"
    "v_exp_f32 v229, v229\n"
    "v_mfma_f32_16x16x32_bf16 v[232:235], a[172:175], a[120:123], v[232:235]\n"
    "ds_read_b128 a[204:207], v15 offset:20480\n"
    "v_accvgpr_write_b32 a0, v224\n"
    "v_mfma_f32_16x16x32_bf16 v[232:235], a[176:179], a[216:219], v[232:235]\n"
    "v_exp_f32 v230, v230\n"
    "v_mfma_f32_16x16x32_bf16 v[232:235], a[180:183], a[232:235], v[232:235]\n"
    "v_exp_f32 v231, v231\n"
    "v_mfma_f32_16x16x32_bf16 v[236:239], a[160:163], a[76:79], v[252:255]\n"
    "v_accvgpr_write_b32 a1, v226\n"
    "v_cvt_pk_bf16_f32 v228, v228, v229\n"
    "v_mfma_f32_16x16x32_bf16 v[236:239], a[164:167], a[92:95], v[236:239]\n"
    "v_cvt_pk_bf16_f32 v230, v230, v231\n"
    "v_accvgpr_write_b32 a4, v228\n"
    "v_mfma_f32_16x16x32_bf16 v[236:239], a[168:171], a[108:111], v[236:239]\n"
    "v_exp_f32 v232, v232\n"
    "v_mfma_f32_16x16x32_bf16 v[236:239], a[172:175], a[124:127], v[236:239]\n"
    "v_exp_f32 v233, v233\n"
    "v_mfma_f32_16x16x32_bf16 v[236:239], a[176:179], a[220:223], v[236:239]\n"
    "v_exp_f32 v234, v234\n"
    "v_mfma_f32_16x16x32_bf16 v[236:239], a[180:183], a[236:239], v[236:239]\n"
    "v_exp_f32 v235, v235\n"
    "v_mfma_f32_16x16x32_bf16 a[244:247], a[48:51], v[212:215], a[244:247]\n"
    "s_waitcnt vmcnt(3) lgkmcnt(0)\n"
    "s_barrier\n"
    "v_accvgpr_write_b32 a5, v230\n"
    "v_mfma_f32_16x16x32_bf16 v[224:227], a[184:187], a[64:67], v[240:243]\n"
    "ds_read_b128 a[160:163], v14 offset:24576\n"
    "v_cvt_pk_bf16_f32 v232, v232, v233\n"
    "v_mfma_f32_16x16x32_bf16 v[224:227], a[188:191], a[80:83], v[224:227]\n"
    "v_exp_f32 v236, v236\n"
    "v_mfma_f32_16x16x32_bf16 v[224:227], a[192:195], a[96:99], v[224:227]\n"
    "v_exp_f32 v237, v237\n"
    "v_mfma_f32_16x16x32_bf16 v[224:227], a[196:199], a[112:115], v[224:227]\n"
    "ds_read_b128 a[164:167], v15 offset:24576\n"
    "v_cvt_pk_bf16_f32 v234, v234, v235\n"
    "v_mfma_f32_16x16x32_bf16 v[224:227], a[200:203], a[208:211], v[224:227]\n"
    "v_exp_f32 v238, v238\n"
    "v_mfma_f32_16x16x32_bf16 v[224:227], a[204:207], a[224:227], v[224:227]\n"
    "v_exp_f32 v239, v239\n"
    "v_mfma_f32_16x16x32_bf16 v[228:231], a[184:187], a[68:71], v[244:247]\n"
    "v_accvgpr_write_b32 a8, v232\n"
    "v_accvgpr_write_b32 a9, v234\n"
    "v_mfma_f32_16x16x32_bf16 v[228:231], a[188:191], a[84:87], v[228:231]\n"
    "ds_read_b128 a[168:171], v14 offset:25600\n"
    "v_cvt_pk_bf16_f32 v236, v236, v237\n"
    "v_mfma_f32_16x16x32_bf16 v[228:231], a[192:195], a[100:103], v[228:231]\n"
    "v_exp_f32 v224, v224\n"
    "v_mfma_f32_16x16x32_bf16 v[228:231], a[196:199], a[116:119], v[228:231]\n"
    "ds_read_b128 a[172:175], v15 offset:25600\n"
    "v_cvt_pk_bf16_f32 v238, v238, v239\n"
    "v_mfma_f32_16x16x32_bf16 v[228:231], a[200:203], a[212:215], v[228:231]\n"
    "v_exp_f32 v225, v225\n"
    "v_mfma_f32_16x16x32_bf16 v[228:231], a[204:207], a[228:231], v[228:231]\n"
    "v_exp_f32 v226, v226\n"
    "v_mfma_f32_16x16x32_bf16 a[248:251], a[48:51], v[216:219], a[248:251]\n"
    "v_accvgpr_write_b32 a12, v236\n"
    "v_accvgpr_write_b32 a13, v238\n"
    "v_mfma_f32_16x16x32_bf16 v[232:235], a[184:187], a[72:75], v[248:251]\n"
    "v_exp_f32 v227, v227\n"
    "v_mfma_f32_16x16x32_bf16 v[232:235], a[188:191], a[88:91], v[232:235]\n"
    "ds_read_b128 a[176:179], v14 offset:26624\n"
    "v_cvt_pk_bf16_f32 v224, v224, v225\n"
    "v_mfma_f32_16x16x32_bf16 v[232:235], a[192:195], a[104:107], v[232:235]\n"
    "v_exp_f32 v228, v228\n"
    "v_mfma_f32_16x16x32_bf16 v[232:235], a[196:199], a[120:123], v[232:235]\n"
    "v_exp_f32 v229, v229\n"
    "v_mfma_f32_16x16x32_bf16 v[232:235], a[200:203], a[216:219], v[232:235]\n"
    "ds_read_b128 a[180:183], v15 offset:26624\n"
    "v_cvt_pk_bf16_f32 v226, v226, v227\n"
    "v_mfma_f32_16x16x32_bf16 v[232:235], a[204:207], a[232:235], v[232:235]\n"
    "v_exp_f32 v230, v230\n"
    "v_mfma_f32_16x16x32_bf16 v[236:239], a[184:187], a[76:79], v[252:255]\n"
    "v_exp_f32 v231, v231\n"
    "v_mfma_f32_16x16x32_bf16 v[236:239], a[188:191], a[92:95], v[236:239]\n"
    "v_accvgpr_write_b32 a2, v224\n"
    "v_accvgpr_write_b32 a3, v226\n"
    "v_mfma_f32_16x16x32_bf16 v[236:239], a[192:195], a[108:111], v[236:239]\n"
    "v_exp_f32 v232, v232\n"
    "v_mfma_f32_16x16x32_bf16 v[236:239], a[196:199], a[124:127], v[236:239]\n"
    "v_cvt_pk_bf16_f32 v228, v228, v229\n"
    "v_cvt_pk_bf16_f32 v230, v230, v231\n"
    "v_mfma_f32_16x16x32_bf16 v[236:239], a[200:203], a[220:223], v[236:239]\n"
    "v_exp_f32 v233, v233\n"
    "v_mfma_f32_16x16x32_bf16 v[236:239], a[204:207], a[236:239], v[236:239]\n"
    "v_exp_f32 v234, v234\n"
    "v_mfma_f32_16x16x32_bf16 a[252:255], a[48:51], v[220:223], a[252:255]\n"
    "s_waitcnt lgkmcnt(0)\n"
    "v_accvgpr_write_b32 a6, v228\n"
    "v_accvgpr_write_b32 a7, v230\n"
    "v_mfma_f32_16x16x32_bf16 v[224:227], a[160:163], a[64:67], v[240:243]\n"
    "v_exp_f32 v235, v235\n"
    "v_mfma_f32_16x16x32_bf16 v[224:227], a[164:167], a[80:83], v[224:227]\n"
    "ds_read_b128 a[184:187], v14 offset:30720\n"
    "v_cvt_pk_bf16_f32 v232, v232, v233\n"
    "v_mfma_f32_16x16x32_bf16 v[224:227], a[168:171], a[96:99], v[224:227]\n"
    "v_exp_f32 v236, v236\n"
    "v_mfma_f32_16x16x32_bf16 v[224:227], a[172:175], a[112:115], v[224:227]\n"
    "v_exp_f32 v237, v237\n"
    "v_mfma_f32_16x16x32_bf16 v[224:227], a[176:179], a[208:211], v[224:227]\n"
    "ds_read_b128 a[188:191], v15 offset:30720\n"
    "v_cvt_pk_bf16_f32 v234, v234, v235\n"
    "v_mfma_f32_16x16x32_bf16 v[224:227], a[180:183], a[224:227], v[224:227]\n"
    "v_exp_f32 v238, v238\n"
    "v_mfma_f32_16x16x32_bf16 v[228:231], a[160:163], a[68:71], v[244:247]\n"
    "v_exp_f32 v239, v239\n"
    "v_mfma_f32_16x16x32_bf16 v[228:231], a[164:167], a[84:87], v[228:231]\n"
    "v_accvgpr_write_b32 a10, v232\n"
    "v_accvgpr_write_b32 a11, v234\n"
    "v_mfma_f32_16x16x32_bf16 v[228:231], a[168:171], a[100:103], v[228:231]\n"
    "ds_read_b128 a[192:195], v14 offset:31744\n"
    "v_cvt_pk_bf16_f32 v236, v236, v237\n"
    "v_mfma_f32_16x16x32_bf16 v[228:231], a[172:175], a[116:119], v[228:231]\n"
    "v_exp_f32 v224, v224\n"
    "v_mfma_f32_16x16x32_bf16 v[228:231], a[176:179], a[212:215], v[228:231]\n"
    "ds_read_b128 a[196:199], v15 offset:31744\n"
    "v_cvt_pk_bf16_f32 v238, v238, v239\n"
    "v_mfma_f32_16x16x32_bf16 v[228:231], a[180:183], a[228:231], v[228:231]\n"
    "v_exp_f32 v225, v225\n"
    "v_mfma_f32_16x16x32_bf16 a[240:243], a[48:51], a[0:3], a[240:243]\n"
    "v_exp_f32 v226, v226\n"
    "v_mfma_f32_16x16x32_bf16 v[232:235], a[160:163], a[72:75], v[248:251]\n"
    "v_accvgpr_write_b32 a14, v236\n"
    "v_accvgpr_write_b32 a15, v238\n"
    "v_mfma_f32_16x16x32_bf16 v[232:235], a[164:167], a[88:91], v[232:235]\n"
    "v_exp_f32 v227, v227\n"
    "v_mfma_f32_16x16x32_bf16 v[232:235], a[168:171], a[104:107], v[232:235]\n"
    "ds_read_b128 a[200:203], v14 offset:32768\n"
    "v_cvt_pk_bf16_f32 v224, v224, v225\n"
    "v_mfma_f32_16x16x32_bf16 v[232:235], a[172:175], a[120:123], v[232:235]\n"
    "v_exp_f32 v228, v228\n"
    "v_mfma_f32_16x16x32_bf16 v[232:235], a[176:179], a[216:219], v[232:235]\n"
    "v_exp_f32 v229, v229\n"
    "v_mfma_f32_16x16x32_bf16 v[232:235], a[180:183], a[232:235], v[232:235]\n"
    "ds_read_b128 a[204:207], v15 offset:32768\n"
    "v_cvt_pk_bf16_f32 v226, v226, v227\n"
    "v_mfma_f32_16x16x32_bf16 v[236:239], a[160:163], a[76:79], v[252:255]\n"
    "v_exp_f32 v230, v230\n"
    "v_mfma_f32_16x16x32_bf16 v[236:239], a[164:167], a[92:95], v[236:239]\n"
    "v_exp_f32 v231, v231\n"
    "v_mfma_f32_16x16x32_bf16 v[236:239], a[168:171], a[108:111], v[236:239]\n"
    "v_accvgpr_write_b32 a16, v224\n"
    "v_accvgpr_write_b32 a17, v226\n"
    "v_mfma_f32_16x16x32_bf16 v[236:239], a[172:175], a[124:127], v[236:239]\n"
    "v_exp_f32 v232, v232\n"
    "v_mfma_f32_16x16x32_bf16 v[236:239], a[176:179], a[220:223], v[236:239]\n"
    "v_cvt_pk_bf16_f32 v228, v228, v229\n"
    "v_cvt_pk_bf16_f32 v230, v230, v231\n"
    "v_mfma_f32_16x16x32_bf16 v[236:239], a[180:183], a[236:239], v[236:239]\n"
    "v_exp_f32 v233, v233\n"
    "v_mfma_f32_16x16x32_bf16 a[244:247], a[48:51], a[4:7], a[244:247]\n"
    "s_waitcnt vmcnt(0) lgkmcnt(0)\n"
    "v_exp_f32 v234, v234\n"
    "s_barrier\n"
    "v_mfma_f32_16x16x32_bf16 v[224:227], a[184:187], a[64:67], v[240:243]\n"
    "v_accvgpr_write_b32 a20, v228\n"
    "v_accvgpr_write_b32 a21, v230\n"
    "v_mfma_f32_16x16x32_bf16 v[224:227], a[188:191], a[80:83], v[224:227]\n"
    "v_exp_f32 v235, v235\n"
    "v_mfma_f32_16x16x32_bf16 v[224:227], a[192:195], a[96:99], v[224:227]\n"
    "ds_read_b128 a[160:163], v14 offset:36864\n"
    "v_cvt_pk_bf16_f32 v232, v232, v233\n"
    "v_mfma_f32_16x16x32_bf16 v[224:227], a[196:199], a[112:115], v[224:227]\n"
    "v_exp_f32 v236, v236\n"
    "v_mfma_f32_16x16x32_bf16 v[224:227], a[200:203], a[208:211], v[224:227]\n"
    "v_exp_f32 v237, v237\n"
    "v_mfma_f32_16x16x32_bf16 v[224:227], a[204:207], a[224:227], v[224:227]\n"
    "ds_read_b128 a[164:167], v15 offset:36864\n"
    "v_cvt_pk_bf16_f32 v234, v234, v235\n"
    "v_mfma_f32_16x16x32_bf16 v[228:231], a[184:187], a[68:71], v[244:247]\n"
    "v_exp_f32 v238, v238\n"
    "v_mfma_f32_16x16x32_bf16 v[228:231], a[188:191], a[84:87], v[228:231]\n"
    "v_exp_f32 v239, v239\n"
    "v_mfma_f32_16x16x32_bf16 v[228:231], a[192:195], a[100:103], v[228:231]\n"
    "v_accvgpr_write_b32 a24, v232\n"
    "v_accvgpr_write_b32 a25, v234\n"
    "v_mfma_f32_16x16x32_bf16 v[228:231], a[196:199], a[116:119], v[228:231]\n"
    "ds_read_b128 a[168:171], v14 offset:37888\n"
    "v_cvt_pk_bf16_f32 v236, v236, v237\n"
    "v_mfma_f32_16x16x32_bf16 v[228:231], a[200:203], a[212:215], v[228:231]\n"
    "v_exp_f32 v224, v224\n"
    "v_mfma_f32_16x16x32_bf16 v[228:231], a[204:207], a[228:231], v[228:231]\n"
    "ds_read_b128 a[172:175], v15 offset:37888\n"
    "v_cvt_pk_bf16_f32 v238, v238, v239\n"
    "v_mfma_f32_16x16x32_bf16 a[248:251], a[48:51], a[8:11], a[248:251]\n"
    "v_exp_f32 v225, v225\n"
    "v_mfma_f32_16x16x32_bf16 v[232:235], a[184:187], a[72:75], v[248:251]\n"
    "v_exp_f32 v226, v226\n"
    "v_mfma_f32_16x16x32_bf16 v[232:235], a[188:191], a[88:91], v[232:235]\n"
    "v_accvgpr_write_b32 a28, v236\n"
    "v_accvgpr_write_b32 a29, v238\n"
    "v_mfma_f32_16x16x32_bf16 v[232:235], a[192:195], a[104:107], v[232:235]\n"
    "v_exp_f32 v227, v227\n"
    "v_mfma_f32_16x16x32_bf16 v[232:235], a[196:199], a[120:123], v[232:235]\n"
    "ds_read_b128 a[176:179], v14 offset:38912\n"
    "v_cvt_pk_bf16_f32 v224, v224, v225\n"
    "v_mfma_f32_16x16x32_bf16 v[232:235], a[200:203], a[216:219], v[232:235]\n"
    "v_exp_f32 v228, v228\n"
    "v_mfma_f32_16x16x32_bf16 v[232:235], a[204:207], a[232:235], v[232:235]\n"
    "v_exp_f32 v229, v229\n"
    "v_mfma_f32_16x16x32_bf16 v[236:239], a[184:187], a[76:79], v[252:255]\n"
    "ds_read_b128 a[180:183], v15 offset:38912\n"
    "v_cvt_pk_bf16_f32 v226, v226, v227\n"
    "v_mfma_f32_16x16x32_bf16 v[236:239], a[188:191], a[92:95], v[236:239]\n"
    "v_exp_f32 v230, v230\n"
    "v_mfma_f32_16x16x32_bf16 v[236:239], a[192:195], a[108:111], v[236:239]\n"
    "v_exp_f32 v231, v231\n"
    "v_accvgpr_write_b32 a18, v224\n"
    "v_accvgpr_write_b32 a19, v226\n"
    "v_mfma_f32_16x16x32_bf16 v[236:239], a[196:199], a[124:127], v[236:239]\n"
    "v_exp_f32 v232, v232\n"
    "v_mfma_f32_16x16x32_bf16 v[236:239], a[200:203], a[220:223], v[236:239]\n"
    "v_cvt_pk_bf16_f32 v228, v228, v229\n"
    "v_cvt_pk_bf16_f32 v230, v230, v231\n"
    "v_mfma_f32_16x16x32_bf16 v[236:239], a[204:207], a[236:239], v[236:239]\n"
    "v_exp_f32 v233, v233\n"
    "v_mfma_f32_16x16x32_bf16 a[252:255], a[48:51], a[12:15], a[252:255]\n"
    "s_waitcnt lgkmcnt(0)\n"
    "v_exp_f32 v234, v234\n"
    "v_mfma_f32_16x16x32_bf16 v[224:227], a[160:163], a[64:67], v[240:243]\n"
    "v_accvgpr_write_b32 a22, v228\n"
    "v_accvgpr_write_b32 a23, v230\n"
    "v_mfma_f32_16x16x32_bf16 v[224:227], a[164:167], a[80:83], v[224:227]\n"
    "v_exp_f32 v235, v235\n"
    "v_mfma_f32_16x16x32_bf16 v[224:227], a[168:171], a[96:99], v[224:227]\n"
    "ds_read_b128 a[184:187], v14 offset:43008\n"
    "v_cvt_pk_bf16_f32 v232, v232, v233\n"
    "v_mfma_f32_16x16x32_bf16 v[224:227], a[172:175], a[112:115], v[224:227]\n"
    "v_exp_f32 v236, v236\n"
    "v_mfma_f32_16x16x32_bf16 v[224:227], a[176:179], a[208:211], v[224:227]\n"
    "v_exp_f32 v237, v237\n"
    "v_mfma_f32_16x16x32_bf16 v[224:227], a[180:183], a[224:227], v[224:227]\n"
    "ds_read_b128 a[188:191], v15 offset:43008\n"
    "v_cvt_pk_bf16_f32 v234, v234, v235\n"
    "v_mfma_f32_16x16x32_bf16 v[228:231], a[160:163], a[68:71], v[244:247]\n"
    "v_exp_f32 v238, v238\n"
    "v_mfma_f32_16x16x32_bf16 v[228:231], a[164:167], a[84:87], v[228:231]\n"
    "v_exp_f32 v239, v239\n"
    "v_mfma_f32_16x16x32_bf16 v[228:231], a[168:171], a[100:103], v[228:231]\n"
    "v_accvgpr_write_b32 a26, v232\n"
    "v_accvgpr_write_b32 a27, v234\n"
    "v_mfma_f32_16x16x32_bf16 v[228:231], a[172:175], a[116:119], v[228:231]\n"
    "ds_read_b128 a[192:195], v14 offset:44032\n"
    "v_cvt_pk_bf16_f32 v236, v236, v237\n"
    "v_mfma_f32_16x16x32_bf16 v[228:231], a[176:179], a[212:215], v[228:231]\n"
    "v_exp_f32 v224, v224\n"
    "v_mfma_f32_16x16x32_bf16 v[228:231], a[180:183], a[228:231], v[228:231]\n"
    "ds_read_b128 a[196:199], v15 offset:44032\n"
    "v_cvt_pk_bf16_f32 v238, v238, v239\n"
    "v_mfma_f32_16x16x32_bf16 a[240:243], a[48:51], a[16:19], a[240:243]\n"
    "v_exp_f32 v225, v225\n"
    "v_mfma_f32_16x16x32_bf16 v[232:235], a[160:163], a[72:75], v[248:251]\n"
    "v_exp_f32 v226, v226\n"
    "v_mfma_f32_16x16x32_bf16 v[232:235], a[164:167], a[88:91], v[232:235]\n"
    "v_accvgpr_write_b32 a30, v236\n"
    "v_accvgpr_write_b32 a31, v238\n"
    "v_mfma_f32_16x16x32_bf16 v[232:235], a[168:171], a[104:107], v[232:235]\n"
    "v_exp_f32 v227, v227\n"
    "v_mfma_f32_16x16x32_bf16 v[232:235], a[172:175], a[120:123], v[232:235]\n"
    "ds_read_b128 a[200:203], v14 offset:45056\n"
    "v_cvt_pk_bf16_f32 v224, v224, v225\n"
    "v_mfma_f32_16x16x32_bf16 v[232:235], a[176:179], a[216:219], v[232:235]\n"
    "v_exp_f32 v228, v228\n"
    "v_mfma_f32_16x16x32_bf16 v[232:235], a[180:183], a[232:235], v[232:235]\n"
    "v_exp_f32 v229, v229\n"
    "v_mfma_f32_16x16x32_bf16 v[236:239], a[160:163], a[76:79], v[252:255]\n"
    "ds_read_b128 a[204:207], v15 offset:45056\n"
    "v_cvt_pk_bf16_f32 v226, v226, v227\n"
    "v_mfma_f32_16x16x32_bf16 v[236:239], a[164:167], a[92:95], v[236:239]\n"
    "v_exp_f32 v230, v230\n"
    "v_mfma_f32_16x16x32_bf16 v[236:239], a[168:171], a[108:111], v[236:239]\n"
    "v_exp_f32 v231, v231\n"
    "v_accvgpr_write_b32 a32, v224\n"
    "v_accvgpr_write_b32 a33, v226\n"
    "v_mfma_f32_16x16x32_bf16 v[236:239], a[172:175], a[124:127], v[236:239]\n"
    "v_exp_f32 v232, v232\n"
    "v_mfma_f32_16x16x32_bf16 v[236:239], a[176:179], a[220:223], v[236:239]\n"
    "v_cvt_pk_bf16_f32 v228, v228, v229\n"
    "v_cvt_pk_bf16_f32 v230, v230, v231\n"
    "v_mfma_f32_16x16x32_bf16 v[236:239], a[180:183], a[236:239], v[236:239]\n"
    "v_exp_f32 v233, v233\n"
    "v_mfma_f32_16x16x32_bf16 a[244:247], a[48:51], a[20:23], a[244:247]\n"
    "s_waitcnt lgkmcnt(0)\n"
    "v_exp_f32 v234, v234\n"
    "s_barrier\n"
    "v_mfma_f32_16x16x32_bf16 v[224:227], a[184:187], a[64:67], v[240:243]\n"
    "v_accvgpr_write_b32 a36, v228\n"
    "v_accvgpr_write_b32 a37, v230\n"
    "v_mfma_f32_16x16x32_bf16 v[224:227], a[188:191], a[80:83], v[224:227]\n"
    "v_exp_f32 v235, v235\n"
    "v_mfma_f32_16x16x32_bf16 v[224:227], a[192:195], a[96:99], v[224:227]\n"
    "v_exp_f32 v236, v236\n"
    "v_mfma_f32_16x16x32_bf16 v[224:227], a[196:199], a[112:115], v[224:227]\n"
    "v_exp_f32 v237, v237\n"
    "v_mfma_f32_16x16x32_bf16 v[224:227], a[200:203], a[208:211], v[224:227]\n"
    "v_cvt_pk_bf16_f32 v232, v232, v233\n"
    "v_cvt_pk_bf16_f32 v234, v234, v235\n"
    "v_mfma_f32_16x16x32_bf16 v[224:227], a[204:207], a[224:227], v[224:227]\n"
    "ds_read_b64_tr_b16 a[128:129], v8 offset:0\n"
    "ds_read_b64_tr_b16 a[130:131], v8 offset:2048\n"
    "v_mfma_f32_16x16x32_bf16 v[228:231], a[184:187], a[68:71], v[244:247]\n"
    "v_exp_f32 v238, v238\n"
    "v_mfma_f32_16x16x32_bf16 v[228:231], a[188:191], a[84:87], v[228:231]\n"
    "v_exp_f32 v239, v239\n"
    "v_mfma_f32_16x16x32_bf16 v[228:231], a[192:195], a[100:103], v[228:231]\n"
    "v_accvgpr_write_b32 a40, v232\n"
    "v_accvgpr_write_b32 a41, v234\n"
    "ds_read_b64_tr_b16 a[132:133], v8 offset:4096\n"
    "v_mfma_f32_16x16x32_bf16 v[228:231], a[196:199], a[116:119], v[228:231]\n"
    "ds_read_b64_tr_b16 a[134:135], v8 offset:6144\n"
    "ds_read_b64_tr_b16 a[136:137], v8 offset:8192\n"
    "v_mfma_f32_16x16x32_bf16 v[228:231], a[200:203], a[212:215], v[228:231]\n"
    "ds_read_b64_tr_b16 a[138:139], v8 offset:10240\n"
    "v_cvt_pk_bf16_f32 v236, v236, v237\n"
    "v_mfma_f32_16x16x32_bf16 v[228:231], a[204:207], a[228:231], v[228:231]\n"
    "v_exp_f32 v224, v224\n"
    "v_mfma_f32_16x16x32_bf16 a[248:251], a[48:51], a[24:27], a[248:251]\n"
    "ds_read_b64_tr_b16 a[140:141], v8 offset:12288\n"
    "v_cvt_pk_bf16_f32 v238, v238, v239\n"
    "v_mfma_f32_16x16x32_bf16 v[232:235], a[184:187], a[72:75], v[248:251]\n"
    "v_exp_f32 v225, v225\n"
    "ds_read_b64_tr_b16 a[142:143], v8 offset:14336\n"
    "v_mfma_f32_16x16x32_bf16 v[232:235], a[188:191], a[88:91], v[232:235]\n"
    "v_exp_f32 v226, v226\n"
    "v_accvgpr_write_b32 a44, v236\n"
    "v_accvgpr_write_b32 a45, v238\n"
    "ds_read_b64_tr_b16 a[144:145], v8 offset:16384\n"
    "v_mfma_f32_16x16x32_bf16 v[232:235], a[192:195], a[104:107], v[232:235]\n"
    "v_exp_f32 v227, v227\n"
    "ds_read_b64_tr_b16 a[146:147], v8 offset:18432\n"
    "v_mfma_f32_16x16x32_bf16 v[232:235], a[196:199], a[120:123], v[232:235]\n"
    "ds_read_b64_tr_b16 a[148:149], v8 offset:20480\n"
    "ds_read_b64_tr_b16 a[150:151], v8 offset:22528\n"
    "v_mfma_f32_16x16x32_bf16 v[232:235], a[200:203], a[216:219], v[232:235]\n"
    "v_exp_f32 v228, v228\n"
    "v_mfma_f32_16x16x32_bf16 v[232:235], a[204:207], a[232:235], v[232:235]\n"
    "ds_read_b64_tr_b16 a[152:153], v8 offset:24576\n"
    "v_cvt_pk_bf16_f32 v224, v224, v225\n"
    "v_mfma_f32_16x16x32_bf16 v[236:239], a[184:187], a[76:79], v[252:255]\n"
    "v_exp_f32 v229, v229\n"
    "v_cvt_pk_bf16_f32 v226, v226, v227\n"
    "ds_read_b64_tr_b16 a[154:155], v8 offset:26624\n"
    "v_mfma_f32_16x16x32_bf16 v[236:239], a[188:191], a[92:95], v[236:239]\n"
    "v_exp_f32 v230, v230\n"
    "ds_read_b64_tr_b16 a[156:157], v8 offset:28672\n"
    "v_mfma_f32_16x16x32_bf16 v[236:239], a[192:195], a[108:111], v[236:239]\n"
    "v_exp_f32 v231, v231\n"
    "v_accvgpr_write_b32 a34, v224\n"
    "v_accvgpr_write_b32 a35, v226\n"
    "ds_read_b64_tr_b16 a[158:159], v8 offset:30720\n"
    "v_mfma_f32_16x16x32_bf16 v[236:239], a[196:199], a[124:127], v[236:239]\n"
    "v_exp_f32 v232, v232\n"
    "v_mfma_f32_16x16x32_bf16 v[236:239], a[200:203], a[220:223], v[236:239]\n"
    "v_cvt_pk_bf16_f32 v228, v228, v229\n"
    "v_cvt_pk_bf16_f32 v230, v230, v231\n"
    "v_mfma_f32_16x16x32_bf16 v[236:239], a[204:207], a[236:239], v[236:239]\n"
    "v_exp_f32 v233, v233\n"
    "v_mfma_f32_16x16x32_bf16 a[252:255], a[48:51], a[28:31], a[252:255]\n"
    "v_exp_f32 v234, v234\n"
    ".L_pv_%=:\n"
    "s_waitcnt lgkmcnt(8)\n"
    "v_mfma_f32_16x16x32_bf16 a[160:163], a[128:131], v[16:19], 0\n"
    "v_accvgpr_write_b32 a38, v228\n"
    "v_accvgpr_write_b32 a39, v230\n"
    "v_mfma_f32_16x16x32_bf16 a[160:163], a[132:135], v[32:35], a[160:163]\n"
    "v_exp_f32 v235, v235\n"
    "v_mfma_f32_16x16x32_bf16 a[160:163], a[136:139], v[48:51], a[160:163]\n"
    "v_exp_f32 v236, v236\n"
    "v_mfma_f32_16x16x32_bf16 a[160:163], a[140:143], v[64:67], a[160:163]\n"
    "s_waitcnt lgkmcnt(0)\n"
    "v_exp_f32 v237, v237\n"
    "v_mfma_f32_16x16x32_bf16 a[160:163], a[144:147], v[80:83], a[160:163]\n"
    "s_add_u32 m0, s85, 65536\n"
    "v_cvt_pk_bf16_f32 v232, v232, v233\n"
    "v_cvt_pk_bf16_f32 v234, v234, v235\n"
    "v_mfma_f32_16x16x32_bf16 a[160:163], a[148:151], v[96:99], a[160:163]\n"
    "ds_read_b64_tr_b16 a[64:65], v9 offset:0\n"
    "ds_read_b64_tr_b16 a[66:67], v9 offset:2048\n"
    "v_mfma_f32_16x16x32_bf16 a[160:163], a[152:155], v[112:115], a[160:163]\n"
    "v_exp_f32 v238, v238\n"
    "v_mfma_f32_16x16x32_bf16 a[160:163], a[156:159], v[128:131], a[160:163]\n"
    "v_exp_f32 v239, v239\n"
    "v_mfma_f32_16x16x32_bf16 a[164:167], a[128:131], v[20:23], 0\n"
    "s_add_u32 s88, s84, 128\n"
    "v_accvgpr_write_b32 a42, v232\n"
    "v_accvgpr_write_b32 a43, v234\n"
    "v_mfma_f32_16x16x32_bf16 a[164:167], a[132:135], v[36:39], a[164:167]\n"
    "ds_read_b64_tr_b16 a[68:69], v9 offset:4096\n"
    "ds_read_b64_tr_b16 a[70:71], v9 offset:6144\n"
    "v_mfma_f32_16x16x32_bf16 a[164:167], a[136:139], v[52:55], a[164:167]\n"
    "v_cvt_pk_bf16_f32 v236, v236, v237\n"
    "v_cvt_pk_bf16_f32 v238, v238, v239\n"
    "v_mfma_f32_16x16x32_bf16 a[164:167], a[140:143], v[68:71], a[164:167]\n"
    "ds_read_b64_tr_b16 a[72:73], v9 offset:8192\n"
    "ds_read_b64_tr_b16 a[74:75], v9 offset:10240\n"
    "v_mfma_f32_16x16x32_bf16 a[164:167], a[144:147], v[84:87], a[164:167]\n"
    "v_accvgpr_write_b32 a46, v236\n"
    "v_accvgpr_write_b32 a47, v238\n"
    "v_mfma_f32_16x16x32_bf16 a[164:167], a[148:151], v[100:103], a[164:167]\n"
    "buffer_load_dwordx4 v1, s[60:63], s88 offen offset:0 lds\n"
    "v_mfma_f32_16x16x32_bf16 a[164:167], a[152:155], v[116:119], a[164:167]\n"
    "ds_read_b64_tr_b16 a[76:77], v9 offset:12288\n"
    "ds_read_b64_tr_b16 a[78:79], v9 offset:14336\n"
    "v_mfma_f32_16x16x32_bf16 a[164:167], a[156:159], v[132:135], a[164:167]\n"
    "v_mfma_f32_16x16x32_bf16 a[168:171], a[128:131], v[24:27], 0\n"
    "buffer_load_dwordx4 v2, s[60:63], s88 offen offset:1024 lds\n"
    "v_mfma_f32_16x16x32_bf16 a[168:171], a[132:135], v[40:43], a[168:171]\n"
    "ds_read_b64_tr_b16 a[80:81], v9 offset:16384\n"
    "ds_read_b64_tr_b16 a[82:83], v9 offset:18432\n"
    "v_mfma_f32_16x16x32_bf16 a[168:171], a[136:139], v[56:59], a[168:171]\n"
    "v_mfma_f32_16x16x32_bf16 a[168:171], a[140:143], v[72:75], a[168:171]\n"
    "ds_read_b64_tr_b16 a[84:85], v9 offset:20480\n"
    "v_mfma_f32_16x16x32_bf16 a[168:171], a[144:147], v[88:91], a[168:171]\n"
    "buffer_load_dwordx4 v3, s[60:63], s88 offen offset:2048 lds\n"
    "v_mfma_f32_16x16x32_bf16 a[168:171], a[148:151], v[104:107], a[168:171]\n"
    "ds_read_b64_tr_b16 a[86:87], v9 offset:22528\n"
    "v_mfma_f32_16x16x32_bf16 a[168:171], a[152:155], v[120:123], a[168:171]\n"
    "ds_read_b64_tr_b16 a[88:89], v9 offset:24576\n"
    "v_mfma_f32_16x16x32_bf16 a[168:171], a[156:159], v[136:139], a[168:171]\n"
    "ds_read_b64_tr_b16 a[90:91], v9 offset:26624\n"
    "v_mfma_f32_16x16x32_bf16 a[172:175], a[128:131], v[28:31], 0\n"
    "buffer_load_dwordx4 v4, s[60:63], s88 offen offset:3072 lds\n"
    "v_mfma_f32_16x16x32_bf16 a[172:175], a[132:135], v[44:47], a[172:175]\n"
    "ds_read_b64_tr_b16 a[92:93], v9 offset:28672\n"
    "v_mfma_f32_16x16x32_bf16 a[172:175], a[136:139], v[60:63], a[172:175]\n"
    "ds_read_b64_tr_b16 a[94:95], v9 offset:30720\n"
    "v_mfma_f32_16x16x32_bf16 a[172:175], a[140:143], v[76:79], a[172:175]\n"
    "v_mfma_f32_16x16x32_bf16 a[172:175], a[144:147], v[92:95], a[172:175]\n"
    "s_add_u32 m0, s85, 69632\n"
    "v_mfma_f32_16x16x32_bf16 a[172:175], a[148:151], v[108:111], a[172:175]\n"
    "v_mfma_f32_16x16x32_bf16 a[172:175], a[152:155], v[124:127], a[172:175]\n"
    "v_mfma_f32_16x16x32_bf16 a[172:175], a[156:159], v[140:143], a[172:175]\n"
    "s_waitcnt lgkmcnt(8)\n"
    "v_mfma_f32_16x16x32_bf16 a[176:179], a[64:67], v[16:19], 0\n"
    "s_add_u32 s88, s84, 12416\n"
    "v_mfma_f32_16x16x32_bf16 a[176:179], a[68:71], v[32:35], a[176:179]\n"
    "v_mfma_f32_16x16x32_bf16 a[176:179], a[72:75], v[48:51], a[176:179]\n"
    "v_mfma_f32_16x16x32_bf16 a[176:179], a[76:79], v[64:67], a[176:179]\n"
    "s_waitcnt lgkmcnt(0)\n"
    "v_mfma_f32_16x16x32_bf16 a[176:179], a[80:83], v[80:83], a[176:179]\n"
    "buffer_load_dwordx4 v1, s[60:63], s88 offen offset:0 lds\n"
    "v_mfma_f32_16x16x32_bf16 a[176:179], a[84:87], v[96:99], a[176:179]\n"
    "ds_read_b64_tr_b16 a[128:129], v8 offset:32768\n"
    "ds_read_b64_tr_b16 a[130:131], v8 offset:34816\n"
    "v_mfma_f32_16x16x32_bf16 a[176:179], a[88:91], v[112:115], a[176:179]\n"
    "v_mfma_f32_16x16x32_bf16 a[176:179], a[92:95], v[128:131], a[176:179]\n"
    "ds_read_b64_tr_b16 a[132:133], v8 offset:36864\n"
    "v_mfma_f32_16x16x32_bf16 a[240:243], a[48:51], a[32:35], a[240:243]\n"
    "buffer_load_dwordx4 v2, s[60:63], s88 offen offset:1024 lds\n"
    "v_mfma_f32_16x16x32_bf16 a[180:183], a[64:67], v[20:23], 0\n"
    "ds_read_b64_tr_b16 a[134:135], v8 offset:38912\n"
    "v_mfma_f32_16x16x32_bf16 a[180:183], a[68:71], v[36:39], a[180:183]\n"
    "ds_read_b64_tr_b16 a[136:137], v8 offset:40960\n"
    "v_mfma_f32_16x16x32_bf16 a[180:183], a[72:75], v[52:55], a[180:183]\n"
    "ds_read_b64_tr_b16 a[138:139], v8 offset:43008\n"
    "v_mfma_f32_16x16x32_bf16 a[180:183], a[76:79], v[68:71], a[180:183]\n"
    "buffer_load_dwordx4 v3, s[60:63], s88 offen offset:2048 lds\n"
    "v_mfma_f32_16x16x32_bf16 a[180:183], a[80:83], v[84:87], a[180:183]\n"
    "ds_read_b64_tr_b16 a[140:141], v8 offset:45056\n"
    "v_mfma_f32_16x16x32_bf16 a[180:183], a[84:87], v[100:103], a[180:183]\n"
    "ds_read_b64_tr_b16 a[142:143], v8 offset:47104\n"
    "v_mfma_f32_16x16x32_bf16 a[180:183], a[88:91], v[116:119], a[180:183]\n"
    "v_mfma_f32_16x16x32_bf16 a[180:183], a[92:95], v[132:135], a[180:183]\n"
    "buffer_load_dwordx4 v4, s[60:63], s88 offen offset:3072 lds\n"
    "v_mfma_f32_16x16x32_bf16 a[244:247], a[48:51], a[36:39], a[244:247]\n"
    "ds_read_b64_tr_b16 a[144:145], v8 offset:49152\n"
    "ds_read_b64_tr_b16 a[146:147], v8 offset:51200\n"
    "v_mfma_f32_16x16x32_bf16 a[184:187], a[64:67], v[24:27], 0\n"
    "v_mfma_f32_16x16x32_bf16 a[184:187], a[68:71], v[40:43], a[184:187]\n"
    "ds_read_b64_tr_b16 a[148:149], v8 offset:53248\n"
    "v_mfma_f32_16x16x32_bf16 a[184:187], a[72:75], v[56:59], a[184:187]\n"
    "s_add_u32 m0, s85, 98304\n"
    "ds_read_b64_tr_b16 a[150:151], v8 offset:55296\n"
    "v_mfma_f32_16x16x32_bf16 a[184:187], a[76:79], v[72:75], a[184:187]\n"
    "v_mfma_f32_16x16x32_bf16 a[184:187], a[80:83], v[88:91], a[184:187]\n"
    "ds_read_b64_tr_b16 a[152:153], v8 offset:57344\n"
    "v_mfma_f32_16x16x32_bf16 a[184:187], a[84:87], v[104:107], a[184:187]\n"
    "ds_read_b64_tr_b16 a[154:155], v8 offset:59392\n"
    "v_mfma_f32_16x16x32_bf16 a[184:187], a[88:91], v[120:123], a[184:187]\n"
    "s_add_u32 s88, s84, 98432\n"
    "v_mfma_f32_16x16x32_bf16 a[184:187], a[92:95], v[136:139], a[184:187]\n"
    "ds_read_b64_tr_b16 a[156:157], v8 offset:61440\n"
    "v_mfma_f32_16x16x32_bf16 a[248:251], a[48:51], a[40:43], a[248:251]\n"
    "ds_read_b64_tr_b16 a[158:159], v8 offset:63488\n"
    "v_mfma_f32_16x16x32_bf16 a[188:191], a[64:67], v[28:31], 0\n"
    "v_mfma_f32_16x16x32_bf16 a[188:191], a[68:71], v[44:47], a[188:191]\n"
    "buffer_load_dwordx4 v1, s[60:63], s88 offen offset:0 lds\n"
    "v_mfma_f32_16x16x32_bf16 a[188:191], a[72:75], v[60:63], a[188:191]\n"
    "v_mfma_f32_16x16x32_bf16 a[188:191], a[76:79], v[76:79], a[188:191]\n"
    "v_mfma_f32_16x16x32_bf16 a[188:191], a[80:83], v[92:95], a[188:191]\n"
    "v_mfma_f32_16x16x32_bf16 a[188:191], a[84:87], v[108:111], a[188:191]\n"
    "buffer_load_dwordx4 v2, s[60:63], s88 offen offset:1024 lds\n"
    "v_mfma_f32_16x16x32_bf16 a[188:191], a[88:91], v[124:127], a[188:191]\n"
    "v_mfma_f32_16x16x32_bf16 a[188:191], a[92:95], v[140:143], a[188:191]\n"
    "v_mfma_f32_16x16x32_bf16 a[252:255], a[48:51], a[44:47], a[252:255]\n"
    "s_waitcnt lgkmcnt(8)\n"
    "v_mfma_f32_16x16x32_bf16 a[160:163], a[128:131], v[144:147], a[160:163]\n"
    "buffer_load_dwordx4 v3, s[60:63], s88 offen offset:2048 lds\n"
    "v_mfma_f32_16x16x32_bf16 a[160:163], a[132:135], v[160:163], a[160:163]\n"
    "v_mfma_f32_16x16x32_bf16 a[160:163], a[136:139], v[176:179], a[160:163]\n"
    "v_mfma_f32_16x16x32_bf16 a[160:163], a[140:143], v[192:195], a[160:163]\n"
    "s_waitcnt lgkmcnt(0)\n"
    "v_mfma_f32_16x16x32_bf16 a[160:163], a[144:147], v[208:211], a[160:163]\n"
    "ds_read_b64_tr_b16 a[64:65], v9 offset:32768\n"
    "v_accvgpr_read_b32 v240, a240\n"
    "v_mfma_f32_16x16x32_bf16 a[160:163], a[148:151], a[0:3], a[160:163]\n"
    "ds_read_b64_tr_b16 a[66:67], v9 offset:34816\n"
    "v_accvgpr_read_b32 v241, a244\n"
    "v_mfma_f32_16x16x32_bf16 a[160:163], a[152:155], a[16:19], a[160:163]\n"
    "buffer_load_dwordx4 v4, s[60:63], s88 offen offset:3072 lds\n"
    "v_mfma_f32_16x16x32_bf16 a[160:163], a[156:159], a[32:35], a[160:163]\n"
    "ds_read_b64_tr_b16 a[68:69], v9 offset:36864\n"
    "v_accvgpr_read_b32 v242, a248\n"
    "v_mfma_f32_16x16x32_bf16 a[164:167], a[128:131], v[148:151], a[164:167]\n"
    "s_add_u32 m0, s85, 102400\n"
    "ds_read_b64_tr_b16 a[70:71], v9 offset:38912\n"
    "v_accvgpr_read_b32 v243, a252\n"
    "v_mfma_f32_16x16x32_bf16 a[164:167], a[132:135], v[164:167], a[164:167]\n"
    "v_max3_f32 v244, v240, v241, v242\n"
    "v_min3_f32 v245, v240, v241, v242\n"
    "v_mfma_f32_16x16x32_bf16 a[164:167], a[136:139], v[180:183], a[164:167]\n"
    "ds_read_b64_tr_b16 a[72:73], v9 offset:40960\n"
    "v_cmp_u_f32 s[86:87], v240, v241\n"
    "v_mfma_f32_16x16x32_bf16 a[164:167], a[140:143], v[196:199], a[164:167]\n"
    "ds_read_b64_tr_b16 a[74:75], v9 offset:43008\n"
    "v_cmp_u_f32 vcc, v242, v243\n"
    "v_mfma_f32_16x16x32_bf16 a[164:167], a[144:147], v[212:215], a[164:167]\n"
    "s_add_u32 s88, s84, 110720\n"
    "v_max_f32 v244, v244, v243\n"
    "v_mfma_f32_16x16x32_bf16 a[164:167], a[148:151], a[4:7], a[164:167]\n"
    "ds_read_b64_tr_b16 a[76:77], v9 offset:45056\n"
    "v_min_f32 v245, v245, v243\n"
    "v_mfma_f32_16x16x32_bf16 a[164:167], a[152:155], a[20:23], a[164:167]\n"
    "s_or_b64 s[86:87], s[86:87], vcc\n"
    "ds_read_b64_tr_b16 a[78:79], v9 offset:47104\n"
    "v_mfma_f32_16x16x32_bf16 a[164:167], a[156:159], a[36:39], a[164:167]\n"
    "v_cmp_lt_f32 vcc, 0x57800000, v244\n"
    "v_mfma_f32_16x16x32_bf16 a[168:171], a[128:131], v[152:155], a[168:171]\n"
    "buffer_load_dwordx4 v1, s[60:63], s88 offen offset:0 lds\n"
    "v_mfma_f32_16x16x32_bf16 a[168:171], a[132:135], v[168:171], a[168:171]\n"
    "ds_read_b64_tr_b16 a[80:81], v9 offset:49152\n"
    "ds_read_b64_tr_b16 a[82:83], v9 offset:51200\n"
    "v_mfma_f32_16x16x32_bf16 a[168:171], a[136:139], v[184:187], a[168:171]\n"
    "v_rcp_f32 v240, v240\n"
    "v_mfma_f32_16x16x32_bf16 a[168:171], a[140:143], v[200:203], a[168:171]\n"
    "s_or_b64 s[86:87], s[86:87], vcc\n"
    "ds_read_b64_tr_b16 a[84:85], v9 offset:53248\n"
    "v_cmp_gt_f32 vcc, 0x27800000, v245\n"
    "v_mfma_f32_16x16x32_bf16 a[168:171], a[144:147], v[216:219], a[168:171]\n"
    "buffer_load_dwordx4 v2, s[60:63], s88 offen offset:1024 lds\n"
    "v_mfma_f32_16x16x32_bf16 a[168:171], a[148:151], a[8:11], a[168:171]\n"
    "v_rcp_f32 v241, v241\n"
    "v_mfma_f32_16x16x32_bf16 a[168:171], a[152:155], a[24:27], a[168:171]\n"
    "s_or_b64 s[86:87], s[86:87], vcc\n"
    "ds_read_b64_tr_b16 a[86:87], v9 offset:55296\n"
    "ds_read_b64_tr_b16 a[88:89], v9 offset:57344\n"
    "v_mfma_f32_16x16x32_bf16 a[168:171], a[156:159], a[40:43], a[168:171]\n"
    "s_or_b32 s86, s86, s87\n"
    "ds_read_b64_tr_b16 a[90:91], v9 offset:59392\n"
    "s_lshl2_add_u32 s87, s76, 163776\n"
    "v_mfma_f32_16x16x32_bf16 a[172:175], a[128:131], v[156:159], a[172:175]\n"
    "buffer_load_dwordx4 v3, s[60:63], s88 offen offset:2048 lds\n"
    "v_mfma_f32_16x16x32_bf16 a[172:175], a[132:135], v[172:175], a[172:175]\n"
    "ds_read_b64_tr_b16 a[92:93], v9 offset:61440\n"
    "v_mov_b32 v244, s86\n"
    "v_mfma_f32_16x16x32_bf16 a[172:175], a[136:139], v[188:191], a[172:175]\n"
    "ds_read_b64_tr_b16 a[94:95], v9 offset:63488\n"
    "v_mov_b32 v245, s87\n"
    "v_mfma_f32_16x16x32_bf16 a[172:175], a[140:143], v[204:207], a[172:175]\n"
    "v_rcp_f32 v242, v242\n"
    "v_mfma_f32_16x16x32_bf16 a[172:175], a[144:147], v[220:223], a[172:175]\n"
    "buffer_load_dwordx4 v4, s[60:63], s88 offen offset:3072 lds\n"
    "v_mfma_f32_16x16x32_bf16 a[172:175], a[148:151], a[12:15], a[172:175]\n"
    "ds_write_b32 v245, v244 offset:0\n"
    "v_mfma_f32_16x16x32_bf16 a[172:175], a[152:155], a[28:31], a[172:175]\n"
    "v_rcp_f32 v243, v243\n"
    "v_mfma_f32_16x16x32_bf16 a[172:175], a[156:159], a[44:47], a[172:175]\n"
    "s_waitcnt lgkmcnt(9)\n"
    "v_mfma_f32_16x16x32_bf16 a[176:179], a[64:67], v[144:147], a[176:179]\n"
    "v_add_u32 v11, 65536, v8\n"
    "v_add_u32 v12, 65536, v9\n"
    "v_mfma_f32_16x16x32_bf16 a[176:179], a[68:71], v[160:163], a[176:179]\n"
    "v_add_u32 v13, 65536, v10\n"
    "v_add_u32 v14, 65536, v6\n"
    "v_mfma_f32_16x16x32_bf16 a[176:179], a[72:75], v[176:179], a[176:179]\n"
    "v_mfma_f32_16x16x32_bf16 a[176:179], a[76:79], v[192:195], a[176:179]\n"
    "s_waitcnt lgkmcnt(1)\n"
    "v_mfma_f32_16x16x32_bf16 a[176:179], a[80:83], v[208:211], a[176:179]\n"
    "ds_read_b64_tr_b16 a[128:129], v10 offset:0\n"
    "v_mfma_f32_16x16x32_bf16 a[176:179], a[84:87], a[0:3], a[176:179]\n"
    "ds_read_b64_tr_b16 a[130:131], v10 offset:2048\n"
    "v_mfma_f32_16x16x32_bf16 a[176:179], a[88:91], a[16:19], a[176:179]\n"
    "v_mfma_f32_16x16x32_bf16 a[176:179], a[92:95], a[32:35], a[176:179]\n"
    "ds_read_b64_tr_b16 a[132:133], v10 offset:4096\n"
    "v_mfma_f32_16x16x32_bf16 a[180:183], a[64:67], v[148:151], a[180:183]\n"
    "ds_read_b64_tr_b16 a[134:135], v10 offset:6144\n"
    "v_mfma_f32_16x16x32_bf16 a[180:183], a[68:71], v[164:167], a[180:183]\n"
    "v_mfma_f32_16x16x32_bf16 a[180:183], a[72:75], v[180:183], a[180:183]\n"
    "ds_read_b64_tr_b16 a[136:137], v10 offset:8192\n"
    "v_mfma_f32_16x16x32_bf16 a[180:183], a[76:79], v[196:199], a[180:183]\n"
    "ds_read_b64_tr_b16 a[138:139], v10 offset:10240\n"
    "v_mfma_f32_16x16x32_bf16 a[180:183], a[80:83], v[212:215], a[180:183]\n"
    "v_mfma_f32_16x16x32_bf16 a[180:183], a[84:87], a[4:7], a[180:183]\n"
    "ds_read_b64_tr_b16 a[140:141], v10 offset:12288\n"
    "v_mfma_f32_16x16x32_bf16 a[180:183], a[88:91], a[20:23], a[180:183]\n"
    "ds_read_b64_tr_b16 a[142:143], v10 offset:14336\n"
    "v_mfma_f32_16x16x32_bf16 a[180:183], a[92:95], a[36:39], a[180:183]\n"
    "v_mfma_f32_16x16x32_bf16 a[184:187], a[64:67], v[152:155], a[184:187]\n"
    "ds_read_b64_tr_b16 a[144:145], v10 offset:16384\n"
    "v_mfma_f32_16x16x32_bf16 a[184:187], a[68:71], v[168:171], a[184:187]\n"
    "ds_read_b64_tr_b16 a[146:147], v10 offset:18432\n"
    "v_mfma_f32_16x16x32_bf16 a[184:187], a[72:75], v[184:187], a[184:187]\n"
    "v_mfma_f32_16x16x32_bf16 a[184:187], a[76:79], v[200:203], a[184:187]\n"
    "ds_read_b64_tr_b16 a[148:149], v10 offset:20480\n"
    "v_mfma_f32_16x16x32_bf16 a[184:187], a[80:83], v[216:219], a[184:187]\n"
    "ds_read_b64_tr_b16 a[150:151], v10 offset:22528\n"
    "v_mfma_f32_16x16x32_bf16 a[184:187], a[84:87], a[8:11], a[184:187]\n"
    "v_mfma_f32_16x16x32_bf16 a[184:187], a[88:91], a[24:27], a[184:187]\n"
    "ds_read_b64_tr_b16 a[152:153], v10 offset:24576\n"
    "v_mfma_f32_16x16x32_bf16 a[184:187], a[92:95], a[40:43], a[184:187]\n"
    "ds_read_b64_tr_b16 a[154:155], v10 offset:26624\n"
    "v_mfma_f32_16x16x32_bf16 a[188:191], a[64:67], v[156:159], a[188:191]\n"
    "v_mfma_f32_16x16x32_bf16 a[188:191], a[68:71], v[172:175], a[188:191]\n"
    "ds_read_b64_tr_b16 a[156:157], v10 offset:28672\n"
    "v_mfma_f32_16x16x32_bf16 a[188:191], a[72:75], v[188:191], a[188:191]\n"
    "ds_read_b64_tr_b16 a[158:159], v10 offset:30720\n"
    "v_mfma_f32_16x16x32_bf16 a[188:191], a[76:79], v[204:207], a[188:191]\n"
    "v_mfma_f32_16x16x32_bf16 a[188:191], a[80:83], v[220:223], a[188:191]\n"
    "v_mfma_f32_16x16x32_bf16 a[188:191], a[84:87], a[12:15], a[188:191]\n"
    "v_mfma_f32_16x16x32_bf16 a[188:191], a[88:91], a[28:31], a[188:191]\n"
    "v_mfma_f32_16x16x32_bf16 a[188:191], a[92:95], a[44:47], a[188:191]\n"
    "s_waitcnt lgkmcnt(8)\n"
    "v_mfma_f32_16x16x32_bf16 a[96:99], a[128:131], v[16:19], 0\n"
    "v_accvgpr_read_b32 v224, a160\n"
    "v_accvgpr_read_b32 v225, a161\n"
    "v_mfma_f32_16x16x32_bf16 a[96:99], a[132:135], v[32:35], a[96:99]\n"
    "v_accvgpr_read_b32 v226, a162\n"
    "v_accvgpr_read_b32 v227, a163\n"
    "v_mfma_f32_16x16x32_bf16 a[96:99], a[136:139], v[48:51], a[96:99]\n"
    "v_accvgpr_read_b32 v228, a176\n"
    "v_accvgpr_read_b32 v229, a177\n"
    "v_mfma_f32_16x16x32_bf16 a[96:99], a[140:143], v[64:67], a[96:99]\n"
    "s_waitcnt lgkmcnt(0)\n"
    "v_accvgpr_read_b32 v230, a178\n"
    "v_accvgpr_read_b32 v231, a179\n"
    "v_mfma_f32_16x16x32_bf16 a[96:99], a[144:147], v[80:83], a[96:99]\n"
    "ds_read_b64_tr_b16 a[64:65], v6 offset:0\n"
    "v_mul_f32 v224, v224, v240\n"
    "v_mfma_f32_16x16x32_bf16 a[96:99], a[148:151], v[96:99], a[96:99]\n"
    "ds_read_b64_tr_b16 a[66:67], v6 offset:2048\n"
    "v_mul_f32 v225, v225, v240\n"
    "v_mfma_f32_16x16x32_bf16 a[96:99], a[152:155], v[112:115], a[96:99]\n"
    "v_mul_f32 v226, v226, v240\n"
    "v_mul_f32 v227, v227, v240\n"
    "v_mfma_f32_16x16x32_bf16 a[96:99], a[156:159], v[128:131], a[96:99]\n"
    "ds_read_b64_tr_b16 a[68:69], v6 offset:4096\n"
    "v_mul_f32 v228, v228, v240\n"
    "v_mfma_f32_16x16x32_bf16 a[100:103], a[128:131], v[20:23], 0\n"
    "ds_read_b64_tr_b16 a[70:71], v6 offset:6144\n"
    "v_mul_f32 v229, v229, v240\n"
    "v_mfma_f32_16x16x32_bf16 a[100:103], a[132:135], v[36:39], a[100:103]\n"
    "v_mul_f32 v230, v230, v240\n"
    "v_mul_f32 v231, v231, v240\n"
    "v_mfma_f32_16x16x32_bf16 a[100:103], a[136:139], v[52:55], a[100:103]\n"
    "ds_read_b64_tr_b16 a[72:73], v6 offset:8192\n"
    "v_cvt_pk_bf16_f32 v244, v224, v225\n"
    "v_mfma_f32_16x16x32_bf16 a[100:103], a[140:143], v[68:71], a[100:103]\n"
    "ds_read_b64_tr_b16 a[74:75], v6 offset:10240\n"
    "v_cvt_pk_bf16_f32 v245, v226, v227\n"
    "v_mfma_f32_16x16x32_bf16 a[100:103], a[144:147], v[84:87], a[100:103]\n"
    "v_cvt_pk_bf16_f32 v246, v228, v229\n"
    "v_cvt_pk_bf16_f32 v247, v230, v231\n"
    "v_mfma_f32_16x16x32_bf16 a[100:103], a[148:151], v[100:103], a[100:103]\n"
    "s_add_u32 s90, s4, s81\n"
    "ds_read_b64_tr_b16 a[76:77], v6 offset:12288\n"
    "v_mfma_f32_16x16x32_bf16 a[100:103], a[152:155], v[116:119], a[100:103]\n"
    "buffer_store_dwordx4 v[244:247], v0, s[40:43], s90 offen offset:0 sc1\n"
    "v_mfma_f32_16x16x32_bf16 a[100:103], a[156:159], v[132:135], a[100:103]\n"
    "ds_read_b64_tr_b16 a[78:79], v6 offset:14336\n"
    "v_mfma_f32_16x16x32_bf16 a[104:107], a[128:131], v[24:27], 0\n"
    "ds_read_b64_tr_b16 a[80:81], v6 offset:16384\n"
    "v_mfma_f32_16x16x32_bf16 a[104:107], a[132:135], v[40:43], a[104:107]\n"
    "ds_read_b64_tr_b16 a[82:83], v6 offset:18432\n"
    "v_mfma_f32_16x16x32_bf16 a[104:107], a[136:139], v[56:59], a[104:107]\n"
    "v_mfma_f32_16x16x32_bf16 a[104:107], a[140:143], v[72:75], a[104:107]\n"
    "ds_read_b64_tr_b16 a[84:85], v6 offset:20480\n"
    "v_mfma_f32_16x16x32_bf16 a[104:107], a[144:147], v[88:91], a[104:107]\n"
    "ds_read_b64_tr_b16 a[86:87], v6 offset:22528\n"
    "v_mfma_f32_16x16x32_bf16 a[104:107], a[148:151], v[104:107], a[104:107]\n"
    "v_mfma_f32_16x16x32_bf16 a[104:107], a[152:155], v[120:123], a[104:107]\n"
    "ds_read_b64_tr_b16 a[88:89], v6 offset:24576\n"
    "v_mfma_f32_16x16x32_bf16 a[104:107], a[156:159], v[136:139], a[104:107]\n"
    "ds_read_b64_tr_b16 a[90:91], v6 offset:26624\n"
    "v_mfma_f32_16x16x32_bf16 a[108:111], a[128:131], v[28:31], 0\n"
    "v_mfma_f32_16x16x32_bf16 a[108:111], a[132:135], v[44:47], a[108:111]\n"
    "ds_read_b64_tr_b16 a[92:93], v6 offset:28672\n"
    "v_mfma_f32_16x16x32_bf16 a[108:111], a[136:139], v[60:63], a[108:111]\n"
    "ds_read_b64_tr_b16 a[94:95], v6 offset:30720\n"
    "v_mfma_f32_16x16x32_bf16 a[108:111], a[140:143], v[76:79], a[108:111]\n"
    "v_mfma_f32_16x16x32_bf16 a[108:111], a[144:147], v[92:95], a[108:111]\n"
    "v_mfma_f32_16x16x32_bf16 a[108:111], a[148:151], v[108:111], a[108:111]\n"
    "v_mfma_f32_16x16x32_bf16 a[108:111], a[152:155], v[124:127], a[108:111]\n"
    "v_mfma_f32_16x16x32_bf16 a[108:111], a[156:159], v[140:143], a[108:111]\n"
    "s_waitcnt lgkmcnt(8)\n"
    "v_mfma_f32_16x16x32_bf16 a[112:115], a[64:67], v[16:19], 0\n"
    "s_lshr_b32 s94, %[bid], 3\n"
    "v_accvgpr_read_b32 v232, a164\n"
    "v_accvgpr_read_b32 v233, a165\n"
    "v_mfma_f32_16x16x32_bf16 a[112:115], a[68:71], v[32:35], a[112:115]\n"
    "s_lshl2_add_u32 s94, s94, s76\n"
    "v_accvgpr_read_b32 v234, a166\n"
    "v_accvgpr_read_b32 v235, a167\n"
    "v_mfma_f32_16x16x32_bf16 a[112:115], a[72:75], v[48:51], a[112:115]\n"
    "s_lshl_b32 s94, s94, 8\n"
    "v_bfe_u32 v5, v0, 4, 1\n"
    "v_lshl_add_u32 v5, v5, 7, s94\n"
    "v_mfma_f32_16x16x32_bf16 a[112:115], a[76:79], v[64:67], a[112:115]\n"
    "s_waitcnt lgkmcnt(0)\n"
    "v_min_u32 v5, .L_end_%=-.L_c_%=-4, v5\n"
    "v_accvgpr_read_b32 v236, a180\n"
    "v_mfma_f32_16x16x32_bf16 a[112:115], a[80:83], v[80:83], a[112:115]\n"
    "s_getpc_b64 s[86:87]\n"
    ".L_cw3_%=:\n"
    "s_add_u32 s86, s86, .L_c_%=-.L_cw3_%=\n"
    "s_addc_u32 s87, s87, 0\n"
    "ds_read_b64_tr_b16 a[128:129], v10 offset:32768\n"
    "v_accvgpr_read_b32 v237, a181\n"
    "v_mfma_f32_16x16x32_bf16 a[112:115], a[84:87], v[96:99], a[112:115]\n"
    "ds_read_b64_tr_b16 a[130:131], v10 offset:34816\n"
    "v_accvgpr_read_b32 v238, a182\n"
    "v_mfma_f32_16x16x32_bf16 a[112:115], a[88:91], v[112:115], a[112:115]\n"
    "global_load_dword a57, v5, s[86:87]\n"
    "v_mfma_f32_16x16x32_bf16 a[112:115], a[92:95], v[128:131], a[112:115]\n"
    "ds_read_b64_tr_b16 a[132:133], v10 offset:36864\n"
    "v_accvgpr_read_b32 v239, a183\n"
    "v_mfma_f32_16x16x32_bf16 a[116:119], a[64:67], v[20:23], 0\n"
    "ds_read_b64_tr_b16 a[134:135], v10 offset:38912\n"
    "v_mul_f32 v232, v232, v241\n"
    "v_mfma_f32_16x16x32_bf16 a[116:119], a[68:71], v[36:39], a[116:119]\n"
    "v_mul_f32 v233, v233, v241\n"
    "v_mul_f32 v234, v234, v241\n"
    "v_mfma_f32_16x16x32_bf16 a[116:119], a[72:75], v[52:55], a[116:119]\n"
    "ds_read_b64_tr_b16 a[136:137], v10 offset:40960\n"
    "v_mul_f32 v235, v235, v241\n"
    "v_mfma_f32_16x16x32_bf16 a[116:119], a[76:79], v[68:71], a[116:119]\n"
    "ds_read_b64_tr_b16 a[138:139], v10 offset:43008\n"
    "v_mul_f32 v236, v236, v241\n"
    "v_mfma_f32_16x16x32_bf16 a[116:119], a[80:83], v[84:87], a[116:119]\n"
    "v_mul_f32 v237, v237, v241\n"
    "v_mul_f32 v238, v238, v241\n"
    "v_mfma_f32_16x16x32_bf16 a[116:119], a[84:87], v[100:103], a[116:119]\n"
    "ds_read_b64_tr_b16 a[140:141], v10 offset:45056\n"
    "v_mul_f32 v239, v239, v241\n"
    "v_mfma_f32_16x16x32_bf16 a[116:119], a[88:91], v[116:119], a[116:119]\n"
    "ds_read_b64_tr_b16 a[142:143], v10 offset:47104\n"
    "v_cvt_pk_bf16_f32 v252, v232, v233\n"
    "v_mfma_f32_16x16x32_bf16 a[116:119], a[92:95], v[132:135], a[116:119]\n"
    "v_cvt_pk_bf16_f32 v253, v234, v235\n"
    "v_cvt_pk_bf16_f32 v254, v236, v237\n"
    "v_mfma_f32_16x16x32_bf16 a[120:123], a[64:67], v[24:27], 0\n"
    "ds_read_b64_tr_b16 a[144:145], v10 offset:49152\n"
    "v_cvt_pk_bf16_f32 v255, v238, v239\n"
    "v_mfma_f32_16x16x32_bf16 a[120:123], a[68:71], v[40:43], a[120:123]\n"
    "s_add_u32 s90, s4, s3\n"
    "ds_read_b64_tr_b16 a[146:147], v10 offset:51200\n"
    "v_mfma_f32_16x16x32_bf16 a[120:123], a[72:75], v[56:59], a[120:123]\n"
    "buffer_store_dwordx4 v[252:255], v0, s[40:43], s90 offen offset:0 sc1\n"
    "v_mfma_f32_16x16x32_bf16 a[120:123], a[76:79], v[72:75], a[120:123]\n"
    "ds_read_b64_tr_b16 a[148:149], v10 offset:53248\n"
    "v_mfma_f32_16x16x32_bf16 a[120:123], a[80:83], v[88:91], a[120:123]\n"
    "ds_read_b64_tr_b16 a[150:151], v10 offset:55296\n"
    "v_mfma_f32_16x16x32_bf16 a[120:123], a[84:87], v[104:107], a[120:123]\n"
    "v_mfma_f32_16x16x32_bf16 a[120:123], a[88:91], v[120:123], a[120:123]\n"
    "ds_read_b64_tr_b16 a[152:153], v10 offset:57344\n"
    "v_mfma_f32_16x16x32_bf16 a[120:123], a[92:95], v[136:139], a[120:123]\n"
    "ds_read_b64_tr_b16 a[154:155], v10 offset:59392\n"
    "v_mfma_f32_16x16x32_bf16 a[124:127], a[64:67], v[28:31], 0\n"
    "v_mfma_f32_16x16x32_bf16 a[124:127], a[68:71], v[44:47], a[124:127]\n"
    "ds_read_b64_tr_b16 a[156:157], v10 offset:61440\n"
    "v_mfma_f32_16x16x32_bf16 a[124:127], a[72:75], v[60:63], a[124:127]\n"
    "ds_read_b64_tr_b16 a[158:159], v10 offset:63488\n"
    "v_mfma_f32_16x16x32_bf16 a[124:127], a[76:79], v[76:79], a[124:127]\n"
    "v_mfma_f32_16x16x32_bf16 a[124:127], a[80:83], v[92:95], a[124:127]\n"
    "v_mfma_f32_16x16x32_bf16 a[124:127], a[84:87], v[108:111], a[124:127]\n"
    "v_mfma_f32_16x16x32_bf16 a[124:127], a[88:91], v[124:127], a[124:127]\n"
    "v_mfma_f32_16x16x32_bf16 a[124:127], a[92:95], v[140:143], a[124:127]\n"
    "s_waitcnt lgkmcnt(8)\n"
    "v_mfma_f32_16x16x32_bf16 a[96:99], a[128:131], v[144:147], a[96:99]\n"
    "v_accvgpr_read_b32 v224, a168\n"
    "v_accvgpr_read_b32 v225, a169\n"
    "v_mfma_f32_16x16x32_bf16 a[96:99], a[132:135], v[160:163], a[96:99]\n"
    "v_accvgpr_read_b32 v226, a170\n"
    "v_accvgpr_read_b32 v227, a171\n"
    "v_mfma_f32_16x16x32_bf16 a[96:99], a[136:139], v[176:179], a[96:99]\n"
    "v_accvgpr_read_b32 v228, a184\n"
    "v_accvgpr_read_b32 v229, a185\n"
    "v_mfma_f32_16x16x32_bf16 a[96:99], a[140:143], v[192:195], a[96:99]\n"
    "s_waitcnt lgkmcnt(0)\n"
    "v_accvgpr_read_b32 v230, a186\n"
    "v_accvgpr_read_b32 v231, a187\n"
    "v_mfma_f32_16x16x32_bf16 a[96:99], a[144:147], v[208:211], a[96:99]\n"
    "ds_read_b64_tr_b16 a[64:65], v6 offset:32768\n"
    "v_mul_f32 v224, v224, v242\n"
    "v_mfma_f32_16x16x32_bf16 a[96:99], a[148:151], a[0:3], a[96:99]\n"
    "ds_read_b64_tr_b16 a[66:67], v6 offset:34816\n"
    "v_mul_f32 v225, v225, v242\n"
    "v_mfma_f32_16x16x32_bf16 a[96:99], a[152:155], a[16:19], a[96:99]\n"
    "v_mul_f32 v226, v226, v242\n"
    "v_mul_f32 v227, v227, v242\n"
    "v_mfma_f32_16x16x32_bf16 a[96:99], a[156:159], a[32:35], a[96:99]\n"
    "ds_read_b64_tr_b16 a[68:69], v6 offset:36864\n"
    "v_mul_f32 v228, v228, v242\n"
    "v_mfma_f32_16x16x32_bf16 a[100:103], a[128:131], v[148:151], a[100:103]\n"
    "ds_read_b64_tr_b16 a[70:71], v6 offset:38912\n"
    "v_mul_f32 v229, v229, v242\n"
    "v_mfma_f32_16x16x32_bf16 a[100:103], a[132:135], v[164:167], a[100:103]\n"
    "v_mul_f32 v230, v230, v242\n"
    "v_mul_f32 v231, v231, v242\n"
    "v_mfma_f32_16x16x32_bf16 a[100:103], a[136:139], v[180:183], a[100:103]\n"
    "ds_read_b64_tr_b16 a[72:73], v6 offset:40960\n"
    "v_cvt_pk_bf16_f32 v244, v224, v225\n"
    "v_mfma_f32_16x16x32_bf16 a[100:103], a[140:143], v[196:199], a[100:103]\n"
    "ds_read_b64_tr_b16 a[74:75], v6 offset:43008\n"
    "v_cvt_pk_bf16_f32 v245, v226, v227\n"
    "v_mfma_f32_16x16x32_bf16 a[100:103], a[144:147], v[212:215], a[100:103]\n"
    "v_cvt_pk_bf16_f32 v246, v228, v229\n"
    "v_cvt_pk_bf16_f32 v247, v230, v231\n"
    "v_mfma_f32_16x16x32_bf16 a[100:103], a[148:151], a[4:7], a[100:103]\n"
    "s_add_u32 s90, s4, s39\n"
    "ds_read_b64_tr_b16 a[76:77], v6 offset:45056\n"
    "v_mfma_f32_16x16x32_bf16 a[100:103], a[152:155], a[20:23], a[100:103]\n"
    "buffer_store_dwordx4 v[244:247], v0, s[40:43], s90 offen offset:0 sc1\n"
    "v_mfma_f32_16x16x32_bf16 a[100:103], a[156:159], a[36:39], a[100:103]\n"
    "ds_read_b64_tr_b16 a[78:79], v6 offset:47104\n"
    "v_mfma_f32_16x16x32_bf16 a[104:107], a[128:131], v[152:155], a[104:107]\n"
    "ds_read_b64_tr_b16 a[80:81], v6 offset:49152\n"
    "v_mfma_f32_16x16x32_bf16 a[104:107], a[132:135], v[168:171], a[104:107]\n"
    "ds_read_b64_tr_b16 a[82:83], v6 offset:51200\n"
    "v_mfma_f32_16x16x32_bf16 a[104:107], a[136:139], v[184:187], a[104:107]\n"
    "v_mfma_f32_16x16x32_bf16 a[104:107], a[140:143], v[200:203], a[104:107]\n"
    "ds_read_b64_tr_b16 a[84:85], v6 offset:53248\n"
    "v_mfma_f32_16x16x32_bf16 a[104:107], a[144:147], v[216:219], a[104:107]\n"
    "ds_read_b64_tr_b16 a[86:87], v6 offset:55296\n"
    "v_mfma_f32_16x16x32_bf16 a[104:107], a[148:151], a[8:11], a[104:107]\n"
    "v_mfma_f32_16x16x32_bf16 a[104:107], a[152:155], a[24:27], a[104:107]\n"
    "ds_read_b64_tr_b16 a[88:89], v6 offset:57344\n"
    "v_mfma_f32_16x16x32_bf16 a[104:107], a[156:159], a[40:43], a[104:107]\n"
    "ds_read_b64_tr_b16 a[90:91], v6 offset:59392\n"
    "v_mfma_f32_16x16x32_bf16 a[108:111], a[128:131], v[156:159], a[108:111]\n"
    "v_mfma_f32_16x16x32_bf16 a[108:111], a[132:135], v[172:175], a[108:111]\n"
    "ds_read_b64_tr_b16 a[92:93], v6 offset:61440\n"
    "v_mfma_f32_16x16x32_bf16 a[108:111], a[136:139], v[188:191], a[108:111]\n"
    "ds_read_b64_tr_b16 a[94:95], v6 offset:63488\n"
    "v_mfma_f32_16x16x32_bf16 a[108:111], a[140:143], v[204:207], a[108:111]\n"
    "v_mfma_f32_16x16x32_bf16 a[108:111], a[144:147], v[220:223], a[108:111]\n"
    "v_mfma_f32_16x16x32_bf16 a[108:111], a[148:151], a[12:15], a[108:111]\n"
    "v_mfma_f32_16x16x32_bf16 a[108:111], a[152:155], a[28:31], a[108:111]\n"
    "v_mfma_f32_16x16x32_bf16 a[108:111], a[156:159], a[44:47], a[108:111]\n"
    "s_waitcnt vmcnt(4) lgkmcnt(0)\n"
    "s_barrier\n"
    "v_mfma_f32_16x16x32_bf16 a[112:115], a[64:67], v[144:147], a[112:115]\n"
    "v_accvgpr_read_b32 v232, a172\n"
    "v_accvgpr_read_b32 v233, a173\n"
    "v_mfma_f32_16x16x32_bf16 a[112:115], a[68:71], v[160:163], a[112:115]\n"
    "v_mov_b32 v245, 163776\n"
    "v_accvgpr_read_b32 v234, a174\n"
    "v_mfma_f32_16x16x32_bf16 a[112:115], a[72:75], v[176:179], a[112:115]\n"
    "ds_read_b128 v[248:251], v245 offset:0\n"
    "v_accvgpr_read_b32 v235, a175\n"
    "v_mfma_f32_16x16x32_bf16 a[112:115], a[76:79], v[192:195], a[112:115]\n"
    "v_accvgpr_read_b32 v236, a188\n"
    "v_accvgpr_read_b32 v237, a189\n"
    "v_mfma_f32_16x16x32_bf16 a[112:115], a[80:83], v[208:211], a[112:115]\n"
    "ds_read_b64_tr_b16 a[128:129], v11 offset:0\n"
    "v_accvgpr_read_b32 v238, a190\n"
    "v_mfma_f32_16x16x32_bf16 a[112:115], a[84:87], a[0:3], a[112:115]\n"
    "ds_read_b64_tr_b16 a[130:131], v11 offset:2048\n"
    "v_accvgpr_read_b32 v239, a191\n"
    "v_mfma_f32_16x16x32_bf16 a[112:115], a[88:91], a[16:19], a[112:115]\n"
    "v_mul_f32 v232, v232, v243\n"
    "v_mul_f32 v233, v233, v243\n"
    "v_mfma_f32_16x16x32_bf16 a[112:115], a[92:95], a[32:35], a[112:115]\n"
    "ds_read_b64_tr_b16 a[132:133], v11 offset:4096\n"
    "v_mul_f32 v234, v234, v243\n"
    "v_mfma_f32_16x16x32_bf16 a[116:119], a[64:67], v[148:151], a[116:119]\n"
    "s_add_u32 m0, s85, 0\n"
    "ds_read_b64_tr_b16 a[134:135], v11 offset:6144\n"
    "v_mul_f32 v235, v235, v243\n"
    "v_mfma_f32_16x16x32_bf16 a[116:119], a[68:71], v[164:167], a[116:119]\n"
    "v_mul_f32 v236, v236, v243\n"
    "v_mul_f32 v237, v237, v243\n"
    "v_mfma_f32_16x16x32_bf16 a[116:119], a[72:75], v[180:183], a[116:119]\n"
    "ds_read_b64_tr_b16 a[136:137], v11 offset:8192\n"
    "v_mul_f32 v238, v238, v243\n"
    "v_mfma_f32_16x16x32_bf16 a[116:119], a[76:79], v[196:199], a[116:119]\n"
    "s_add_u32 s88, s84, 256\n"
    "ds_read_b64_tr_b16 a[138:139], v11 offset:10240\n"
    "v_mul_f32 v239, v239, v243\n"
    "v_mfma_f32_16x16x32_bf16 a[116:119], a[80:83], v[212:215], a[116:119]\n"
    "v_cvt_pk_bf16_f32 v252, v232, v233\n"
    "v_cvt_pk_bf16_f32 v253, v234, v235\n"
    "v_mfma_f32_16x16x32_bf16 a[116:119], a[84:87], a[4:7], a[116:119]\n"
    "s_waitcnt lgkmcnt(6)\n"
    "ds_read_b64_tr_b16 a[140:141], v11 offset:12288\n"
    "v_cvt_pk_bf16_f32 v254, v236, v237\n"
    "v_mfma_f32_16x16x32_bf16 a[116:119], a[88:91], a[20:23], a[116:119]\n"
    "ds_read_b64_tr_b16 a[142:143], v11 offset:14336\n"
    "v_or3_b32 v248, v248, v249, v250\n"
    "v_mfma_f32_16x16x32_bf16 a[116:119], a[92:95], a[36:39], a[116:119]\n"
    "buffer_load_dwordx4 v1, s[60:63], s88 offen offset:0 lds\n"
    "v_mfma_f32_16x16x32_bf16 a[120:123], a[64:67], v[152:155], a[120:123]\n"
    "ds_read_b64_tr_b16 a[144:145], v11 offset:16384\n"
    "v_or_b32 v248, v248, v251\n"
    "v_mfma_f32_16x16x32_bf16 a[120:123], a[68:71], v[168:171], a[120:123]\n"
    "s_nop 0\n"
    "ds_read_b64_tr_b16 a[146:147], v11 offset:18432\n"
    "v_readfirstlane_b32 s86, v248\n"
    "v_mfma_f32_16x16x32_bf16 a[120:123], a[72:75], v[184:187], a[120:123]\n"
    "buffer_load_dwordx4 v2, s[60:63], s88 offen offset:1024 lds\n"
    "v_mfma_f32_16x16x32_bf16 a[120:123], a[76:79], v[200:203], a[120:123]\n"
    "s_nop 0\n"
    "ds_read_b64_tr_b16 a[148:149], v11 offset:20480\n"
    "v_cvt_pk_bf16_f32 v255, v238, v239\n"
    "v_mfma_f32_16x16x32_bf16 a[120:123], a[80:83], v[216:219], a[120:123]\n"
    "buffer_load_dwordx4 v3, s[60:63], s88 offen offset:2048 lds\n"
    "v_mfma_f32_16x16x32_bf16 a[120:123], a[84:87], a[8:11], a[120:123]\n"
    "s_andn2_b32 s86, s86, s92\n"
    "s_cbranch_scc1 .L_redo_%=\n"
    "ds_read_b64_tr_b16 a[150:151], v11 offset:22528\n"
    "s_add_u32 s90, s4, s99\n"
    "v_mfma_f32_16x16x32_bf16 a[120:123], a[88:91], a[24:27], a[120:123]\n"
    "buffer_store_dwordx4 v[252:255], v0, s[40:43], s90 offen offset:0 sc1\n"
    "v_mfma_f32_16x16x32_bf16 a[120:123], a[92:95], a[40:43], a[120:123]\n"
    "ds_read_b64_tr_b16 a[152:153], v11 offset:24576\n"
    "ds_read_b64_tr_b16 a[154:155], v11 offset:26624\n"
    "v_mfma_f32_16x16x32_bf16 a[124:127], a[64:67], v[156:159], a[124:127]\n"
    "buffer_load_dwordx4 v4, s[60:63], s88 offen offset:3072 lds\n"
    "v_mfma_f32_16x16x32_bf16 a[124:127], a[68:71], v[172:175], a[124:127]\n"
    "ds_read_b64_tr_b16 a[156:157], v11 offset:28672\n"
    "v_mfma_f32_16x16x32_bf16 a[124:127], a[72:75], v[188:191], a[124:127]\n"
    "s_add_u32 m0, s85, 4096\n"
    "ds_read_b64_tr_b16 a[158:159], v11 offset:30720\n"
    "v_mfma_f32_16x16x32_bf16 a[124:127], a[76:79], v[204:207], a[124:127]\n"
    "v_mfma_f32_16x16x32_bf16 a[124:127], a[80:83], v[220:223], a[124:127]\n"
    "v_mfma_f32_16x16x32_bf16 a[124:127], a[84:87], a[12:15], a[124:127]\n"
    "s_add_u32 s88, s84, 12544\n"
    "v_mfma_f32_16x16x32_bf16 a[124:127], a[88:91], a[28:31], a[124:127]\n"
    "v_mfma_f32_16x16x32_bf16 a[124:127], a[92:95], a[44:47], a[124:127]\n"
    "s_waitcnt lgkmcnt(8)\n"
    "v_mfma_f32_16x16x32_bf16 a[160:163], a[128:131], v[16:19], 0\n"
    "buffer_load_dwordx4 v1, s[60:63], s88 offen offset:0 lds\n"
    "v_mfma_f32_16x16x32_bf16 a[160:163], a[132:135], v[32:35], a[160:163]\n"
    "v_accvgpr_read_b32 v224, a96\n"
    "v_accvgpr_read_b32 v225, a97\n"
    "v_mfma_f32_16x16x32_bf16 a[160:163], a[136:139], v[48:51], a[160:163]\n"
    "v_accvgpr_read_b32 v226, a98\n"
    "v_accvgpr_read_b32 v227, a99\n"
    "v_mfma_f32_16x16x32_bf16 a[160:163], a[140:143], v[64:67], a[160:163]\n"
    "s_waitcnt lgkmcnt(0)\n"
    "v_accvgpr_read_b32 v228, a112\n"
    "v_accvgpr_read_b32 v229, a113\n"
    "v_mfma_f32_16x16x32_bf16 a[160:163], a[144:147], v[80:83], a[160:163]\n"
    "ds_read_b64_tr_b16 a[64:65], v12 offset:0\n"
    "v_accvgpr_read_b32 v230, a114\n"
    "v_mfma_f32_16x16x32_bf16 a[160:163], a[148:151], v[96:99], a[160:163]\n"
    "buffer_load_dwordx4 v2, s[60:63], s88 offen offset:1024 lds\n"
    "v_mfma_f32_16x16x32_bf16 a[160:163], a[152:155], v[112:115], a[160:163]\n"
    "ds_read_b64_tr_b16 a[66:67], v12 offset:2048\n"
    "v_accvgpr_read_b32 v231, a115\n"
    "v_mfma_f32_16x16x32_bf16 a[160:163], a[156:159], v[128:131], a[160:163]\n"
    "ds_read_b64_tr_b16 a[68:69], v12 offset:4096\n"
    "v_mul_f32 v224, v224, v240\n"
    "v_mfma_f32_16x16x32_bf16 a[164:167], a[128:131], v[20:23], 0\n"
    "buffer_load_dwordx4 v3, s[60:63], s88 offen offset:2048 lds\n"
    "v_mfma_f32_16x16x32_bf16 a[164:167], a[132:135], v[36:39], a[164:167]\n"
    "ds_read_b64_tr_b16 a[70:71], v12 offset:6144\n"
    "v_mul_f32 v225, v225, v240\n"
    "v_mfma_f32_16x16x32_bf16 a[164:167], a[136:139], v[52:55], a[164:167]\n"
    "ds_read_b64_tr_b16 a[72:73], v12 offset:8192\n"
    "v_mul_f32 v226, v226, v240\n"
    "v_mfma_f32_16x16x32_bf16 a[164:167], a[140:143], v[68:71], a[164:167]\n"
    "buffer_load_dwordx4 v4, s[60:63], s88 offen offset:3072 lds\n"
    "v_mfma_f32_16x16x32_bf16 a[164:167], a[144:147], v[84:87], a[164:167]\n"
    "ds_read_b64_tr_b16 a[74:75], v12 offset:10240\n"
    "v_mul_f32 v227, v227, v240\n"
    "v_mfma_f32_16x16x32_bf16 a[164:167], a[148:151], v[100:103], a[164:167]\n"
    "s_add_u32 m0, s85, 32768\n"
    "ds_read_b64_tr_b16 a[76:77], v12 offset:12288\n"
    "v_mul_f32 v228, v228, v240\n"
    "v_mfma_f32_16x16x32_bf16 a[164:167], a[152:155], v[116:119], a[164:167]\n"
    "ds_read_b64_tr_b16 a[78:79], v12 offset:14336\n"
    "v_mul_f32 v229, v229, v240\n"
    "v_mfma_f32_16x16x32_bf16 a[164:167], a[156:159], v[132:135], a[164:167]\n"
    "v_mul_f32 v230, v230, v240\n"
    "v_mul_f32 v231, v231, v240\n"
    "v_mfma_f32_16x16x32_bf16 a[168:171], a[128:131], v[24:27], 0\n"
    "s_add_u32 s88, s84, 98560\n"
    "ds_read_b64_tr_b16 a[80:81], v12 offset:16384\n"
    "v_cvt_pk_bf16_f32 v244, v224, v225\n"
    "v_mfma_f32_16x16x32_bf16 a[168:171], a[132:135], v[40:43], a[168:171]\n"
    "ds_read_b64_tr_b16 a[82:83], v12 offset:18432\n"
    "v_cvt_pk_bf16_f32 v245, v226, v227\n"
    "v_mfma_f32_16x16x32_bf16 a[168:171], a[136:139], v[56:59], a[168:171]\n"
    "v_cvt_pk_bf16_f32 v246, v228, v229\n"
    "v_cvt_pk_bf16_f32 v247, v230, v231\n"
    "v_mfma_f32_16x16x32_bf16 a[168:171], a[140:143], v[72:75], a[168:171]\n"
    "buffer_load_dwordx4 v1, s[60:63], s88 offen offset:0 lds\n"
    "v_mfma_f32_16x16x32_bf16 a[168:171], a[144:147], v[88:91], a[168:171]\n"
    "s_add_u32 s90, s5, s81\n"
    "ds_read_b64_tr_b16 a[84:85], v12 offset:20480\n"
    "ds_read_b64_tr_b16 a[86:87], v12 offset:22528\n"
    "v_mfma_f32_16x16x32_bf16 a[168:171], a[148:151], v[104:107], a[168:171]\n"
    "buffer_store_dwordx4 v[244:247], v0, s[40:43], s90 offen offset:0 sc1\n"
    "v_mfma_f32_16x16x32_bf16 a[168:171], a[152:155], v[120:123], a[168:171]\n"
    "buffer_load_dwordx4 v2, s[60:63], s88 offen offset:1024 lds\n"
    "v_mfma_f32_16x16x32_bf16 a[168:171], a[156:159], v[136:139], a[168:171]\n"
    "ds_read_b64_tr_b16 a[88:89], v12 offset:24576\n"
    "ds_read_b64_tr_b16 a[90:91], v12 offset:26624\n"
    "v_mfma_f32_16x16x32_bf16 a[172:175], a[128:131], v[28:31], 0\n"
    "v_mfma_f32_16x16x32_bf16 a[172:175], a[132:135], v[44:47], a[172:175]\n"
    "buffer_load_dwordx4 v3, s[60:63], s88 offen offset:2048 lds\n"
    "v_mfma_f32_16x16x32_bf16 a[172:175], a[136:139], v[60:63], a[172:175]\n"
    "ds_read_b64_tr_b16 a[92:93], v12 offset:28672\n"
    "ds_read_b64_tr_b16 a[94:95], v12 offset:30720\n"
    "v_mfma_f32_16x16x32_bf16 a[172:175], a[140:143], v[76:79], a[172:175]\n"
    "v_mfma_f32_16x16x32_bf16 a[172:175], a[144:147], v[92:95], a[172:175]\n"
    "buffer_load_dwordx4 v4, s[60:63], s88 offen offset:3072 lds\n"
    "v_mfma_f32_16x16x32_bf16 a[172:175], a[148:151], v[108:111], a[172:175]\n"
    "v_mfma_f32_16x16x32_bf16 a[172:175], a[152:155], v[124:127], a[172:175]\n"
    "v_mfma_f32_16x16x32_bf16 a[172:175], a[156:159], v[140:143], a[172:175]\n"
    "s_waitcnt lgkmcnt(8)\n"
    "v_mfma_f32_16x16x32_bf16 a[176:179], a[64:67], v[16:19], 0\n"
    "v_accvgpr_read_b32 v232, a100\n"
    "v_accvgpr_read_b32 v233, a101\n"
    "v_mfma_f32_16x16x32_bf16 a[176:179], a[68:71], v[32:35], a[176:179]\n"
    "v_accvgpr_read_b32 v234, a102\n"
    "v_accvgpr_read_b32 v235, a103\n"
    "v_mfma_f32_16x16x32_bf16 a[176:179], a[72:75], v[48:51], a[176:179]\n"
    "v_accvgpr_read_b32 v236, a116\n"
    "v_accvgpr_read_b32 v237, a117\n"
    "v_mfma_f32_16x16x32_bf16 a[176:179], a[76:79], v[64:67], a[176:179]\n"
    "s_waitcnt lgkmcnt(0)\n"
    "v_accvgpr_read_b32 v238, a118\n"
    "v_accvgpr_read_b32 v239, a119\n"
    "v_mfma_f32_16x16x32_bf16 a[176:179], a[80:83], v[80:83], a[176:179]\n"
    "s_add_u32 s94, s97, 3\n"
    "ds_read_b64_tr_b16 a[128:129], v11 offset:32768\n"
    "v_accvgpr_write_b32 a53, s94\n"
    "v_mfma_f32_16x16x32_bf16 a[176:179], a[84:87], v[96:99], a[176:179]\n"
    "s_waitcnt vmcnt(10)\n"
    "ds_read_b64_tr_b16 a[130:131], v11 offset:34816\n"
    "v_mul_f32 v232, v232, v241\n"
    "v_mfma_f32_16x16x32_bf16 a[176:179], a[88:91], v[112:115], a[176:179]\n"
    "buffer_store_dwordx2 a[52:53], v7, s[40:43], s23 offen offset:0 sc1\n"
    "v_mfma_f32_16x16x32_bf16 a[176:179], a[92:95], v[128:131], a[176:179]\n"
    "ds_read_b64_tr_b16 a[132:133], v11 offset:36864\n"
    "v_mul_f32 v233, v233, v241\n"
    "v_mfma_f32_16x16x32_bf16 a[180:183], a[64:67], v[20:23], 0\n"
    "ds_read_b64_tr_b16 a[134:135], v11 offset:38912\n"
    "v_mul_f32 v234, v234, v241\n"
    "v_mfma_f32_16x16x32_bf16 a[180:183], a[68:71], v[36:39], a[180:183]\n"
    "v_mul_f32 v235, v235, v241\n"
    "v_mul_f32 v236, v236, v241\n"
    "v_mfma_f32_16x16x32_bf16 a[180:183], a[72:75], v[52:55], a[180:183]\n"
    "ds_read_b64_tr_b16 a[136:137], v11 offset:40960\n"
    "v_mul_f32 v237, v237, v241\n"
    "v_mfma_f32_16x16x32_bf16 a[180:183], a[76:79], v[68:71], a[180:183]\n"
    "ds_read_b64_tr_b16 a[138:139], v11 offset:43008\n"
    "v_mul_f32 v238, v238, v241\n"
    "v_mfma_f32_16x16x32_bf16 a[180:183], a[80:83], v[84:87], a[180:183]\n"
    "s_add_u32 m0, s85, 36864\n"
    "v_mul_f32 v239, v239, v241\n"
    "v_cvt_pk_bf16_f32 v252, v232, v233\n"
    "v_mfma_f32_16x16x32_bf16 a[180:183], a[84:87], v[100:103], a[180:183]\n"
    "ds_read_b64_tr_b16 a[140:141], v11 offset:45056\n"
    "v_cvt_pk_bf16_f32 v253, v234, v235\n"
    "v_mfma_f32_16x16x32_bf16 a[180:183], a[88:91], v[116:119], a[180:183]\n"
    "s_add_u32 s88, s84, 110848\n"
    "ds_read_b64_tr_b16 a[142:143], v11 offset:47104\n"
    "v_cvt_pk_bf16_f32 v254, v236, v237\n"
    "v_mfma_f32_16x16x32_bf16 a[180:183], a[92:95], v[132:135], a[180:183]\n"
    "v_cvt_pk_bf16_f32 v255, v238, v239\n"
    "s_add_u32 s90, s5, s3\n"
    "v_mfma_f32_16x16x32_bf16 a[184:187], a[64:67], v[24:27], 0\n"
    "buffer_load_dwordx4 v1, s[60:63], s88 offen offset:0 lds\n"
    "v_mfma_f32_16x16x32_bf16 a[184:187], a[68:71], v[40:43], a[184:187]\n"
    "ds_read_b64_tr_b16 a[144:145], v11 offset:49152\n"
    "ds_read_b64_tr_b16 a[146:147], v11 offset:51200\n"
    "v_mfma_f32_16x16x32_bf16 a[184:187], a[72:75], v[56:59], a[184:187]\n"
    "buffer_store_dwordx4 v[252:255], v0, s[40:43], s90 offen offset:0 sc1\n"
    "v_mfma_f32_16x16x32_bf16 a[184:187], a[76:79], v[72:75], a[184:187]\n"
    "buffer_load_dwordx4 v2, s[60:63], s88 offen offset:1024 lds\n"
    "v_mfma_f32_16x16x32_bf16 a[184:187], a[80:83], v[88:91], a[184:187]\n"
    "ds_read_b64_tr_b16 a[148:149], v11 offset:53248\n"
    "ds_read_b64_tr_b16 a[150:151], v11 offset:55296\n"
    "v_mfma_f32_16x16x32_bf16 a[184:187], a[84:87], v[104:107], a[184:187]\n"
    "buffer_load_dwordx4 v3, s[60:63], s88 offen offset:2048 lds\n"
    "v_mfma_f32_16x16x32_bf16 a[184:187], a[88:91], v[120:123], a[184:187]\n"
    "ds_read_b64_tr_b16 a[152:153], v11 offset:57344\n"
    "v_mfma_f32_16x16x32_bf16 a[184:187], a[92:95], v[136:139], a[184:187]\n"
    "buffer_load_dwordx4 v4, s[60:63], s88 offen offset:3072 lds\n"
    "v_mfma_f32_16x16x32_bf16 a[188:191], a[64:67], v[28:31], 0\n"
    "ds_read_b64_tr_b16 a[154:155], v11 offset:59392\n"
    "v_mfma_f32_16x16x32_bf16 a[188:191], a[68:71], v[44:47], a[188:191]\n"
    "ds_read_b64_tr_b16 a[156:157], v11 offset:61440\n"
    "v_mfma_f32_16x16x32_bf16 a[188:191], a[72:75], v[60:63], a[188:191]\n"
    "ds_read_b64_tr_b16 a[158:159], v11 offset:63488\n"
    "v_mfma_f32_16x16x32_bf16 a[188:191], a[76:79], v[76:79], a[188:191]\n"
    "v_mfma_f32_16x16x32_bf16 a[188:191], a[80:83], v[92:95], a[188:191]\n"
    "v_lshrrev_b32 v248, 4, v0\n"
    "v_add_u32 v248, 0, v248\n"
    "v_mfma_f32_16x16x32_bf16 a[188:191], a[84:87], v[108:111], a[188:191]\n"
    "v_mul_u32_u24 v249, 205, v248\n"
    "v_mfma_f32_16x16x32_bf16 a[188:191], a[88:91], v[124:127], a[188:191]\n"
    "v_lshrrev_b32 v249, 12, v249\n"
    "v_mfma_f32_16x16x32_bf16 a[188:191], a[92:95], v[140:143], a[188:191]\n"
    "s_waitcnt lgkmcnt(8)\n"
    "v_mul_u32_u24 v250, 20, v249\n"
    "v_mfma_f32_16x16x32_bf16 a[160:163], a[128:131], v[144:147], a[160:163]\n"
    "v_sub_u32 v250, v248, v250\n"
    "v_accvgpr_read_b32 v224, a104\n"
    "v_mfma_f32_16x16x32_bf16 a[160:163], a[132:135], v[160:163], a[160:163]\n"
    "v_bfe_u32 v251, v249, 2, 1\n"
    "v_lshlrev_b32 v251, 1, v251\n"
    "v_mfma_f32_16x16x32_bf16 a[160:163], a[136:139], v[176:179], a[160:163]\n"
    "v_xor_b32 v250, v250, v251\n"
    "v_accvgpr_read_b32 v225, a105\n"
    "v_mfma_f32_16x16x32_bf16 a[160:163], a[140:143], v[192:195], a[160:163]\n"
    "s_waitcnt lgkmcnt(0)\n"
    "v_mul_u32_u24 v249, 1280, v249\n"
    "v_accvgpr_read_b32 v226, a106\n"
    "v_mfma_f32_16x16x32_bf16 a[160:163], a[144:147], v[208:211], a[160:163]\n"
    "ds_read_b64_tr_b16 a[64:65], v12 offset:32768\n"
    "v_lshl_add_u32 v1, v250, 4, v249\n"
    "v_mfma_f32_16x16x32_bf16 a[160:163], a[148:151], a[0:3], a[160:163]\n"
    "ds_read_b64_tr_b16 a[66:67], v12 offset:34816\n"
    "v_lshrrev_b32 v248, 4, v0\n"
    "v_mfma_f32_16x16x32_bf16 a[160:163], a[152:155], a[16:19], a[160:163]\n"
    "v_add_u32 v248, 64, v248\n"
    "v_accvgpr_read_b32 v227, a107\n"
    "v_mfma_f32_16x16x32_bf16 a[160:163], a[156:159], a[32:35], a[160:163]\n"
    "ds_read_b64_tr_b16 a[68:69], v12 offset:36864\n"
    "v_mul_u32_u24 v249, 205, v248\n"
    "v_mfma_f32_16x16x32_bf16 a[164:167], a[128:131], v[148:151], a[164:167]\n"
    "ds_read_b64_tr_b16 a[70:71], v12 offset:38912\n"
    "v_lshrrev_b32 v249, 12, v249\n"
    "v_mfma_f32_16x16x32_bf16 a[164:167], a[132:135], v[164:167], a[164:167]\n"
    "v_mul_u32_u24 v250, 20, v249\n"
    "v_sub_u32 v250, v248, v250\n"
    "v_mfma_f32_16x16x32_bf16 a[164:167], a[136:139], v[180:183], a[164:167]\n"
    "ds_read_b64_tr_b16 a[72:73], v12 offset:40960\n"
    "v_bfe_u32 v251, v249, 2, 1\n"
    "v_mfma_f32_16x16x32_bf16 a[164:167], a[140:143], v[196:199], a[164:167]\n"
    "ds_read_b64_tr_b16 a[74:75], v12 offset:43008\n"
    "v_lshlrev_b32 v251, 1, v251\n"
    "v_mfma_f32_16x16x32_bf16 a[164:167], a[144:147], v[212:215], a[164:167]\n"
    "v_xor_b32 v250, v250, v251\n"
    "v_mul_u32_u24 v249, 1280, v249\n"
    "v_mfma_f32_16x16x32_bf16 a[164:167], a[148:151], a[4:7], a[164:167]\n"
    "ds_read_b64_tr_b16 a[76:77], v12 offset:45056\n"
    "v_lshl_add_u32 v2, v250, 4, v249\n"
    "v_mfma_f32_16x16x32_bf16 a[164:167], a[152:155], a[20:23], a[164:167]\n"
    "ds_read_b64_tr_b16 a[78:79], v12 offset:47104\n"
    "v_lshrrev_b32 v248, 4, v0\n"
    "v_mfma_f32_16x16x32_bf16 a[164:167], a[156:159], a[36:39], a[164:167]\n"
    "v_add_u32 v248, 128, v248\n"
    "v_accvgpr_read_b32 v228, a120\n"
    "v_mfma_f32_16x16x32_bf16 a[168:171], a[128:131], v[152:155], a[168:171]\n"
    "ds_read_b64_tr_b16 a[80:81], v12 offset:49152\n"
    "v_mul_u32_u24 v249, 205, v248\n"
    "v_mfma_f32_16x16x32_bf16 a[168:171], a[132:135], v[168:171], a[168:171]\n"
    "ds_read_b64_tr_b16 a[82:83], v12 offset:51200\n"
    "v_lshrrev_b32 v249, 12, v249\n"
    "v_mfma_f32_16x16x32_bf16 a[168:171], a[136:139], v[184:187], a[168:171]\n"
    "v_mul_u32_u24 v250, 20, v249\n"
    "v_sub_u32 v250, v248, v250\n"
    "v_mfma_f32_16x16x32_bf16 a[168:171], a[140:143], v[200:203], a[168:171]\n"
    "ds_read_b64_tr_b16 a[84:85], v12 offset:53248\n"
    "v_bfe_u32 v251, v249, 2, 1\n"
    "v_mfma_f32_16x16x32_bf16 a[168:171], a[144:147], v[216:219], a[168:171]\n"
    "ds_read_b64_tr_b16 a[86:87], v12 offset:55296\n"
    "v_lshlrev_b32 v251, 1, v251\n"
    "v_mfma_f32_16x16x32_bf16 a[168:171], a[148:151], a[8:11], a[168:171]\n"
    "v_xor_b32 v250, v250, v251\n"
    "v_accvgpr_read_b32 v229, a121\n"
    "v_mfma_f32_16x16x32_bf16 a[168:171], a[152:155], a[24:27], a[168:171]\n"
    "ds_read_b64_tr_b16 a[88:89], v12 offset:57344\n"
    "v_accvgpr_read_b32 v230, a122\n"
    "v_mfma_f32_16x16x32_bf16 a[168:171], a[156:159], a[40:43], a[168:171]\n"
    "ds_read_b64_tr_b16 a[90:91], v12 offset:59392\n"
    "v_accvgpr_read_b32 v231, a123\n"
    "v_mfma_f32_16x16x32_bf16 a[172:175], a[128:131], v[156:159], a[172:175]\n"
    "v_mul_f32 v224, v224, v242\n"
    "v_mul_f32 v225, v225, v242\n"
    "v_mfma_f32_16x16x32_bf16 a[172:175], a[132:135], v[172:175], a[172:175]\n"
    "ds_read_b64_tr_b16 a[92:93], v12 offset:61440\n"
    "v_mul_f32 v226, v226, v242\n"
    "v_mfma_f32_16x16x32_bf16 a[172:175], a[136:139], v[188:191], a[172:175]\n"
    "ds_read_b64_tr_b16 a[94:95], v12 offset:63488\n"
    "v_mul_f32 v227, v227, v242\n"
    "v_mfma_f32_16x16x32_bf16 a[172:175], a[140:143], v[204:207], a[172:175]\n"
    "v_mul_f32 v228, v228, v242\n"
    "v_mul_f32 v229, v229, v242\n"
    "v_mfma_f32_16x16x32_bf16 a[172:175], a[144:147], v[220:223], a[172:175]\n"
    "v_mul_f32 v230, v230, v242\n"
    "v_mul_f32 v231, v231, v242\n"
    "v_mfma_f32_16x16x32_bf16 a[172:175], a[148:151], a[12:15], a[172:175]\n"
    "v_cvt_pk_bf16_f32 v244, v224, v225\n"
    "v_cvt_pk_bf16_f32 v245, v226, v227\n"
    "v_mfma_f32_16x16x32_bf16 a[172:175], a[152:155], a[28:31], a[172:175]\n"
    "v_cvt_pk_bf16_f32 v246, v228, v229\n"
    "v_cvt_pk_bf16_f32 v247, v230, v231\n"
    "v_mfma_f32_16x16x32_bf16 a[172:175], a[156:159], a[44:47], a[172:175]\n"
    "s_waitcnt lgkmcnt(8)\n"
    "s_add_u32 s90, s5, s39\n"
    "v_mul_u32_u24 v249, 1280, v249\n"
    "buffer_store_dwordx4 v[244:247], v0, s[40:43], s90 offen offset:0 sc1\n"
    "v_mfma_f32_16x16x32_bf16 a[176:179], a[64:67], v[144:147], a[176:179]\n"
    "v_lshl_add_u32 v3, v250, 4, v249\n"
    "v_lshrrev_b32 v248, 4, v0\n"
    "v_mfma_f32_16x16x32_bf16 a[176:179], a[68:71], v[160:163], a[176:179]\n"
    "v_add_u32 v248, 192, v248\n"
    "v_mul_u32_u24 v249, 205, v248\n"
    "v_mfma_f32_16x16x32_bf16 a[176:179], a[72:75], v[176:179], a[176:179]\n"
    "v_lshrrev_b32 v249, 12, v249\n"
    "v_mul_u32_u24 v250, 20, v249\n"
    "v_mfma_f32_16x16x32_bf16 a[176:179], a[76:79], v[192:195], a[176:179]\n"
    "s_waitcnt lgkmcnt(0)\n"
    "v_sub_u32 v250, v248, v250\n"
    "v_bfe_u32 v251, v249, 2, 1\n"
    "v_mfma_f32_16x16x32_bf16 a[176:179], a[80:83], v[208:211], a[176:179]\n"
    "ds_read_b64_tr_b16 a[128:129], v13 offset:0\n"
    "v_lshlrev_b32 v251, 1, v251\n"
    "v_mfma_f32_16x16x32_bf16 a[176:179], a[84:87], a[0:3], a[176:179]\n"
    "ds_read_b64_tr_b16 a[130:131], v13 offset:2048\n"
    "v_xor_b32 v250, v250, v251\n"
    "v_mfma_f32_16x16x32_bf16 a[176:179], a[88:91], a[16:19], a[176:179]\n"
    "v_mul_u32_u24 v249, 1280, v249\n"
    "v_lshl_add_u32 v4, v250, 4, v249\n"
    "v_mfma_f32_16x16x32_bf16 a[176:179], a[92:95], a[32:35], a[176:179]\n"
    "ds_read_b64_tr_b16 a[132:133], v13 offset:4096\n"
    "v_lshrrev_b32 v248, 4, v0\n"
    "v_mfma_f32_16x16x32_bf16 a[180:183], a[64:67], v[148:151], a[180:183]\n"
    "ds_read_b64_tr_b16 a[134:135], v13 offset:6144\n"
    "v_add_u32 v248, 256, v248\n"
    "v_mfma_f32_16x16x32_bf16 a[180:183], a[68:71], v[164:167], a[180:183]\n"
    "v_mul_u32_u24 v249, 205, v248\n"
    "v_lshrrev_b32 v249, 12, v249\n"
    "v_mfma_f32_16x16x32_bf16 a[180:183], a[72:75], v[180:183], a[180:183]\n"
    "ds_read_b64_tr_b16 a[136:137], v13 offset:8192\n"
    "v_mul_u32_u24 v250, 20, v249\n"
    "v_mfma_f32_16x16x32_bf16 a[180:183], a[76:79], v[196:199], a[180:183]\n"
    "ds_read_b64_tr_b16 a[138:139], v13 offset:10240\n"
    "v_sub_u32 v250, v248, v250\n"
    "v_mfma_f32_16x16x32_bf16 a[180:183], a[80:83], v[212:215], a[180:183]\n"
    "v_bfe_u32 v251, v249, 2, 1\n"
    "v_lshlrev_b32 v251, 1, v251\n"
    "v_mfma_f32_16x16x32_bf16 a[180:183], a[84:87], a[4:7], a[180:183]\n"
    "ds_read_b64_tr_b16 a[140:141], v13 offset:12288\n"
    "v_xor_b32 v250, v250, v251\n"
    "v_mfma_f32_16x16x32_bf16 a[180:183], a[88:91], a[20:23], a[180:183]\n"
    "ds_read_b64_tr_b16 a[142:143], v13 offset:14336\n"
    "v_mul_u32_u24 v249, 1280, v249\n"
    "v_mfma_f32_16x16x32_bf16 a[180:183], a[92:95], a[36:39], a[180:183]\n"
    "v_lshl_add_u32 v5, v250, 4, v249\n"
    "v_accvgpr_read_b32 v232, a108\n"
    "v_mfma_f32_16x16x32_bf16 a[184:187], a[64:67], v[152:155], a[184:187]\n"
    "s_mul_i32 s77, s76, 10240\n"
    "ds_read_b64_tr_b16 a[144:145], v13 offset:16384\n"
    "v_accvgpr_read_b32 v233, a109\n"
    "v_mfma_f32_16x16x32_bf16 a[184:187], a[68:71], v[168:171], a[184:187]\n"
    "s_add_u32 s44, s64, 20480\n"
    "s_addc_u32 s45, s65, 0\n"
    "ds_read_b64_tr_b16 a[146:147], v13 offset:18432\n"
    "v_accvgpr_read_b32 v234, a110\n"
    "v_mfma_f32_16x16x32_bf16 a[184:187], a[72:75], v[184:187], a[184:187]\n"
    "s_sub_u32 s46, s66, 20480\n"
    "v_accvgpr_read_b32 v235, a111\n"
    "v_accvgpr_read_b32 v236, a124\n"
    "v_mfma_f32_16x16x32_bf16 a[184:187], a[76:79], v[200:203], a[184:187]\n"
    "s_mov_b32 s47, 0x20000\n"
    "ds_read_b64_tr_b16 a[148:149], v13 offset:20480\n"
    "v_lshrrev_b32 v248, 4, v0\n"
    "v_mfma_f32_16x16x32_bf16 a[184:187], a[80:83], v[216:219], a[184:187]\n"
    "ds_read_b64_tr_b16 a[150:151], v13 offset:22528\n"
    "v_cmp_gt_u32 vcc, s26, v248\n"
    "v_mfma_f32_16x16x32_bf16 a[184:187], a[84:87], a[8:11], a[184:187]\n"
    "v_add_u32 v248, s25, v248\n"
    "v_mov_b32 v249, s27\n"
    "v_mfma_f32_16x16x32_bf16 a[184:187], a[88:91], a[24:27], a[184:187]\n"
    "ds_read_b64_tr_b16 a[152:153], v13 offset:24576\n"
    "v_cndmask_b32 v248, v249, v248, vcc\n"
    "v_mfma_f32_16x16x32_bf16 a[184:187], a[92:95], a[40:43], a[184:187]\n"
    "ds_read_b64_tr_b16 a[154:155], v13 offset:26624\n"
    "v_lshlrev_b32 v15, 6, v248\n"
    "v_mfma_f32_16x16x32_bf16 a[188:191], a[64:67], v[156:159], a[188:191]\n"
    "v_accvgpr_read_b32 v237, a125\n"
    "v_accvgpr_read_b32 v238, a126\n"
    "v_mfma_f32_16x16x32_bf16 a[188:191], a[68:71], v[172:175], a[188:191]\n"
    "ds_read_b64_tr_b16 a[156:157], v13 offset:28672\n"
    "v_accvgpr_read_b32 v239, a127\n"
    "v_mfma_f32_16x16x32_bf16 a[188:191], a[72:75], v[188:191], a[188:191]\n"
    "ds_read_b64_tr_b16 a[158:159], v13 offset:30720\n"
    "v_mul_f32 v232, v232, v243\n"
    "v_mfma_f32_16x16x32_bf16 a[188:191], a[76:79], v[204:207], a[188:191]\n"
    "v_mul_f32 v233, v233, v243\n"
    "v_mul_f32 v234, v234, v243\n"
    "v_mfma_f32_16x16x32_bf16 a[188:191], a[80:83], v[220:223], a[188:191]\n"
    "v_mul_f32 v235, v235, v243\n"
    "v_mul_f32 v236, v236, v243\n"
    "v_mfma_f32_16x16x32_bf16 a[188:191], a[84:87], a[12:15], a[188:191]\n"
    "v_mul_f32 v237, v237, v243\n"
    "v_mul_f32 v238, v238, v243\n"
    "v_mfma_f32_16x16x32_bf16 a[188:191], a[88:91], a[28:31], a[188:191]\n"
    "v_mul_f32 v239, v239, v243\n"
    "v_cvt_pk_bf16_f32 v252, v232, v233\n"
    "v_mfma_f32_16x16x32_bf16 a[188:191], a[92:95], a[44:47], a[188:191]\n"
    "s_waitcnt lgkmcnt(8)\n"
    "v_cvt_pk_bf16_f32 v253, v234, v235\n"
    "v_cvt_pk_bf16_f32 v254, v236, v237\n"
    "v_cvt_pk_bf16_f32 v255, v238, v239\n"
    "s_add_u32 s90, s5, s99\n"
    "buffer_store_dwordx4 v[252:255], v0, s[40:43], s90 offen offset:0 sc1\n"
    "v_mfma_f32_16x16x32_bf16 a[96:99], a[128:131], v[16:19], 0\n"
    "buffer_load_dwordx2 v[250:251], v15, s[40:43], s24 offen offset:0 sc1\n"
    "v_mfma_f32_16x16x32_bf16 a[96:99], a[132:135], v[32:35], a[96:99]\n"
    "v_accvgpr_read_b32 v224, a160\n"
    "v_accvgpr_read_b32 v225, a161\n"
    "v_mfma_f32_16x16x32_bf16 a[96:99], a[136:139], v[48:51], a[96:99]\n"
    "v_accvgpr_read_b32 v226, a162\n"
    "v_accvgpr_read_b32 v227, a163\n"
    "v_mfma_f32_16x16x32_bf16 a[96:99], a[140:143], v[64:67], a[96:99]\n"
    "s_waitcnt lgkmcnt(0)\n"
    "v_accvgpr_read_b32 v228, a176\n"
    "v_accvgpr_read_b32 v229, a177\n"
    "v_mfma_f32_16x16x32_bf16 a[96:99], a[144:147], v[80:83], a[96:99]\n"
    "ds_read_b64_tr_b16 a[64:65], v14 offset:0\n"
    "v_accvgpr_read_b32 v230, a178\n"
    "v_mfma_f32_16x16x32_bf16 a[96:99], a[148:151], v[96:99], a[96:99]\n"
    "ds_read_b64_tr_b16 a[66:67], v14 offset:2048\n"
    "v_accvgpr_read_b32 v231, a179\n"
    "v_mfma_f32_16x16x32_bf16 a[96:99], a[152:155], v[112:115], a[96:99]\n"
    "v_mul_f32 v224, v224, v240\n"
    "v_mul_f32 v225, v225, v240\n"
    "v_mfma_f32_16x16x32_bf16 a[96:99], a[156:159], v[128:131], a[96:99]\n"
    "ds_read_b64_tr_b16 a[68:69], v14 offset:4096\n"
    "v_mul_f32 v226, v226, v240\n"
    "v_mfma_f32_16x16x32_bf16 a[100:103], a[128:131], v[20:23], 0\n"
    "ds_read_b64_tr_b16 a[70:71], v14 offset:6144\n"
    "v_mul_f32 v227, v227, v240\n"
    "v_mfma_f32_16x16x32_bf16 a[100:103], a[132:135], v[36:39], a[100:103]\n"
    "v_mul_f32 v228, v228, v240\n"
    "v_mul_f32 v229, v229, v240\n"
    "v_mfma_f32_16x16x32_bf16 a[100:103], a[136:139], v[52:55], a[100:103]\n"
    "ds_read_b64_tr_b16 a[72:73], v14 offset:8192\n"
    "v_mul_f32 v230, v230, v240\n"
    "v_mfma_f32_16x16x32_bf16 a[100:103], a[140:143], v[68:71], a[100:103]\n"
    "ds_read_b64_tr_b16 a[74:75], v14 offset:10240\n"
    "v_mul_f32 v231, v231, v240\n"
    "v_mfma_f32_16x16x32_bf16 a[100:103], a[144:147], v[84:87], a[100:103]\n"
    "v_cvt_pk_bf16_f32 v244, v224, v225\n"
    "v_cvt_pk_bf16_f32 v245, v226, v227\n"
    "v_mfma_f32_16x16x32_bf16 a[100:103], a[148:151], v[100:103], a[100:103]\n"
    "ds_read_b64_tr_b16 a[76:77], v14 offset:12288\n"
    "v_cvt_pk_bf16_f32 v246, v228, v229\n"
    "v_mfma_f32_16x16x32_bf16 a[100:103], a[152:155], v[116:119], a[100:103]\n"
    "ds_read_b64_tr_b16 a[78:79], v14 offset:14336\n"
    "v_cvt_pk_bf16_f32 v247, v230, v231\n"
    "v_mfma_f32_16x16x32_bf16 a[100:103], a[156:159], v[132:135], a[100:103]\n"
    "s_add_u32 s90, s6, s81\n"
    "v_mfma_f32_16x16x32_bf16 a[104:107], a[128:131], v[24:27], 0\n"
    "buffer_store_dwordx4 v[244:247], v0, s[40:43], s90 offen offset:0 sc1\n"
    "v_mfma_f32_16x16x32_bf16 a[104:107], a[132:135], v[40:43], a[104:107]\n"
    "ds_read_b64_tr_b16 a[80:81], v14 offset:16384\n"
    "ds_read_b64_tr_b16 a[82:83], v14 offset:18432\n"
    "v_mfma_f32_16x16x32_bf16 a[104:107], a[136:139], v[56:59], a[104:107]\n"
    "v_mfma_f32_16x16x32_bf16 a[104:107], a[140:143], v[72:75], a[104:107]\n"
    "ds_read_b64_tr_b16 a[84:85], v14 offset:20480\n"
    "v_mfma_f32_16x16x32_bf16 a[104:107], a[144:147], v[88:91], a[104:107]\n"
    "ds_read_b64_tr_b16 a[86:87], v14 offset:22528\n"
    "v_mfma_f32_16x16x32_bf16 a[104:107], a[148:151], v[104:107], a[104:107]\n"
    "v_mfma_f32_16x16x32_bf16 a[104:107], a[152:155], v[120:123], a[104:107]\n"
    "ds_read_b64_tr_b16 a[88:89], v14 offset:24576\n"
    "v_mfma_f32_16x16x32_bf16 a[104:107], a[156:159], v[136:139], a[104:107]\n"
    "ds_read_b64_tr_b16 a[90:91], v14 offset:26624\n"
    "v_mfma_f32_16x16x32_bf16 a[108:111], a[128:131], v[28:31], 0\n"
    "v_mfma_f32_16x16x32_bf16 a[108:111], a[132:135], v[44:47], a[108:111]\n"
    "ds_read_b64_tr_b16 a[92:93], v14 offset:28672\n"
    "v_mfma_f32_16x16x32_bf16 a[108:111], a[136:139], v[60:63], a[108:111]\n"
    "ds_read_b64_tr_b16 a[94:95], v14 offset:30720\n"
    "v_mfma_f32_16x16x32_bf16 a[108:111], a[140:143], v[76:79], a[108:111]\n"
    "v_mfma_f32_16x16x32_bf16 a[108:111], a[144:147], v[92:95], a[108:111]\n"
    "v_mfma_f32_16x16x32_bf16 a[108:111], a[148:151], v[108:111], a[108:111]\n"
    "v_mfma_f32_16x16x32_bf16 a[108:111], a[152:155], v[124:127], a[108:111]\n"
    "v_mfma_f32_16x16x32_bf16 a[108:111], a[156:159], v[140:143], a[108:111]\n"
    "s_waitcnt lgkmcnt(8)\n"
    "v_mfma_f32_16x16x32_bf16 a[112:115], a[64:67], v[16:19], 0\n"
    "v_accvgpr_read_b32 v232, a164\n"
    "v_accvgpr_read_b32 v233, a165\n"
    "v_mfma_f32_16x16x32_bf16 a[112:115], a[68:71], v[32:35], a[112:115]\n"
    "v_accvgpr_read_b32 v234, a166\n"
    "v_accvgpr_read_b32 v235, a167\n"
    "v_mfma_f32_16x16x32_bf16 a[112:115], a[72:75], v[48:51], a[112:115]\n"
    "v_accvgpr_read_b32 v236, a180\n"
    "v_accvgpr_read_b32 v237, a181\n"
    "v_mfma_f32_16x16x32_bf16 a[112:115], a[76:79], v[64:67], a[112:115]\n"
    "s_waitcnt lgkmcnt(0)\n"
    "v_accvgpr_read_b32 v238, a182\n"
    "v_accvgpr_read_b32 v239, a183\n"
    "v_mfma_f32_16x16x32_bf16 a[112:115], a[80:83], v[80:83], a[112:115]\n"
    "s_add_u32 s94, s97, 4\n"
    "ds_read_b64_tr_b16 a[128:129], v13 offset:32768\n"
    "v_accvgpr_write_b32 a53, s94\n"
    "v_mfma_f32_16x16x32_bf16 a[112:115], a[84:87], v[96:99], a[112:115]\n"
    "s_waitcnt vmcnt(2)\n"
    "ds_read_b64_tr_b16 a[130:131], v13 offset:34816\n"
    "v_mul_f32 v232, v232, v241\n"
    "v_mfma_f32_16x16x32_bf16 a[112:115], a[88:91], v[112:115], a[112:115]\n"
    "buffer_store_dwordx2 a[52:53], v7, s[40:43], s23 offen offset:0 sc1\n"
    "v_mfma_f32_16x16x32_bf16 a[112:115], a[92:95], v[128:131], a[112:115]\n"
    "ds_read_b64_tr_b16 a[132:133], v13 offset:36864\n"
    "v_mul_f32 v233, v233, v241\n"
    "v_mfma_f32_16x16x32_bf16 a[116:119], a[64:67], v[20:23], 0\n"
    "s_min_u32 s94, s22, 3\n"
    "ds_read_b64_tr_b16 a[134:135], v13 offset:38912\n"
    "v_mul_f32 v234, v234, v241\n"
    "v_mfma_f32_16x16x32_bf16 a[116:119], a[68:71], v[36:39], a[116:119]\n"
    "s_add_u32 s94, s94, s97\n"
    "v_mul_f32 v235, v235, v241\n"
    "v_mul_f32 v236, v236, v241\n"
    "v_mfma_f32_16x16x32_bf16 a[116:119], a[72:75], v[52:55], a[116:119]\n"
    "s_waitcnt vmcnt(2)\n"
    "ds_read_b64_tr_b16 a[136:137], v13 offset:40960\n"
    "v_xor_b32 v248, s96, v250\n"
    "v_mfma_f32_16x16x32_bf16 a[116:119], a[76:79], v[68:71], a[116:119]\n"
    "ds_read_b64_tr_b16 a[138:139], v13 offset:43008\n"
    "v_subrev_u32 v249, s94, v251\n"
    "v_mfma_f32_16x16x32_bf16 a[116:119], a[80:83], v[84:87], a[116:119]\n"
    "v_lshrrev_b32 v249, 4, v249\n"
    "v_or_b32 v248, v248, v249\n"
    "v_mfma_f32_16x16x32_bf16 a[116:119], a[84:87], v[100:103], a[116:119]\n"
    "ds_read_b64_tr_b16 a[140:141], v13 offset:45056\n"
    "v_cmp_ne_u32 vcc, 0, v248\n"
    "v_mfma_f32_16x16x32_bf16 a[116:119], a[88:91], v[116:119], a[116:119]\n"
    "s_nop 0\n"
    "ds_read_b64_tr_b16 a[142:143], v13 offset:47104\n"
    "v_mul_f32 v237, v237, v241\n"
    "v_mfma_f32_16x16x32_bf16 a[116:119], a[92:95], v[132:135], a[116:119]\n"
    "s_cbranch_vccnz .L_spin0_%=\n"
    ".L_back0_%=:\n"
    "v_mul_f32 v238, v238, v241\n"
    "v_mul_f32 v239, v239, v241\n"
    "v_mfma_f32_16x16x32_bf16 a[120:123], a[64:67], v[24:27], 0\n"
    "s_add_u32 s89, s20, s81\n"
    "ds_read_b64_tr_b16 a[144:145], v13 offset:49152\n"
    "v_cvt_pk_bf16_f32 v252, v232, v233\n"
    "v_mfma_f32_16x16x32_bf16 a[120:123], a[68:71], v[40:43], a[120:123]\n"
    "ds_read_b64_tr_b16 a[146:147], v13 offset:51200\n"
    "v_cvt_pk_bf16_f32 v253, v234, v235\n"
    "v_mfma_f32_16x16x32_bf16 a[120:123], a[72:75], v[56:59], a[120:123]\n"
    "v_cvt_pk_bf16_f32 v254, v236, v237\n"
    "v_cvt_pk_bf16_f32 v255, v238, v239\n"
    "v_mfma_f32_16x16x32_bf16 a[120:123], a[76:79], v[72:75], a[120:123]\n"
    "s_add_u32 s89, s89, 0\n"
    "ds_read_b64_tr_b16 a[148:149], v13 offset:53248\n"
    "s_add_u32 s90, s6, s3\n"
    "v_mfma_f32_16x16x32_bf16 a[120:123], a[80:83], v[88:91], a[120:123]\n"
    "buffer_store_dwordx4 v[252:255], v0, s[40:43], s90 offen offset:0 sc1\n"
    "v_mfma_f32_16x16x32_bf16 a[120:123], a[84:87], v[104:107], a[120:123]\n"
    "ds_read_b64_tr_b16 a[150:151], v13 offset:55296\n"
    "v_mfma_f32_16x16x32_bf16 a[120:123], a[88:91], v[120:123], a[120:123]\n"
    "ds_read_b64_tr_b16 a[152:153], v13 offset:57344\n"
    "v_mfma_f32_16x16x32_bf16 a[120:123], a[92:95], v[136:139], a[120:123]\n"
    "buffer_load_dwordx4 a[192:195], v0, s[40:43], s89 offen offset:0 sc1\n"
    "v_mfma_f32_16x16x32_bf16 a[124:127], a[64:67], v[28:31], 0\n"
    "ds_read_b64_tr_b16 a[154:155], v13 offset:59392\n"
    "v_mfma_f32_16x16x32_bf16 a[124:127], a[68:71], v[44:47], a[124:127]\n"
    "ds_read_b64_tr_b16 a[156:157], v13 offset:61440\n"
    "v_mfma_f32_16x16x32_bf16 a[124:127], a[72:75], v[60:63], a[124:127]\n"
    "ds_read_b64_tr_b16 a[158:159], v13 offset:63488\n"
    "v_mfma_f32_16x16x32_bf16 a[124:127], a[76:79], v[76:79], a[124:127]\n"
    "buffer_load_dwordx4 a[196:199], v0, s[40:43], s89 offen offset:1024 sc1\n"
    "v_mfma_f32_16x16x32_bf16 a[124:127], a[80:83], v[92:95], a[124:127]\n"
    "v_mfma_f32_16x16x32_bf16 a[124:127], a[84:87], v[108:111], a[124:127]\n"
    "v_mfma_f32_16x16x32_bf16 a[124:127], a[88:91], v[124:127], a[124:127]\n"
    "v_mfma_f32_16x16x32_bf16 a[124:127], a[92:95], v[140:143], a[124:127]\n"
    "s_waitcnt lgkmcnt(8)\n"
    "v_mfma_f32_16x16x32_bf16 a[96:99], a[128:131], v[144:147], a[96:99]\n"
    "buffer_load_dwordx4 a[200:203], v0, s[40:43], s89 offen offset:2048 sc1\n"
    "v_mfma_f32_16x16x32_bf16 a[96:99], a[132:135], v[160:163], a[96:99]\n"
    "v_accvgpr_read_b32 v224, a168\n"
    "v_accvgpr_read_b32 v225, a169\n"
    "v_mfma_f32_16x16x32_bf16 a[96:99], a[136:139], v[176:179], a[96:99]\n"
    "buffer_load_dwordx4 a[204:207], v0, s[40:43], s89 offen offset:3072 sc1\n"
    "v_mfma_f32_16x16x32_bf16 a[96:99], a[140:143], v[192:195], a[96:99]\n"
    "s_waitcnt lgkmcnt(0)\n"
    "v_accvgpr_read_b32 v226, a170\n"
    "v_accvgpr_read_b32 v227, a171\n"
    "v_mfma_f32_16x16x32_bf16 a[96:99], a[144:147], v[208:211], a[96:99]\n"
    "ds_read_b64_tr_b16 a[64:65], v14 offset:32768\n"
    "v_accvgpr_read_b32 v228, a184\n"
    "v_mfma_f32_16x16x32_bf16 a[96:99], a[148:151], a[0:3], a[96:99]\n"
    "ds_read_b64_tr_b16 a[66:67], v14 offset:34816\n"
    "v_accvgpr_read_b32 v229, a185\n"
    "v_mfma_f32_16x16x32_bf16 a[96:99], a[152:155], a[16:19], a[96:99]\n"
    "s_add_u32 s89, s20, s3\n"
    "v_accvgpr_read_b32 v230, a186\n"
    "v_accvgpr_read_b32 v231, a187\n"
    "v_mfma_f32_16x16x32_bf16 a[96:99], a[156:159], a[32:35], a[96:99]\n"
    "ds_read_b64_tr_b16 a[68:69], v14 offset:36864\n"
    "v_mul_f32 v224, v224, v242\n"
    "v_mfma_f32_16x16x32_bf16 a[100:103], a[128:131], v[148:151], a[100:103]\n"
    "ds_read_b64_tr_b16 a[70:71], v14 offset:38912\n"
    "v_mul_f32 v225, v225, v242\n"
    "v_mfma_f32_16x16x32_bf16 a[100:103], a[132:135], v[164:167], a[100:103]\n"
    "v_mul_f32 v226, v226, v242\n"
    "v_mul_f32 v227, v227, v242\n"
    "v_mfma_f32_16x16x32_bf16 a[100:103], a[136:139], v[180:183], a[100:103]\n"
    "s_add_u32 s89, s89, 0\n"
    "ds_read_b64_tr_b16 a[72:73], v14 offset:40960\n"
    "v_mul_f32 v228, v228, v242\n"
    "v_mfma_f32_16x16x32_bf16 a[100:103], a[140:143], v[196:199], a[100:103]\n"
    "ds_read_b64_tr_b16 a[74:75], v14 offset:43008\n"
    "v_mul_f32 v229, v229, v242\n"
    "v_mfma_f32_16x16x32_bf16 a[100:103], a[144:147], v[212:215], a[100:103]\n"
    "v_mul_f32 v230, v230, v242\n"
    "v_mul_f32 v231, v231, v242\n"
    "v_mfma_f32_16x16x32_bf16 a[100:103], a[148:151], a[4:7], a[100:103]\n"
    "ds_read_b64_tr_b16 a[76:77], v14 offset:45056\n"
    "v_cvt_pk_bf16_f32 v244, v224, v225\n"
    "v_mfma_f32_16x16x32_bf16 a[100:103], a[152:155], a[20:23], a[100:103]\n"
    "ds_read_b64_tr_b16 a[78:79], v14 offset:47104\n"
    "v_cvt_pk_bf16_f32 v245, v226, v227\n"
    "v_mfma_f32_16x16x32_bf16 a[100:103], a[156:159], a[36:39], a[100:103]\n"
    "buffer_load_dwordx4 a[208:211], v0, s[40:43], s89 offen offset:0 sc1\n"
    "v_mfma_f32_16x16x32_bf16 a[104:107], a[128:131], v[152:155], a[104:107]\n"
    "ds_read_b64_tr_b16 a[80:81], v14 offset:49152\n"
    "v_cvt_pk_bf16_f32 v246, v228, v229\n"
    "v_mfma_f32_16x16x32_bf16 a[104:107], a[132:135], v[168:171], a[104:107]\n"
    "ds_read_b64_tr_b16 a[82:83], v14 offset:51200\n"
    "v_cvt_pk_bf16_f32 v247, v230, v231\n"
    "v_mfma_f32_16x16x32_bf16 a[104:107], a[136:139], v[184:187], a[104:107]\n"
    "buffer_load_dwordx4 a[212:215], v0, s[40:43], s89 offen offset:1024 sc1\n"
    "v_mfma_f32_16x16x32_bf16 a[104:107], a[140:143], v[200:203], a[104:107]\n"
    "s_add_u32 s90, s6, s39\n"
    "ds_read_b64_tr_b16 a[84:85], v14 offset:53248\n"
    "v_mfma_f32_16x16x32_bf16 a[104:107], a[144:147], v[216:219], a[104:107]\n"
    "buffer_store_dwordx4 v[244:247], v0, s[40:43], s90 offen offset:0 sc1\n"
    "v_mfma_f32_16x16x32_bf16 a[104:107], a[148:151], a[8:11], a[104:107]\n"
    "buffer_load_dwordx4 a[216:219], v0, s[40:43], s89 offen offset:2048 sc1\n"
    "v_mfma_f32_16x16x32_bf16 a[104:107], a[152:155], a[24:27], a[104:107]\n"
    "ds_read_b64_tr_b16 a[86:87], v14 offset:55296\n"
    "ds_read_b64_tr_b16 a[88:89], v14 offset:57344\n"
    "v_mfma_f32_16x16x32_bf16 a[104:107], a[156:159], a[40:43], a[104:107]\n"
    "ds_read_b64_tr_b16 a[90:91], v14 offset:59392\n"
    "v_mfma_f32_16x16x32_bf16 a[108:111], a[128:131], v[156:159], a[108:111]\n"
    "v_mfma_f32_16x16x32_bf16 a[108:111], a[132:135], v[172:175], a[108:111]\n"
    "buffer_load_dwordx4 a[220:223], v0, s[40:43], s89 offen offset:3072 sc1\n"
    "v_mfma_f32_16x16x32_bf16 a[108:111], a[136:139], v[188:191], a[108:111]\n"
    "ds_read_b64_tr_b16 a[92:93], v14 offset:61440\n"
    "ds_read_b64_tr_b16 a[94:95], v14 offset:63488\n"
    "v_mfma_f32_16x16x32_bf16 a[108:111], a[140:143], v[204:207], a[108:111]\n"
    "v_mfma_f32_16x16x32_bf16 a[108:111], a[144:147], v[220:223], a[108:111]\n"
    "v_mfma_f32_16x16x32_bf16 a[108:111], a[148:151], a[12:15], a[108:111]\n"
    "s_add_u32 s89, s20, s39\n"
    "v_mfma_f32_16x16x32_bf16 a[108:111], a[152:155], a[28:31], a[108:111]\n"
    "v_mfma_f32_16x16x32_bf16 a[108:111], a[156:159], a[44:47], a[108:111]\n"
    "s_waitcnt lgkmcnt(0)\n"
    "s_barrier\n"
    "v_mfma_f32_16x16x32_bf16 a[112:115], a[64:67], v[144:147], a[112:115]\n"
    "s_add_u32 s89, s89, 0\n"
    "v_accvgpr_read_b32 v232, a172\n"
    "v_accvgpr_read_b32 v233, a173\n"
    "v_mfma_f32_16x16x32_bf16 a[112:115], a[68:71], v[160:163], a[112:115]\n"
    "v_accvgpr_read_b32 v234, a174\n"
    "v_accvgpr_read_b32 v235, a175\n"
    "v_mfma_f32_16x16x32_bf16 a[112:115], a[72:75], v[176:179], a[112:115]\n"
    "v_accvgpr_read_b32 v236, a188\n"
    "v_accvgpr_read_b32 v237, a189\n"
    "v_mfma_f32_16x16x32_bf16 a[112:115], a[76:79], v[192:195], a[112:115]\n"
    "v_accvgpr_read_b32 v238, a190\n"
    "v_accvgpr_read_b32 v239, a191\n"
    "v_mfma_f32_16x16x32_bf16 a[112:115], a[80:83], v[208:211], a[112:115]\n"
    "ds_read_b64_tr_b16 a[128:129], v8 offset:0\n"
    "v_mul_f32 v232, v232, v243\n"
    "v_mfma_f32_16x16x32_bf16 a[112:115], a[84:87], a[0:3], a[112:115]\n"
    "ds_read_b64_tr_b16 a[130:131], v8 offset:2048\n"
    "v_mul_f32 v233, v233, v243\n"
    "v_mfma_f32_16x16x32_bf16 a[112:115], a[88:91], a[16:19], a[112:115]\n"
    "buffer_load_dwordx4 a[224:227], v0, s[40:43], s89 offen offset:0 sc1\n"
    "v_mfma_f32_16x16x32_bf16 a[112:115], a[92:95], a[32:35], a[112:115]\n"
    "ds_read_b64_tr_b16 a[132:133], v8 offset:4096\n"
    "v_mul_f32 v234, v234, v243\n"
    "v_mfma_f32_16x16x32_bf16 a[116:119], a[64:67], v[148:151], a[116:119]\n"
    "ds_read_b64_tr_b16 a[134:135], v8 offset:6144\n"
    "v_mul_f32 v235, v235, v243\n"
    "v_mfma_f32_16x16x32_bf16 a[116:119], a[68:71], v[164:167], a[116:119]\n"
    "buffer_load_dwordx4 a[228:231], v0, s[40:43], s89 offen offset:1024 sc1\n"
    "v_mfma_f32_16x16x32_bf16 a[116:119], a[72:75], v[180:183], a[116:119]\n"
    "ds_read_b64_tr_b16 a[136:137], v8 offset:8192\n"
    "v_mul_f32 v236, v236, v243\n"
    "v_mfma_f32_16x16x32_bf16 a[116:119], a[76:79], v[196:199], a[116:119]\n"
    "ds_read_b64_tr_b16 a[138:139], v8 offset:10240\n"
    "v_mul_f32 v237, v237, v243\n"
    "v_mfma_f32_16x16x32_bf16 a[116:119], a[80:83], v[212:215], a[116:119]\n"
    "buffer_load_dwordx4 a[232:235], v0, s[40:43], s89 offen offset:2048 sc1\n"
    "v_mfma_f32_16x16x32_bf16 a[116:119], a[84:87], a[4:7], a[116:119]\n"
    "ds_read_b64_tr_b16 a[140:141], v8 offset:12288\n"
    "v_mul_f32 v238, v238, v243\n"
    "v_mfma_f32_16x16x32_bf16 a[116:119], a[88:91], a[20:23], a[116:119]\n"
    "ds_read_b64_tr_b16 a[142:143], v8 offset:14336\n"
    "v_mul_f32 v239, v239, v243\n"
    "v_mfma_f32_16x16x32_bf16 a[116:119], a[92:95], a[36:39], a[116:119]\n"
    "buffer_load_dwordx4 a[236:239], v0, s[40:43], s89 offen offset:3072 sc1\n"
    "v_mfma_f32_16x16x32_bf16 a[120:123], a[64:67], v[152:155], a[120:123]\n"
    "ds_read_b64_tr_b16 a[144:145], v8 offset:16384\n"
    "v_cvt_pk_bf16_f32 v252, v232, v233\n"
    "v_mfma_f32_16x16x32_bf16 a[120:123], a[68:71], v[168:171], a[120:123]\n"
    "ds_read_b64_tr_b16 a[146:147], v8 offset:18432\n"
    "v_cvt_pk_bf16_f32 v253, v234, v235\n"
    "v_mfma_f32_16x16x32_bf16 a[120:123], a[72:75], v[184:187], a[120:123]\n"
    "v_cvt_pk_bf16_f32 v254, v236, v237\n"
    "v_cvt_pk_bf16_f32 v255, v238, v239\n"
    "v_mfma_f32_16x16x32_bf16 a[120:123], a[76:79], v[200:203], a[120:123]\n"
    "s_add_u32 s89, s20, s99\n"
    "ds_read_b64_tr_b16 a[148:149], v8 offset:20480\n"
    "s_add_u32 s90, s6, s99\n"
    "v_mfma_f32_16x16x32_bf16 a[120:123], a[80:83], v[216:219], a[120:123]\n"
    "buffer_store_dwordx4 v[252:255], v0, s[40:43], s90 offen offset:0 sc1\n"
    "v_mfma_f32_16x16x32_bf16 a[120:123], a[84:87], a[8:11], a[120:123]\n"
    "ds_read_b64_tr_b16 a[150:151], v8 offset:22528\n"
    "v_mfma_f32_16x16x32_bf16 a[120:123], a[88:91], a[24:27], a[120:123]\n"
    "ds_read_b64_tr_b16 a[152:153], v8 offset:24576\n"
    "v_mfma_f32_16x16x32_bf16 a[120:123], a[92:95], a[40:43], a[120:123]\n"
    "s_add_u32 s89, s89, 0\n"
    "ds_read_b64_tr_b16 a[154:155], v8 offset:26624\n"
    "v_mfma_f32_16x16x32_bf16 a[124:127], a[64:67], v[156:159], a[124:127]\n"
    "v_mfma_f32_16x16x32_bf16 a[124:127], a[68:71], v[172:175], a[124:127]\n"
    "ds_read_b64_tr_b16 a[156:157], v8 offset:28672\n"
    "v_mfma_f32_16x16x32_bf16 a[124:127], a[72:75], v[188:191], a[124:127]\n"
    "ds_read_b64_tr_b16 a[158:159], v8 offset:30720\n"
    "v_mfma_f32_16x16x32_bf16 a[124:127], a[76:79], v[204:207], a[124:127]\n"
    "buffer_load_dwordx4 a[240:243], v0, s[40:43], s89 offen offset:0 sc1\n"
    "v_mfma_f32_16x16x32_bf16 a[124:127], a[80:83], v[220:223], a[124:127]\n"
    "v_mfma_f32_16x16x32_bf16 a[124:127], a[84:87], a[12:15], a[124:127]\n"
    "v_mfma_f32_16x16x32_bf16 a[124:127], a[88:91], a[28:31], a[124:127]\n"
    "buffer_load_dwordx4 a[244:247], v0, s[40:43], s89 offen offset:1024 sc1\n"
    "v_mfma_f32_16x16x32_bf16 a[124:127], a[92:95], a[44:47], a[124:127]\n"
    "s_waitcnt lgkmcnt(8)\n"
    "v_mfma_f32_16x16x32_bf16 a[160:163], a[128:131], v[16:19], 0\n"
    "v_accvgpr_read_b32 v224, a96\n"
    "v_accvgpr_read_b32 v225, a97\n"
    "v_mfma_f32_16x16x32_bf16 a[160:163], a[132:135], v[32:35], a[160:163]\n"
    "v_accvgpr_read_b32 v226, a98\n"
    "v_accvgpr_read_b32 v227, a99\n"
    "v_mfma_f32_16x16x32_bf16 a[160:163], a[136:139], v[48:51], a[160:163]\n"
    "buffer_load_dwordx4 a[248:251], v0, s[40:43], s89 offen offset:2048 sc1\n"
    "v_mfma_f32_16x16x32_bf16 a[160:163], a[140:143], v[64:67], a[160:163]\n"
    "s_waitcnt lgkmcnt(0)\n"
    "v_accvgpr_read_b32 v228, a112\n"
    "v_accvgpr_read_b32 v229, a113\n"
    "v_mfma_f32_16x16x32_bf16 a[160:163], a[144:147], v[80:83], a[160:163]\n"
    "ds_read_b64_tr_b16 a[64:65], v9 offset:0\n"
    "v_accvgpr_read_b32 v230, a114\n"
    "v_mfma_f32_16x16x32_bf16 a[160:163], a[148:151], v[96:99], a[160:163]\n"
    "ds_read_b64_tr_b16 a[66:67], v9 offset:2048\n"
    "v_accvgpr_read_b32 v231, a115\n"
    "v_mfma_f32_16x16x32_bf16 a[160:163], a[152:155], v[112:115], a[160:163]\n"
    "buffer_load_dwordx4 a[252:255], v0, s[40:43], s89 offen offset:3072 sc1\n"
    "v_mfma_f32_16x16x32_bf16 a[160:163], a[156:159], v[128:131], a[160:163]\n"
    "ds_read_b64_tr_b16 a[68:69], v9 offset:4096\n"
    "v_mul_f32 v224, v224, v240\n"
    "v_mfma_f32_16x16x32_bf16 a[164:167], a[128:131], v[20:23], 0\n"
    "ds_read_b64_tr_b16 a[70:71], v9 offset:6144\n"
    "v_mul_f32 v225, v225, v240\n"
    "v_mfma_f32_16x16x32_bf16 a[164:167], a[132:135], v[36:39], a[164:167]\n"
    "v_mul_f32 v226, v226, v240\n"
    "v_mul_f32 v227, v227, v240\n"
    "v_mfma_f32_16x16x32_bf16 a[164:167], a[136:139], v[52:55], a[164:167]\n"
    "ds_read_b64_tr_b16 a[72:73], v9 offset:8192\n"
    "v_lshrrev_b32 v248, 6, v0\n"
    "v_mfma_f32_16x16x32_bf16 a[164:167], a[140:143], v[68:71], a[164:167]\n"
    "ds_read_b64_tr_b16 a[74:75], v9 offset:10240\n"
    "v_mul_u32_u24 v248, 320, v248\n"
    "v_mfma_f32_16x16x32_bf16 a[164:167], a[144:147], v[84:87], a[164:167]\n"
    "v_bfe_u32 v249, v0, 4, 2\n"
    "v_lshl_add_u32 v248, v249, 3, v248\n"
    "v_mfma_f32_16x16x32_bf16 a[164:167], a[148:151], v[100:103], a[164:167]\n"
    "ds_read_b64_tr_b16 a[76:77], v9 offset:12288\n"
    "v_add_u32 v248, 65536, v248\n"
    "v_mfma_f32_16x16x32_bf16 a[164:167], a[152:155], v[116:119], a[164:167]\n"
    "ds_read_b64_tr_b16 a[78:79], v9 offset:14336\n"
    "v_bfe_u32 v249, v0, 8, 1\n"
    "v_mfma_f32_16x16x32_bf16 a[164:167], a[156:159], v[132:135], a[164:167]\n"
    "v_lshl_add_u32 v11, v249, 5, v248\n"
    "v_xor_b32 v249, 1, v249\n"
    "v_mfma_f32_16x16x32_bf16 a[168:171], a[128:131], v[24:27], 0\n"
    "ds_read_b64_tr_b16 a[80:81], v9 offset:16384\n"
    "v_lshl_add_u32 v12, v249, 5, v248\n"
    "v_mfma_f32_16x16x32_bf16 a[168:171], a[132:135], v[40:43], a[168:171]\n"
    "v_add_u32 v13, 40960, v11\n"
    "v_add_u32 v14, 40960, v12\n"
    "v_mfma_f32_16x16x32_bf16 a[168:171], a[136:139], v[56:59], a[168:171]\n"
    "s_add_u32 m0, s77, 65536\n"
    "ds_read_b64_tr_b16 a[82:83], v9 offset:18432\n"
    "v_mul_f32 v228, v228, v240\n"
    "v_mfma_f32_16x16x32_bf16 a[168:171], a[140:143], v[72:75], a[168:171]\n"
    "ds_read_b64_tr_b16 a[84:85], v9 offset:20480\n"
    "v_mul_f32 v229, v229, v240\n"
    "v_mfma_f32_16x16x32_bf16 a[168:171], a[144:147], v[88:91], a[168:171]\n"
    "ds_read_b64_tr_b16 a[86:87], v9 offset:22528\n"
    "v_mul_f32 v230, v230, v240\n"
    "v_mfma_f32_16x16x32_bf16 a[168:171], a[148:151], v[104:107], a[168:171]\n"
    "buffer_load_dwordx4 v1, s[64:67], s12 offen offset:0 lds\n"
    "v_mfma_f32_16x16x32_bf16 a[168:171], a[152:155], v[120:123], a[168:171]\n"
    "ds_read_b64_tr_b16 a[88:89], v9 offset:24576\n"
    "v_mul_f32 v231, v231, v240\n"
    "v_mfma_f32_16x16x32_bf16 a[168:171], a[156:159], v[136:139], a[168:171]\n"
    "s_add_u32 m0, s77, 66560\n"
    "ds_read_b64_tr_b16 a[90:91], v9 offset:26624\n"
    "v_cvt_pk_bf16_f32 v244, v224, v225\n"
    "v_mfma_f32_16x16x32_bf16 a[172:175], a[128:131], v[28:31], 0\n"
    "v_cvt_pk_bf16_f32 v245, v226, v227\n"
    "v_cvt_pk_bf16_f32 v246, v228, v229\n"
    "v_mfma_f32_16x16x32_bf16 a[172:175], a[132:135], v[44:47], a[172:175]\n"
    "ds_read_b64_tr_b16 a[92:93], v9 offset:28672\n"
    "v_cvt_pk_bf16_f32 v247, v230, v231\n"
    "v_mfma_f32_16x16x32_bf16 a[172:175], a[136:139], v[60:63], a[172:175]\n"
    "buffer_load_dwordx4 v2, s[64:67], s12 offen offset:0 lds\n"
    "v_mfma_f32_16x16x32_bf16 a[172:175], a[140:143], v[76:79], a[172:175]\n"
    "s_add_u32 s90, s7, s81\n"
    "ds_read_b64_tr_b16 a[94:95], v9 offset:30720\n"
    "v_mfma_f32_16x16x32_bf16 a[172:175], a[144:147], v[92:95], a[172:175]\n"
    "buffer_store_dwordx4 v[244:247], v0, s[40:43], s90 offen offset:0 sc1\n"
    "v_mfma_f32_16x16x32_bf16 a[172:175], a[148:151], v[108:111], a[172:175]\n"
    "s_add_u32 m0, s77, 67584\n"
    "v_mfma_f32_16x16x32_bf16 a[172:175], a[152:155], v[124:127], a[172:175]\n"
    "v_mfma_f32_16x16x32_bf16 a[172:175], a[156:159], v[140:143], a[172:175]\n"
    "s_waitcnt lgkmcnt(8)\n"
    "v_mfma_f32_16x16x32_bf16 a[176:179], a[64:67], v[16:19], 0\n"
    "buffer_load_dwordx4 v3, s[64:67], s12 offen offset:0 lds\n"
    "v_mfma_f32_16x16x32_bf16 a[176:179], a[68:71], v[32:35], a[176:179]\n"
    "v_accvgpr_read_b32 v232, a100\n"
    "v_accvgpr_read_b32 v233, a101\n"
    "v_mfma_f32_16x16x32_bf16 a[176:179], a[72:75], v[48:51], a[176:179]\n"
    "v_accvgpr_read_b32 v234, a102\n"
    "v_accvgpr_read_b32 v235, a103\n"
    "v_mfma_f32_16x16x32_bf16 a[176:179], a[76:79], v[64:67], a[176:179]\n"
    "s_waitcnt lgkmcnt(0)\n"
    "v_accvgpr_read_b32 v236, a116\n"
    "v_accvgpr_read_b32 v237, a117\n"
    "v_mfma_f32_16x16x32_bf16 a[176:179], a[80:83], v[80:83], a[176:179]\n"
    "s_add_u32 s94, s97, 5\n"
    "ds_read_b64_tr_b16 a[128:129], v8 offset:32768\n"
    "v_accvgpr_write_b32 a53, s94\n"
    "v_mfma_f32_16x16x32_bf16 a[176:179], a[84:87], v[96:99], a[176:179]\n"
    "s_waitcnt vmcnt(8)\n"
    "ds_read_b64_tr_b16 a[130:131], v8 offset:34816\n"
    "v_accvgpr_read_b32 v238, a118\n"
    "v_mfma_f32_16x16x32_bf16 a[176:179], a[88:91], v[112:115], a[176:179]\n"
    "buffer_store_dwordx2 a[52:53], v7, s[40:43], s23 offen offset:0 sc1\n"
    "v_mfma_f32_16x16x32_bf16 a[176:179], a[92:95], v[128:131], a[176:179]\n"
    "ds_read_b64_tr_b16 a[132:133], v8 offset:36864\n"
    "v_accvgpr_read_b32 v239, a119\n"
    "v_mfma_f32_16x16x32_bf16 a[180:183], a[64:67], v[20:23], 0\n"
    "ds_read_b64_tr_b16 a[134:135], v8 offset:38912\n"
    "v_mul_f32 v232, v232, v241\n"
    "v_mfma_f32_16x16x32_bf16 a[180:183], a[68:71], v[36:39], a[180:183]\n"
    "v_mul_f32 v233, v233, v241\n"
    "v_mul_f32 v234, v234, v241\n"
    "v_mfma_f32_16x16x32_bf16 a[180:183], a[72:75], v[52:55], a[180:183]\n"
    "ds_read_b64_tr_b16 a[136:137], v8 offset:40960\n"
    "v_mul_f32 v235, v235, v241\n"
    "v_mfma_f32_16x16x32_bf16 a[180:183], a[76:79], v[68:71], a[180:183]\n"
    "ds_read_b64_tr_b16 a[138:139], v8 offset:43008\n"
    "v_mul_f32 v236, v236, v241\n"
    "v_mfma_f32_16x16x32_bf16 a[180:183], a[80:83], v[84:87], a[180:183]\n"
    "s_add_u32 m0, s77, 68608\n"
    "v_mul_f32 v237, v237, v241\n"
    "v_mul_f32 v238, v238, v241\n"
    "v_mfma_f32_16x16x32_bf16 a[180:183], a[84:87], v[100:103], a[180:183]\n"
    "ds_read_b64_tr_b16 a[140:141], v8 offset:45056\n"
    "v_mul_f32 v239, v239, v241\n"
    "v_mfma_f32_16x16x32_bf16 a[180:183], a[88:91], v[116:119], a[180:183]\n"
    "ds_read_b64_tr_b16 a[142:143], v8 offset:47104\n"
    "v_cvt_pk_bf16_f32 v252, v232, v233\n"
    "v_mfma_f32_16x16x32_bf16 a[180:183], a[92:95], v[132:135], a[180:183]\n"
    "buffer_load_dwordx4 v4, s[64:67], s12 offen offset:0 lds\n"
    "v_mfma_f32_16x16x32_bf16 a[184:187], a[64:67], v[24:27], 0\n"
    "ds_read_b64_tr_b16 a[144:145], v8 offset:49152\n"
    "v_cvt_pk_bf16_f32 v253, v234, v235\n"
    "v_mfma_f32_16x16x32_bf16 a[184:187], a[68:71], v[40:43], a[184:187]\n"
    "ds_read_b64_tr_b16 a[146:147], v8 offset:51200\n"
    "v_cvt_pk_bf16_f32 v254, v236, v237\n"
    "v_mfma_f32_16x16x32_bf16 a[184:187], a[72:75], v[56:59], a[184:187]\n"
    "s_add_u32 m0, s77, 69632\n"
    "v_cvt_pk_bf16_f32 v255, v238, v239\n"
    "s_add_u32 s90, s7, s3\n"
    "v_mfma_f32_16x16x32_bf16 a[184:187], a[76:79], v[72:75], a[184:187]\n"
    "buffer_store_dwordx4 v[252:255], v0, s[40:43], s90 offen offset:0 sc1\n"
    "v_mfma_f32_16x16x32_bf16 a[184:187], a[80:83], v[88:91], a[184:187]\n"
    "ds_read_b64_tr_b16 a[148:149], v8 offset:53248\n"
    "ds_read_b64_tr_b16 a[150:151], v8 offset:55296\n"
    "v_mfma_f32_16x16x32_bf16 a[184:187], a[84:87], v[104:107], a[184:187]\n"
    "buffer_load_dwordx4 v5, s[64:67], s12 offen offset:0 lds\n"
    "v_mfma_f32_16x16x32_bf16 a[184:187], a[88:91], v[120:123], a[184:187]\n"
    "ds_read_b64_tr_b16 a[152:153], v8 offset:57344\n"
    "v_mfma_f32_16x16x32_bf16 a[184:187], a[92:95], v[136:139], a[184:187]\n"
    "ds_read_b64_tr_b16 a[154:155], v8 offset:59392\n"
    "v_mfma_f32_16x16x32_bf16 a[188:191], a[64:67], v[28:31], 0\n"
    "s_add_u32 m0, s77, 70656\n"
    "v_mfma_f32_16x16x32_bf16 a[188:191], a[68:71], v[44:47], a[188:191]\n"
    "ds_read_b64_tr_b16 a[156:157], v8 offset:61440\n"
    "v_mfma_f32_16x16x32_bf16 a[188:191], a[72:75], v[60:63], a[188:191]\n"
    "ds_read_b64_tr_b16 a[158:159], v8 offset:63488\n"
    "v_mfma_f32_16x16x32_bf16 a[188:191], a[76:79], v[76:79], a[188:191]\n"
    "buffer_load_dwordx4 v1, s[44:47], s12 offen offset:0 lds\n"
    "v_mfma_f32_16x16x32_bf16 a[188:191], a[80:83], v[92:95], a[188:191]\n"
    "v_mfma_f32_16x16x32_bf16 a[188:191], a[84:87], v[108:111], a[188:191]\n"
    "v_mfma_f32_16x16x32_bf16 a[188:191], a[88:91], v[124:127], a[188:191]\n"
    "s_add_u32 m0, s77, 71680\n"
    "v_mfma_f32_16x16x32_bf16 a[188:191], a[92:95], v[140:143], a[188:191]\n"
    "s_waitcnt lgkmcnt(8)\n"
    "v_mfma_f32_16x16x32_bf16 a[160:163], a[128:131], v[144:147], a[160:163]\n"
    "v_accvgpr_read_b32 v224, a104\n"
    "v_accvgpr_read_b32 v225, a105\n"
    "v_mfma_f32_16x16x32_bf16 a[160:163], a[132:135], v[160:163], a[160:163]\n"
    "v_accvgpr_read_b32 v226, a106\n"
    "v_accvgpr_read_b32 v227, a107\n"
    "v_mfma_f32_16x16x32_bf16 a[160:163], a[136:139], v[176:179], a[160:163]\n"
    "buffer_load_dwordx4 v2, s[44:47], s12 offen offset:0 lds\n"
    "v_mfma_f32_16x16x32_bf16 a[160:163], a[140:143], v[192:195], a[160:163]\n"
    "s_waitcnt lgkmcnt(0)\n"
    "v_accvgpr_read_b32 v228, a120\n"
    "v_accvgpr_read_b32 v229, a121\n"
    "v_mfma_f32_16x16x32_bf16 a[160:163], a[144:147], v[208:211], a[160:163]\n"
    "ds_read_b64_tr_b16 a[64:65], v9 offset:32768\n"
    "v_accvgpr_read_b32 v230, a122\n"
    "v_mfma_f32_16x16x32_bf16 a[160:163], a[148:151], a[0:3], a[160:163]\n"
    "s_add_u32 m0, s77, 72704\n"
    "ds_read_b64_tr_b16 a[66:67], v9 offset:34816\n"
    "v_accvgpr_read_b32 v231, a123\n"
    "v_mfma_f32_16x16x32_bf16 a[160:163], a[152:155], a[16:19], a[160:163]\n"
    "v_mul_f32 v224, v224, v242\n"
    "v_mul_f32 v225, v225, v242\n"
    "v_mfma_f32_16x16x32_bf16 a[160:163], a[156:159], a[32:35], a[160:163]\n"
    "ds_read_b64_tr_b16 a[68:69], v9 offset:36864\n"
    "v_mul_f32 v226, v226, v242\n"
    "v_mfma_f32_16x16x32_bf16 a[164:167], a[128:131], v[148:151], a[164:167]\n"
    "ds_read_b64_tr_b16 a[70:71], v9 offset:38912\n"
    "v_mul_f32 v227, v227, v242\n"
    "v_mfma_f32_16x16x32_bf16 a[164:167], a[132:135], v[164:167], a[164:167]\n"
    "buffer_load_dwordx4 v3, s[44:47], s12 offen offset:0 lds\n"
    "v_mfma_f32_16x16x32_bf16 a[164:167], a[136:139], v[180:183], a[164:167]\n"
    "ds_read_b64_tr_b16 a[72:73], v9 offset:40960\n"
    "v_mul_f32 v228, v228, v242\n"
    "v_mfma_f32_16x16x32_bf16 a[164:167], a[140:143], v[196:199], a[164:167]\n"
    "s_add_u32 m0, s77, 73728\n"
    "ds_read_b64_tr_b16 a[74:75], v9 offset:43008\n"
    "v_mul_f32 v229, v229, v242\n"
    "v_mfma_f32_16x16x32_bf16 a[164:167], a[144:147], v[212:215], a[164:167]\n"
    "v_mul_f32 v230, v230, v242\n"
    "v_mul_f32 v231, v231, v242\n"
    "v_mfma_f32_16x16x32_bf16 a[164:167], a[148:151], a[4:7], a[164:167]\n"
    "ds_read_b64_tr_b16 a[76:77], v9 offset:45056\n"
    "v_cvt_pk_bf16_f32 v244, v224, v225\n"
    "v_mfma_f32_16x16x32_bf16 a[164:167], a[152:155], a[20:23], a[164:167]\n"
    "ds_read_b64_tr_b16 a[78:79], v9 offset:47104\n"
    "v_cvt_pk_bf16_f32 v245, v226, v227\n"
    "v_mfma_f32_16x16x32_bf16 a[164:167], a[156:159], a[36:39], a[164:167]\n"
    "buffer_load_dwordx4 v4, s[44:47], s12 offen offset:0 lds\n"
    "v_mfma_f32_16x16x32_bf16 a[168:171], a[128:131], v[152:155], a[168:171]\n"
    "ds_read_b64_tr_b16 a[80:81], v9 offset:49152\n"
    "v_cvt_pk_bf16_f32 v246, v228, v229\n"
    "v_mfma_f32_16x16x32_bf16 a[168:171], a[132:135], v[168:171], a[168:171]\n"
    "s_add_u32 m0, s77, 74752\n"
    "ds_read_b64_tr_b16 a[82:83], v9 offset:51200\n"
    "v_cvt_pk_bf16_f32 v247, v230, v231\n"
    "v_mfma_f32_16x16x32_bf16 a[168:171], a[136:139], v[184:187], a[168:171]\n"
    "s_add_u32 s90, s7, s39\n"
    "v_mfma_f32_16x16x32_bf16 a[168:171], a[140:143], v[200:203], a[168:171]\n"
    "buffer_store_dwordx4 v[244:247], v0, s[40:43], s90 offen offset:0 sc1\n"
    "v_mfma_f32_16x16x32_bf16 a[168:171], a[144:147], v[216:219], a[168:171]\n"
    "ds_read_b64_tr_b16 a[84:85], v9 offset:53248\n"
    "ds_read_b64_tr_b16 a[86:87], v9 offset:55296\n"
    "v_mfma_f32_16x16x32_bf16 a[168:171], a[148:151], a[8:11], a[168:171]\n"
    "buffer_load_dwordx4 v5, s[44:47], s12 offen offset:0 lds\n"
    "v_mfma_f32_16x16x32_bf16 a[168:171], a[152:155], a[24:27], a[168:171]\n"
    "ds_read_b64_tr_b16 a[88:89], v9 offset:57344\n"
    "v_mfma_f32_16x16x32_bf16 a[168:171], a[156:159], a[40:43], a[168:171]\n"
    "ds_read_b64_tr_b16 a[90:91], v9 offset:59392\n"
    "v_mfma_f32_16x16x32_bf16 a[172:175], a[128:131], v[156:159], a[172:175]\n"
    "v_mfma_f32_16x16x32_bf16 a[172:175], a[132:135], v[172:175], a[172:175]\n"
    "ds_read_b64_tr_b16 a[92:93], v9 offset:61440\n"
    "v_mfma_f32_16x16x32_bf16 a[172:175], a[136:139], v[188:191], a[172:175]\n"
    "ds_read_b64_tr_b16 a[94:95], v9 offset:63488\n"
    "v_mfma_f32_16x16x32_bf16 a[172:175], a[140:143], v[204:207], a[172:175]\n"
    "v_mfma_f32_16x16x32_bf16 a[172:175], a[144:147], v[220:223], a[172:175]\n"
    "buffer_load_dwordx2 v[250:251], v15, s[40:43], s24 offen offset:0 sc1\n"
    "v_mfma_f32_16x16x32_bf16 a[172:175], a[148:151], a[12:15], a[172:175]\n"
    "v_mfma_f32_16x16x32_bf16 a[172:175], a[152:155], a[28:31], a[172:175]\n"
    "v_mfma_f32_16x16x32_bf16 a[172:175], a[156:159], a[44:47], a[172:175]\n"
    "s_waitcnt lgkmcnt(8)\n"
    "v_mfma_f32_16x16x32_bf16 a[176:179], a[64:67], v[144:147], a[176:179]\n"
    "v_accvgpr_read_b32 v232, a108\n"
    "v_accvgpr_read_b32 v233, a109\n"
    "v_mfma_f32_16x16x32_bf16 a[176:179], a[68:71], v[160:163], a[176:179]\n"
    "v_accvgpr_read_b32 v234, a110\n"
    "v_accvgpr_read_b32 v235, a111\n"
    "v_mfma_f32_16x16x32_bf16 a[176:179], a[72:75], v[176:179], a[176:179]\n"
    "v_accvgpr_read_b32 v236, a124\n"
    "v_accvgpr_read_b32 v237, a125\n"
    "v_mfma_f32_16x16x32_bf16 a[176:179], a[76:79], v[192:195], a[176:179]\n"
    "s_waitcnt lgkmcnt(0)\n"
    "v_accvgpr_read_b32 v238, a126\n"
    "v_accvgpr_read_b32 v239, a127\n"
    "v_mfma_f32_16x16x32_bf16 a[176:179], a[80:83], v[208:211], a[176:179]\n"
    "s_add_u32 m0, s77, 106496\n"
    "ds_read_b64_tr_b16 a[128:129], v10 offset:0\n"
    "v_mul_f32 v232, v232, v243\n"
    "v_mfma_f32_16x16x32_bf16 a[176:179], a[84:87], a[0:3], a[176:179]\n"
    "ds_read_b64_tr_b16 a[130:131], v10 offset:2048\n"
    "v_mul_f32 v233, v233, v243\n"
    "v_mfma_f32_16x16x32_bf16 a[176:179], a[88:91], a[16:19], a[176:179]\n"
    "v_mul_f32 v234, v234, v243\n"
    "v_mul_f32 v235, v235, v243\n"
    "v_mfma_f32_16x16x32_bf16 a[176:179], a[92:95], a[32:35], a[176:179]\n"
    "ds_read_b64_tr_b16 a[132:133], v10 offset:4096\n"
    "v_mul_f32 v236, v236, v243\n"
    "v_mfma_f32_16x16x32_bf16 a[180:183], a[64:67], v[148:151], a[180:183]\n"
    "buffer_load_dwordx4 v1, s[64:67], s13 offen offset:0 lds\n"
    "v_mfma_f32_16x16x32_bf16 a[180:183], a[68:71], v[164:167], a[180:183]\n"
    "ds_read_b64_tr_b16 a[134:135], v10 offset:6144\n"
    "v_mul_f32 v237, v237, v243\n"
    "v_mfma_f32_16x16x32_bf16 a[180:183], a[72:75], v[180:183], a[180:183]\n"
    "s_add_u32 m0, s77, 107520\n"
    "ds_read_b64_tr_b16 a[136:137], v10 offset:8192\n"
    "v_mul_f32 v238, v238, v243\n"
    "v_mfma_f32_16x16x32_bf16 a[180:183], a[76:79], v[196:199], a[180:183]\n"
    "ds_read_b64_tr_b16 a[138:139], v10 offset:10240\n"
    "v_mul_f32 v239, v239, v243\n"
    "v_mfma_f32_16x16x32_bf16 a[180:183], a[80:83], v[212:215], a[180:183]\n"
    "v_cvt_pk_bf16_f32 v252, v232, v233\n"
    "v_cvt_pk_bf16_f32 v253, v234, v235\n"
    "v_mfma_f32_16x16x32_bf16 a[180:183], a[84:87], a[4:7], a[180:183]\n"
    "ds_read_b64_tr_b16 a[140:141], v10 offset:12288\n"
    "v_cvt_pk_bf16_f32 v254, v236, v237\n"
    "v_mfma_f32_16x16x32_bf16 a[180:183], a[88:91], a[20:23], a[180:183]\n"
    "buffer_load_dwordx4 v2, s[64:67], s13 offen offset:0 lds\n"
    "v_mfma_f32_16x16x32_bf16 a[180:183], a[92:95], a[36:39], a[180:183]\n"
    "ds_read_b64_tr_b16 a[142:143], v10 offset:14336\n"
    "v_cvt_pk_bf16_f32 v255, v238, v239\n"
    "v_mfma_f32_16x16x32_bf16 a[184:187], a[64:67], v[152:155], a[184:187]\n"
    "s_add_u32 m0, s77, 108544\n"
    "ds_read_b64_tr_b16 a[144:145], v10 offset:16384\n"
    "s_add_u32 s90, s7, s99\n"
    "v_mfma_f32_16x16x32_bf16 a[184:187], a[68:71], v[168:171], a[184:187]\n"
    "buffer_store_dwordx4 v[252:255], v0, s[40:43], s90 offen offset:0 sc1\n"
    "v_mfma_f32_16x16x32_bf16 a[184:187], a[72:75], v[184:187], a[184:187]\n"
    "ds_read_b64_tr_b16 a[146:147], v10 offset:18432\n"
    "v_accvgpr_read_b32 v224, a160\n"
    "v_mfma_f32_16x16x32_bf16 a[184:187], a[76:79], v[200:203], a[184:187]\n"
    "ds_read_b64_tr_b16 a[148:149], v10 offset:20480\n"
    "v_accvgpr_read_b32 v225, a161\n"
    "v_mfma_f32_16x16x32_bf16 a[184:187], a[80:83], v[216:219], a[184:187]\n"
    "buffer_load_dwordx4 v3, s[64:67], s13 offen offset:0 lds\n"
    "v_mfma_f32_16x16x32_bf16 a[184:187], a[84:87], a[8:11], a[184:187]\n"
    "ds_read_b64_tr_b16 a[150:151], v10 offset:22528\n"
    "v_accvgpr_read_b32 v226, a162\n"
    "v_mfma_f32_16x16x32_bf16 a[184:187], a[88:91], a[24:27], a[184:187]\n"
    "s_add_u32 m0, s77, 109568\n"
    "ds_read_b64_tr_b16 a[152:153], v10 offset:24576\n"
    "v_accvgpr_read_b32 v227, a163\n"
    "v_mfma_f32_16x16x32_bf16 a[184:187], a[92:95], a[40:43], a[184:187]\n"
    "ds_read_b64_tr_b16 a[154:155], v10 offset:26624\n"
    "v_accvgpr_read_b32 v228, a176\n"
    "v_mfma_f32_16x16x32_bf16 a[188:191], a[64:67], v[156:159], a[188:191]\n"
    "v_accvgpr_read_b32 v229, a177\n"
    "v_accvgpr_read_b32 v230, a178\n"
    "v_mfma_f32_16x16x32_bf16 a[188:191], a[68:71], v[172:175], a[188:191]\n"
    "ds_read_b64_tr_b16 a[156:157], v10 offset:28672\n"
    "v_accvgpr_read_b32 v231, a179\n"
    "v_mfma_f32_16x16x32_bf16 a[188:191], a[72:75], v[188:191], a[188:191]\n"
    "buffer_load_dwordx4 v4, s[64:67], s13 offen offset:0 lds\n"
    "v_mfma_f32_16x16x32_bf16 a[188:191], a[76:79], v[204:207], a[188:191]\n"
    "ds_read_b64_tr_b16 a[158:159], v10 offset:30720\n"
    "v_mul_f32 v224, v224, v240\n"
    "v_mfma_f32_16x16x32_bf16 a[188:191], a[80:83], v[220:223], a[188:191]\n"
    "s_add_u32 m0, s77, 110592\n"
    "v_mul_f32 v225, v225, v240\n"
    "v_mul_f32 v226, v226, v240\n"
    "v_mfma_f32_16x16x32_bf16 a[188:191], a[84:87], a[12:15], a[188:191]\n"
    "v_mul_f32 v227, v227, v240\n"
    "v_mul_f32 v228, v228, v240\n"
    "v_mfma_f32_16x16x32_bf16 a[188:191], a[88:91], a[28:31], a[188:191]\n"
    "v_mul_f32 v229, v229, v240\n"
    "v_mul_f32 v230, v230, v240\n"
    "v_mfma_f32_16x16x32_bf16 a[188:191], a[92:95], a[44:47], a[188:191]\n"
    "s_waitcnt lgkmcnt(8)\n"
    "v_mul_f32 v231, v231, v240\n"
    "v_cvt_pk_bf16_f32 v244, v224, v225\n"
    "v_mfma_f32_16x16x32_bf16 a[96:99], a[128:131], v[16:19], 0\n"
    "v_cvt_pk_bf16_f32 v245, v226, v227\n"
    "v_cvt_pk_bf16_f32 v246, v228, v229\n"
    "v_cvt_pk_bf16_f32 v247, v230, v231\n"
    "s_add_u32 s90, s8, s81\n"
    "buffer_store_dwordx4 v[244:247], v0, s[40:43], s90 offen offset:0 sc1\n"
    "v_mfma_f32_16x16x32_bf16 a[96:99], a[132:135], v[32:35], a[96:99]\n"
    "buffer_load_dwordx4 v5, s[64:67], s13 offen offset:0 lds\n"
    "v_mfma_f32_16x16x32_bf16 a[96:99], a[136:139], v[48:51], a[96:99]\n"
    "v_accvgpr_read_b32 v232, a164\n"
    "v_accvgpr_read_b32 v233, a165\n"
    "v_mfma_f32_16x16x32_bf16 a[96:99], a[140:143], v[64:67], a[96:99]\n"
    "s_waitcnt lgkmcnt(0)\n"
    "v_accvgpr_read_b32 v234, a166\n"
    "v_accvgpr_read_b32 v235, a167\n"
    "v_mfma_f32_16x16x32_bf16 a[96:99], a[144:147], v[80:83], a[96:99]\n"
    "s_add_u32 m0, s77, 111616\n"
    "ds_read_b64_tr_b16 a[64:65], v6 offset:0\n"
    "v_accvgpr_read_b32 v236, a180\n"
    "v_mfma_f32_16x16x32_bf16 a[96:99], a[148:151], v[96:99], a[96:99]\n"
    "ds_read_b64_tr_b16 a[66:67], v6 offset:2048\n"
    "v_accvgpr_read_b32 v237, a181\n"
    "v_mfma_f32_16x16x32_bf16 a[96:99], a[152:155], v[112:115], a[96:99]\n"
    "buffer_load_dwordx4 v1, s[44:47], s13 offen offset:0 lds\n"
    "v_mfma_f32_16x16x32_bf16 a[96:99], a[156:159], v[128:131], a[96:99]\n"
    "ds_read_b64_tr_b16 a[68:69], v6 offset:4096\n"
    "v_accvgpr_read_b32 v238, a182\n"
    "v_mfma_f32_16x16x32_bf16 a[100:103], a[128:131], v[20:23], 0\n"
    "ds_read_b64_tr_b16 a[70:71], v6 offset:6144\n"
    "v_accvgpr_read_b32 v239, a183\n"
    "v_mfma_f32_16x16x32_bf16 a[100:103], a[132:135], v[36:39], a[100:103]\n"
    "s_add_u32 m0, s77, 112640\n"
    "v_mul_f32 v232, v232, v241\n"
    "v_mul_f32 v233, v233, v241\n"
    "v_mfma_f32_16x16x32_bf16 a[100:103], a[136:139], v[52:55], a[100:103]\n"
    "ds_read_b64_tr_b16 a[72:73], v6 offset:8192\n"
    "v_mul_f32 v234, v234, v241\n"
    "v_mfma_f32_16x16x32_bf16 a[100:103], a[140:143], v[68:71], a[100:103]\n"
    "ds_read_b64_tr_b16 a[74:75], v6 offset:10240\n"
    "v_mul_f32 v235, v235, v241\n"
    "v_mfma_f32_16x16x32_bf16 a[100:103], a[144:147], v[84:87], a[100:103]\n"
    "buffer_load_dwordx4 v2, s[44:47], s13 offen offset:0 lds\n"
    "v_mfma_f32_16x16x32_bf16 a[100:103], a[148:151], v[100:103], a[100:103]\n"
    "ds_read_b64_tr_b16 a[76:77], v6 offset:12288\n"
    "v_mul_f32 v236, v236, v241\n"
    "v_mfma_f32_16x16x32_bf16 a[100:103], a[152:155], v[116:119], a[100:103]\n"
    "ds_read_b64_tr_b16 a[78:79], v6 offset:14336\n"
    "v_mul_f32 v237, v237, v241\n"
    "v_mfma_f32_16x16x32_bf16 a[100:103], a[156:159], v[132:135], a[100:103]\n"
    "s_add_u32 m0, s77, 113664\n"
    "v_mul_f32 v238, v238, v241\n"
    "v_mul_f32 v239, v239, v241\n"
    "v_mfma_f32_16x16x32_bf16 a[104:107], a[128:131], v[24:27], 0\n"
    "ds_read_b64_tr_b16 a[80:81], v6 offset:16384\n"
    "v_cvt_pk_bf16_f32 v252, v232, v233\n"
    "v_mfma_f32_16x16x32_bf16 a[104:107], a[132:135], v[40:43], a[104:107]\n"
    "v_cvt_pk_bf16_f32 v253, v234, v235\n"
    "v_cvt_pk_bf16_f32 v254, v236, v237\n"
    "v_mfma_f32_16x16x32_bf16 a[104:107], a[136:139], v[56:59], a[104:107]\n"
    "v_cvt_pk_bf16_f32 v255, v238, v239\n"
    "s_add_u32 s90, s8, s3\n"
    "buffer_store_dwordx4 v[252:255], v0, s[40:43], s90 offen offset:0 sc1\n"
    "v_mfma_f32_16x16x32_bf16 a[104:107], a[140:143], v[72:75], a[104:107]\n"
    "ds_read_b64_tr_b16 a[82:83], v6 offset:18432\n"
    "ds_read_b64_tr_b16 a[84:85], v6 offset:20480\n"
    "v_mfma_f32_16x16x32_bf16 a[104:107], a[144:147], v[88:91], a[104:107]\n"
    "buffer_load_dwordx4 v3, s[44:47], s13 offen offset:0 lds\n"
    "v_mfma_f32_16x16x32_bf16 a[104:107], a[148:151], v[104:107], a[104:107]\n"
    "s_add_u32 m0, s77, 114688\n"
    "ds_read_b64_tr_b16 a[86:87], v6 offset:22528\n"
    "v_accvgpr_read_b32 v224, a168\n"
    "v_mfma_f32_16x16x32_bf16 a[104:107], a[152:155], v[120:123], a[104:107]\n"
    "ds_read_b64_tr_b16 a[88:89], v6 offset:24576\n"
    "v_accvgpr_read_b32 v225, a169\n"
    "v_mfma_f32_16x16x32_bf16 a[104:107], a[156:159], v[136:139], a[104:107]\n"
    "ds_read_b64_tr_b16 a[90:91], v6 offset:26624\n"
    "v_accvgpr_read_b32 v226, a170\n"
    "v_mfma_f32_16x16x32_bf16 a[108:111], a[128:131], v[28:31], 0\n"
    "buffer_load_dwordx4 v4, s[44:47], s13 offen offset:0 lds\n"
    "v_mfma_f32_16x16x32_bf16 a[108:111], a[132:135], v[44:47], a[108:111]\n"
    "ds_read_b64_tr_b16 a[92:93], v6 offset:28672\n"
    "v_accvgpr_read_b32 v227, a171\n"
    "v_mfma_f32_16x16x32_bf16 a[108:111], a[136:139], v[60:63], a[108:111]\n"
    "ds_read_b64_tr_b16 a[94:95], v6 offset:30720\n"
    "v_accvgpr_read_b32 v228, a184\n"
    "v_mfma_f32_16x16x32_bf16 a[108:111], a[140:143], v[76:79], a[108:111]\n"
    "s_add_u32 m0, s77, 115712\n"
    "v_accvgpr_read_b32 v229, a185\n"
    "v_accvgpr_read_b32 v230, a186\n"
    "v_mfma_f32_16x16x32_bf16 a[108:111], a[144:147], v[92:95], a[108:111]\n"
    "v_accvgpr_read_b32 v231, a187\n"
    "v_mul_f32 v224, v224, v242\n"
    "v_mfma_f32_16x16x32_bf16 a[108:111], a[148:151], v[108:111], a[108:111]\n"
    "v_mul_f32 v225, v225, v242\n"
    "v_mul_f32 v226, v226, v242\n"
    "v_mfma_f32_16x16x32_bf16 a[108:111], a[152:155], v[124:127], a[108:111]\n"
    "buffer_load_dwordx4 v5, s[44:47], s13 offen offset:0 lds\n"
    "v_mfma_f32_16x16x32_bf16 a[108:111], a[156:159], v[140:143], a[108:111]\n"
    "s_waitcnt lgkmcnt(8)\n"
    "v_mul_f32 v227, v227, v242\n"
    "v_mul_f32 v228, v228, v242\n"
    "v_mfma_f32_16x16x32_bf16 a[112:115], a[64:67], v[16:19], 0\n"
    "v_mul_f32 v229, v229, v242\n"
    "v_mul_f32 v230, v230, v242\n"
    "v_mfma_f32_16x16x32_bf16 a[112:115], a[68:71], v[32:35], a[112:115]\n"
    "v_mul_f32 v231, v231, v242\n"
    "v_cvt_pk_bf16_f32 v244, v224, v225\n"
    "v_mfma_f32_16x16x32_bf16 a[112:115], a[72:75], v[48:51], a[112:115]\n"
    "v_cvt_pk_bf16_f32 v245, v226, v227\n"
    "v_cvt_pk_bf16_f32 v246, v228, v229\n"
    "v_mfma_f32_16x16x32_bf16 a[112:115], a[76:79], v[64:67], a[112:115]\n"
    "s_waitcnt lgkmcnt(0)\n"
    "v_cvt_pk_bf16_f32 v247, v230, v231\n"
    "s_add_u32 s90, s8, s39\n"
    "v_mfma_f32_16x16x32_bf16 a[112:115], a[80:83], v[80:83], a[112:115]\n"
    "buffer_store_dwordx4 v[244:247], v0, s[40:43], s90 offen offset:0 sc1\n"
    "v_mfma_f32_16x16x32_bf16 a[112:115], a[84:87], v[96:99], a[112:115]\n"
    "s_add_u32 s94, s97, 6\n"
    "ds_read_b64_tr_b16 a[128:129], v10 offset:32768\n"
    "ds_read_b64_tr_b16 a[130:131], v10 offset:34816\n"
    "v_mfma_f32_16x16x32_bf16 a[112:115], a[88:91], v[112:115], a[112:115]\n"
    "v_accvgpr_write_b32 a53, s94\n"
    "v_accvgpr_read_b32 v232, a172\n"
    "v_mfma_f32_16x16x32_bf16 a[112:115], a[92:95], v[128:131], a[112:115]\n"
    "s_waitcnt vmcnt(11)\n"
    "ds_read_b64_tr_b16 a[132:133], v10 offset:36864\n"
    "v_accvgpr_read_b32 v233, a173\n"
    "v_mfma_f32_16x16x32_bf16 a[116:119], a[64:67], v[20:23], 0\n"
    "buffer_store_dwordx2 a[52:53], v7, s[40:43], s23 offen offset:0 sc1\n"
    "v_mfma_f32_16x16x32_bf16 a[116:119], a[68:71], v[36:39], a[116:119]\n"
    "ds_read_b64_tr_b16 a[134:135], v10 offset:38912\n"
    "v_accvgpr_read_b32 v234, a174\n"
    "v_mfma_f32_16x16x32_bf16 a[116:119], a[72:75], v[52:55], a[116:119]\n"
    "ds_read_b64_tr_b16 a[136:137], v10 offset:40960\n"
    "v_accvgpr_read_b32 v235, a175\n"
    "v_mfma_f32_16x16x32_bf16 a[116:119], a[76:79], v[68:71], a[116:119]\n"
    "ds_read_b64_tr_b16 a[138:139], v10 offset:43008\n"
    "v_accvgpr_read_b32 v236, a188\n"
    "v_mfma_f32_16x16x32_bf16 a[116:119], a[80:83], v[84:87], a[116:119]\n"
    "s_min_u32 s94, s22, 5\n"
    "v_accvgpr_read_b32 v237, a189\n"
    "v_accvgpr_read_b32 v238, a190\n"
    "v_mfma_f32_16x16x32_bf16 a[116:119], a[84:87], v[100:103], a[116:119]\n"
    "s_add_u32 s94, s94, s97\n"
    "ds_read_b64_tr_b16 a[140:141], v10 offset:45056\n"
    "v_accvgpr_read_b32 v239, a191\n"
    "v_mfma_f32_16x16x32_bf16 a[116:119], a[88:91], v[116:119], a[116:119]\n"
    "ds_read_b64_tr_b16 a[142:143], v10 offset:47104\n"
    "v_xor_b32 v248, s96, v250\n"
    "v_mfma_f32_16x16x32_bf16 a[116:119], a[92:95], v[132:135], a[116:119]\n"
    "v_subrev_u32 v249, s94, v251\n"
    "v_lshrrev_b32 v249, 4, v249\n"
    "v_mfma_f32_16x16x32_bf16 a[120:123], a[64:67], v[24:27], 0\n"
    "ds_read_b64_tr_b16 a[144:145], v10 offset:49152\n"
    "v_or_b32 v248, v248, v249\n"
    "v_mfma_f32_16x16x32_bf16 a[120:123], a[68:71], v[40:43], a[120:123]\n"
    "v_cmp_ne_u32 vcc, 0, v248\n"
    "s_nop 0\n"
    "v_mfma_f32_16x16x32_bf16 a[120:123], a[72:75], v[56:59], a[120:123]\n"
    "s_cbranch_vccnz .L_spin1_%=\n"
    ".L_back1_%=:\n"
    "ds_read_b64_tr_b16 a[146:147], v10 offset:51200\n"
    "v_mul_f32 v232, v232, v243\n"
    "v_mfma_f32_16x16x32_bf16 a[120:123], a[76:79], v[72:75], a[120:123]\n"
    "s_add_u32 s89, s20, s39\n"
    "ds_read_b64_tr_b16 a[148:149], v10 offset:53248\n"
    "v_mul_f32 v233, v233, v243\n"
    "v_mfma_f32_16x16x32_bf16 a[120:123], a[80:83], v[88:91], a[120:123]\n"
    "ds_read_b64_tr_b16 a[150:151], v10 offset:55296\n"
    "v_mul_f32 v234, v234, v243\n"
    "v_mfma_f32_16x16x32_bf16 a[120:123], a[84:87], v[104:107], a[120:123]\n"
    "s_add_u32 s89, s89, 4096\n"
    "v_mul_f32 v235, v235, v243\n"
    "v_mul_f32 v236, v236, v243\n"
    "v_mfma_f32_16x16x32_bf16 a[120:123], a[88:91], v[120:123], a[120:123]\n"
    "v_mul_f32 v237, v237, v243\n"
    "v_mul_f32 v238, v238, v243\n"
    "v_mul_f32 v239, v239, v243\n"
    "v_cvt_pk_bf16_f32 v252, v232, v233\n"
    "v_cvt_pk_bf16_f32 v253, v234, v235\n"
    "v_cvt_pk_bf16_f32 v254, v236, v237\n"
    "v_cvt_pk_bf16_f32 v255, v238, v239\n"
    "s_add_u32 s90, s8, s99\n"
    "buffer_store_dwordx4 v[252:255], v0, s[40:43], s90 offen offset:0 sc1\n"
    "v_mfma_f32_16x16x32_bf16 a[120:123], a[92:95], v[136:139], a[120:123]\n"
    "ds_read_b64_tr_b16 a[152:153], v10 offset:57344\n"
    "ds_read_b64_tr_b16 a[154:155], v10 offset:59392\n"
    "v_mfma_f32_16x16x32_bf16 a[124:127], a[64:67], v[28:31], 0\n"
    "v_mfma_f32_16x16x32_bf16 a[124:127], a[68:71], v[44:47], a[124:127]\n"
    "buffer_load_dwordx4 a[160:163], v0, s[40:43], s89 offen offset:0 sc1\n"
    "v_mfma_f32_16x16x32_bf16 a[124:127], a[72:75], v[60:63], a[124:127]\n"
    "ds_read_b64_tr_b16 a[156:157], v10 offset:61440\n"
    "ds_read_b64_tr_b16 a[158:159], v10 offset:63488\n"
    "v_mfma_f32_16x16x32_bf16 a[124:127], a[76:79], v[76:79], a[124:127]\n"
    "v_mfma_f32_16x16x32_bf16 a[124:127], a[80:83], v[92:95], a[124:127]\n"
    "v_mfma_f32_16x16x32_bf16 a[124:127], a[84:87], v[108:111], a[124:127]\n"
    "buffer_load_dwordx4 a[164:167], v0, s[40:43], s89 offen offset:1024 sc1\n"
    "v_mfma_f32_16x16x32_bf16 a[124:127], a[88:91], v[124:127], a[124:127]\n"
    "v_mfma_f32_16x16x32_bf16 a[124:127], a[92:95], v[140:143], a[124:127]\n"
    "s_waitcnt lgkmcnt(8)\n"
    "v_mfma_f32_16x16x32_bf16 a[96:99], a[128:131], v[144:147], a[96:99]\n"
    "v_mfma_f32_16x16x32_bf16 a[96:99], a[132:135], v[160:163], a[96:99]\n"
    "buffer_load_dwordx4 a[168:171], v0, s[40:43], s89 offen offset:2048 sc1\n"
    "v_mfma_f32_16x16x32_bf16 a[96:99], a[136:139], v[176:179], a[96:99]\n"
    "v_mfma_f32_16x16x32_bf16 a[96:99], a[140:143], v[192:195], a[96:99]\n"
    "s_waitcnt lgkmcnt(0)\n"
    "v_mfma_f32_16x16x32_bf16 a[96:99], a[144:147], v[208:211], a[96:99]\n"
    "ds_read_b64_tr_b16 a[64:65], v6 offset:32768\n"
    "v_mfma_f32_16x16x32_bf16 a[96:99], a[148:151], a[0:3], a[96:99]\n"
    "buffer_load_dwordx4 a[172:175], v0, s[40:43], s89 offen offset:3072 sc1\n"
    "v_mfma_f32_16x16x32_bf16 a[96:99], a[152:155], a[16:19], a[96:99]\n"
    "ds_read_b64_tr_b16 a[66:67], v6 offset:34816\n"
    "v_mfma_f32_16x16x32_bf16 a[96:99], a[156:159], a[32:35], a[96:99]\n"
    "ds_read_b64_tr_b16 a[68:69], v6 offset:36864\n"
    "v_mfma_f32_16x16x32_bf16 a[100:103], a[128:131], v[148:151], a[100:103]\n"
    "ds_read_b64_tr_b16 a[70:71], v6 offset:38912\n"
    "v_mfma_f32_16x16x32_bf16 a[100:103], a[132:135], v[164:167], a[100:103]\n"
    "s_add_u32 s89, s20, s99\n"
    "v_mfma_f32_16x16x32_bf16 a[100:103], a[136:139], v[180:183], a[100:103]\n"
    "ds_read_b64_tr_b16 a[72:73], v6 offset:40960\n"
    "v_mfma_f32_16x16x32_bf16 a[100:103], a[140:143], v[196:199], a[100:103]\n"
    "ds_read_b64_tr_b16 a[74:75], v6 offset:43008\n"
    "v_mfma_f32_16x16x32_bf16 a[100:103], a[144:147], v[212:215], a[100:103]\n"
    "v_mfma_f32_16x16x32_bf16 a[100:103], a[148:151], a[4:7], a[100:103]\n"
    "s_add_u32 s89, s89, 4096\n"
    "ds_read_b64_tr_b16 a[76:77], v6 offset:45056\n"
    "v_mfma_f32_16x16x32_bf16 a[100:103], a[152:155], a[20:23], a[100:103]\n"
    "ds_read_b64_tr_b16 a[78:79], v6 offset:47104\n"
    "v_mfma_f32_16x16x32_bf16 a[100:103], a[156:159], a[36:39], a[100:103]\n"
    "v_mfma_f32_16x16x32_bf16 a[104:107], a[128:131], v[152:155], a[104:107]\n"
    "ds_read_b64_tr_b16 a[80:81], v6 offset:49152\n"
    "v_mfma_f32_16x16x32_bf16 a[104:107], a[132:135], v[168:171], a[104:107]\n"
    "buffer_load_dwordx4 a[176:179], v0, s[40:43], s89 offen offset:0 sc1\n"
    "v_mfma_f32_16x16x32_bf16 a[104:107], a[136:139], v[184:187], a[104:107]\n"
    "ds_read_b64_tr_b16 a[82:83], v6 offset:51200\n"
    "v_mfma_f32_16x16x32_bf16 a[104:107], a[140:143], v[200:203], a[104:107]\n"
    "ds_read_b64_tr_b16 a[84:85], v6 offset:53248\n"
    "v_mfma_f32_16x16x32_bf16 a[104:107], a[144:147], v[216:219], a[104:107]\n"
    "ds_read_b64_tr_b16 a[86:87], v6 offset:55296\n"
    "v_mfma_f32_16x16x32_bf16 a[104:107], a[148:151], a[8:11], a[104:107]\n"
    "buffer_load_dwordx4 a[180:183], v0, s[40:43], s89 offen offset:1024 sc1\n"
    "v_mfma_f32_16x16x32_bf16 a[104:107], a[152:155], a[24:27], a[104:107]\n"
    "ds_read_b64_tr_b16 a[88:89], v6 offset:57344\n"
    "v_mfma_f32_16x16x32_bf16 a[104:107], a[156:159], a[40:43], a[104:107]\n"
    "ds_read_b64_tr_b16 a[90:91], v6 offset:59392\n"
    "v_mfma_f32_16x16x32_bf16 a[108:111], a[128:131], v[156:159], a[108:111]\n"
    "v_mfma_f32_16x16x32_bf16 a[108:111], a[132:135], v[172:175], a[108:111]\n"
    "buffer_load_dwordx4 a[184:187], v0, s[40:43], s89 offen offset:2048 sc1\n"
    "v_mfma_f32_16x16x32_bf16 a[108:111], a[136:139], v[188:191], a[108:111]\n"
    "ds_read_b64_tr_b16 a[92:93], v6 offset:61440\n"
    "ds_read_b64_tr_b16 a[94:95], v6 offset:63488\n"
    "v_mfma_f32_16x16x32_bf16 a[108:111], a[140:143], v[204:207], a[108:111]\n"
    "v_mfma_f32_16x16x32_bf16 a[108:111], a[144:147], v[220:223], a[108:111]\n"
    "v_mfma_f32_16x16x32_bf16 a[108:111], a[148:151], a[12:15], a[108:111]\n"
    "buffer_load_dwordx4 a[188:191], v0, s[40:43], s89 offen offset:3072 sc1\n"
    "v_mfma_f32_16x16x32_bf16 a[108:111], a[152:155], a[28:31], a[108:111]\n"
    "v_mfma_f32_16x16x32_bf16 a[108:111], a[156:159], a[44:47], a[108:111]\n"
    "s_waitcnt lgkmcnt(0)\n"
    "s_barrier\n"
    "v_mfma_f32_16x16x32_bf16 a[112:115], a[64:67], v[144:147], a[112:115]\n"
    "v_mfma_f32_16x16x32_bf16 a[112:115], a[68:71], v[160:163], a[112:115]\n"
    "v_mfma_f32_16x16x32_bf16 a[112:115], a[72:75], v[176:179], a[112:115]\n"
    "v_mfma_f32_16x16x32_bf16 a[112:115], a[76:79], v[192:195], a[112:115]\n"
    "buffer_load_dwordx2 v[250:251], v15, s[40:43], s24 offen offset:0 sc1\n"
    "v_mfma_f32_16x16x32_bf16 a[112:115], a[80:83], v[208:211], a[112:115]\n"
    "ds_read_b64_tr_b16 a[128:129], v11 offset:0\n"
    "v_mfma_f32_16x16x32_bf16 a[112:115], a[84:87], a[0:3], a[112:115]\n"
    "ds_read_b64_tr_b16 a[130:131], v11 offset:5120\n"
    "v_mfma_f32_16x16x32_bf16 a[112:115], a[88:91], a[16:19], a[112:115]\n"
    "s_add_u32 s89, s20, s3\n"
    "v_mfma_f32_16x16x32_bf16 a[112:115], a[92:95], a[32:35], a[112:115]\n"
    "ds_read_b64_tr_b16 a[132:133], v11 offset:10240\n"
    "v_mfma_f32_16x16x32_bf16 a[116:119], a[64:67], v[148:151], a[116:119]\n"
    "ds_read_b64_tr_b16 a[134:135], v11 offset:15360\n"
    "v_mfma_f32_16x16x32_bf16 a[116:119], a[68:71], v[164:167], a[116:119]\n"
    "v_mfma_f32_16x16x32_bf16 a[116:119], a[72:75], v[180:183], a[116:119]\n"
    "s_add_u32 s89, s89, 4096\n"
    "ds_read_b64_tr_b16 a[136:137], v11 offset:20480\n"
    "v_mfma_f32_16x16x32_bf16 a[116:119], a[76:79], v[196:199], a[116:119]\n"
    "ds_read_b64_tr_b16 a[138:139], v11 offset:25600\n"
    "v_mfma_f32_16x16x32_bf16 a[116:119], a[80:83], v[212:215], a[116:119]\n"
    "v_mfma_f32_16x16x32_bf16 a[116:119], a[84:87], a[4:7], a[116:119]\n"
    "ds_read_b64_tr_b16 a[140:141], v11 offset:30720\n"
    "v_mfma_f32_16x16x32_bf16 a[116:119], a[88:91], a[20:23], a[116:119]\n"
    "buffer_load_dwordx4 a[144:147], v0, s[40:43], s89 offen offset:0 sc1\n"
    "v_mfma_f32_16x16x32_bf16 a[116:119], a[92:95], a[36:39], a[116:119]\n"
    "ds_read_b64_tr_b16 a[142:143], v11 offset:35840\n"
    "v_mfma_f32_16x16x32_bf16 a[120:123], a[64:67], v[152:155], a[120:123]\n"
    "v_mfma_f32_16x16x32_bf16 a[120:123], a[68:71], v[168:171], a[120:123]\n"
    "v_mfma_f32_16x16x32_bf16 a[120:123], a[72:75], v[184:187], a[120:123]\n"
    "buffer_load_dwordx4 a[148:151], v0, s[40:43], s89 offen offset:1024 sc1\n"
    "v_mfma_f32_16x16x32_bf16 a[120:123], a[76:79], v[200:203], a[120:123]\n"
    "v_mfma_f32_16x16x32_bf16 a[120:123], a[80:83], v[216:219], a[120:123]\n"
    "v_mfma_f32_16x16x32_bf16 a[120:123], a[84:87], a[8:11], a[120:123]\n"
    "v_mfma_f32_16x16x32_bf16 a[120:123], a[88:91], a[24:27], a[120:123]\n"
    "buffer_load_dwordx4 a[152:155], v0, s[40:43], s89 offen offset:2048 sc1\n"
    "v_mfma_f32_16x16x32_bf16 a[120:123], a[92:95], a[40:43], a[120:123]\n"
    "v_mfma_f32_16x16x32_bf16 a[124:127], a[64:67], v[156:159], a[124:127]\n"
    "v_mfma_f32_16x16x32_bf16 a[124:127], a[68:71], v[172:175], a[124:127]\n"
    "v_mfma_f32_16x16x32_bf16 a[124:127], a[72:75], v[188:191], a[124:127]\n"
    "buffer_load_dwordx4 a[156:159], v0, s[40:43], s89 offen offset:3072 sc1\n"
    "v_mfma_f32_16x16x32_bf16 a[124:127], a[76:79], v[204:207], a[124:127]\n"
    "v_mfma_f32_16x16x32_bf16 a[124:127], a[80:83], v[220:223], a[124:127]\n"
    "v_mfma_f32_16x16x32_bf16 a[124:127], a[84:87], a[12:15], a[124:127]\n"
    "v_mfma_f32_16x16x32_bf16 a[124:127], a[88:91], a[28:31], a[124:127]\n"
    "v_mfma_f32_16x16x32_bf16 a[124:127], a[92:95], a[44:47], a[124:127]\n"
    ".L_c_%=:\n"
    "s_waitcnt lgkmcnt(4)\n"
    "v_mfma_f32_16x16x32_bf16 v[16:19], a[128:131], a[192:195], 0\n"
    "v_subrev_u32 v8, 65536, v11\n"
    "v_subrev_u32 v9, 65536, v12\n"
    "v_mfma_f32_16x16x32_bf16 v[16:19], a[132:135], a[196:199], v[16:19]\n"
    "s_waitcnt lgkmcnt(0)\n"
    "v_mfma_f32_16x16x32_bf16 v[16:19], a[136:139], a[200:203], v[16:19]\n"
    "s_add_u32 m0, s77, 0\n"
    "ds_read_b64_tr_b16 v[192:193], v12 offset:0\n"
    "v_accvgpr_read_b32 v208, a96\n"
    "v_mfma_f32_16x16x32_bf16 v[16:19], a[140:143], a[204:207], v[16:19]\n"
    "ds_read_b64_tr_b16 v[194:195], v12 offset:5120\n"
    "v_accvgpr_read_b32 v209, a97\n"
    "v_mfma_f32_16x16x32_bf16 v[20:23], a[128:131], a[208:211], 0\n"
    "ds_read_b64_tr_b16 v[196:197], v12 offset:10240\n"
    "v_accvgpr_read_b32 v210, a98\n"
    "v_mfma_f32_16x16x32_bf16 v[20:23], a[132:135], a[212:215], v[20:23]\n"
    "ds_read_b64_tr_b16 v[198:199], v12 offset:15360\n"
    "v_accvgpr_read_b32 v211, a99\n"
    "v_mfma_f32_16x16x32_bf16 v[20:23], a[136:139], a[216:219], v[20:23]\n"
    "buffer_load_dwordx4 v1, s[64:67], s14 offen offset:0 lds\n"
    "v_mfma_f32_16x16x32_bf16 v[20:23], a[140:143], a[220:223], v[20:23]\n"
    "ds_read_b64_tr_b16 v[200:201], v12 offset:20480\n"
    "ds_read_b64_tr_b16 v[202:203], v12 offset:25600\n"
    "v_mfma_f32_16x16x32_bf16 v[24:27], a[128:131], a[224:227], 0\n"
    "s_add_u32 m0, s77, 1024\n"
    "ds_read_b64_tr_b16 v[204:205], v12 offset:30720\n"
    "v_accvgpr_read_b32 v212, a112\n"
    "v_mfma_f32_16x16x32_bf16 v[24:27], a[132:135], a[228:231], v[24:27]\n"
    "ds_read_b64_tr_b16 v[206:207], v12 offset:35840\n"
    "v_accvgpr_read_b32 v213, a113\n"
    "v_mfma_f32_16x16x32_bf16 v[24:27], a[136:139], a[232:235], v[24:27]\n"
    "v_accvgpr_read_b32 v214, a114\n"
    "v_accvgpr_read_b32 v215, a115\n"
    "v_mfma_f32_16x16x32_bf16 v[24:27], a[140:143], a[236:239], v[24:27]\n"
    "buffer_load_dwordx4 v2, s[64:67], s14 offen offset:0 lds\n"
    "v_mfma_f32_16x16x32_bf16 v[28:31], a[128:131], a[240:243], 0\n"
    "v_accvgpr_read_b32 v216, a100\n"
    "v_accvgpr_read_b32 v217, a101\n"
    "v_mfma_f32_16x16x32_bf16 v[28:31], a[132:135], a[244:247], v[28:31]\n"
    "v_accvgpr_read_b32 v218, a102\n"
    "v_accvgpr_read_b32 v219, a103\n"
    "v_mfma_f32_16x16x32_bf16 v[28:31], a[136:139], a[248:251], v[28:31]\n"
    "v_accvgpr_read_b32 v220, a116\n"
    "v_accvgpr_read_b32 v221, a117\n"
    "v_mfma_f32_16x16x32_bf16 v[28:31], a[140:143], a[252:255], v[28:31]\n"
    "s_waitcnt lgkmcnt(4)\n"
    "v_accvgpr_read_b32 v222, a118\n"
    "v_accvgpr_read_b32 v223, a119\n"
    "v_mfma_f32_16x16x32_bf16 v[32:35], v[192:195], a[192:195], 0\n"
    "s_add_u32 s89, s20, s81\n"
    "v_accvgpr_read_b32 v224, a104\n"
    "v_accvgpr_read_b32 v225, a105\n"
    "v_mfma_f32_16x16x32_bf16 v[32:35], v[196:199], a[196:199], v[32:35]\n"
    "s_waitcnt lgkmcnt(0)\n"
    "v_accvgpr_read_b32 v226, a106\n"
    "v_accvgpr_read_b32 v227, a107\n"
    "v_mfma_f32_16x16x32_bf16 v[32:35], v[200:203], a[200:203], v[32:35]\n"
    "ds_read_b64_tr_b16 v[176:177], v11 offset:64\n"
    "v_accvgpr_read_b32 v228, a120\n"
    "v_mfma_f32_16x16x32_bf16 v[32:35], v[204:207], a[204:207], v[32:35]\n"
    "ds_read_b64_tr_b16 v[178:179], v11 offset:5184\n"
    "v_accvgpr_read_b32 v229, a121\n"
    "v_mfma_f32_16x16x32_bf16 v[36:39], v[192:195], a[208:211], 0\n"
    "s_add_u32 s89, s89, 4096\n"
    "ds_read_b64_tr_b16 v[180:181], v11 offset:10304\n"
    "v_accvgpr_read_b32 v230, a122\n"
    "v_mfma_f32_16x16x32_bf16 v[36:39], v[196:199], a[212:215], v[36:39]\n"
    "ds_read_b64_tr_b16 v[182:183], v11 offset:15424\n"
    "v_accvgpr_read_b32 v231, a123\n"
    "v_mfma_f32_16x16x32_bf16 v[36:39], v[200:203], a[216:219], v[36:39]\n"
    "ds_read_b64_tr_b16 v[184:185], v11 offset:20544\n"
    "v_accvgpr_read_b32 v232, a108\n"
    "v_mfma_f32_16x16x32_bf16 v[36:39], v[204:207], a[220:223], v[36:39]\n"
    "v_accvgpr_read_b32 v233, a109\n"
    "v_accvgpr_read_b32 v234, a110\n"
    "v_mfma_f32_16x16x32_bf16 v[40:43], v[192:195], a[224:227], 0\n"
    "v_accvgpr_read_b32 v235, a111\n"
    "v_accvgpr_read_b32 v236, a124\n"
    "v_accvgpr_read_b32 v237, a125\n"
    "v_accvgpr_read_b32 v238, a126\n"
    "v_accvgpr_read_b32 v239, a127\n"
    "v_mfma_f32_16x16x32_bf16 v[40:43], v[196:199], a[228:231], v[40:43]\n"
    "ds_read_b64_tr_b16 v[186:187], v11 offset:25664\n"
    "ds_read_b64_tr_b16 v[188:189], v11 offset:30784\n"
    "v_mfma_f32_16x16x32_bf16 v[40:43], v[200:203], a[232:235], v[40:43]\n"
    "buffer_load_dwordx4 a[128:131], v0, s[40:43], s89 offen offset:0 sc1\n"
    "v_mfma_f32_16x16x32_bf16 v[40:43], v[204:207], a[236:239], v[40:43]\n"
    "ds_read_b64_tr_b16 v[190:191], v11 offset:35904\n"
    "v_mul_f32 v208, v208, v240\n"
    "v_mfma_f32_16x16x32_bf16 v[44:47], v[192:195], a[240:243], 0\n"
    "buffer_load_dwordx4 a[132:135], v0, s[40:43], s89 offen offset:1024 sc1\n"
    "v_mfma_f32_16x16x32_bf16 v[44:47], v[196:199], a[244:247], v[44:47]\n"
    "v_mul_f32 v209, v209, v240\n"
    "v_mul_f32 v210, v210, v240\n"
    "v_mfma_f32_16x16x32_bf16 v[44:47], v[200:203], a[248:251], v[44:47]\n"
    "v_mul_f32 v211, v211, v240\n"
    "v_mul_f32 v212, v212, v240\n"
    "v_mfma_f32_16x16x32_bf16 v[44:47], v[204:207], a[252:255], v[44:47]\n"
    "s_waitcnt lgkmcnt(4)\n"
    "v_mul_f32 v213, v213, v240\n"
    "v_mul_f32 v214, v214, v240\n"
    "v_mfma_f32_16x16x32_bf16 v[48:51], v[176:179], a[192:195], 0\n"
    "buffer_load_dwordx4 a[136:139], v0, s[40:43], s89 offen offset:2048 sc1\n"
    "v_mfma_f32_16x16x32_bf16 v[48:51], v[180:183], a[196:199], v[48:51]\n"
    "s_waitcnt lgkmcnt(0)\n"
    "v_mul_f32 v215, v215, v240\n"
    "v_cvt_pk_bf16_f32 v244, v208, v209\n"
    "v_mfma_f32_16x16x32_bf16 v[48:51], v[184:187], a[200:203], v[48:51]\n"
    "ds_read_b64_tr_b16 v[192:193], v12 offset:64\n"
    "v_cvt_pk_bf16_f32 v245, v210, v211\n"
    "v_mfma_f32_16x16x32_bf16 v[48:51], v[188:191], a[204:207], v[48:51]\n"
    "ds_read_b64_tr_b16 v[194:195], v12 offset:5184\n"
    "v_cvt_pk_bf16_f32 v246, v212, v213\n"
    "v_mfma_f32_16x16x32_bf16 v[52:55], v[176:179], a[208:211], 0\n"
    "ds_read_b64_tr_b16 v[196:197], v12 offset:10304\n"
    "v_cvt_pk_bf16_f32 v247, v214, v215\n"
    "v_mfma_f32_16x16x32_bf16 v[52:55], v[180:183], a[212:215], v[52:55]\n"
    "buffer_load_dwordx4 a[140:143], v0, s[40:43], s89 offen offset:3072 sc1\n"
    "v_mfma_f32_16x16x32_bf16 v[52:55], v[184:187], a[216:219], v[52:55]\n"
    "s_add_u32 s90, s9, s81\n"
    "ds_read_b64_tr_b16 v[198:199], v12 offset:15424\n"
    "ds_read_b64_tr_b16 v[200:201], v12 offset:20544\n"
    "v_mfma_f32_16x16x32_bf16 v[52:55], v[188:191], a[220:223], v[52:55]\n"
    "buffer_store_dwordx4 v[244:247], v0, s[40:43], s90 offen offset:0 sc1\n"
    "v_mfma_f32_16x16x32_bf16 v[56:59], v[176:179], a[224:227], 0\n"
    "ds_read_b64_tr_b16 v[202:203], v12 offset:25664\n"
    "ds_read_b64_tr_b16 v[204:205], v12 offset:30784\n"
    "v_mfma_f32_16x16x32_bf16 v[56:59], v[180:183], a[228:231], v[56:59]\n"
    "ds_read_b64_tr_b16 v[206:207], v12 offset:35904\n"
    "v_mfma_f32_16x16x32_bf16 v[56:59], v[184:187], a[232:235], v[56:59]\n"
    "s_add_u32 s94, s97, 7\n"
    "v_accvgpr_write_b32 a53, s94\n"
    "s_waitcnt vmcnt(20)\n"
    "v_mfma_f32_16x16x32_bf16 v[56:59], v[188:191], a[236:239], v[56:59]\n"
    "buffer_store_dwordx2 a[52:53], v7, s[40:43], s23 offen offset:0 sc1\n"
    "v_mfma_f32_16x16x32_bf16 v[60:63], v[176:179], a[240:243], 0\n"
    "v_mfma_f32_16x16x32_bf16 v[60:63], v[180:183], a[244:247], v[60:63]\n"
    "v_mfma_f32_16x16x32_bf16 v[60:63], v[184:187], a[248:251], v[60:63]\n"
    "v_mfma_f32_16x16x32_bf16 v[60:63], v[188:191], a[252:255], v[60:63]\n"
    "s_waitcnt lgkmcnt(4)\n"
    "v_mfma_f32_16x16x32_bf16 v[64:67], v[192:195], a[192:195], 0\n"
    "s_add_u32 s89, s20, s81\n"
    "v_mfma_f32_16x16x32_bf16 v[64:67], v[196:199], a[196:199], v[64:67]\n"
    "s_waitcnt lgkmcnt(0)\n"
    "v_mfma_f32_16x16x32_bf16 v[64:67], v[200:203], a[200:203], v[64:67]\n"
    "ds_read_b64_tr_b16 v[176:177], v11 offset:128\n"
    "v_mfma_f32_16x16x32_bf16 v[64:67], v[204:207], a[204:207], v[64:67]\n"
    "s_add_u32 s89, s89, 8192\n"
    "ds_read_b64_tr_b16 v[178:179], v11 offset:5248\n"
    "v_mfma_f32_16x16x32_bf16 v[68:71], v[192:195], a[208:211], 0\n"
    "ds_read_b64_tr_b16 v[180:181], v11 offset:10368\n"
    "v_mul_f32 v216, v216, v241\n"
    "v_mfma_f32_16x16x32_bf16 v[68:71], v[196:199], a[212:215], v[68:71]\n"
    "ds_read_b64_tr_b16 v[182:183], v11 offset:15488\n"
    "v_mul_f32 v217, v217, v241\n"
    "v_mfma_f32_16x16x32_bf16 v[68:71], v[200:203], a[216:219], v[68:71]\n"
    "ds_read_b64_tr_b16 v[184:185], v11 offset:20608\n"
    "v_mul_f32 v218, v218, v241\n"
    "v_mfma_f32_16x16x32_bf16 v[68:71], v[204:207], a[220:223], v[68:71]\n"
    "ds_read_b64_tr_b16 v[186:187], v11 offset:25728\n"
    "v_mul_f32 v219, v219, v241\n"
    "v_mfma_f32_16x16x32_bf16 v[72:75], v[192:195], a[224:227], 0\n"
    "buffer_load_dwordx4 a[64:67], v0, s[40:43], s89 offen offset:0 sc1\n"
    "v_mfma_f32_16x16x32_bf16 v[72:75], v[196:199], a[228:231], v[72:75]\n"
    "ds_read_b64_tr_b16 v[188:189], v11 offset:30848\n"
    "ds_read_b64_tr_b16 v[190:191], v11 offset:35968\n"
    "v_mfma_f32_16x16x32_bf16 v[72:75], v[200:203], a[232:235], v[72:75]\n"
    "v_mul_f32 v220, v220, v241\n"
    "v_mul_f32 v221, v221, v241\n"
    "v_mfma_f32_16x16x32_bf16 v[72:75], v[204:207], a[236:239], v[72:75]\n"
    "buffer_load_dwordx4 a[68:71], v0, s[40:43], s89 offen offset:1024 sc1\n"
    "v_mfma_f32_16x16x32_bf16 v[76:79], v[192:195], a[240:243], 0\n"
    "v_mul_f32 v222, v222, v241\n"
    "v_mul_f32 v223, v223, v241\n"
    "v_mfma_f32_16x16x32_bf16 v[76:79], v[196:199], a[244:247], v[76:79]\n"
    "v_cvt_pk_bf16_f32 v252, v216, v217\n"
    "v_cvt_pk_bf16_f32 v253, v218, v219\n"
    "v_mfma_f32_16x16x32_bf16 v[76:79], v[200:203], a[248:251], v[76:79]\n"
    "buffer_load_dwordx4 a[72:75], v0, s[40:43], s89 offen offset:2048 sc1\n"
    "v_mfma_f32_16x16x32_bf16 v[76:79], v[204:207], a[252:255], v[76:79]\n"
    "s_waitcnt lgkmcnt(4)\n"
    "v_cvt_pk_bf16_f32 v254, v220, v221\n"
    "v_cvt_pk_bf16_f32 v255, v222, v223\n"
    "v_mfma_f32_16x16x32_bf16 v[80:83], v[176:179], a[192:195], 0\n"
    "s_add_u32 s90, s9, s3\n"
    "v_mfma_f32_16x16x32_bf16 v[80:83], v[180:183], a[196:199], v[80:83]\n"
    "s_waitcnt lgkmcnt(0)\n"
    "v_mfma_f32_16x16x32_bf16 v[80:83], v[184:187], a[200:203], v[80:83]\n"
    "buffer_load_dwordx4 a[76:79], v0, s[40:43], s89 offen offset:3072 sc1\n"
    "v_mfma_f32_16x16x32_bf16 v[80:83], v[188:191], a[204:207], v[80:83]\n"
    "ds_read_b64_tr_b16 v[192:193], v12 offset:128\n"
    "ds_read_b64_tr_b16 v[194:195], v12 offset:5248\n"
    "v_mfma_f32_16x16x32_bf16 v[84:87], v[176:179], a[208:211], 0\n"
    "buffer_store_dwordx4 v[252:255], v0, s[40:43], s90 offen offset:0 sc1\n"
    "v_mfma_f32_16x16x32_bf16 v[84:87], v[180:183], a[212:215], v[84:87]\n"
    "ds_read_b64_tr_b16 v[196:197], v12 offset:10368\n"
    "ds_read_b64_tr_b16 v[198:199], v12 offset:15488\n"
    "v_mfma_f32_16x16x32_bf16 v[84:87], v[184:187], a[216:219], v[84:87]\n"
    "s_add_u32 s89, s20, s3\n"
    "ds_read_b64_tr_b16 v[200:201], v12 offset:20608\n"
    "v_mfma_f32_16x16x32_bf16 v[84:87], v[188:191], a[220:223], v[84:87]\n"
    "ds_read_b64_tr_b16 v[202:203], v12 offset:25728\n"
    "v_mfma_f32_16x16x32_bf16 v[88:91], v[176:179], a[224:227], 0\n"
    "ds_read_b64_tr_b16 v[204:205], v12 offset:30848\n"
    "v_mfma_f32_16x16x32_bf16 v[88:91], v[180:183], a[228:231], v[88:91]\n"
    "s_add_u32 s89, s89, 8192\n"
    "ds_read_b64_tr_b16 v[206:207], v12 offset:35968\n"
    "v_mfma_f32_16x16x32_bf16 v[88:91], v[184:187], a[232:235], v[88:91]\n"
    "v_mfma_f32_16x16x32_bf16 v[88:91], v[188:191], a[236:239], v[88:91]\n"
    "v_mfma_f32_16x16x32_bf16 v[92:95], v[176:179], a[240:243], 0\n"
    "v_mfma_f32_16x16x32_bf16 v[92:95], v[180:183], a[244:247], v[92:95]\n"
    "buffer_load_dwordx4 a[80:83], v0, s[40:43], s89 offen offset:0 sc1\n"
    "v_mfma_f32_16x16x32_bf16 v[92:95], v[184:187], a[248:251], v[92:95]\n"
    "v_mfma_f32_16x16x32_bf16 v[92:95], v[188:191], a[252:255], v[92:95]\n"
    "s_waitcnt lgkmcnt(4)\n"
    "v_mfma_f32_16x16x32_bf16 v[96:99], v[192:195], a[192:195], 0\n"
    "v_mul_f32 v224, v224, v242\n"
    "v_mul_f32 v225, v225, v242\n"
    "v_mfma_f32_16x16x32_bf16 v[96:99], v[196:199], a[196:199], v[96:99]\n"
    "s_waitcnt lgkmcnt(0)\n"
    "v_mul_f32 v226, v226, v242\n"
    "v_mul_f32 v227, v227, v242\n"
    "v_mfma_f32_16x16x32_bf16 v[96:99], v[200:203], a[200:203], v[96:99]\n"
    "buffer_load_dwordx4 a[84:87], v0, s[40:43], s89 offen offset:1024 sc1\n"
    "v_mfma_f32_16x16x32_bf16 v[96:99], v[204:207], a[204:207], v[96:99]\n"
    "ds_read_b64_tr_b16 v[176:177], v11 offset:192\n"
    "ds_read_b64_tr_b16 v[178:179], v11 offset:5312\n"
    "v_mfma_f32_16x16x32_bf16 v[100:103], v[192:195], a[208:211], 0\n"
    "ds_read_b64_tr_b16 v[180:181], v11 offset:10432\n"
    "v_mul_f32 v228, v228, v242\n"
    "v_mfma_f32_16x16x32_bf16 v[100:103], v[196:199], a[212:215], v[100:103]\n"
    "buffer_load_dwordx4 a[88:91], v0, s[40:43], s89 offen offset:2048 sc1\n"
    "v_mfma_f32_16x16x32_bf16 v[100:103], v[200:203], a[216:219], v[100:103]\n"
    "ds_read_b64_tr_b16 v[182:183], v11 offset:15552\n"
    "ds_read_b64_tr_b16 v[184:185], v11 offset:20672\n"
    "v_mfma_f32_16x16x32_bf16 v[100:103], v[204:207], a[220:223], v[100:103]\n"
    "ds_read_b64_tr_b16 v[186:187], v11 offset:25792\n"
    "v_mul_f32 v229, v229, v242\n"
    "v_mfma_f32_16x16x32_bf16 v[104:107], v[192:195], a[224:227], 0\n"
    "ds_read_b64_tr_b16 v[188:189], v11 offset:30912\n"
    "v_mul_f32 v230, v230, v242\n"
    "v_mfma_f32_16x16x32_bf16 v[104:107], v[196:199], a[228:231], v[104:107]\n"
    "buffer_load_dwordx4 a[92:95], v0, s[40:43], s89 offen offset:3072 sc1\n"
    "v_mfma_f32_16x16x32_bf16 v[104:107], v[200:203], a[232:235], v[104:107]\n"
    "ds_read_b64_tr_b16 v[190:191], v11 offset:36032\n"
    "v_mul_f32 v231, v231, v242\n"
    "v_mfma_f32_16x16x32_bf16 v[104:107], v[204:207], a[236:239], v[104:107]\n"
    "v_cvt_pk_bf16_f32 v244, v224, v225\n"
    "v_cvt_pk_bf16_f32 v245, v226, v227\n"
    "v_mfma_f32_16x16x32_bf16 v[108:111], v[192:195], a[240:243], 0\n"
    "s_add_u32 m0, s77, 2048\n"
    "v_cvt_pk_bf16_f32 v246, v228, v229\n"
    "v_cvt_pk_bf16_f32 v247, v230, v231\n"
    "v_mfma_f32_16x16x32_bf16 v[108:111], v[196:199], a[244:247], v[108:111]\n"
    "s_add_u32 s90, s9, s39\n"
    "v_mfma_f32_16x16x32_bf16 v[108:111], v[200:203], a[248:251], v[108:111]\n"
    "buffer_store_dwordx4 v[244:247], v0, s[40:43], s90 offen offset:0 sc1\n"
    "v_mfma_f32_16x16x32_bf16 v[108:111], v[204:207], a[252:255], v[108:111]\n"
    "s_waitcnt lgkmcnt(4)\n"
    "v_mfma_f32_16x16x32_bf16 v[112:115], v[176:179], a[192:195], 0\n"
    "buffer_load_dwordx4 v3, s[64:67], s14 offen offset:0 lds\n"
    "v_mfma_f32_16x16x32_bf16 v[112:115], v[180:183], a[196:199], v[112:115]\n"
    "s_waitcnt lgkmcnt(0)\n"
    "v_mfma_f32_16x16x32_bf16 v[112:115], v[184:187], a[200:203], v[112:115]\n"
    "ds_read_b64_tr_b16 v[192:193], v12 offset:192\n"
    "v_mfma_f32_16x16x32_bf16 v[112:115], v[188:191], a[204:207], v[112:115]\n"
    "s_add_u32 m0, s77, 3072\n"
    "ds_read_b64_tr_b16 v[194:195], v12 offset:5312\n"
    "v_mfma_f32_16x16x32_bf16 v[116:119], v[176:179], a[208:211], 0\n"
    "ds_read_b64_tr_b16 v[196:197], v12 offset:10432\n"
    "v_mfma_f32_16x16x32_bf16 v[116:119], v[180:183], a[212:215], v[116:119]\n"
    "ds_read_b64_tr_b16 v[198:199], v12 offset:15552\n"
    "v_mfma_f32_16x16x32_bf16 v[116:119], v[184:187], a[216:219], v[116:119]\n"
    "ds_read_b64_tr_b16 v[200:201], v12 offset:20672\n"
    "v_mfma_f32_16x16x32_bf16 v[116:119], v[188:191], a[220:223], v[116:119]\n"
    "buffer_load_dwordx4 v4, s[64:67], s14 offen offset:0 lds\n"
    "v_mfma_f32_16x16x32_bf16 v[120:123], v[176:179], a[224:227], 0\n"
    "ds_read_b64_tr_b16 v[202:203], v12 offset:25792\n"
    "ds_read_b64_tr_b16 v[204:205], v12 offset:30912\n"
    "v_mfma_f32_16x16x32_bf16 v[120:123], v[180:183], a[228:231], v[120:123]\n"
    "ds_read_b64_tr_b16 v[206:207], v12 offset:36032\n"
    "v_mfma_f32_16x16x32_bf16 v[120:123], v[184:187], a[232:235], v[120:123]\n"
    "s_add_u32 m0, s77, 4096\n"
    "v_mfma_f32_16x16x32_bf16 v[120:123], v[188:191], a[236:239], v[120:123]\n"
    "v_mfma_f32_16x16x32_bf16 v[124:127], v[176:179], a[240:243], 0\n"
    "v_mul_f32 v232, v232, v243\n"
    "v_mul_f32 v233, v233, v243\n"
    "v_mfma_f32_16x16x32_bf16 v[124:127], v[180:183], a[244:247], v[124:127]\n"
    "v_mul_f32 v234, v234, v243\n"
    "v_mul_f32 v235, v235, v243\n"
    "v_mfma_f32_16x16x32_bf16 v[124:127], v[184:187], a[248:251], v[124:127]\n"
    "buffer_load_dwordx4 v5, s[64:67], s14 offen offset:0 lds\n"
    "v_mfma_f32_16x16x32_bf16 v[124:127], v[188:191], a[252:255], v[124:127]\n"
    "s_waitcnt lgkmcnt(4)\n"
    "v_mul_f32 v236, v236, v243\n"
    "v_mul_f32 v237, v237, v243\n"
    "v_mfma_f32_16x16x32_bf16 v[128:131], v[192:195], a[192:195], 0\n"
    "v_mul_f32 v238, v238, v243\n"
    "v_mul_f32 v239, v239, v243\n"
    "v_mfma_f32_16x16x32_bf16 v[128:131], v[196:199], a[196:199], v[128:131]\n"
    "s_waitcnt lgkmcnt(0)\n"
    "v_cvt_pk_bf16_f32 v252, v232, v233\n"
    "v_cvt_pk_bf16_f32 v253, v234, v235\n"
    "v_mfma_f32_16x16x32_bf16 v[128:131], v[200:203], a[200:203], v[128:131]\n"
    "s_add_u32 s89, s20, s39\n"
    "ds_read_b64_tr_b16 v[176:177], v11 offset:256\n"
    "v_cvt_pk_bf16_f32 v254, v236, v237\n"
    "v_mfma_f32_16x16x32_bf16 v[128:131], v[204:207], a[204:207], v[128:131]\n"
    "ds_read_b64_tr_b16 v[178:179], v11 offset:5376\n"
    "v_cvt_pk_bf16_f32 v255, v238, v239\n"
    "v_mfma_f32_16x16x32_bf16 v[132:135], v[192:195], a[208:211], 0\n"
    "s_add_u32 s90, s9, s99\n"
    "ds_read_b64_tr_b16 v[180:181], v11 offset:10496\n"
    "v_mfma_f32_16x16x32_bf16 v[132:135], v[196:199], a[212:215], v[132:135]\n"
    "buffer_store_dwordx4 v[252:255], v0, s[40:43], s90 offen offset:0 sc1\n"
    "v_mfma_f32_16x16x32_bf16 v[132:135], v[200:203], a[216:219], v[132:135]\n"
    "s_add_u32 s89, s89, 8192\n"
    "ds_read_b64_tr_b16 v[182:183], v11 offset:15616\n"
    "ds_read_b64_tr_b16 v[184:185], v11 offset:20736\n"
    "v_mfma_f32_16x16x32_bf16 v[132:135], v[204:207], a[220:223], v[132:135]\n"
    "ds_read_b64_tr_b16 v[186:187], v11 offset:25856\n"
    "v_mfma_f32_16x16x32_bf16 v[136:139], v[192:195], a[224:227], 0\n"
    "ds_read_b64_tr_b16 v[188:189], v11 offset:30976\n"
    "v_mfma_f32_16x16x32_bf16 v[136:139], v[196:199], a[228:231], v[136:139]\n"
    "buffer_load_dwordx4 a[96:99], v0, s[40:43], s89 offen offset:0 sc1\n"
    "v_mfma_f32_16x16x32_bf16 v[136:139], v[200:203], a[232:235], v[136:139]\n"
    "ds_read_b64_tr_b16 v[190:191], v11 offset:36096\n"
    "v_mfma_f32_16x16x32_bf16 v[136:139], v[204:207], a[236:239], v[136:139]\n"
    "v_mfma_f32_16x16x32_bf16 v[140:143], v[192:195], a[240:243], 0\n"
    "v_mfma_f32_16x16x32_bf16 v[140:143], v[196:199], a[244:247], v[140:143]\n"
    "buffer_load_dwordx4 a[100:103], v0, s[40:43], s89 offen offset:1024 sc1\n"
    "v_mfma_f32_16x16x32_bf16 v[140:143], v[200:203], a[248:251], v[140:143]\n"
    "v_mfma_f32_16x16x32_bf16 v[140:143], v[204:207], a[252:255], v[140:143]\n"
    "s_waitcnt lgkmcnt(4)\n"
    "v_mfma_f32_16x16x32_bf16 v[144:147], v[176:179], a[192:195], 0\n"
    "buffer_load_dwordx4 a[104:107], v0, s[40:43], s89 offen offset:2048 sc1\n"
    "v_mfma_f32_16x16x32_bf16 v[144:147], v[180:183], a[196:199], v[144:147]\n"
    "s_waitcnt lgkmcnt(0)\n"
    "v_mfma_f32_16x16x32_bf16 v[144:147], v[184:187], a[200:203], v[144:147]\n"
    "ds_read_b64_tr_b16 v[192:193], v12 offset:256\n"
    "v_mfma_f32_16x16x32_bf16 v[144:147], v[188:191], a[204:207], v[144:147]\n"
    "ds_read_b64_tr_b16 v[194:195], v12 offset:5376\n"
    "v_mfma_f32_16x16x32_bf16 v[148:151], v[176:179], a[208:211], 0\n"
    "buffer_load_dwordx4 a[108:111], v0, s[40:43], s89 offen offset:3072 sc1\n"
    "v_mfma_f32_16x16x32_bf16 v[148:151], v[180:183], a[212:215], v[148:151]\n"
    "ds_read_b64_tr_b16 v[196:197], v12 offset:10496\n"
    "ds_read_b64_tr_b16 v[198:199], v12 offset:15616\n"
    "v_mfma_f32_16x16x32_bf16 v[148:151], v[184:187], a[216:219], v[148:151]\n"
    "ds_read_b64_tr_b16 v[200:201], v12 offset:20736\n"
    "v_mfma_f32_16x16x32_bf16 v[148:151], v[188:191], a[220:223], v[148:151]\n"
    "ds_read_b64_tr_b16 v[202:203], v12 offset:25856\n"
    "v_mfma_f32_16x16x32_bf16 v[152:155], v[176:179], a[224:227], 0\n"
    "s_add_u32 s89, s20, s99\n"
    "ds_read_b64_tr_b16 v[204:205], v12 offset:30976\n"
    "v_mfma_f32_16x16x32_bf16 v[152:155], v[180:183], a[228:231], v[152:155]\n"
    "ds_read_b64_tr_b16 v[206:207], v12 offset:36096\n"
    "v_mfma_f32_16x16x32_bf16 v[152:155], v[184:187], a[232:235], v[152:155]\n"
    "v_mfma_f32_16x16x32_bf16 v[152:155], v[188:191], a[236:239], v[152:155]\n"
    "s_add_u32 s89, s89, 8192\n"
    "v_mfma_f32_16x16x32_bf16 v[156:159], v[176:179], a[240:243], 0\n"
    "v_mfma_f32_16x16x32_bf16 v[156:159], v[180:183], a[244:247], v[156:159]\n"
    "v_mfma_f32_16x16x32_bf16 v[156:159], v[184:187], a[248:251], v[156:159]\n"
    "v_mfma_f32_16x16x32_bf16 v[156:159], v[188:191], a[252:255], v[156:159]\n"
    "s_waitcnt lgkmcnt(0)\n"
    "s_barrier\n"
    "v_mfma_f32_16x16x32_bf16 v[160:163], v[192:195], a[192:195], 0\n"
    "buffer_load_dwordx4 a[112:115], v0, s[40:43], s89 offen offset:0 sc1\n"
    "v_mfma_f32_16x16x32_bf16 v[160:163], v[196:199], a[196:199], v[160:163]\n"
    "ds_read_b64_tr_b16 v[176:177], v13 offset:0\n"
    "ds_read_b64_tr_b16 v[178:179], v13 offset:5120\n"
    "v_mfma_f32_16x16x32_bf16 v[160:163], v[200:203], a[200:203], v[160:163]\n"
    "ds_read_b64_tr_b16 v[180:181], v13 offset:10240\n"
    "v_mfma_f32_16x16x32_bf16 v[160:163], v[204:207], a[204:207], v[160:163]\n"
    "buffer_load_dwordx4 a[116:119], v0, s[40:43], s89 offen offset:1024 sc1\n"
    "v_mfma_f32_16x16x32_bf16 v[164:167], v[192:195], a[208:211], 0\n"
    "ds_read_b64_tr_b16 v[182:183], v13 offset:15360\n"
    "ds_read_b64_tr_b16 v[184:185], v13 offset:20480\n"
    "v_mfma_f32_16x16x32_bf16 v[164:167], v[196:199], a[212:215], v[164:167]\n"
    "ds_read_b64_tr_b16 v[186:187], v13 offset:25600\n"
    "v_mfma_f32_16x16x32_bf16 v[164:167], v[200:203], a[216:219], v[164:167]\n"
    "buffer_load_dwordx4 a[120:123], v0, s[40:43], s89 offen offset:2048 sc1\n"
    "v_mfma_f32_16x16x32_bf16 v[164:167], v[204:207], a[220:223], v[164:167]\n"
    "ds_read_b64_tr_b16 v[188:189], v13 offset:30720\n"
    "ds_read_b64_tr_b16 v[190:191], v13 offset:35840\n"
    "v_mfma_f32_16x16x32_bf16 v[168:171], v[192:195], a[224:227], 0\n"
    "v_mfma_f32_16x16x32_bf16 v[168:171], v[196:199], a[228:231], v[168:171]\n"
    "v_mfma_f32_16x16x32_bf16 v[168:171], v[200:203], a[232:235], v[168:171]\n"
    "buffer_load_dwordx4 a[124:127], v0, s[40:43], s89 offen offset:3072 sc1\n"
    "v_mfma_f32_16x16x32_bf16 v[168:171], v[204:207], a[236:239], v[168:171]\n"
    "v_mfma_f32_16x16x32_bf16 v[172:175], v[192:195], a[240:243], 0\n"
    "v_mfma_f32_16x16x32_bf16 v[172:175], v[196:199], a[244:247], v[172:175]\n"
    "v_mfma_f32_16x16x32_bf16 v[172:175], v[200:203], a[248:251], v[172:175]\n"
    "v_mfma_f32_16x16x32_bf16 v[172:175], v[204:207], a[252:255], v[172:175]\n"
    "s_waitcnt vmcnt(26) lgkmcnt(4)\n"
    "v_mfma_f32_16x16x32_bf16 v[16:19], v[176:179], a[128:131], v[16:19]\n"
    "s_add_u32 m0, s77, 5120\n"
    "v_mfma_f32_16x16x32_bf16 v[16:19], v[180:183], a[132:135], v[16:19]\n"
    "s_waitcnt vmcnt(24) lgkmcnt(0)\n"
    "v_mfma_f32_16x16x32_bf16 v[16:19], v[184:187], a[136:139], v[16:19]\n"
    "buffer_load_dwordx4 v1, s[44:47], s14 offen offset:0 lds\n"
    "v_mfma_f32_16x16x32_bf16 v[16:19], v[188:191], a[140:143], v[16:19]\n"
    "ds_read_b64_tr_b16 v[192:193], v14 offset:0\n"
    "ds_read_b64_tr_b16 v[194:195], v14 offset:5120\n"
    "v_mfma_f32_16x16x32_bf16 v[20:23], v[176:179], a[144:147], v[20:23]\n"
    "ds_read_b64_tr_b16 v[196:197], v14 offset:10240\n"
    "v_mfma_f32_16x16x32_bf16 v[20:23], v[180:183], a[148:151], v[20:23]\n"
    "s_add_u32 m0, s77, 6144\n"
    "ds_read_b64_tr_b16 v[198:199], v14 offset:15360\n"
    "v_mfma_f32_16x16x32_bf16 v[20:23], v[184:187], a[152:155], v[20:23]\n"
    "ds_read_b64_tr_b16 v[200:201], v14 offset:20480\n"
    "v_mfma_f32_16x16x32_bf16 v[20:23], v[188:191], a[156:159], v[20:23]\n"
    "ds_read_b64_tr_b16 v[202:203], v14 offset:25600\n"
    "v_mfma_f32_16x16x32_bf16 v[24:27], v[176:179], a[160:163], v[24:27]\n"
    "buffer_load_dwordx4 v2, s[44:47], s14 offen offset:0 lds\n"
    "v_mfma_f32_16x16x32_bf16 v[24:27], v[180:183], a[164:167], v[24:27]\n"
    "ds_read_b64_tr_b16 v[204:205], v14 offset:30720\n"
    "ds_read_b64_tr_b16 v[206:207], v14 offset:35840\n"
    "v_mfma_f32_16x16x32_bf16 v[24:27], v[184:187], a[168:171], v[24:27]\n"
    "v_mfma_f32_16x16x32_bf16 v[24:27], v[188:191], a[172:175], v[24:27]\n"
    "s_add_u32 m0, s77, 7168\n"
    "v_mfma_f32_16x16x32_bf16 v[28:31], v[176:179], a[176:179], v[28:31]\n"
    "v_mfma_f32_16x16x32_bf16 v[28:31], v[180:183], a[180:183], v[28:31]\n"
    "v_mfma_f32_16x16x32_bf16 v[28:31], v[184:187], a[184:187], v[28:31]\n"
    "buffer_load_dwordx4 v3, s[44:47], s14 offen offset:0 lds\n"
    "v_mfma_f32_16x16x32_bf16 v[28:31], v[188:191], a[188:191], v[28:31]\n"
    "s_waitcnt lgkmcnt(4)\n"
    "v_mfma_f32_16x16x32_bf16 v[32:35], v[192:195], a[128:131], v[32:35]\n"
    "v_mfma_f32_16x16x32_bf16 v[32:35], v[196:199], a[132:135], v[32:35]\n"
    "s_waitcnt lgkmcnt(0)\n"
    "s_add_u32 m0, s77, 8192\n"
    "v_mfma_f32_16x16x32_bf16 v[32:35], v[200:203], a[136:139], v[32:35]\n"
    "ds_read_b64_tr_b16 v[176:177], v13 offset:64\n"
    "v_mfma_f32_16x16x32_bf16 v[32:35], v[204:207], a[140:143], v[32:35]\n"
    "buffer_load_dwordx4 v4, s[44:47], s14 offen offset:0 lds\n"
    "v_mfma_f32_16x16x32_bf16 v[36:39], v[192:195], a[144:147], v[36:39]\n"
    "ds_read_b64_tr_b16 v[178:179], v13 offset:5184\n"
    "ds_read_b64_tr_b16 v[180:181], v13 offset:10304\n"
    "v_mfma_f32_16x16x32_bf16 v[36:39], v[196:199], a[148:151], v[36:39]\n"
    "ds_read_b64_tr_b16 v[182:183], v13 offset:15424\n"
    "v_mfma_f32_16x16x32_bf16 v[36:39], v[200:203], a[152:155], v[36:39]\n"
    "s_add_u32 m0, s77, 9216\n"
    "ds_read_b64_tr_b16 v[184:185], v13 offset:20544\n"
    "v_mfma_f32_16x16x32_bf16 v[36:39], v[204:207], a[156:159], v[36:39]\n"
    "ds_read_b64_tr_b16 v[186:187], v13 offset:25664\n"
    "v_mfma_f32_16x16x32_bf16 v[40:43], v[192:195], a[160:163], v[40:43]\n"
    "ds_read_b64_tr_b16 v[188:189], v13 offset:30784\n"
    "v_mfma_f32_16x16x32_bf16 v[40:43], v[196:199], a[164:167], v[40:43]\n"
    "buffer_load_dwordx4 v5, s[44:47], s14 offen offset:0 lds\n"
    "v_mfma_f32_16x16x32_bf16 v[40:43], v[200:203], a[168:171], v[40:43]\n"
    "ds_read_b64_tr_b16 v[190:191], v13 offset:35904\n"
    "v_mfma_f32_16x16x32_bf16 v[40:43], v[204:207], a[172:175], v[40:43]\n"
    "v_mfma_f32_16x16x32_bf16 v[44:47], v[192:195], a[176:179], v[44:47]\n"
    "s_add_u32 m0, s77, 65536\n"
    "v_mfma_f32_16x16x32_bf16 v[44:47], v[196:199], a[180:183], v[44:47]\n"
    "v_mfma_f32_16x16x32_bf16 v[44:47], v[200:203], a[184:187], v[44:47]\n"
    "v_mfma_f32_16x16x32_bf16 v[44:47], v[204:207], a[188:191], v[44:47]\n"
    "s_waitcnt lgkmcnt(4)\n"
    "v_mfma_f32_16x16x32_bf16 v[48:51], v[176:179], a[128:131], v[48:51]\n"
    "buffer_load_dwordx4 v1, s[64:67], s15 offen offset:0 lds\n"
    "v_mfma_f32_16x16x32_bf16 v[48:51], v[180:183], a[132:135], v[48:51]\n"
    "s_waitcnt lgkmcnt(0)\n"
    "v_mfma_f32_16x16x32_bf16 v[48:51], v[184:187], a[136:139], v[48:51]\n"
    "ds_read_b64_tr_b16 v[192:193], v14 offset:64\n"
    "v_mfma_f32_16x16x32_bf16 v[48:51], v[188:191], a[140:143], v[48:51]\n"
    "s_add_u32 s94, s97, 8\n"
    "ds_read_b64_tr_b16 v[194:195], v14 offset:5184\n"
    "v_accvgpr_write_b32 a53, s94\n"
    "v_mfma_f32_16x16x32_bf16 v[52:55], v[176:179], a[144:147], v[52:55]\n"
    "s_waitcnt vmcnt(14)\n"
    "ds_read_b64_tr_b16 v[196:197], v14 offset:10304\n"
    "v_mfma_f32_16x16x32_bf16 v[52:55], v[180:183], a[148:151], v[52:55]\n"
    "buffer_store_dwordx2 a[52:53], v7, s[40:43], s23 offen offset:0 sc1\n"
    "v_mfma_f32_16x16x32_bf16 v[52:55], v[184:187], a[152:155], v[52:55]\n"
    "ds_read_b64_tr_b16 v[198:199], v14 offset:15424\n"
    "ds_read_b64_tr_b16 v[200:201], v14 offset:20544\n"
    "v_mfma_f32_16x16x32_bf16 v[52:55], v[188:191], a[156:159], v[52:55]\n"
    "ds_read_b64_tr_b16 v[202:203], v14 offset:25664\n"
    "v_mfma_f32_16x16x32_bf16 v[56:59], v[176:179], a[160:163], v[56:59]\n"
    "s_min_u32 s94, s22, 6\n"
    "ds_read_b64_tr_b16 v[204:205], v14 offset:30784\n"
    "s_add_u32 s94, s94, s97\n"
    "v_mfma_f32_16x16x32_bf16 v[56:59], v[180:183], a[164:167], v[56:59]\n"
    "ds_read_b64_tr_b16 v[206:207], v14 offset:35904\n"
    "v_xor_b32 v248, s96, v250\n"
    "v_mfma_f32_16x16x32_bf16 v[56:59], v[184:187], a[168:171], v[56:59]\n"
    "v_subrev_u32 v249, s94, v251\n"
    "v_lshrrev_b32 v249, 4, v249\n"
    "v_mfma_f32_16x16x32_bf16 v[56:59], v[188:191], a[172:175], v[56:59]\n"
    "v_or_b32 v248, v248, v249\n"
    "v_cmp_ne_u32 vcc, 0, v248\n"
    "v_mfma_f32_16x16x32_bf16 v[60:63], v[176:179], a[176:179], v[60:63]\n"
    "s_nop 0\n"
    "s_cbranch_vccnz .L_spin3_%=\n"
    ".L_back3_%=:\n"
    "v_mfma_f32_16x16x32_bf16 v[60:63], v[180:183], a[180:183], v[60:63]\n"
    "v_mfma_f32_16x16x32_bf16 v[60:63], v[184:187], a[184:187], v[60:63]\n"
    "v_mfma_f32_16x16x32_bf16 v[60:63], v[188:191], a[188:191], v[60:63]\n"
    "s_waitcnt lgkmcnt(4)\n"
    "v_mfma_f32_16x16x32_bf16 v[64:67], v[192:195], a[128:131], v[64:67]\n"
    "s_add_u32 s89, s20, s81\n"
    "v_mfma_f32_16x16x32_bf16 v[64:67], v[196:199], a[132:135], v[64:67]\n"
    "s_waitcnt lgkmcnt(0)\n"
    "v_mfma_f32_16x16x32_bf16 v[64:67], v[200:203], a[136:139], v[64:67]\n"
    "ds_read_b64_tr_b16 v[176:177], v13 offset:128\n"
    "v_mfma_f32_16x16x32_bf16 v[64:67], v[204:207], a[140:143], v[64:67]\n"
    "s_add_u32 s89, s89, 12288\n"
    "ds_read_b64_tr_b16 v[178:179], v13 offset:5248\n"
    "v_mfma_f32_16x16x32_bf16 v[68:71], v[192:195], a[144:147], v[68:71]\n"
    "ds_read_b64_tr_b16 v[180:181], v13 offset:10368\n"
    "v_mfma_f32_16x16x32_bf16 v[68:71], v[196:199], a[148:151], v[68:71]\n"
    "ds_read_b64_tr_b16 v[182:183], v13 offset:15488\n"
    "v_mfma_f32_16x16x32_bf16 v[68:71], v[200:203], a[152:155], v[68:71]\n"
    "buffer_load_dwordx4 a[192:195], v0, s[40:43], s89 offen offset:0 sc1\n"
    "v_mfma_f32_16x16x32_bf16 v[68:71], v[204:207], a[156:159], v[68:71]\n"
    "ds_read_b64_tr_b16 v[184:185], v13 offset:20608\n"
    "ds_read_b64_tr_b16 v[186:187], v13 offset:25728\n"
    "v_mfma_f32_16x16x32_bf16 v[72:75], v[192:195], a[160:163], v[72:75]\n"
    "ds_read_b64_tr_b16 v[188:189], v13 offset:30848\n"
    "v_mfma_f32_16x16x32_bf16 v[72:75], v[196:199], a[164:167], v[72:75]\n"
    "buffer_load_dwordx4 a[196:199], v0, s[40:43], s89 offen offset:1024 sc1\n"
    "v_mfma_f32_16x16x32_bf16 v[72:75], v[200:203], a[168:171], v[72:75]\n"
    "ds_read_b64_tr_b16 v[190:191], v13 offset:35968\n"
    "v_mfma_f32_16x16x32_bf16 v[72:75], v[204:207], a[172:175], v[72:75]\n"
    "v_mfma_f32_16x16x32_bf16 v[76:79], v[192:195], a[176:179], v[76:79]\n"
    "buffer_load_dwordx4 a[200:203], v0, s[40:43], s89 offen offset:2048 sc1\n"
    "v_mfma_f32_16x16x32_bf16 v[76:79], v[196:199], a[180:183], v[76:79]\n"
    "v_mfma_f32_16x16x32_bf16 v[76:79], v[200:203], a[184:187], v[76:79]\n"
    "v_mfma_f32_16x16x32_bf16 v[76:79], v[204:207], a[188:191], v[76:79]\n"
    "s_waitcnt lgkmcnt(4)\n"
    "v_mfma_f32_16x16x32_bf16 v[80:83], v[176:179], a[128:131], v[80:83]\n"
    "buffer_load_dwordx4 a[204:207], v0, s[40:43], s89 offen offset:3072 sc1\n"
    "v_mfma_f32_16x16x32_bf16 v[80:83], v[180:183], a[132:135], v[80:83]\n"
    "s_waitcnt lgkmcnt(0)\n"
    "v_mfma_f32_16x16x32_bf16 v[80:83], v[184:187], a[136:139], v[80:83]\n"
    "ds_read_b64_tr_b16 v[192:193], v14 offset:128\n"
    "v_mfma_f32_16x16x32_bf16 v[80:83], v[188:191], a[140:143], v[80:83]\n"
    "s_add_u32 s89, s20, s3\n"
    "ds_read_b64_tr_b16 v[194:195], v14 offset:5248\n"
    "v_mfma_f32_16x16x32_bf16 v[84:87], v[176:179], a[144:147], v[84:87]\n"
    "ds_read_b64_tr_b16 v[196:197], v14 offset:10368\n"
    "v_mfma_f32_16x16x32_bf16 v[84:87], v[180:183], a[148:151], v[84:87]\n"
    "ds_read_b64_tr_b16 v[198:199], v14 offset:15488\n"
    "v_mfma_f32_16x16x32_bf16 v[84:87], v[184:187], a[152:155], v[84:87]\n"
    "s_add_u32 s89, s89, 12288\n"
    "ds_read_b64_tr_b16 v[200:201], v14 offset:20608\n"
    "v_mfma_f32_16x16x32_bf16 v[84:87], v[188:191], a[156:159], v[84:87]\n"
    "ds_read_b64_tr_b16 v[202:203], v14 offset:25728\n"
    "v_mfma_f32_16x16x32_bf16 v[88:91], v[176:179], a[160:163], v[88:91]\n"
    "ds_read_b64_tr_b16 v[204:205], v14 offset:30848\n"
    "v_mfma_f32_16x16x32_bf16 v[88:91], v[180:183], a[164:167], v[88:91]\n"
    "buffer_load_dwordx4 a[208:211], v0, s[40:43], s89 offen offset:0 sc1\n"
    "v_mfma_f32_16x16x32_bf16 v[88:91], v[184:187], a[168:171], v[88:91]\n"
    "ds_read_b64_tr_b16 v[206:207], v14 offset:35968\n"
    "v_mfma_f32_16x16x32_bf16 v[88:91], v[188:191], a[172:175], v[88:91]\n"
    "v_mfma_f32_16x16x32_bf16 v[92:95], v[176:179], a[176:179], v[92:95]\n"
    "v_mfma_f32_16x16x32_bf16 v[92:95], v[180:183], a[180:183], v[92:95]\n"
    "buffer_load_dwordx4 a[212:215], v0, s[40:43], s89 offen offset:1024 sc1\n"
    "v_mfma_f32_16x16x32_bf16 v[92:95], v[184:187], a[184:187], v[92:95]\n"
    "v_mfma_f32_16x16x32_bf16 v[92:95], v[188:191], a[188:191], v[92:95]\n"
    "s_waitcnt lgkmcnt(4)\n"
    "v_mfma_f32_16x16x32_bf16 v[96:99], v[192:195], a[128:131], v[96:99]\n"
    "buffer_load_dwordx4 a[216:219], v0, s[40:43], s89 offen offset:2048 sc1\n"
    "v_mfma_f32_16x16x32_bf16 v[96:99], v[196:199], a[132:135], v[96:99]\n"
    "s_waitcnt lgkmcnt(0)\n"
    "v_mfma_f32_16x16x32_bf16 v[96:99], v[200:203], a[136:139], v[96:99]\n"
    "ds_read_b64_tr_b16 v[176:177], v13 offset:192\n"
    "v_mfma_f32_16x16x32_bf16 v[96:99], v[204:207], a[140:143], v[96:99]\n"
    "buffer_load_dwordx4 a[220:223], v0, s[40:43], s89 offen offset:3072 sc1\n"
    "v_mfma_f32_16x16x32_bf16 v[100:103], v[192:195], a[144:147], v[100:103]\n"
    "ds_read_b64_tr_b16 v[178:179], v13 offset:5312\n"
    "ds_read_b64_tr_b16 v[180:181], v13 offset:10432\n"
    "v_mfma_f32_16x16x32_bf16 v[100:103], v[196:199], a[148:151], v[100:103]\n"
    "ds_read_b64_tr_b16 v[182:183], v13 offset:15552\n"
    "v_mfma_f32_16x16x32_bf16 v[100:103], v[200:203], a[152:155], v[100:103]\n"
    "s_add_u32 m0, s77, 66560\n"
    "ds_read_b64_tr_b16 v[184:185], v13 offset:20672\n"
    "v_mfma_f32_16x16x32_bf16 v[100:103], v[204:207], a[156:159], v[100:103]\n"
    "ds_read_b64_tr_b16 v[186:187], v13 offset:25792\n"
    "v_mfma_f32_16x16x32_bf16 v[104:107], v[192:195], a[160:163], v[104:107]\n"
    "ds_read_b64_tr_b16 v[188:189], v13 offset:30912\n"
    "v_mfma_f32_16x16x32_bf16 v[104:107], v[196:199], a[164:167], v[104:107]\n"
    "ds_read_b64_tr_b16 v[190:191], v13 offset:36032\n"
    "v_mfma_f32_16x16x32_bf16 v[104:107], v[200:203], a[168:171], v[104:107]\n"
    "buffer_load_dwordx4 v2, s[64:67], s15 offen offset:0 lds\n"
    "v_mfma_f32_16x16x32_bf16 v[104:107], v[204:207], a[172:175], v[104:107]\n"
    "v_mfma_f32_16x16x32_bf16 v[108:111], v[192:195], a[176:179], v[108:111]\n"
    "v_mfma_f32_16x16x32_bf16 v[108:111], v[196:199], a[180:183], v[108:111]\n"
    "s_add_u32 m0, s77, 67584\n"
    "v_mfma_f32_16x16x32_bf16 v[108:111], v[200:203], a[184:187], v[108:111]\n"
    "v_mfma_f32_16x16x32_bf16 v[108:111], v[204:207], a[188:191], v[108:111]\n"
    "s_waitcnt lgkmcnt(4)\n"
    "v_mfma_f32_16x16x32_bf16 v[112:115], v[176:179], a[128:131], v[112:115]\n"
    "buffer_load_dwordx4 v3, s[64:67], s15 offen offset:0 lds\n"
    "v_mfma_f32_16x16x32_bf16 v[112:115], v[180:183], a[132:135], v[112:115]\n"
    "s_waitcnt lgkmcnt(0)\n"
    "v_mfma_f32_16x16x32_bf16 v[112:115], v[184:187], a[136:139], v[112:115]\n"
    "ds_read_b64_tr_b16 v[192:193], v14 offset:192\n"
    "v_mfma_f32_16x16x32_bf16 v[112:115], v[188:191], a[140:143], v[112:115]\n"
    "s_add_u32 m0, s77, 68608\n"
    "ds_read_b64_tr_b16 v[194:195], v14 offset:5312\n"
    "v_mfma_f32_16x16x32_bf16 v[116:119], v[176:179], a[144:147], v[116:119]\n"
    "ds_read_b64_tr_b16 v[196:197], v14 offset:10432\n"
    "v_mfma_f32_16x16x32_bf16 v[116:119], v[180:183], a[148:151], v[116:119]\n"
    "ds_read_b64_tr_b16 v[198:199], v14 offset:15552\n"
    "v_mfma_f32_16x16x32_bf16 v[116:119], v[184:187], a[152:155], v[116:119]\n"
    "ds_read_b64_tr_b16 v[200:201], v14 offset:20672\n"
    "v_mfma_f32_16x16x32_bf16 v[116:119], v[188:191], a[156:159], v[116:119]\n"
    "buffer_load_dwordx4 v4, s[64:67], s15 offen offset:0 lds\n"
    "v_mfma_f32_16x16x32_bf16 v[120:123], v[176:179], a[160:163], v[120:123]\n"
    "ds_read_b64_tr_b16 v[202:203], v14 offset:25792\n"
    "ds_read_b64_tr_b16 v[204:205], v14 offset:30912\n"
    "v_mfma_f32_16x16x32_bf16 v[120:123], v[180:183], a[164:167], v[120:123]\n"
    "ds_read_b64_tr_b16 v[206:207], v14 offset:36032\n"
    "v_mfma_f32_16x16x32_bf16 v[120:123], v[184:187], a[168:171], v[120:123]\n"
    "s_add_u32 m0, s77, 69632\n"
    "v_mfma_f32_16x16x32_bf16 v[120:123], v[188:191], a[172:175], v[120:123]\n"
    "v_mfma_f32_16x16x32_bf16 v[124:127], v[176:179], a[176:179], v[124:127]\n"
    "v_mfma_f32_16x16x32_bf16 v[124:127], v[180:183], a[180:183], v[124:127]\n"
    "buffer_load_dwordx4 v5, s[64:67], s15 offen offset:0 lds\n"
    "v_mfma_f32_16x16x32_bf16 v[124:127], v[184:187], a[184:187], v[124:127]\n"
    "v_mfma_f32_16x16x32_bf16 v[124:127], v[188:191], a[188:191], v[124:127]\n"
    "s_waitcnt lgkmcnt(4)\n"
    "v_mfma_f32_16x16x32_bf16 v[128:131], v[192:195], a[128:131], v[128:131]\n"
    "s_add_u32 m0, s77, 70656\n"
    "v_mfma_f32_16x16x32_bf16 v[128:131], v[196:199], a[132:135], v[128:131]\n"
    "s_waitcnt lgkmcnt(0)\n"
    "v_mfma_f32_16x16x32_bf16 v[128:131], v[200:203], a[136:139], v[128:131]\n"
    "ds_read_b64_tr_b16 v[176:177], v13 offset:256\n"
    "v_mfma_f32_16x16x32_bf16 v[128:131], v[204:207], a[140:143], v[128:131]\n"
    "buffer_load_dwordx4 v1, s[44:47], s15 offen offset:0 lds\n"
    "v_mfma_f32_16x16x32_bf16 v[132:135], v[192:195], a[144:147], v[132:135]\n"
    "ds_read_b64_tr_b16 v[178:179], v13 offset:5376\n"
    "ds_read_b64_tr_b16 v[180:181], v13 offset:10496\n"
    "v_mfma_f32_16x16x32_bf16 v[132:135], v[196:199], a[148:151], v[132:135]\n"
    "ds_read_b64_tr_b16 v[182:183], v13 offset:15616\n"
    "v_mfma_f32_16x16x32_bf16 v[132:135], v[200:203], a[152:155], v[132:135]\n"
    "ds_read_b64_tr_b16 v[184:185], v13 offset:20736\n"
    "v_mfma_f32_16x16x32_bf16 v[132:135], v[204:207], a[156:159], v[132:135]\n"
    "s_add_u32 s89, s20, s39\n"
    "ds_read_b64_tr_b16 v[186:187], v13 offset:25856\n"
    "v_mfma_f32_16x16x32_bf16 v[136:139], v[192:195], a[160:163], v[136:139]\n"
    "ds_read_b64_tr_b16 v[188:189], v13 offset:30976\n"
    "v_mfma_f32_16x16x32_bf16 v[136:139], v[196:199], a[164:167], v[136:139]\n"
    "ds_read_b64_tr_b16 v[190:191], v13 offset:36096\n"
    "v_mfma_f32_16x16x32_bf16 v[136:139], v[200:203], a[168:171], v[136:139]\n"
    "s_add_u32 s89, s89, 12288\n"
    "v_mfma_f32_16x16x32_bf16 v[136:139], v[204:207], a[172:175], v[136:139]\n"
    "v_mfma_f32_16x16x32_bf16 v[140:143], v[192:195], a[176:179], v[140:143]\n"
    "v_mfma_f32_16x16x32_bf16 v[140:143], v[196:199], a[180:183], v[140:143]\n"
    "buffer_load_dwordx4 a[224:227], v0, s[40:43], s89 offen offset:0 sc1\n"
    "v_mfma_f32_16x16x32_bf16 v[140:143], v[200:203], a[184:187], v[140:143]\n"
    "v_mfma_f32_16x16x32_bf16 v[140:143], v[204:207], a[188:191], v[140:143]\n"
    "s_waitcnt lgkmcnt(4)\n"
    "v_mfma_f32_16x16x32_bf16 v[144:147], v[176:179], a[128:131], v[144:147]\n"
    "buffer_load_dwordx4 a[228:231], v0, s[40:43], s89 offen offset:1024 sc1\n"
    "v_mfma_f32_16x16x32_bf16 v[144:147], v[180:183], a[132:135], v[144:147]\n"
    "s_waitcnt lgkmcnt(0)\n"
    "v_mfma_f32_16x16x32_bf16 v[144:147], v[184:187], a[136:139], v[144:147]\n"
    "ds_read_b64_tr_b16 v[192:193], v14 offset:256\n"
    "v_mfma_f32_16x16x32_bf16 v[144:147], v[188:191], a[140:143], v[144:147]\n"
    "ds_read_b64_tr_b16 v[194:195], v14 offset:5376\n"
    "v_mfma_f32_16x16x32_bf16 v[148:151], v[176:179], a[144:147], v[148:151]\n"
    "buffer_load_dwordx4 a[232:235], v0, s[40:43], s89 offen offset:2048 sc1\n"
    "v_mfma_f32_16x16x32_bf16 v[148:151], v[180:183], a[148:151], v[148:151]\n"
    "ds_read_b64_tr_b16 v[196:197], v14 offset:10496\n"
    "ds_read_b64_tr_b16 v[198:199], v14 offset:15616\n"
    "v_mfma_f32_16x16x32_bf16 v[148:151], v[184:187], a[152:155], v[148:151]\n"
    "ds_read_b64_tr_b16 v[200:201], v14 offset:20736\n"
    "v_mfma_f32_16x16x32_bf16 v[148:151], v[188:191], a[156:159], v[148:151]\n"
    "buffer_load_dwordx4 a[236:239], v0, s[40:43], s89 offen offset:3072 sc1\n"
    "v_mfma_f32_16x16x32_bf16 v[152:155], v[176:179], a[160:163], v[152:155]\n"
    "ds_read_b64_tr_b16 v[202:203], v14 offset:25856\n"
    "ds_read_b64_tr_b16 v[204:205], v14 offset:30976\n"
    "v_mfma_f32_16x16x32_bf16 v[152:155], v[180:183], a[164:167], v[152:155]\n"
    "ds_read_b64_tr_b16 v[206:207], v14 offset:36096\n"
    "v_mfma_f32_16x16x32_bf16 v[152:155], v[184:187], a[168:171], v[152:155]\n"
    "s_add_u32 s89, s20, s99\n"
    "v_mfma_f32_16x16x32_bf16 v[152:155], v[188:191], a[172:175], v[152:155]\n"
    "v_mfma_f32_16x16x32_bf16 v[156:159], v[176:179], a[176:179], v[156:159]\n"
    "v_mfma_f32_16x16x32_bf16 v[156:159], v[180:183], a[180:183], v[156:159]\n"
    "s_add_u32 s89, s89, 12288\n"
    "v_mfma_f32_16x16x32_bf16 v[156:159], v[184:187], a[184:187], v[156:159]\n"
    "v_mfma_f32_16x16x32_bf16 v[156:159], v[188:191], a[188:191], v[156:159]\n"
    "s_waitcnt vmcnt(19) lgkmcnt(0)\n"
    "s_barrier\n"
    "v_mfma_f32_16x16x32_bf16 v[160:163], v[192:195], a[128:131], v[160:163]\n"
    "ds_read_b64_tr_b16 v[176:177], v8 offset:0\n"
    "v_mfma_f32_16x16x32_bf16 v[160:163], v[196:199], a[132:135], v[160:163]\n"
    "buffer_load_dwordx4 a[240:243], v0, s[40:43], s89 offen offset:0 sc1\n"
    "v_mfma_f32_16x16x32_bf16 v[160:163], v[200:203], a[136:139], v[160:163]\n"
    "ds_read_b64_tr_b16 v[178:179], v8 offset:5120\n"
    "ds_read_b64_tr_b16 v[180:181], v8 offset:10240\n"
    "v_mfma_f32_16x16x32_bf16 v[160:163], v[204:207], a[140:143], v[160:163]\n"
    "ds_read_b64_tr_b16 v[182:183], v8 offset:15360\n"
    "v_mfma_f32_16x16x32_bf16 v[164:167], v[192:195], a[144:147], v[164:167]\n"
    "buffer_load_dwordx4 a[244:247], v0, s[40:43], s89 offen offset:1024 sc1\n"
    "v_mfma_f32_16x16x32_bf16 v[164:167], v[196:199], a[148:151], v[164:167]\n"
    "ds_read_b64_tr_b16 v[184:185], v8 offset:20480\n"
    "ds_read_b64_tr_b16 v[186:187], v8 offset:25600\n"
    "v_mfma_f32_16x16x32_bf16 v[164:167], v[200:203], a[152:155], v[164:167]\n"
    "ds_read_b64_tr_b16 v[188:189], v8 offset:30720\n"
    "v_mfma_f32_16x16x32_bf16 v[164:167], v[204:207], a[156:159], v[164:167]\n"
    "buffer_load_dwordx4 a[248:251], v0, s[40:43], s89 offen offset:2048 sc1\n"
    "v_mfma_f32_16x16x32_bf16 v[168:171], v[192:195], a[160:163], v[168:171]\n"
    "ds_read_b64_tr_b16 v[190:191], v8 offset:35840\n"
    "v_mfma_f32_16x16x32_bf16 v[168:171], v[196:199], a[164:167], v[168:171]\n"
    "v_mfma_f32_16x16x32_bf16 v[168:171], v[200:203], a[168:171], v[168:171]\n"
    "buffer_load_dwordx4 a[252:255], v0, s[40:43], s89 offen offset:3072 sc1\n"
    "v_mfma_f32_16x16x32_bf16 v[168:171], v[204:207], a[172:175], v[168:171]\n"
    "v_mfma_f32_16x16x32_bf16 v[172:175], v[192:195], a[176:179], v[172:175]\n"
    "v_mfma_f32_16x16x32_bf16 v[172:175], v[196:199], a[180:183], v[172:175]\n"
    "v_mfma_f32_16x16x32_bf16 v[172:175], v[200:203], a[184:187], v[172:175]\n"
    "v_mfma_f32_16x16x32_bf16 v[172:175], v[204:207], a[188:191], v[172:175]\n"
    "s_waitcnt lgkmcnt(4)\n"
    "v_mfma_f32_16x16x32_bf16 v[16:19], v[176:179], a[64:67], v[16:19]\n"
    "buffer_load_dwordx2 v[250:251], v15, s[40:43], s24 offen offset:0 sc1\n"
    "v_mfma_f32_16x16x32_bf16 v[16:19], v[180:183], a[68:71], v[16:19]\n"
    "s_waitcnt lgkmcnt(0)\n"
    "v_mfma_f32_16x16x32_bf16 v[16:19], v[184:187], a[72:75], v[16:19]\n"
    "ds_read_b64_tr_b16 v[192:193], v9 offset:0\n"
    "v_mfma_f32_16x16x32_bf16 v[16:19], v[188:191], a[76:79], v[16:19]\n"
    "ds_read_b64_tr_b16 v[194:195], v9 offset:5120\n"
    "v_mfma_f32_16x16x32_bf16 v[20:23], v[176:179], a[80:83], v[20:23]\n"
    "s_add_u32 m0, s77, 71680\n"
    "ds_read_b64_tr_b16 v[196:197], v9 offset:10240\n"
    "v_mfma_f32_16x16x32_bf16 v[20:23], v[180:183], a[84:87], v[20:23]\n"
    "ds_read_b64_tr_b16 v[198:199], v9 offset:15360\n"
    "v_mfma_f32_16x16x32_bf16 v[20:23], v[184:187], a[88:91], v[20:23]\n"
    "ds_read_b64_tr_b16 v[200:201], v9 offset:20480\n"
    "v_mfma_f32_16x16x32_bf16 v[20:23], v[188:191], a[92:95], v[20:23]\n"
    "buffer_load_dwordx4 v2, s[44:47], s15 offen offset:0 lds\n"
    "v_mfma_f32_16x16x32_bf16 v[24:27], v[176:179], a[96:99], v[24:27]\n"
    "ds_read_b64_tr_b16 v[202:203], v9 offset:25600\n"
    "ds_read_b64_tr_b16 v[204:205], v9 offset:30720\n"
    "v_mfma_f32_16x16x32_bf16 v[24:27], v[180:183], a[100:103], v[24:27]\n"
    "ds_read_b64_tr_b16 v[206:207], v9 offset:35840\n"
    "v_mfma_f32_16x16x32_bf16 v[24:27], v[184:187], a[104:107], v[24:27]\n"
    "s_add_u32 m0, s77, 72704\n"
    "v_mfma_f32_16x16x32_bf16 v[24:27], v[188:191], a[108:111], v[24:27]\n"
    "v_mfma_f32_16x16x32_bf16 v[28:31], v[176:179], a[112:115], v[28:31]\n"
    "v_mfma_f32_16x16x32_bf16 v[28:31], v[180:183], a[116:119], v[28:31]\n"
    "v_mfma_f32_16x16x32_bf16 v[28:31], v[184:187], a[120:123], v[28:31]\n"
    "buffer_load_dwordx4 v3, s[44:47], s15 offen offset:0 lds\n"
    "v_mfma_f32_16x16x32_bf16 v[28:31], v[188:191], a[124:127], v[28:31]\n"
    "s_waitcnt lgkmcnt(4)\n"
    "v_mfma_f32_16x16x32_bf16 v[32:35], v[192:195], a[64:67], v[32:35]\n"
    "v_mfma_f32_16x16x32_bf16 v[32:35], v[196:199], a[68:71], v[32:35]\n"
    "s_waitcnt lgkmcnt(0)\n"
    "s_add_u32 m0, s77, 73728\n"
    "v_mfma_f32_16x16x32_bf16 v[32:35], v[200:203], a[72:75], v[32:35]\n"
    "ds_read_b64_tr_b16 v[176:177], v8 offset:64\n"
    "v_mfma_f32_16x16x32_bf16 v[32:35], v[204:207], a[76:79], v[32:35]\n"
    "ds_read_b64_tr_b16 v[178:179], v8 offset:5184\n"
    "v_mfma_f32_16x16x32_bf16 v[36:39], v[192:195], a[80:83], v[36:39]\n"
    "buffer_load_dwordx4 v4, s[44:47], s15 offen offset:0 lds\n"
    "v_mfma_f32_16x16x32_bf16 v[36:39], v[196:199], a[84:87], v[36:39]\n"
    "s_mov_b32 s79, 0\n"
    "ds_read_b64_tr_b16 v[180:181], v8 offset:10304\n"
    "ds_read_b64_tr_b16 v[182:183], v8 offset:15424\n"
    "v_mfma_f32_16x16x32_bf16 v[36:39], v[200:203], a[88:91], v[36:39]\n"
    "ds_read_b64_tr_b16 v[184:185], v8 offset:20544\n"
    "v_mfma_f32_16x16x32_bf16 v[36:39], v[204:207], a[92:95], v[36:39]\n"
    "ds_read_b64_tr_b16 v[186:187], v8 offset:25664\n"
    "v_mfma_f32_16x16x32_bf16 v[40:43], v[192:195], a[96:99], v[40:43]\n"
    "s_add_u32 m0, s77, 74752\n"
    "ds_read_b64_tr_b16 v[188:189], v8 offset:30784\n"
    "v_mfma_f32_16x16x32_bf16 v[40:43], v[196:199], a[100:103], v[40:43]\n"
    "ds_read_b64_tr_b16 v[190:191], v8 offset:35904\n"
    "v_mfma_f32_16x16x32_bf16 v[40:43], v[200:203], a[104:107], v[40:43]\n"
    "v_mfma_f32_16x16x32_bf16 v[40:43], v[204:207], a[108:111], v[40:43]\n"
    "buffer_load_dwordx4 v5, s[44:47], s15 offen offset:0 lds\n"
    "v_mfma_f32_16x16x32_bf16 v[44:47], v[192:195], a[112:115], v[44:47]\n"
    "v_mfma_f32_16x16x32_bf16 v[44:47], v[196:199], a[116:119], v[44:47]\n"
    "v_mfma_f32_16x16x32_bf16 v[44:47], v[200:203], a[120:123], v[44:47]\n"
    "s_add_u32 m0, s77, 106496\n"
    "s_mul_i32 s79, s79, 5120\n"
    "v_mfma_f32_16x16x32_bf16 v[44:47], v[204:207], a[124:127], v[44:47]\n"
    "s_waitcnt lgkmcnt(4)\n"
    "v_mfma_f32_16x16x32_bf16 v[48:51], v[176:179], a[64:67], v[48:51]\n"
    "v_mfma_f32_16x16x32_bf16 v[48:51], v[180:183], a[68:71], v[48:51]\n"
    "s_waitcnt lgkmcnt(0)\n"
    "v_mfma_f32_16x16x32_bf16 v[48:51], v[184:187], a[72:75], v[48:51]\n"
    "buffer_load_dwordx4 v1, s[64:67], s16 offen offset:0 lds\n"
    "v_mfma_f32_16x16x32_bf16 v[48:51], v[188:191], a[76:79], v[48:51]\n"
    "ds_read_b64_tr_b16 v[192:193], v9 offset:64\n"
    "ds_read_b64_tr_b16 v[194:195], v9 offset:5184\n"
    "v_mfma_f32_16x16x32_bf16 v[52:55], v[176:179], a[80:83], v[52:55]\n"
    "ds_read_b64_tr_b16 v[196:197], v9 offset:10304\n"
    "v_mfma_f32_16x16x32_bf16 v[52:55], v[180:183], a[84:87], v[52:55]\n"
    "s_add_u32 m0, s77, 107520\n"
    "ds_read_b64_tr_b16 v[198:199], v9 offset:15424\n"
    "v_mfma_f32_16x16x32_bf16 v[52:55], v[184:187], a[88:91], v[52:55]\n"
    "ds_read_b64_tr_b16 v[200:201], v9 offset:20544\n"
    "v_mfma_f32_16x16x32_bf16 v[52:55], v[188:191], a[92:95], v[52:55]\n"
    "ds_read_b64_tr_b16 v[202:203], v9 offset:25664\n"
    "v_mfma_f32_16x16x32_bf16 v[56:59], v[176:179], a[96:99], v[56:59]\n"
    "buffer_load_dwordx4 v2, s[64:67], s16 offen offset:0 lds\n"
    "v_mfma_f32_16x16x32_bf16 v[56:59], v[180:183], a[100:103], v[56:59]\n"
    "s_add_u32 s79, s79, s21\n"
    "ds_read_b64_tr_b16 v[204:205], v9 offset:30784\n"
    "ds_read_b64_tr_b16 v[206:207], v9 offset:35904\n"
    "v_mfma_f32_16x16x32_bf16 v[56:59], v[184:187], a[104:107], v[56:59]\n"
    "v_mfma_f32_16x16x32_bf16 v[56:59], v[188:191], a[108:111], v[56:59]\n"
    "v_mfma_f32_16x16x32_bf16 v[60:63], v[176:179], a[112:115], v[60:63]\n"
    "s_add_u32 s89, s20, s81\n"
    "v_mfma_f32_16x16x32_bf16 v[60:63], v[180:183], a[116:119], v[60:63]\n"
    "v_mfma_f32_16x16x32_bf16 v[60:63], v[184:187], a[120:123], v[60:63]\n"
    "v_mfma_f32_16x16x32_bf16 v[60:63], v[188:191], a[124:127], v[60:63]\n"
    "s_waitcnt lgkmcnt(4)\n"
    "s_add_u32 s89, s89, 16384\n"
    "v_mfma_f32_16x16x32_bf16 v[64:67], v[192:195], a[64:67], v[64:67]\n"
    "v_mfma_f32_16x16x32_bf16 v[64:67], v[196:199], a[68:71], v[64:67]\n"
    "s_waitcnt lgkmcnt(0)\n"
    "v_mfma_f32_16x16x32_bf16 v[64:67], v[200:203], a[72:75], v[64:67]\n"
    "buffer_load_dwordx4 a[128:131], v0, s[40:43], s89 offen offset:0 sc1\n"
    "v_mfma_f32_16x16x32_bf16 v[64:67], v[204:207], a[76:79], v[64:67]\n"
    "s_mov_b32 s80, 1\n"
    "ds_read_b64_tr_b16 v[176:177], v8 offset:128\n"
    "ds_read_b64_tr_b16 v[178:179], v8 offset:5248\n"
    "v_mfma_f32_16x16x32_bf16 v[68:71], v[192:195], a[80:83], v[68:71]\n"
    "ds_read_b64_tr_b16 v[180:181], v8 offset:10368\n"
    "v_mfma_f32_16x16x32_bf16 v[68:71], v[196:199], a[84:87], v[68:71]\n"
    "ds_read_b64_tr_b16 v[182:183], v8 offset:15488\n"
    "v_mfma_f32_16x16x32_bf16 v[68:71], v[200:203], a[88:91], v[68:71]\n"
    "buffer_load_dwordx4 a[132:135], v0, s[40:43], s89 offen offset:1024 sc1\n"
    "v_mfma_f32_16x16x32_bf16 v[68:71], v[204:207], a[92:95], v[68:71]\n"
    "ds_read_b64_tr_b16 v[184:185], v8 offset:20608\n"
    "ds_read_b64_tr_b16 v[186:187], v8 offset:25728\n"
    "v_mfma_f32_16x16x32_bf16 v[72:75], v[192:195], a[96:99], v[72:75]\n"
    "ds_read_b64_tr_b16 v[188:189], v8 offset:30848\n"
    "v_mfma_f32_16x16x32_bf16 v[72:75], v[196:199], a[100:103], v[72:75]\n"
    "buffer_load_dwordx4 a[136:139], v0, s[40:43], s89 offen offset:2048 sc1\n"
    "v_mfma_f32_16x16x32_bf16 v[72:75], v[200:203], a[104:107], v[72:75]\n"
    "ds_read_b64_tr_b16 v[190:191], v8 offset:35968\n"
    "v_mfma_f32_16x16x32_bf16 v[72:75], v[204:207], a[108:111], v[72:75]\n"
    "v_mfma_f32_16x16x32_bf16 v[76:79], v[192:195], a[112:115], v[76:79]\n"
    "buffer_load_dwordx4 a[140:143], v0, s[40:43], s89 offen offset:3072 sc1\n"
    "v_mfma_f32_16x16x32_bf16 v[76:79], v[196:199], a[116:119], v[76:79]\n"
    "s_mul_i32 s80, s80, 5120\n"
    "v_mfma_f32_16x16x32_bf16 v[76:79], v[200:203], a[120:123], v[76:79]\n"
    "v_mfma_f32_16x16x32_bf16 v[76:79], v[204:207], a[124:127], v[76:79]\n"
    "s_waitcnt lgkmcnt(4)\n"
    "v_mfma_f32_16x16x32_bf16 v[80:83], v[176:179], a[64:67], v[80:83]\n"
    "s_add_u32 s89, s20, s3\n"
    "v_mfma_f32_16x16x32_bf16 v[80:83], v[180:183], a[68:71], v[80:83]\n"
    "s_waitcnt lgkmcnt(0)\n"
    "v_mfma_f32_16x16x32_bf16 v[80:83], v[184:187], a[72:75], v[80:83]\n"
    "ds_read_b64_tr_b16 v[192:193], v9 offset:128\n"
    "v_mfma_f32_16x16x32_bf16 v[80:83], v[188:191], a[76:79], v[80:83]\n"
    "s_add_u32 s89, s89, 16384\n"
    "ds_read_b64_tr_b16 v[194:195], v9 offset:5248\n"
    "v_mfma_f32_16x16x32_bf16 v[84:87], v[176:179], a[80:83], v[84:87]\n"
    "ds_read_b64_tr_b16 v[196:197], v9 offset:10368\n"
    "v_mfma_f32_16x16x32_bf16 v[84:87], v[180:183], a[84:87], v[84:87]\n"
    "ds_read_b64_tr_b16 v[198:199], v9 offset:15488\n"
    "v_mfma_f32_16x16x32_bf16 v[84:87], v[184:187], a[88:91], v[84:87]\n"
    "buffer_load_dwordx4 a[144:147], v0, s[40:43], s89 offen offset:0 sc1\n"
    "v_mfma_f32_16x16x32_bf16 v[84:87], v[188:191], a[92:95], v[84:87]\n"
    "s_add_u32 s80, s80, s21\n"
    "ds_read_b64_tr_b16 v[200:201], v9 offset:20608\n"
    "ds_read_b64_tr_b16 v[202:203], v9 offset:25728\n"
    "v_mfma_f32_16x16x32_bf16 v[88:91], v[176:179], a[96:99], v[88:91]\n"
    "ds_read_b64_tr_b16 v[204:205], v9 offset:30848\n"
    "v_mfma_f32_16x16x32_bf16 v[88:91], v[180:183], a[100:103], v[88:91]\n"
    "ds_read_b64_tr_b16 v[206:207], v9 offset:35968\n"
    "v_mfma_f32_16x16x32_bf16 v[88:91], v[184:187], a[104:107], v[88:91]\n"
    "buffer_load_dwordx4 a[148:151], v0, s[40:43], s89 offen offset:1024 sc1\n"
    "v_mfma_f32_16x16x32_bf16 v[88:91], v[188:191], a[108:111], v[88:91]\n"
    "v_mfma_f32_16x16x32_bf16 v[92:95], v[176:179], a[112:115], v[92:95]\n"
    "v_mfma_f32_16x16x32_bf16 v[92:95], v[180:183], a[116:119], v[92:95]\n"
    "buffer_load_dwordx4 a[152:155], v0, s[40:43], s89 offen offset:2048 sc1\n"
    "v_mfma_f32_16x16x32_bf16 v[92:95], v[184:187], a[120:123], v[92:95]\n"
    "v_mfma_f32_16x16x32_bf16 v[92:95], v[188:191], a[124:127], v[92:95]\n"
    "s_waitcnt lgkmcnt(4)\n"
    "v_mfma_f32_16x16x32_bf16 v[96:99], v[192:195], a[64:67], v[96:99]\n"
    "s_mov_b32 s82, 2\n"
    "v_mfma_f32_16x16x32_bf16 v[96:99], v[196:199], a[68:71], v[96:99]\n"
    "s_waitcnt lgkmcnt(0)\n"
    "v_mfma_f32_16x16x32_bf16 v[96:99], v[200:203], a[72:75], v[96:99]\n"
    "buffer_load_dwordx4 a[156:159], v0, s[40:43], s89 offen offset:3072 sc1\n"
    "v_mfma_f32_16x16x32_bf16 v[96:99], v[204:207], a[76:79], v[96:99]\n"
    "ds_read_b64_tr_b16 v[176:177], v8 offset:192\n"
    "ds_read_b64_tr_b16 v[178:179], v8 offset:5312\n"
    "v_mfma_f32_16x16x32_bf16 v[100:103], v[192:195], a[80:83], v[100:103]\n"
    "s_add_u32 m0, s77, 108544\n"
    "ds_read_b64_tr_b16 v[180:181], v8 offset:10432\n"
    "v_mfma_f32_16x16x32_bf16 v[100:103], v[196:199], a[84:87], v[100:103]\n"
    "ds_read_b64_tr_b16 v[182:183], v8 offset:15552\n"
    "v_mfma_f32_16x16x32_bf16 v[100:103], v[200:203], a[88:91], v[100:103]\n"
    "ds_read_b64_tr_b16 v[184:185], v8 offset:20672\n"
    "v_mfma_f32_16x16x32_bf16 v[100:103], v[204:207], a[92:95], v[100:103]\n"
    "buffer_load_dwordx4 v3, s[64:67], s16 offen offset:0 lds\n"
    "v_mfma_f32_16x16x32_bf16 v[104:107], v[192:195], a[96:99], v[104:107]\n"
    "ds_read_b64_tr_b16 v[186:187], v8 offset:25792\n"
    "ds_read_b64_tr_b16 v[188:189], v8 offset:30912\n"
    "v_mfma_f32_16x16x32_bf16 v[104:107], v[196:199], a[100:103], v[104:107]\n"
    "ds_read_b64_tr_b16 v[190:191], v8 offset:36032\n"
    "v_mfma_f32_16x16x32_bf16 v[104:107], v[200:203], a[104:107], v[104:107]\n"
    "s_mul_i32 s82, s82, 5120\n"
    "v_mfma_f32_16x16x32_bf16 v[104:107], v[204:207], a[108:111], v[104:107]\n"
    "s_add_u32 m0, s77, 109568\n"
    "v_mfma_f32_16x16x32_bf16 v[108:111], v[192:195], a[112:115], v[108:111]\n"
    "v_mfma_f32_16x16x32_bf16 v[108:111], v[196:199], a[116:119], v[108:111]\n"
    "v_mfma_f32_16x16x32_bf16 v[108:111], v[200:203], a[120:123], v[108:111]\n"
    "buffer_load_dwordx4 v4, s[64:67], s16 offen offset:0 lds\n"
    "v_mfma_f32_16x16x32_bf16 v[108:111], v[204:207], a[124:127], v[108:111]\n"
    "s_waitcnt lgkmcnt(4)\n"
    "v_mfma_f32_16x16x32_bf16 v[112:115], v[176:179], a[64:67], v[112:115]\n"
    "v_mfma_f32_16x16x32_bf16 v[112:115], v[180:183], a[68:71], v[112:115]\n"
    "s_waitcnt lgkmcnt(0)\n"
    "s_add_u32 m0, s77, 110592\n"
    "v_mfma_f32_16x16x32_bf16 v[112:115], v[184:187], a[72:75], v[112:115]\n"
    "ds_read_b64_tr_b16 v[192:193], v9 offset:192\n"
    "v_mfma_f32_16x16x32_bf16 v[112:115], v[188:191], a[76:79], v[112:115]\n"
    "ds_read_b64_tr_b16 v[194:195], v9 offset:5312\n"
    "v_mfma_f32_16x16x32_bf16 v[116:119], v[176:179], a[80:83], v[116:119]\n"
    "s_add_u32 s82, s82, s21\n"
    "ds_read_b64_tr_b16 v[196:197], v9 offset:10432\n"
    "v_mfma_f32_16x16x32_bf16 v[116:119], v[180:183], a[84:87], v[116:119]\n"
    "buffer_load_dwordx4 v5, s[64:67], s16 offen offset:0 lds\n"
    "v_mfma_f32_16x16x32_bf16 v[116:119], v[184:187], a[88:91], v[116:119]\n"
    "ds_read_b64_tr_b16 v[198:199], v9 offset:15552\n"
    "ds_read_b64_tr_b16 v[200:201], v9 offset:20672\n"
    "v_mfma_f32_16x16x32_bf16 v[116:119], v[188:191], a[92:95], v[116:119]\n"
    "ds_read_b64_tr_b16 v[202:203], v9 offset:25792\n"
    "v_mfma_f32_16x16x32_bf16 v[120:123], v[176:179], a[96:99], v[120:123]\n"
    "s_add_u32 m0, s77, 111616\n"
    "ds_read_b64_tr_b16 v[204:205], v9 offset:30912\n"
    "v_mfma_f32_16x16x32_bf16 v[120:123], v[180:183], a[100:103], v[120:123]\n"
    "ds_read_b64_tr_b16 v[206:207], v9 offset:36032\n"
    "v_mfma_f32_16x16x32_bf16 v[120:123], v[184:187], a[104:107], v[120:123]\n"
    "v_mfma_f32_16x16x32_bf16 v[120:123], v[188:191], a[108:111], v[120:123]\n"
    "buffer_load_dwordx4 v1, s[44:47], s16 offen offset:0 lds\n"
    "v_mfma_f32_16x16x32_bf16 v[124:127], v[176:179], a[112:115], v[124:127]\n"
    "v_mfma_f32_16x16x32_bf16 v[124:127], v[180:183], a[116:119], v[124:127]\n"
    "v_mfma_f32_16x16x32_bf16 v[124:127], v[184:187], a[120:123], v[124:127]\n"
    "s_mov_b32 s83, 3\n"
    "v_mfma_f32_16x16x32_bf16 v[124:127], v[188:191], a[124:127], v[124:127]\n"
    "s_waitcnt lgkmcnt(4)\n"
    "s_add_u32 m0, s77, 112640\n"
    "v_mfma_f32_16x16x32_bf16 v[128:131], v[192:195], a[64:67], v[128:131]\n"
    "v_mfma_f32_16x16x32_bf16 v[128:131], v[196:199], a[68:71], v[128:131]\n"
    "s_waitcnt lgkmcnt(0)\n"
    "v_mfma_f32_16x16x32_bf16 v[128:131], v[200:203], a[72:75], v[128:131]\n"
    "buffer_load_dwordx4 v2, s[44:47], s16 offen offset:0 lds\n"
    "v_mfma_f32_16x16x32_bf16 v[128:131], v[204:207], a[76:79], v[128:131]\n"
    "ds_read_b64_tr_b16 v[176:177], v8 offset:256\n"
    "ds_read_b64_tr_b16 v[178:179], v8 offset:5376\n"
    "v_mfma_f32_16x16x32_bf16 v[132:135], v[192:195], a[80:83], v[132:135]\n"
    "ds_read_b64_tr_b16 v[180:181], v8 offset:10496\n"
    "v_mfma_f32_16x16x32_bf16 v[132:135], v[196:199], a[84:87], v[132:135]\n"
    "s_add_u32 s89, s20, s39\n"
    "ds_read_b64_tr_b16 v[182:183], v8 offset:15616\n"
    "v_mfma_f32_16x16x32_bf16 v[132:135], v[200:203], a[88:91], v[132:135]\n"
    "ds_read_b64_tr_b16 v[184:185], v8 offset:20736\n"
    "v_mfma_f32_16x16x32_bf16 v[132:135], v[204:207], a[92:95], v[132:135]\n"
    "ds_read_b64_tr_b16 v[186:187], v8 offset:25856\n"
    "v_mfma_f32_16x16x32_bf16 v[136:139], v[192:195], a[96:99], v[136:139]\n"
    "s_mul_i32 s83, s83, 5120\n"
    "ds_read_b64_tr_b16 v[188:189], v8 offset:30976\n"
    "v_mfma_f32_16x16x32_bf16 v[136:139], v[196:199], a[100:103], v[136:139]\n"
    "s_add_u32 s89, s89, 16384\n"
    "ds_read_b64_tr_b16 v[190:191], v8 offset:36096\n"
    "v_mfma_f32_16x16x32_bf16 v[136:139], v[200:203], a[104:107], v[136:139]\n"
    "v_mfma_f32_16x16x32_bf16 v[136:139], v[204:207], a[108:111], v[136:139]\n"
    "v_mfma_f32_16x16x32_bf16 v[140:143], v[192:195], a[112:115], v[140:143]\n"
    "buffer_load_dwordx4 a[160:163], v0, s[40:43], s89 offen offset:0 sc1\n"
    "v_mfma_f32_16x16x32_bf16 v[140:143], v[196:199], a[116:119], v[140:143]\n"
    "v_mfma_f32_16x16x32_bf16 v[140:143], v[200:203], a[120:123], v[140:143]\n"
    "v_mfma_f32_16x16x32_bf16 v[140:143], v[204:207], a[124:127], v[140:143]\n"
    "s_waitcnt lgkmcnt(4)\n"
    "v_mfma_f32_16x16x32_bf16 v[144:147], v[176:179], a[64:67], v[144:147]\n"
    "buffer_load_dwordx4 a[164:167], v0, s[40:43], s89 offen offset:1024 sc1\n"
    "v_mfma_f32_16x16x32_bf16 v[144:147], v[180:183], a[68:71], v[144:147]\n"
    "s_waitcnt lgkmcnt(0)\n"
    "v_mfma_f32_16x16x32_bf16 v[144:147], v[184:187], a[72:75], v[144:147]\n"
    "s_add_u32 s83, s83, s21\n"
    "ds_read_b64_tr_b16 v[192:193], v9 offset:256\n"
    "v_mfma_f32_16x16x32_bf16 v[144:147], v[188:191], a[76:79], v[144:147]\n"
    "buffer_load_dwordx4 a[168:171], v0, s[40:43], s89 offen offset:2048 sc1\n"
    "v_mfma_f32_16x16x32_bf16 v[148:151], v[176:179], a[80:83], v[148:151]\n"
    "ds_read_b64_tr_b16 v[194:195], v9 offset:5376\n"
    "ds_read_b64_tr_b16 v[196:197], v9 offset:10496\n"
    "v_mfma_f32_16x16x32_bf16 v[148:151], v[180:183], a[84:87], v[148:151]\n"
    "ds_read_b64_tr_b16 v[198:199], v9 offset:15616\n"
    "v_mfma_f32_16x16x32_bf16 v[148:151], v[184:187], a[88:91], v[148:151]\n"
    "buffer_load_dwordx4 a[172:175], v0, s[40:43], s89 offen offset:3072 sc1\n"
    "v_mfma_f32_16x16x32_bf16 v[148:151], v[188:191], a[92:95], v[148:151]\n"
    "ds_read_b64_tr_b16 v[200:201], v9 offset:20736\n"
    "ds_read_b64_tr_b16 v[202:203], v9 offset:25856\n"
    "v_mfma_f32_16x16x32_bf16 v[152:155], v[176:179], a[96:99], v[152:155]\n"
    "ds_read_b64_tr_b16 v[204:205], v9 offset:30976\n"
    "v_mfma_f32_16x16x32_bf16 v[152:155], v[180:183], a[100:103], v[152:155]\n"
    "s_add_u32 s89, s20, s99\n"
    "ds_read_b64_tr_b16 v[206:207], v9 offset:36096\n"
    "v_mfma_f32_16x16x32_bf16 v[152:155], v[184:187], a[104:107], v[152:155]\n"
    "v_mfma_f32_16x16x32_bf16 v[152:155], v[188:191], a[108:111], v[152:155]\n"
    "v_mfma_f32_16x16x32_bf16 v[156:159], v[176:179], a[112:115], v[156:159]\n"
    "v_mfma_f32_16x16x32_bf16 v[156:159], v[180:183], a[116:119], v[156:159]\n"
    "s_add_u32 s89, s89, 16384\n"
    "v_mfma_f32_16x16x32_bf16 v[156:159], v[184:187], a[120:123], v[156:159]\n"
    "v_mfma_f32_16x16x32_bf16 v[156:159], v[188:191], a[124:127], v[156:159]\n"
    "s_waitcnt vmcnt(19) lgkmcnt(0)\n"
    "s_barrier\n"
    "v_mfma_f32_16x16x32_bf16 v[160:163], v[192:195], a[64:67], v[160:163]\n"
    "buffer_load_dwordx4 a[176:179], v0, s[40:43], s89 offen offset:0 sc1\n"
    "v_mfma_f32_16x16x32_bf16 v[160:163], v[196:199], a[68:71], v[160:163]\n"
    "ds_read_b64_tr_b16 v[176:177], v11 offset:0\n"
    "ds_read_b64_tr_b16 v[178:179], v11 offset:5120\n"
    "v_mfma_f32_16x16x32_bf16 v[160:163], v[200:203], a[72:75], v[160:163]\n"
    "ds_read_b64_tr_b16 v[180:181], v11 offset:10240\n"
    "v_mfma_f32_16x16x32_bf16 v[160:163], v[204:207], a[76:79], v[160:163]\n"
    "buffer_load_dwordx4 a[180:183], v0, s[40:43], s89 offen offset:1024 sc1\n"
    "v_mfma_f32_16x16x32_bf16 v[164:167], v[192:195], a[80:83], v[164:167]\n"
    "ds_read_b64_tr_b16 v[182:183], v11 offset:15360\n"
    "ds_read_b64_tr_b16 v[184:185], v11 offset:20480\n"
    "v_mfma_f32_16x16x32_bf16 v[164:167], v[196:199], a[84:87], v[164:167]\n"
    "ds_read_b64_tr_b16 v[186:187], v11 offset:25600\n"
    "v_mfma_f32_16x16x32_bf16 v[164:167], v[200:203], a[88:91], v[164:167]\n"
    "ds_read_b64_tr_b16 v[188:189], v11 offset:30720\n"
    "v_mfma_f32_16x16x32_bf16 v[164:167], v[204:207], a[92:95], v[164:167]\n"
    "buffer_load_dwordx4 a[184:187], v0, s[40:43], s89 offen offset:2048 sc1\n"
    "v_mfma_f32_16x16x32_bf16 v[168:171], v[192:195], a[96:99], v[168:171]\n"
    "ds_read_b64_tr_b16 v[190:191], v11 offset:35840\n"
    "v_mfma_f32_16x16x32_bf16 v[168:171], v[196:199], a[100:103], v[168:171]\n"
    "v_mfma_f32_16x16x32_bf16 v[168:171], v[200:203], a[104:107], v[168:171]\n"
    "buffer_load_dwordx4 a[188:191], v0, s[40:43], s89 offen offset:3072 sc1\n"
    "v_mfma_f32_16x16x32_bf16 v[168:171], v[204:207], a[108:111], v[168:171]\n"
    "v_mfma_f32_16x16x32_bf16 v[172:175], v[192:195], a[112:115], v[172:175]\n"
    "v_mfma_f32_16x16x32_bf16 v[172:175], v[196:199], a[116:119], v[172:175]\n"
    "v_mfma_f32_16x16x32_bf16 v[172:175], v[200:203], a[120:123], v[172:175]\n"
    "v_mfma_f32_16x16x32_bf16 v[172:175], v[204:207], a[124:127], v[172:175]\n"
    "s_waitcnt lgkmcnt(4)\n"
    "v_mfma_f32_16x16x32_bf16 v[16:19], v[176:179], a[192:195], v[16:19]\n"
    "s_min_u32 s94, s22, 8\n"
    "s_add_u32 s94, s94, s97\n"
    "v_mfma_f32_16x16x32_bf16 v[16:19], v[180:183], a[196:199], v[16:19]\n"
    "s_waitcnt lgkmcnt(0)\n"
    "v_xor_b32 v248, s96, v250\n"
    "v_subrev_u32 v249, s94, v251\n"
    "v_mfma_f32_16x16x32_bf16 v[16:19], v[184:187], a[200:203], v[16:19]\n"
    "ds_read_b64_tr_b16 v[192:193], v12 offset:0\n"
    "v_lshrrev_b32 v249, 4, v249\n"
    "v_mfma_f32_16x16x32_bf16 v[16:19], v[188:191], a[204:207], v[16:19]\n"
    "ds_read_b64_tr_b16 v[194:195], v12 offset:5120\n"
    "v_or_b32 v248, v248, v249\n"
    "v_mfma_f32_16x16x32_bf16 v[20:23], v[176:179], a[208:211], v[20:23]\n"
    "ds_read_b64_tr_b16 v[196:197], v12 offset:10240\n"
    "v_cmp_ne_u32 vcc, 0, v248\n"
    "v_mfma_f32_16x16x32_bf16 v[20:23], v[180:183], a[212:215], v[20:23]\n"
    "s_nop 0\n"
    "s_cbranch_vccnz .L_spin4_%=\n"
    ".L_back4_%=:\n"
    "ds_read_b64_tr_b16 v[198:199], v12 offset:15360\n"
    "v_mfma_f32_16x16x32_bf16 v[20:23], v[184:187], a[216:219], v[20:23]\n"
    "ds_read_b64_tr_b16 v[200:201], v12 offset:20480\n"
    "v_mfma_f32_16x16x32_bf16 v[20:23], v[188:191], a[220:223], v[20:23]\n"
    "ds_read_b64_tr_b16 v[202:203], v12 offset:25600\n"
    "v_mfma_f32_16x16x32_bf16 v[24:27], v[176:179], a[224:227], v[24:27]\n"
    "s_add_u32 m0, s77, 113664\n"
    "ds_read_b64_tr_b16 v[204:205], v12 offset:30720\n"
    "v_mfma_f32_16x16x32_bf16 v[24:27], v[180:183], a[228:231], v[24:27]\n"
    "ds_read_b64_tr_b16 v[206:207], v12 offset:35840\n"
    "v_mfma_f32_16x16x32_bf16 v[24:27], v[184:187], a[232:235], v[24:27]\n"
    "v_mfma_f32_16x16x32_bf16 v[24:27], v[188:191], a[236:239], v[24:27]\n"
    "buffer_load_dwordx4 v3, s[44:47], s16 offen offset:0 lds\n"
    "v_mfma_f32_16x16x32_bf16 v[28:31], v[176:179], a[240:243], v[28:31]\n"
    "v_mfma_f32_16x16x32_bf16 v[28:31], v[180:183], a[244:247], v[28:31]\n"
    "v_mfma_f32_16x16x32_bf16 v[28:31], v[184:187], a[248:251], v[28:31]\n"
    "s_add_u32 m0, s77, 114688\n"
    "v_mfma_f32_16x16x32_bf16 v[28:31], v[188:191], a[252:255], v[28:31]\n"
    "s_waitcnt lgkmcnt(4)\n"
    "v_mfma_f32_16x16x32_bf16 v[32:35], v[192:195], a[192:195], v[32:35]\n"
    "v_mfma_f32_16x16x32_bf16 v[32:35], v[196:199], a[196:199], v[32:35]\n"
    "s_waitcnt lgkmcnt(0)\n"
    "v_mfma_f32_16x16x32_bf16 v[32:35], v[200:203], a[200:203], v[32:35]\n"
    "buffer_load_dwordx4 v4, s[44:47], s16 offen offset:0 lds\n"
    "v_mfma_f32_16x16x32_bf16 v[32:35], v[204:207], a[204:207], v[32:35]\n"
    "ds_read_b64_tr_b16 v[176:177], v11 offset:64\n"
    "ds_read_b64_tr_b16 v[178:179], v11 offset:5184\n"
    "v_mfma_f32_16x16x32_bf16 v[36:39], v[192:195], a[208:211], v[36:39]\n"
    "s_add_u32 m0, s77, 115712\n"
    "ds_read_b64_tr_b16 v[180:181], v11 offset:10304\n"
    "v_mfma_f32_16x16x32_bf16 v[36:39], v[196:199], a[212:215], v[36:39]\n"
    "ds_read_b64_tr_b16 v[182:183], v11 offset:15424\n"
    "v_mfma_f32_16x16x32_bf16 v[36:39], v[200:203], a[216:219], v[36:39]\n"
    "ds_read_b64_tr_b16 v[184:185], v11 offset:20544\n"
    "v_mfma_f32_16x16x32_bf16 v[36:39], v[204:207], a[220:223], v[36:39]\n"
    "buffer_load_dwordx4 v5, s[44:47], s16 offen offset:0 lds\n"
    "v_mfma_f32_16x16x32_bf16 v[40:43], v[192:195], a[224:227], v[40:43]\n"
    "ds_read_b64_tr_b16 v[186:187], v11 offset:25664\n"
    "ds_read_b64_tr_b16 v[188:189], v11 offset:30784\n"
    "v_mfma_f32_16x16x32_bf16 v[40:43], v[196:199], a[228:231], v[40:43]\n"
    "ds_read_b64_tr_b16 v[190:191], v11 offset:35904\n"
    "v_mfma_f32_16x16x32_bf16 v[40:43], v[200:203], a[232:235], v[40:43]\n"
    "s_add_u32 m0, s77, 0\n"
    "v_mfma_f32_16x16x32_bf16 v[40:43], v[204:207], a[236:239], v[40:43]\n"
    "v_mfma_f32_16x16x32_bf16 v[44:47], v[192:195], a[240:243], v[44:47]\n"
    "v_mfma_f32_16x16x32_bf16 v[44:47], v[196:199], a[244:247], v[44:47]\n"
    "buffer_load_dwordx4 v1, s[64:67], s17 offen offset:0 lds\n"
    "v_mfma_f32_16x16x32_bf16 v[44:47], v[200:203], a[248:251], v[44:47]\n"
    "v_mfma_f32_16x16x32_bf16 v[44:47], v[204:207], a[252:255], v[44:47]\n"
    "s_waitcnt lgkmcnt(4)\n"
    "v_mfma_f32_16x16x32_bf16 v[48:51], v[176:179], a[192:195], v[48:51]\n"
    "s_add_u32 m0, s77, 1024\n"
    "v_mfma_f32_16x16x32_bf16 v[48:51], v[180:183], a[196:199], v[48:51]\n"
    "s_waitcnt lgkmcnt(0)\n"
    "v_mfma_f32_16x16x32_bf16 v[48:51], v[184:187], a[200:203], v[48:51]\n"
    "ds_read_b64_tr_b16 v[192:193], v12 offset:64\n"
    "v_mfma_f32_16x16x32_bf16 v[48:51], v[188:191], a[204:207], v[48:51]\n"
    "buffer_load_dwordx4 v2, s[64:67], s17 offen offset:0 lds\n"
    "v_mfma_f32_16x16x32_bf16 v[52:55], v[176:179], a[208:211], v[52:55]\n"
    "ds_read_b64_tr_b16 v[194:195], v12 offset:5184\n"
    "ds_read_b64_tr_b16 v[196:197], v12 offset:10304\n"
    "v_mfma_f32_16x16x32_bf16 v[52:55], v[180:183], a[212:215], v[52:55]\n"
    "ds_read_b64_tr_b16 v[198:199], v12 offset:15424\n"
    "v_mfma_f32_16x16x32_bf16 v[52:55], v[184:187], a[216:219], v[52:55]\n"
    "s_add_u32 m0, s77, 2048\n"
    "ds_read_b64_tr_b16 v[200:201], v12 offset:20544\n"
    "v_mfma_f32_16x16x32_bf16 v[52:55], v[188:191], a[220:223], v[52:55]\n"
    "ds_read_b64_tr_b16 v[202:203], v12 offset:25664\n"
    "v_mfma_f32_16x16x32_bf16 v[56:59], v[176:179], a[224:227], v[56:59]\n"
    "ds_read_b64_tr_b16 v[204:205], v12 offset:30784\n"
    "v_mfma_f32_16x16x32_bf16 v[56:59], v[180:183], a[228:231], v[56:59]\n"
    "buffer_load_dwordx4 v3, s[64:67], s17 offen offset:0 lds\n"
    "v_mfma_f32_16x16x32_bf16 v[56:59], v[184:187], a[232:235], v[56:59]\n"
    "ds_read_b64_tr_b16 v[206:207], v12 offset:35904\n"
    "v_mfma_f32_16x16x32_bf16 v[56:59], v[188:191], a[236:239], v[56:59]\n"
    "v_mfma_f32_16x16x32_bf16 v[60:63], v[176:179], a[240:243], v[60:63]\n"
    "v_mfma_f32_16x16x32_bf16 v[60:63], v[180:183], a[244:247], v[60:63]\n"
    "s_add_u32 s89, s20, s81\n"
    "v_mfma_f32_16x16x32_bf16 v[60:63], v[184:187], a[248:251], v[60:63]\n"
    "v_mfma_f32_16x16x32_bf16 v[60:63], v[188:191], a[252:255], v[60:63]\n"
    "s_waitcnt lgkmcnt(4)\n"
    "v_mfma_f32_16x16x32_bf16 v[64:67], v[192:195], a[192:195], v[64:67]\n"
    "s_add_u32 s89, s89, 20480\n"
    "v_mfma_f32_16x16x32_bf16 v[64:67], v[196:199], a[196:199], v[64:67]\n"
    "s_waitcnt lgkmcnt(0)\n"
    "v_mfma_f32_16x16x32_bf16 v[64:67], v[200:203], a[200:203], v[64:67]\n"
    "ds_read_b64_tr_b16 v[176:177], v11 offset:128\n"
    "v_mfma_f32_16x16x32_bf16 v[64:67], v[204:207], a[204:207], v[64:67]\n"
    "buffer_load_dwordx4 a[64:67], v0, s[40:43], s89 offen offset:0 sc1\n"
    "v_mfma_f32_16x16x32_bf16 v[68:71], v[192:195], a[208:211], v[68:71]\n"
    "ds_read_b64_tr_b16 v[178:179], v11 offset:5248\n"
    "ds_read_b64_tr_b16 v[180:181], v11 offset:10368\n"
    "v_mfma_f32_16x16x32_bf16 v[68:71], v[196:199], a[212:215], v[68:71]\n"
    "ds_read_b64_tr_b16 v[182:183], v11 offset:15488\n"
    "v_mfma_f32_16x16x32_bf16 v[68:71], v[200:203], a[216:219], v[68:71]\n"
    "buffer_load_dwordx4 a[68:71], v0, s[40:43], s89 offen offset:1024 sc1\n"
    "v_mfma_f32_16x16x32_bf16 v[68:71], v[204:207], a[220:223], v[68:71]\n"
    "ds_read_b64_tr_b16 v[184:185], v11 offset:20608\n"
    "ds_read_b64_tr_b16 v[186:187], v11 offset:25728\n"
    "v_mfma_f32_16x16x32_bf16 v[72:75], v[192:195], a[224:227], v[72:75]\n"
    "ds_read_b64_tr_b16 v[188:189], v11 offset:30848\n"
    "v_mfma_f32_16x16x32_bf16 v[72:75], v[196:199], a[228:231], v[72:75]\n"
    "buffer_load_dwordx4 a[72:75], v0, s[40:43], s89 offen offset:2048 sc1\n"
    "v_mfma_f32_16x16x32_bf16 v[72:75], v[200:203], a[232:235], v[72:75]\n"
    "ds_read_b64_tr_b16 v[190:191], v11 offset:35968\n"
    "v_mfma_f32_16x16x32_bf16 v[72:75], v[204:207], a[236:239], v[72:75]\n"
    "v_mfma_f32_16x16x32_bf16 v[76:79], v[192:195], a[240:243], v[76:79]\n"
    "buffer_load_dwordx4 a[76:79], v0, s[40:43], s89 offen offset:3072 sc1\n"
    "v_mfma_f32_16x16x32_bf16 v[76:79], v[196:199], a[244:247], v[76:79]\n"
    "v_mfma_f32_16x16x32_bf16 v[76:79], v[200:203], a[248:251], v[76:79]\n"
    "v_mfma_f32_16x16x32_bf16 v[76:79], v[204:207], a[252:255], v[76:79]\n"
    "s_waitcnt lgkmcnt(4)\n"
    "s_add_u32 s89, s20, s3\n"
    "v_mfma_f32_16x16x32_bf16 v[80:83], v[176:179], a[192:195], v[80:83]\n"
    "v_mfma_f32_16x16x32_bf16 v[80:83], v[180:183], a[196:199], v[80:83]\n"
    "s_waitcnt lgkmcnt(0)\n"
    "v_mfma_f32_16x16x32_bf16 v[80:83], v[184:187], a[200:203], v[80:83]\n"
    "s_add_u32 s89, s89, 20480\n"
    "ds_read_b64_tr_b16 v[192:193], v12 offset:128\n"
    "v_mfma_f32_16x16x32_bf16 v[80:83], v[188:191], a[204:207], v[80:83]\n"
    "ds_read_b64_tr_b16 v[194:195], v12 offset:5248\n"
    "v_mfma_f32_16x16x32_bf16 v[84:87], v[176:179], a[208:211], v[84:87]\n"
    "ds_read_b64_tr_b16 v[196:197], v12 offset:10368\n"
    "v_mfma_f32_16x16x32_bf16 v[84:87], v[180:183], a[212:215], v[84:87]\n"
    "buffer_load_dwordx4 a[80:83], v0, s[40:43], s89 offen offset:0 sc1\n"
    "v_mfma_f32_16x16x32_bf16 v[84:87], v[184:187], a[216:219], v[84:87]\n"
    "ds_read_b64_tr_b16 v[198:199], v12 offset:15488\n"
    "ds_read_b64_tr_b16 v[200:201], v12 offset:20608\n"
    "v_mfma_f32_16x16x32_bf16 v[84:87], v[188:191], a[220:223], v[84:87]\n"
    "ds_read_b64_tr_b16 v[202:203], v12 offset:25728\n"
    "v_mfma_f32_16x16x32_bf16 v[88:91], v[176:179], a[224:227], v[88:91]\n"
    "buffer_load_dwordx4 a[84:87], v0, s[40:43], s89 offen offset:1024 sc1\n"
    "v_mfma_f32_16x16x32_bf16 v[88:91], v[180:183], a[228:231], v[88:91]\n"
    "ds_read_b64_tr_b16 v[204:205], v12 offset:30848\n"
    "ds_read_b64_tr_b16 v[206:207], v12 offset:35968\n"
    "v_mfma_f32_16x16x32_bf16 v[88:91], v[184:187], a[232:235], v[88:91]\n"
    "v_mfma_f32_16x16x32_bf16 v[88:91], v[188:191], a[236:239], v[88:91]\n"
    "buffer_load_dwordx4 a[88:91], v0, s[40:43], s89 offen offset:2048 sc1\n"
    "v_mfma_f32_16x16x32_bf16 v[92:95], v[176:179], a[240:243], v[92:95]\n"
    "v_mfma_f32_16x16x32_bf16 v[92:95], v[180:183], a[244:247], v[92:95]\n"
    "v_mfma_f32_16x16x32_bf16 v[92:95], v[184:187], a[248:251], v[92:95]\n"
    "v_mfma_f32_16x16x32_bf16 v[92:95], v[188:191], a[252:255], v[92:95]\n"
    "s_waitcnt lgkmcnt(4)\n"
    "v_mfma_f32_16x16x32_bf16 v[96:99], v[192:195], a[192:195], v[96:99]\n"
    "buffer_load_dwordx4 a[92:95], v0, s[40:43], s89 offen offset:3072 sc1\n"
    "v_mfma_f32_16x16x32_bf16 v[96:99], v[196:199], a[196:199], v[96:99]\n"
    "s_waitcnt lgkmcnt(0)\n"
    "v_mfma_f32_16x16x32_bf16 v[96:99], v[200:203], a[200:203], v[96:99]\n"
    "s_add_u32 m0, s77, 3072\n"
    "ds_read_b64_tr_b16 v[176:177], v11 offset:192\n"
    "v_mfma_f32_16x16x32_bf16 v[96:99], v[204:207], a[204:207], v[96:99]\n"
    "ds_read_b64_tr_b16 v[178:179], v11 offset:5312\n"
    "v_mfma_f32_16x16x32_bf16 v[100:103], v[192:195], a[208:211], v[100:103]\n"
    "ds_read_b64_tr_b16 v[180:181], v11 offset:10432\n"
    "v_mfma_f32_16x16x32_bf16 v[100:103], v[196:199], a[212:215], v[100:103]\n"
    "buffer_load_dwordx4 v4, s[64:67], s17 offen offset:0 lds\n"
    "v_mfma_f32_16x16x32_bf16 v[100:103], v[200:203], a[216:219], v[100:103]\n"
    "ds_read_b64_tr_b16 v[182:183], v11 offset:15552\n"
    "ds_read_b64_tr_b16 v[184:185], v11 offset:20672\n"
    "v_mfma_f32_16x16x32_bf16 v[100:103], v[204:207], a[220:223], v[100:103]\n"
    "ds_read_b64_tr_b16 v[186:187], v11 offset:25792\n"
    "v_mfma_f32_16x16x32_bf16 v[104:107], v[192:195], a[224:227], v[104:107]\n"
    "s_add_u32 m0, s77, 4096\n"
    "ds_read_b64_tr_b16 v[188:189], v11 offset:30912\n"
    "v_mfma_f32_16x16x32_bf16 v[104:107], v[196:199], a[228:231], v[104:107]\n"
    "ds_read_b64_tr_b16 v[190:191], v11 offset:36032\n"
    "v_mfma_f32_16x16x32_bf16 v[104:107], v[200:203], a[232:235], v[104:107]\n"
    "v_mfma_f32_16x16x32_bf16 v[104:107], v[204:207], a[236:239], v[104:107]\n"
    "buffer_load_dwordx4 v5, s[64:67], s17 offen offset:0 lds\n"
    "v_mfma_f32_16x16x32_bf16 v[108:111], v[192:195], a[240:243], v[108:111]\n"
    "v_mfma_f32_16x16x32_bf16 v[108:111], v[196:199], a[244:247], v[108:111]\n"
    "v_mfma_f32_16x16x32_bf16 v[108:111], v[200:203], a[248:251], v[108:111]\n"
    "s_add_u32 m0, s77, 5120\n"
    "v_mfma_f32_16x16x32_bf16 v[108:111], v[204:207], a[252:255], v[108:111]\n"
    "s_waitcnt lgkmcnt(4)\n"
    "v_mfma_f32_16x16x32_bf16 v[112:115], v[176:179], a[192:195], v[112:115]\n"
    "v_mfma_f32_16x16x32_bf16 v[112:115], v[180:183], a[196:199], v[112:115]\n"
    "s_waitcnt lgkmcnt(0)\n"
    "v_mfma_f32_16x16x32_bf16 v[112:115], v[184:187], a[200:203], v[112:115]\n"
    "buffer_load_dwordx4 v1, s[44:47], s17 offen offset:0 lds\n"
    "v_mfma_f32_16x16x32_bf16 v[112:115], v[188:191], a[204:207], v[112:115]\n"
    "ds_read_b64_tr_b16 v[192:193], v12 offset:192\n"
    "ds_read_b64_tr_b16 v[194:195], v12 offset:5312\n"
    "v_mfma_f32_16x16x32_bf16 v[116:119], v[176:179], a[208:211], v[116:119]\n"
    "s_add_u32 m0, s77, 6144\n"
    "ds_read_b64_tr_b16 v[196:197], v12 offset:10432\n"
    "v_mfma_f32_16x16x32_bf16 v[116:119], v[180:183], a[212:215], v[116:119]\n"
    "ds_read_b64_tr_b16 v[198:199], v12 offset:15552\n"
    "v_mfma_f32_16x16x32_bf16 v[116:119], v[184:187], a[216:219], v[116:119]\n"
    "ds_read_b64_tr_b16 v[200:201], v12 offset:20672\n"
    "v_mfma_f32_16x16x32_bf16 v[116:119], v[188:191], a[220:223], v[116:119]\n"
    "buffer_load_dwordx4 v2, s[44:47], s17 offen offset:0 lds\n"
    "v_mfma_f32_16x16x32_bf16 v[120:123], v[176:179], a[224:227], v[120:123]\n"
    "ds_read_b64_tr_b16 v[202:203], v12 offset:25792\n"
    "ds_read_b64_tr_b16 v[204:205], v12 offset:30912\n"
    "v_mfma_f32_16x16x32_bf16 v[120:123], v[180:183], a[228:231], v[120:123]\n"
    "ds_read_b64_tr_b16 v[206:207], v12 offset:36032\n"
    "v_mfma_f32_16x16x32_bf16 v[120:123], v[184:187], a[232:235], v[120:123]\n"
    "s_add_u32 m0, s77, 7168\n"
    "v_mfma_f32_16x16x32_bf16 v[120:123], v[188:191], a[236:239], v[120:123]\n"
    "v_mfma_f32_16x16x32_bf16 v[124:127], v[176:179], a[240:243], v[124:127]\n"
    "v_mfma_f32_16x16x32_bf16 v[124:127], v[180:183], a[244:247], v[124:127]\n"
    "buffer_load_dwordx4 v3, s[44:47], s17 offen offset:0 lds\n"
    "v_mfma_f32_16x16x32_bf16 v[124:127], v[184:187], a[248:251], v[124:127]\n"
    "v_mfma_f32_16x16x32_bf16 v[124:127], v[188:191], a[252:255], v[124:127]\n"
    "s_waitcnt lgkmcnt(4)\n"
    "v_mfma_f32_16x16x32_bf16 v[128:131], v[192:195], a[192:195], v[128:131]\n"
    "s_add_u32 s89, s20, s39\n"
    "v_mfma_f32_16x16x32_bf16 v[128:131], v[196:199], a[196:199], v[128:131]\n"
    "s_waitcnt lgkmcnt(0)\n"
    "v_mfma_f32_16x16x32_bf16 v[128:131], v[200:203], a[200:203], v[128:131]\n"
    "ds_read_b64_tr_b16 v[176:177], v11 offset:256\n"
    "v_mfma_f32_16x16x32_bf16 v[128:131], v[204:207], a[204:207], v[128:131]\n"
    "ds_read_b64_tr_b16 v[178:179], v11 offset:5376\n"
    "v_mfma_f32_16x16x32_bf16 v[132:135], v[192:195], a[208:211], v[132:135]\n"
    "s_add_u32 s89, s89, 20480\n"
    "ds_read_b64_tr_b16 v[180:181], v11 offset:10496\n"
    "v_mfma_f32_16x16x32_bf16 v[132:135], v[196:199], a[212:215], v[132:135]\n"
    "ds_read_b64_tr_b16 v[182:183], v11 offset:15616\n"
    "v_mfma_f32_16x16x32_bf16 v[132:135], v[200:203], a[216:219], v[132:135]\n"
    "ds_read_b64_tr_b16 v[184:185], v11 offset:20736\n"
    "v_mfma_f32_16x16x32_bf16 v[132:135], v[204:207], a[220:223], v[132:135]\n"
    "buffer_load_dwordx4 a[96:99], v0, s[40:43], s89 offen offset:0 sc1\n"
    "v_mfma_f32_16x16x32_bf16 v[136:139], v[192:195], a[224:227], v[136:139]\n"
    "ds_read_b64_tr_b16 v[186:187], v11 offset:25856\n"
    "ds_read_b64_tr_b16 v[188:189], v11 offset:30976\n"
    "v_mfma_f32_16x16x32_bf16 v[136:139], v[196:199], a[228:231], v[136:139]\n"
    "ds_read_b64_tr_b16 v[190:191], v11 offset:36096\n"
    "v_mfma_f32_16x16x32_bf16 v[136:139], v[200:203], a[232:235], v[136:139]\n"
    "buffer_load_dwordx4 a[100:103], v0, s[40:43], s89 offen offset:1024 sc1\n"
    "v_mfma_f32_16x16x32_bf16 v[136:139], v[204:207], a[236:239], v[136:139]\n"
    "v_mfma_f32_16x16x32_bf16 v[140:143], v[192:195], a[240:243], v[140:143]\n"
    "v_mfma_f32_16x16x32_bf16 v[140:143], v[196:199], a[244:247], v[140:143]\n"
    "buffer_load_dwordx4 a[104:107], v0, s[40:43], s89 offen offset:2048 sc1\n"
    "v_mfma_f32_16x16x32_bf16 v[140:143], v[200:203], a[248:251], v[140:143]\n"
    "v_mfma_f32_16x16x32_bf16 v[140:143], v[204:207], a[252:255], v[140:143]\n"
    "s_waitcnt lgkmcnt(4)\n"
    "v_mfma_f32_16x16x32_bf16 v[144:147], v[176:179], a[192:195], v[144:147]\n"
    "buffer_load_dwordx4 a[108:111], v0, s[40:43], s89 offen offset:3072 sc1\n"
    "v_mfma_f32_16x16x32_bf16 v[144:147], v[180:183], a[196:199], v[144:147]\n"
    "s_waitcnt lgkmcnt(0)\n"
    "v_mfma_f32_16x16x32_bf16 v[144:147], v[184:187], a[200:203], v[144:147]\n"
    "ds_read_b64_tr_b16 v[192:193], v12 offset:256\n"
    "v_mfma_f32_16x16x32_bf16 v[144:147], v[188:191], a[204:207], v[144:147]\n"
    "s_add_u32 s89, s20, s99\n"
    "ds_read_b64_tr_b16 v[194:195], v12 offset:5376\n"
    "v_mfma_f32_16x16x32_bf16 v[148:151], v[176:179], a[208:211], v[148:151]\n"
    "ds_read_b64_tr_b16 v[196:197], v12 offset:10496\n"
    "v_mfma_f32_16x16x32_bf16 v[148:151], v[180:183], a[212:215], v[148:151]\n"
    "ds_read_b64_tr_b16 v[198:199], v12 offset:15616\n"
    "v_mfma_f32_16x16x32_bf16 v[148:151], v[184:187], a[216:219], v[148:151]\n"
    "s_add_u32 s89, s89, 20480\n"
    "ds_read_b64_tr_b16 v[200:201], v12 offset:20736\n"
    "v_mfma_f32_16x16x32_bf16 v[148:151], v[188:191], a[220:223], v[148:151]\n"
    "ds_read_b64_tr_b16 v[202:203], v12 offset:25856\n"
    "v_mfma_f32_16x16x32_bf16 v[152:155], v[176:179], a[224:227], v[152:155]\n"
    "ds_read_b64_tr_b16 v[204:205], v12 offset:30976\n"
    "v_mfma_f32_16x16x32_bf16 v[152:155], v[180:183], a[228:231], v[152:155]\n"
    "buffer_load_dwordx4 a[112:115], v0, s[40:43], s89 offen offset:0 sc1\n"
    "v_mfma_f32_16x16x32_bf16 v[152:155], v[184:187], a[232:235], v[152:155]\n"
    "ds_read_b64_tr_b16 v[206:207], v12 offset:36096\n"
    "v_mfma_f32_16x16x32_bf16 v[152:155], v[188:191], a[236:239], v[152:155]\n"
    "v_mfma_f32_16x16x32_bf16 v[156:159], v[176:179], a[240:243], v[156:159]\n"
    "buffer_load_dwordx4 a[116:119], v0, s[40:43], s89 offen offset:1024 sc1\n"
    "v_mfma_f32_16x16x32_bf16 v[156:159], v[180:183], a[244:247], v[156:159]\n"
    "v_mfma_f32_16x16x32_bf16 v[156:159], v[184:187], a[248:251], v[156:159]\n"
    "v_mfma_f32_16x16x32_bf16 v[156:159], v[188:191], a[252:255], v[156:159]\n"
    "s_waitcnt vmcnt(22) lgkmcnt(0)\n"
    "s_barrier\n"
    "v_mfma_f32_16x16x32_bf16 v[160:163], v[192:195], a[192:195], v[160:163]\n"
    "buffer_load_dwordx4 a[120:123], v0, s[40:43], s89 offen offset:2048 sc1\n"
    "v_mfma_f32_16x16x32_bf16 v[160:163], v[196:199], a[196:199], v[160:163]\n"
    "ds_read_b64_tr_b16 v[176:177], v13 offset:0\n"
    "ds_read_b64_tr_b16 v[178:179], v13 offset:5120\n"
    "v_mfma_f32_16x16x32_bf16 v[160:163], v[200:203], a[200:203], v[160:163]\n"
    "buffer_load_dwordx4 a[124:127], v0, s[40:43], s89 offen offset:3072 sc1\n"
    "v_mfma_f32_16x16x32_bf16 v[160:163], v[204:207], a[204:207], v[160:163]\n"
    "ds_read_b64_tr_b16 v[180:181], v13 offset:10240\n"
    "ds_read_b64_tr_b16 v[182:183], v13 offset:15360\n"
    "v_mfma_f32_16x16x32_bf16 v[164:167], v[192:195], a[208:211], v[164:167]\n"
    "ds_read_b64_tr_b16 v[184:185], v13 offset:20480\n"
    "v_mfma_f32_16x16x32_bf16 v[164:167], v[196:199], a[212:215], v[164:167]\n"
    "ds_read_b64_tr_b16 v[186:187], v13 offset:25600\n"
    "v_mfma_f32_16x16x32_bf16 v[164:167], v[200:203], a[216:219], v[164:167]\n"
    "ds_read_b64_tr_b16 v[188:189], v13 offset:30720\n"
    "v_mfma_f32_16x16x32_bf16 v[164:167], v[204:207], a[220:223], v[164:167]\n"
    "ds_read_b64_tr_b16 v[190:191], v13 offset:35840\n"
    "v_mfma_f32_16x16x32_bf16 v[168:171], v[192:195], a[224:227], v[168:171]\n"
    "v_mfma_f32_16x16x32_bf16 v[168:171], v[196:199], a[228:231], v[168:171]\n"
    "v_mfma_f32_16x16x32_bf16 v[168:171], v[200:203], a[232:235], v[168:171]\n"
    "s_cmp_lg_u32 s35, 0\n"
    "s_cselect_b32 s20, 0x7f000000, s20\n"
    "v_mfma_f32_16x16x32_bf16 v[168:171], v[204:207], a[236:239], v[168:171]\n"
    "v_mfma_f32_16x16x32_bf16 v[172:175], v[192:195], a[240:243], v[172:175]\n"
    "v_mfma_f32_16x16x32_bf16 v[172:175], v[196:199], a[244:247], v[172:175]\n"
    "v_mfma_f32_16x16x32_bf16 v[172:175], v[200:203], a[248:251], v[172:175]\n"
    "v_mfma_f32_16x16x32_bf16 v[172:175], v[204:207], a[252:255], v[172:175]\n"
    "s_waitcnt lgkmcnt(4)\n"
    "v_mfma_f32_16x16x32_bf16 v[16:19], v[176:179], a[128:131], v[16:19]\n"
    "s_add_u32 m0, s77, 8192\n"
    "v_mfma_f32_16x16x32_bf16 v[16:19], v[180:183], a[132:135], v[16:19]\n"
    "s_waitcnt lgkmcnt(0)\n"
    "v_mfma_f32_16x16x32_bf16 v[16:19], v[184:187], a[136:139], v[16:19]\n"
    "ds_read_b64_tr_b16 v[192:193], v14 offset:0\n"
    "v_mfma_f32_16x16x32_bf16 v[16:19], v[188:191], a[140:143], v[16:19]\n"
    "buffer_load_dwordx4 v4, s[44:47], s17 offen offset:0 lds\n"
    "v_mfma_f32_16x16x32_bf16 v[20:23], v[176:179], a[144:147], v[20:23]\n"
    "ds_read_b64_tr_b16 v[194:195], v14 offset:5120\n"
    "ds_read_b64_tr_b16 v[196:197], v14 offset:10240\n"
    "v_mfma_f32_16x16x32_bf16 v[20:23], v[180:183], a[148:151], v[20:23]\n"
    "ds_read_b64_tr_b16 v[198:199], v14 offset:15360\n"
    "v_mfma_f32_16x16x32_bf16 v[20:23], v[184:187], a[152:155], v[20:23]\n"
    "s_add_u32 m0, s77, 9216\n"
    "ds_read_b64_tr_b16 v[200:201], v14 offset:20480\n"
    "v_mfma_f32_16x16x32_bf16 v[20:23], v[188:191], a[156:159], v[20:23]\n"
    "ds_read_b64_tr_b16 v[202:203], v14 offset:25600\n"
    "v_mfma_f32_16x16x32_bf16 v[24:27], v[176:179], a[160:163], v[24:27]\n"
    "ds_read_b64_tr_b16 v[204:205], v14 offset:30720\n"
    "v_mfma_f32_16x16x32_bf16 v[24:27], v[180:183], a[164:167], v[24:27]\n"
    "ds_read_b64_tr_b16 v[206:207], v14 offset:35840\n"
    "v_mfma_f32_16x16x32_bf16 v[24:27], v[184:187], a[168:171], v[24:27]\n"
    "buffer_load_dwordx4 v5, s[44:47], s17 offen offset:0 lds\n"
    "v_mfma_f32_16x16x32_bf16 v[24:27], v[188:191], a[172:175], v[24:27]\n"
    "v_mfma_f32_16x16x32_bf16 v[28:31], v[176:179], a[176:179], v[28:31]\n"
    "v_mfma_f32_16x16x32_bf16 v[28:31], v[180:183], a[180:183], v[28:31]\n"
    "s_add_u32 m0, s77, 65536\n"
    "v_mfma_f32_16x16x32_bf16 v[28:31], v[184:187], a[184:187], v[28:31]\n"
    "v_mfma_f32_16x16x32_bf16 v[28:31], v[188:191], a[188:191], v[28:31]\n"
    "s_waitcnt lgkmcnt(4)\n"
    "v_mfma_f32_16x16x32_bf16 v[32:35], v[192:195], a[128:131], v[32:35]\n"
    "v_mfma_f32_16x16x32_bf16 v[32:35], v[196:199], a[132:135], v[32:35]\n"
    "s_waitcnt lgkmcnt(0)\n"
    "v_mfma_f32_16x16x32_bf16 v[32:35], v[200:203], a[136:139], v[32:35]\n"
    "buffer_load_dwordx4 v1, s[64:67], s18 offen offset:0 lds\n"
    "v_mfma_f32_16x16x32_bf16 v[32:35], v[204:207], a[140:143], v[32:35]\n"
    "ds_read_b64_tr_b16 v[176:177], v13 offset:64\n"
    "ds_read_b64_tr_b16 v[178:179], v13 offset:5184\n"
    "v_mfma_f32_16x16x32_bf16 v[36:39], v[192:195], a[144:147], v[36:39]\n"
    "s_add_u32 m0, s77, 66560\n"
    "ds_read_b64_tr_b16 v[180:181], v13 offset:10304\n"
    "v_bfe_u32 v240, v0, 4, 4\n"
    "v_mfma_f32_16x16x32_bf16 v[36:39], v[196:199], a[148:151], v[36:39]\n"
    "ds_read_b64_tr_b16 v[182:183], v13 offset:15424\n"
    "v_lshrrev_b32 v241, 2, v240\n"
    "v_mfma_f32_16x16x32_bf16 v[36:39], v[200:203], a[152:155], v[36:39]\n"
    "ds_read_b64_tr_b16 v[184:185], v13 offset:20544\n"
    "v_lshrrev_b32 v242, 1, v241\n"
    "v_mfma_f32_16x16x32_bf16 v[36:39], v[204:207], a[156:159], v[36:39]\n"
    "ds_read_b64_tr_b16 v[186:187], v13 offset:25664\n"
    "v_and_b32 v243, 3, v240\n"
    "v_mfma_f32_16x16x32_bf16 v[40:43], v[192:195], a[160:163], v[40:43]\n"
    "ds_read_b64_tr_b16 v[188:189], v13 offset:30784\n"
    "v_lshl_add_u32 v242, v242, 2, v243\n"
    "v_mfma_f32_16x16x32_bf16 v[40:43], v[196:199], a[164:167], v[40:43]\n"
    "buffer_load_dwordx4 v2, s[64:67], s18 offen offset:0 lds\n"
    "v_mfma_f32_16x16x32_bf16 v[40:43], v[200:203], a[168:171], v[40:43]\n"
    "ds_read_b64_tr_b16 v[190:191], v13 offset:35904\n"
    "v_and_b32 v243, 1, v242\n"
    "v_mfma_f32_16x16x32_bf16 v[40:43], v[204:207], a[172:175], v[40:43]\n"
    "s_add_u32 m0, s77, 67584\n"
    "v_lshlrev_b32 v243, 4, v243\n"
    "v_mov_b32 v244, 0x3f80\n"
    "v_mfma_f32_16x16x32_bf16 v[44:47], v[192:195], a[176:179], v[44:47]\n"
    "v_lshlrev_b32 v244, v243, v244\n"
    "v_lshrrev_b32 v242, 1, v242\n"
    "v_mfma_f32_16x16x32_bf16 v[44:47], v[196:199], a[180:183], v[44:47]\n"
    "v_and_b32 v241, 1, v241\n"
    "v_lshlrev_b32 v241, 1, v241\n"
    "v_mfma_f32_16x16x32_bf16 v[44:47], v[200:203], a[184:187], v[44:47]\n"
    "buffer_load_dwordx4 v3, s[64:67], s18 offen offset:0 lds\n"
    "v_mfma_f32_16x16x32_bf16 v[44:47], v[204:207], a[188:191], v[44:47]\n"
    "s_waitcnt lgkmcnt(4)\n"
    "v_lshrrev_b32 v243, 8, v0\n"
    "v_sub_u32 v243, v243, v241\n"
    "v_mfma_f32_16x16x32_bf16 v[48:51], v[176:179], a[128:131], v[48:51]\n"
    "v_lshl_add_u32 v243, v243, 2, v242\n"
    "s_sub_u32 s87, 0, s35\n"
    "v_mfma_f32_16x16x32_bf16 v[48:51], v[180:183], a[132:135], v[48:51]\n"
    "s_waitcnt lgkmcnt(0)\n"
    "v_cmp_eq_u32 vcc, 0, v243\n"
    "v_mfma_f32_16x16x32_bf16 v[48:51], v[184:187], a[136:139], v[48:51]\n"
    "s_add_u32 m0, s77, 68608\n"
    "ds_read_b64_tr_b16 v[192:193], v14 offset:64\n"
    "v_mfma_f32_16x16x32_bf16 v[48:51], v[188:191], a[140:143], v[48:51]\n"
    "s_nop 1\n"
    "ds_read_b64_tr_b16 v[194:195], v14 offset:5184\n"
    "v_cndmask_b32 v245, 0, v244, vcc\n"
    "v_mfma_f32_16x16x32_bf16 v[52:55], v[176:179], a[144:147], v[52:55]\n"
    "ds_read_b64_tr_b16 v[196:197], v14 offset:10304\n"
    "v_accvgpr_write_b32 a0, v245\n"
    "v_mfma_f32_16x16x32_bf16 v[52:55], v[180:183], a[148:151], v[52:55]\n"
    "ds_read_b64_tr_b16 v[198:199], v14 offset:15424\n"
    "v_and_b32 v241, s87, v245\n"
    "v_mfma_f32_16x16x32_bf16 v[52:55], v[184:187], a[152:155], v[52:55]\n"
    "buffer_load_dwordx4 v4, s[64:67], s18 offen offset:0 lds\n"
    "v_mfma_f32_16x16x32_bf16 v[52:55], v[188:191], a[156:159], v[52:55]\n"
    "ds_read_b64_tr_b16 v[200:201], v14 offset:20544\n"
    "ds_read_b64_tr_b16 v[202:203], v14 offset:25664\n"
    "v_mfma_f32_16x16x32_bf16 v[56:59], v[176:179], a[160:163], v[56:59]\n"
    "ds_read_b64_tr_b16 v[204:205], v14 offset:30784\n"
    "v_accvgpr_write_b32 a16, v241\n"
    "v_mfma_f32_16x16x32_bf16 v[56:59], v[180:183], a[164:167], v[56:59]\n"
    "s_add_u32 s89, s20, s81\n"
    "ds_read_b64_tr_b16 v[206:207], v14 offset:35904\n"
    "v_xor_b32 v241, v241, v245\n"
    "v_mfma_f32_16x16x32_bf16 v[56:59], v[184:187], a[168:171], v[56:59]\n"
    "v_accvgpr_write_b32 a8, v241\n"
    "v_cmp_eq_u32 vcc, 1, v243\n"
    "v_mfma_f32_16x16x32_bf16 v[56:59], v[188:191], a[172:175], v[56:59]\n"
    "s_nop 1\n"
    "v_cndmask_b32 v245, 0, v244, vcc\n"
    "v_accvgpr_write_b32 a1, v245\n"
    "v_mfma_f32_16x16x32_bf16 v[60:63], v[176:179], a[176:179], v[60:63]\n"
    "s_add_u32 s89, s89, 24576\n"
    "v_and_b32 v241, s87, v245\n"
    "v_accvgpr_write_b32 a17, v241\n"
    "v_mfma_f32_16x16x32_bf16 v[60:63], v[180:183], a[180:183], v[60:63]\n"
    "v_xor_b32 v241, v241, v245\n"
    "v_mfma_f32_16x16x32_bf16 v[60:63], v[184:187], a[184:187], v[60:63]\n"
    "v_accvgpr_write_b32 a9, v241\n"
    "v_mfma_f32_16x16x32_bf16 v[60:63], v[188:191], a[188:191], v[60:63]\n"
    "s_waitcnt lgkmcnt(4)\n"
    "v_cmp_eq_u32 vcc, 2, v243\n"
    "v_mfma_f32_16x16x32_bf16 v[64:67], v[192:195], a[128:131], v[64:67]\n"
    "buffer_load_dwordx4 a[192:195], v0, s[40:43], s89 offen offset:0 sc1\n"
    "v_mfma_f32_16x16x32_bf16 v[64:67], v[196:199], a[132:135], v[64:67]\n"
    "s_waitcnt lgkmcnt(0)\n"
    "s_nop 1\n"
    "v_mfma_f32_16x16x32_bf16 v[64:67], v[200:203], a[136:139], v[64:67]\n"
    "ds_read_b64_tr_b16 v[176:177], v13 offset:128\n"
    "v_cndmask_b32 v245, 0, v244, vcc\n"
    "v_mfma_f32_16x16x32_bf16 v[64:67], v[204:207], a[140:143], v[64:67]\n"
    "ds_read_b64_tr_b16 v[178:179], v13 offset:5248\n"
    "v_accvgpr_write_b32 a2, v245\n"
    "v_mfma_f32_16x16x32_bf16 v[68:71], v[192:195], a[144:147], v[68:71]\n"
    "buffer_load_dwordx4 a[196:199], v0, s[40:43], s89 offen offset:1024 sc1\n"
    "v_mfma_f32_16x16x32_bf16 v[68:71], v[196:199], a[148:151], v[68:71]\n"
    "ds_read_b64_tr_b16 v[180:181], v13 offset:10368\n"
    "ds_read_b64_tr_b16 v[182:183], v13 offset:15488\n"
    "v_mfma_f32_16x16x32_bf16 v[68:71], v[200:203], a[152:155], v[68:71]\n"
    "ds_read_b64_tr_b16 v[184:185], v13 offset:20608\n"
    "v_and_b32 v241, s87, v245\n"
    "v_mfma_f32_16x16x32_bf16 v[68:71], v[204:207], a[156:159], v[68:71]\n"
    "buffer_load_dwordx4 a[200:203], v0, s[40:43], s89 offen offset:2048 sc1\n"
    "v_mfma_f32_16x16x32_bf16 v[72:75], v[192:195], a[160:163], v[72:75]\n"
    "ds_read_b64_tr_b16 v[186:187], v13 offset:25728\n"
    "ds_read_b64_tr_b16 v[188:189], v13 offset:30848\n"
    "v_mfma_f32_16x16x32_bf16 v[72:75], v[196:199], a[164:167], v[72:75]\n"
    "ds_read_b64_tr_b16 v[190:191], v13 offset:35968\n"
    "v_accvgpr_write_b32 a18, v241\n"
    "v_mfma_f32_16x16x32_bf16 v[72:75], v[200:203], a[168:171], v[72:75]\n"
    "v_xor_b32 v241, v241, v245\n"
    "v_accvgpr_write_b32 a10, v241\n"
    "v_mfma_f32_16x16x32_bf16 v[72:75], v[204:207], a[172:175], v[72:75]\n"
    "buffer_load_dwordx4 a[204:207], v0, s[40:43], s89 offen offset:3072 sc1\n"
    "v_mfma_f32_16x16x32_bf16 v[76:79], v[192:195], a[176:179], v[76:79]\n"
    "v_cmp_eq_u32 vcc, 3, v243\n"
    "v_mfma_f32_16x16x32_bf16 v[76:79], v[196:199], a[180:183], v[76:79]\n"
    "s_nop 1\n"
    "v_cndmask_b32 v245, 0, v244, vcc\n"
    "v_accvgpr_write_b32 a3, v245\n"
    "v_mfma_f32_16x16x32_bf16 v[76:79], v[200:203], a[184:187], v[76:79]\n"
    "s_add_u32 s89, s20, s3\n"
    "v_and_b32 v241, s87, v245\n"
    "v_accvgpr_write_b32 a19, v241\n"
    "v_mfma_f32_16x16x32_bf16 v[76:79], v[204:207], a[188:191], v[76:79]\n"
    "s_waitcnt lgkmcnt(4)\n"
    "v_xor_b32 v241, v241, v245\n"
    "v_accvgpr_write_b32 a11, v241\n"
    "v_mfma_f32_16x16x32_bf16 v[80:83], v[176:179], a[128:131], v[80:83]\n"
    "v_cmp_eq_u32 vcc, 4, v243\n"
    "v_mfma_f32_16x16x32_bf16 v[80:83], v[180:183], a[132:135], v[80:83]\n"
    "s_waitcnt lgkmcnt(0)\n"
    "s_add_u32 s89, s89, 24576\n"
    "v_mfma_f32_16x16x32_bf16 v[80:83], v[184:187], a[136:139], v[80:83]\n"
    "s_nop 1\n"
    "ds_read_b64_tr_b16 v[192:193], v14 offset:128\n"
    "v_cndmask_b32 v245, 0, v244, vcc\n"
    "v_mfma_f32_16x16x32_bf16 v[80:83], v[188:191], a[140:143], v[80:83]\n"
    "ds_read_b64_tr_b16 v[194:195], v14 offset:5248\n"
    "v_accvgpr_write_b32 a4, v245\n"
    "v_mfma_f32_16x16x32_bf16 v[84:87], v[176:179], a[144:147], v[84:87]\n"
    "ds_read_b64_tr_b16 v[196:197], v14 offset:10368\n"
    "v_and_b32 v241, s87, v245\n"
    "v_mfma_f32_16x16x32_bf16 v[84:87], v[180:183], a[148:151], v[84:87]\n"
    "buffer_load_dwordx4 a[208:211], v0, s[40:43], s89 offen offset:0 sc1\n"
    "v_mfma_f32_16x16x32_bf16 v[84:87], v[184:187], a[152:155], v[84:87]\n"
    "ds_read_b64_tr_b16 v[198:199], v14 offset:15488\n"
    "ds_read_b64_tr_b16 v[200:201], v14 offset:20608\n"
    "v_mfma_f32_16x16x32_bf16 v[84:87], v[188:191], a[156:159], v[84:87]\n"
    "ds_read_b64_tr_b16 v[202:203], v14 offset:25728\n"
    "v_accvgpr_write_b32 a20, v241\n"
    "v_mfma_f32_16x16x32_bf16 v[88:91], v[176:179], a[160:163], v[88:91]\n"
    "ds_read_b64_tr_b16 v[204:205], v14 offset:30848\n"
    "v_xor_b32 v241, v241, v245\n"
    "v_mfma_f32_16x16x32_bf16 v[88:91], v[180:183], a[164:167], v[88:91]\n"
    "buffer_load_dwordx4 a[212:215], v0, s[40:43], s89 offen offset:1024 sc1\n"
    "v_mfma_f32_16x16x32_bf16 v[88:91], v[184:187], a[168:171], v[88:91]\n"
    "ds_read_b64_tr_b16 v[206:207], v14 offset:35968\n"
    "v_accvgpr_write_b32 a12, v241\n"
    "v_mfma_f32_16x16x32_bf16 v[88:91], v[188:191], a[172:175], v[88:91]\n"
    "buffer_load_dwordx4 a[216:219], v0, s[40:43], s89 offen offset:2048 sc1\n"
    "v_mfma_f32_16x16x32_bf16 v[92:95], v[176:179], a[176:179], v[92:95]\n"
    "v_cmp_eq_u32 vcc, 5, v243\n"
    "s_nop 1\n"
    "v_mfma_f32_16x16x32_bf16 v[92:95], v[180:183], a[180:183], v[92:95]\n"
    "v_cndmask_b32 v245, 0, v244, vcc\n"
    "v_accvgpr_write_b32 a5, v245\n"
    "v_mfma_f32_16x16x32_bf16 v[92:95], v[184:187], a[184:187], v[92:95]\n"
    "v_and_b32 v241, s87, v245\n"
    "v_accvgpr_write_b32 a21, v241\n"
    "v_mfma_f32_16x16x32_bf16 v[92:95], v[188:191], a[188:191], v[92:95]\n"
    "s_waitcnt lgkmcnt(4)\n"
    "v_xor_b32 v241, v241, v245\n"
    "v_accvgpr_write_b32 a13, v241\n"
    "v_mfma_f32_16x16x32_bf16 v[96:99], v[192:195], a[128:131], v[96:99]\n"
    "buffer_load_dwordx4 a[220:223], v0, s[40:43], s89 offen offset:3072 sc1\n"
    "v_mfma_f32_16x16x32_bf16 v[96:99], v[196:199], a[132:135], v[96:99]\n"
    "s_waitcnt lgkmcnt(0)\n"
    "v_cmp_eq_u32 vcc, 6, v243\n"
    "v_mfma_f32_16x16x32_bf16 v[96:99], v[200:203], a[136:139], v[96:99]\n"
    "s_nop 1\n"
    "ds_read_b64_tr_b16 v[176:177], v13 offset:192\n"
    "v_cndmask_b32 v245, 0, v244, vcc\n"
    "v_mfma_f32_16x16x32_bf16 v[96:99], v[204:207], a[140:143], v[96:99]\n"
    "s_add_u32 m0, s77, 69632\n"
    "ds_read_b64_tr_b16 v[178:179], v13 offset:5312\n"
    "v_accvgpr_write_b32 a6, v245\n"
    "v_mfma_f32_16x16x32_bf16 v[100:103], v[192:195], a[144:147], v[100:103]\n"
    "ds_read_b64_tr_b16 v[180:181], v13 offset:10432\n"
    "v_and_b32 v241, s87, v245\n"
    "v_mfma_f32_16x16x32_bf16 v[100:103], v[196:199], a[148:151], v[100:103]\n"
    "ds_read_b64_tr_b16 v[182:183], v13 offset:15552\n"
    "v_accvgpr_write_b32 a22, v241\n"
    "v_mfma_f32_16x16x32_bf16 v[100:103], v[200:203], a[152:155], v[100:103]\n"
    "buffer_load_dwordx4 v5, s[64:67], s18 offen offset:0 lds\n"
    "v_mfma_f32_16x16x32_bf16 v[100:103], v[204:207], a[156:159], v[100:103]\n"
    "ds_read_b64_tr_b16 v[184:185], v13 offset:20672\n"
    "v_xor_b32 v241, v241, v245\n"
    "v_mfma_f32_16x16x32_bf16 v[104:107], v[192:195], a[160:163], v[104:107]\n"
    "ds_read_b64_tr_b16 v[186:187], v13 offset:25792\n"
    "v_accvgpr_write_b32 a14, v241\n"
    "v_mfma_f32_16x16x32_bf16 v[104:107], v[196:199], a[164:167], v[104:107]\n"
    "s_add_u32 m0, s77, 70656\n"
    "ds_read_b64_tr_b16 v[188:189], v13 offset:30912\n"
    "v_cmp_eq_u32 vcc, 7, v243\n"
    "v_mfma_f32_16x16x32_bf16 v[104:107], v[200:203], a[168:171], v[104:107]\n"
    "s_nop 1\n"
    "ds_read_b64_tr_b16 v[190:191], v13 offset:36032\n"
    "v_cndmask_b32 v245, 0, v244, vcc\n"
    "v_mfma_f32_16x16x32_bf16 v[104:107], v[204:207], a[172:175], v[104:107]\n"
    "v_accvgpr_write_b32 a7, v245\n"
    "v_and_b32 v241, s87, v245\n"
    "v_mfma_f32_16x16x32_bf16 v[108:111], v[192:195], a[176:179], v[108:111]\n"
    "v_accvgpr_write_b32 a23, v241\n"
    "v_xor_b32 v241, v241, v245\n"
    "v_mfma_f32_16x16x32_bf16 v[108:111], v[196:199], a[180:183], v[108:111]\n"
    "buffer_load_dwordx4 v1, s[44:47], s18 offen offset:0 lds\n"
    "v_mfma_f32_16x16x32_bf16 v[108:111], v[200:203], a[184:187], v[108:111]\n"
    "v_accvgpr_write_b32 a15, v241\n"
    "v_mul_u32_u24 v240, 1280, v240\n"
    "v_mfma_f32_16x16x32_bf16 v[108:111], v[204:207], a[188:191], v[108:111]\n"
    "s_waitcnt lgkmcnt(4)\n"
    "v_lshrrev_b32 v243, 8, v0\n"
    "v_lshl_add_u32 v10, v243, 4, v240\n"
    "v_mfma_f32_16x16x32_bf16 v[112:115], v[176:179], a[128:131], v[112:115]\n"
    "s_add_u32 m0, s77, 71680\n"
    "v_bfe_u32 v240, v0, 4, 3\n"
    "v_lshlrev_b32 v15, 6, v240\n"
    "v_mfma_f32_16x16x32_bf16 v[112:115], v[180:183], a[132:135], v[112:115]\n"
    "s_waitcnt lgkmcnt(0)\n"
    "s_and_b32 s85, %[bid], 7\n"
    "s_lshl_b32 s85, s85, 2\n"
    "v_mfma_f32_16x16x32_bf16 v[112:115], v[184:187], a[136:139], v[112:115]\n"
    "s_lshr_b32 s85, 0x54321706, s85\n"
    "ds_read_b64_tr_b16 v[192:193], v14 offset:192\n"
    "s_and_b32 s85, s85, 15\n"
    "v_mfma_f32_16x16x32_bf16 v[112:115], v[188:191], a[140:143], v[112:115]\n"
    "buffer_load_dwordx4 v2, s[44:47], s18 offen offset:0 lds\n"
    "v_mfma_f32_16x16x32_bf16 v[116:119], v[176:179], a[144:147], v[116:119]\n"
    "s_lshr_b32 s84, %[bid], 5\n"
    "ds_read_b64_tr_b16 v[194:195], v14 offset:5312\n"
    "ds_read_b64_tr_b16 v[196:197], v14 offset:10432\n"
    "v_mfma_f32_16x16x32_bf16 v[116:119], v[180:183], a[148:151], v[116:119]\n"
    "s_lshl_b32 s84, s84, 6\n"
    "ds_read_b64_tr_b16 v[198:199], v14 offset:15552\n"
    "s_add_u32 s84, s84, 3141632\n"
    "v_mfma_f32_16x16x32_bf16 v[116:119], v[184:187], a[152:155], v[116:119]\n"
    "s_and_b32 s86, %[bid], 31\n"
    "ds_read_b64_tr_b16 v[200:201], v14 offset:20672\n"
    "s_add_u32 s86, s86, s76\n"
    "v_mfma_f32_16x16x32_bf16 v[116:119], v[188:191], a[156:159], v[116:119]\n"
    "s_add_u32 m0, s77, 72704\n"
    "ds_read_b64_tr_b16 v[202:203], v14 offset:25792\n"
    "s_cmp_eq_u32 s86, 0\n"
    "s_cselect_b32 s84, s84, 0x7f000000\n"
    "v_mfma_f32_16x16x32_bf16 v[120:123], v[176:179], a[160:163], v[120:123]\n"
    "s_mov_b32 s86, 0\n"
    "ds_read_b64_tr_b16 v[204:205], v14 offset:30912\n"
    "s_cmp_eq_u32 s85, 0\n"
    "s_cselect_b32 s4, 0x40000000, s86\n"
    "v_mfma_f32_16x16x32_bf16 v[120:123], v[180:183], a[164:167], v[120:123]\n"
    "s_mov_b32 s86, 2625536\n"
    "ds_read_b64_tr_b16 v[206:207], v14 offset:36032\n"
    "s_cmp_eq_u32 s85, 1\n"
    "s_cselect_b32 s5, 0x40000000, s86\n"
    "v_mfma_f32_16x16x32_bf16 v[120:123], v[184:187], a[168:171], v[120:123]\n"
    "buffer_load_dwordx4 v3, s[44:47], s18 offen offset:0 lds\n"
    "v_mfma_f32_16x16x32_bf16 v[120:123], v[188:191], a[172:175], v[120:123]\n"
    "s_mov_b32 s86, 5251072\n"
    "v_mfma_f32_16x16x32_bf16 v[124:127], v[176:179], a[176:179], v[124:127]\n"
    "s_cmp_eq_u32 s85, 2\n"
    "s_cselect_b32 s6, 0x40000000, s86\n"
    "v_mfma_f32_16x16x32_bf16 v[124:127], v[180:183], a[180:183], v[124:127]\n"
    "s_add_u32 m0, s77, 73728\n"
    "v_mfma_f32_16x16x32_bf16 v[124:127], v[184:187], a[184:187], v[124:127]\n"
    "s_mov_b32 s86, 7876608\n"
    "s_cmp_eq_u32 s85, 3\n"
    "s_cselect_b32 s7, 0x40000000, s86\n"
    "v_mfma_f32_16x16x32_bf16 v[124:127], v[188:191], a[188:191], v[124:127]\n"
    "s_waitcnt lgkmcnt(4)\n"
    "v_mfma_f32_16x16x32_bf16 v[128:131], v[192:195], a[128:131], v[128:131]\n"
    "s_mov_b32 s86, 10502144\n"
    "v_mfma_f32_16x16x32_bf16 v[128:131], v[196:199], a[132:135], v[128:131]\n"
    "s_waitcnt lgkmcnt(0)\n"
    "s_cmp_eq_u32 s85, 4\n"
    "s_cselect_b32 s8, 0x40000000, s86\n"
    "v_mfma_f32_16x16x32_bf16 v[128:131], v[200:203], a[136:139], v[128:131]\n"
    "buffer_load_dwordx4 v4, s[44:47], s18 offen offset:0 lds\n"
    "v_mfma_f32_16x16x32_bf16 v[128:131], v[204:207], a[140:143], v[128:131]\n"
    "s_mov_b32 s86, 13127680\n"
    "ds_read_b64_tr_b16 v[176:177], v13 offset:256\n"
    "ds_read_b64_tr_b16 v[178:179], v13 offset:5376\n"
    "v_mfma_f32_16x16x32_bf16 v[132:135], v[192:195], a[144:147], v[132:135]\n"
    "s_add_u32 s89, s20, s39\n"
    "ds_read_b64_tr_b16 v[180:181], v13 offset:10496\n"
    "s_cmp_eq_u32 s85, 5\n"
    "s_cselect_b32 s9, 0x40000000, s86\n"
    "v_mfma_f32_16x16x32_bf16 v[132:135], v[196:199], a[148:151], v[132:135]\n"
    "s_mov_b32 s86, 15753216\n"
    "ds_read_b64_tr_b16 v[182:183], v13 offset:15616\n"
    "v_mfma_f32_16x16x32_bf16 v[132:135], v[200:203], a[152:155], v[132:135]\n"
    "s_cmp_eq_u32 s85, 6\n"
    "s_cselect_b32 s10, 0x40000000, s86\n"
    "ds_read_b64_tr_b16 v[184:185], v13 offset:20736\n"
    "v_mfma_f32_16x16x32_bf16 v[132:135], v[204:207], a[156:159], v[132:135]\n"
    "ds_read_b64_tr_b16 v[186:187], v13 offset:25856\n"
    "v_mfma_f32_16x16x32_bf16 v[136:139], v[192:195], a[160:163], v[136:139]\n"
    "s_add_u32 s89, s89, 24576\n"
    "ds_read_b64_tr_b16 v[188:189], v13 offset:30976\n"
    "s_mov_b32 s86, 18378752\n"
    "v_mfma_f32_16x16x32_bf16 v[136:139], v[196:199], a[164:167], v[136:139]\n"
    "s_cmp_eq_u32 s85, 7\n"
    "s_cselect_b32 s11, 0x40000000, s86\n"
    "ds_read_b64_tr_b16 v[190:191], v13 offset:36096\n"
    "v_mfma_f32_16x16x32_bf16 v[136:139], v[200:203], a[168:171], v[136:139]\n"
    "s_lshl1_add_u32 s87, s76, 0\n"
    "v_mfma_f32_16x16x32_bf16 v[136:139], v[204:207], a[172:175], v[136:139]\n"
    "buffer_load_dwordx4 a[224:227], v0, s[40:43], s89 offen offset:0 sc1\n"
    "v_mfma_f32_16x16x32_bf16 v[140:143], v[192:195], a[176:179], v[140:143]\n"
    "s_cmp_eq_u32 s87, s85\n"
    "s_cselect_b32 s86, 4, 5\n"
    "s_cselect_b32 s79, 0x7f000000, s79\n"
    "s_cselect_b32 s80, 0x7f000000, s80\n"
    "v_mfma_f32_16x16x32_bf16 v[140:143], v[196:199], a[180:183], v[140:143]\n"
    "v_mfma_f32_16x16x32_bf16 v[140:143], v[200:203], a[184:187], v[140:143]\n"
    "v_mfma_f32_16x16x32_bf16 v[140:143], v[204:207], a[188:191], v[140:143]\n"
    "s_waitcnt lgkmcnt(4)\n"
    "s_mul_i32 s86, s86, 10240\n"
    "v_mfma_f32_16x16x32_bf16 v[144:147], v[176:179], a[128:131], v[144:147]\n"
    "buffer_load_dwordx4 a[228:231], v0, s[40:43], s89 offen offset:1024 sc1\n"
    "v_mfma_f32_16x16x32_bf16 v[144:147], v[180:183], a[132:135], v[144:147]\n"
    "s_waitcnt lgkmcnt(0)\n"
    "v_add_u32 v252, s86, v0\n"
    "s_lshl1_add_u32 s87, s76, 1\n"
    "v_mfma_f32_16x16x32_bf16 v[144:147], v[184:187], a[136:139], v[144:147]\n"
    "buffer_load_dwordx4 a[232:235], v0, s[40:43], s89 offen offset:2048 sc1\n"
    "v_mfma_f32_16x16x32_bf16 v[144:147], v[188:191], a[140:143], v[144:147]\n"
    "s_cmp_eq_u32 s87, s85\n"
    "s_cselect_b32 s86, 4, 5\n"
    "s_cselect_b32 s82, 0x7f000000, s82\n"
    "s_cselect_b32 s83, 0x7f000000, s83\n"
    "ds_read_b64_tr_b16 v[192:193], v14 offset:256\n"
    "ds_read_b64_tr_b16 v[194:195], v14 offset:5376\n"
    "v_mfma_f32_16x16x32_bf16 v[148:151], v[176:179], a[144:147], v[148:151]\n"
    "ds_read_b64_tr_b16 v[196:197], v14 offset:10496\n"
    "v_mfma_f32_16x16x32_bf16 v[148:151], v[180:183], a[148:151], v[148:151]\n"
    "buffer_load_dwordx4 a[236:239], v0, s[40:43], s89 offen offset:3072 sc1\n"
    "v_mfma_f32_16x16x32_bf16 v[148:151], v[184:187], a[152:155], v[148:151]\n"
    "s_mul_i32 s86, s86, 10240\n"
    "ds_read_b64_tr_b16 v[198:199], v14 offset:15616\n"
    "ds_read_b64_tr_b16 v[200:201], v14 offset:20736\n"
    "v_mfma_f32_16x16x32_bf16 v[148:151], v[188:191], a[156:159], v[148:151]\n"
    "ds_read_b64_tr_b16 v[202:203], v14 offset:25856\n"
    "v_add_u32 v253, s86, v0\n"
    "v_mfma_f32_16x16x32_bf16 v[152:155], v[176:179], a[160:163], v[152:155]\n"
    "s_and_b32 s86, s76, 1\n"
    "ds_read_b64_tr_b16 v[204:205], v14 offset:30976\n"
    "s_mul_i32 s86, s86, 5\n"
    "v_mfma_f32_16x16x32_bf16 v[152:155], v[180:183], a[164:167], v[152:155]\n"
    "s_add_u32 s89, s20, s99\n"
    "ds_read_b64_tr_b16 v[206:207], v14 offset:36096\n"
    "s_lshr_b32 s87, s76, 1\n"
    "v_mfma_f32_16x16x32_bf16 v[152:155], v[184:187], a[168:171], v[152:155]\n"
    "s_add_u32 s86, s86, s87\n"
    "s_lshl_b32 s86, s86, 10\n"
    "v_mfma_f32_16x16x32_bf16 v[152:155], v[188:191], a[172:175], v[152:155]\n"
    "v_add_u32 v254, s86, v0\n"
    "v_mfma_f32_16x16x32_bf16 v[156:159], v[176:179], a[176:179], v[156:159]\n"
    "s_add_u32 s89, s89, 24576\n"
    "v_mfma_f32_16x16x32_bf16 v[156:159], v[180:183], a[180:183], v[156:159]\n"
    "v_mfma_f32_16x16x32_bf16 v[156:159], v[184:187], a[184:187], v[156:159]\n"
    "v_mfma_f32_16x16x32_bf16 v[156:159], v[188:191], a[188:191], v[156:159]\n"
    "s_waitcnt vmcnt(21) lgkmcnt(0)\n"
    "s_barrier\n"
    "v_mfma_f32_16x16x32_bf16 v[160:163], v[192:195], a[128:131], v[160:163]\n"
    "buffer_load_dwordx4 a[240:243], v0, s[40:43], s89 offen offset:0 sc1\n"
    "v_mfma_f32_16x16x32_bf16 v[160:163], v[196:199], a[132:135], v[160:163]\n"
    "ds_read_b64_tr_b16 v[176:177], v8 offset:0\n"
    "ds_read_b64_tr_b16 v[178:179], v8 offset:5120\n"
    "v_mfma_f32_16x16x32_bf16 v[160:163], v[200:203], a[136:139], v[160:163]\n"
    "ds_read_b64_tr_b16 v[180:181], v8 offset:10240\n"
    "v_mfma_f32_16x16x32_bf16 v[160:163], v[204:207], a[140:143], v[160:163]\n"
    "buffer_load_dwordx4 a[244:247], v0, s[40:43], s89 offen offset:1024 sc1\n"
    "v_mfma_f32_16x16x32_bf16 v[164:167], v[192:195], a[144:147], v[164:167]\n"
    "ds_read_b64_tr_b16 v[182:183], v8 offset:15360\n"
    "ds_read_b64_tr_b16 v[184:185], v8 offset:20480\n"
    "v_mfma_f32_16x16x32_bf16 v[164:167], v[196:199], a[148:151], v[164:167]\n"
    "ds_read_b64_tr_b16 v[186:187], v8 offset:25600\n"
    "v_mfma_f32_16x16x32_bf16 v[164:167], v[200:203], a[152:155], v[164:167]\n"
    "ds_read_b64_tr_b16 v[188:189], v8 offset:30720\n"
    "v_mfma_f32_16x16x32_bf16 v[164:167], v[204:207], a[156:159], v[164:167]\n"
    "buffer_load_dwordx4 a[248:251], v0, s[40:43], s89 offen offset:2048 sc1\n"
    "v_mfma_f32_16x16x32_bf16 v[168:171], v[192:195], a[160:163], v[168:171]\n"
    "ds_read_b64_tr_b16 v[190:191], v8 offset:35840\n"
    "v_mfma_f32_16x16x32_bf16 v[168:171], v[196:199], a[164:167], v[168:171]\n"
    "v_mfma_f32_16x16x32_bf16 v[168:171], v[200:203], a[168:171], v[168:171]\n"
    "buffer_load_dwordx4 a[252:255], v0, s[40:43], s89 offen offset:3072 sc1\n"
    "v_mfma_f32_16x16x32_bf16 v[168:171], v[204:207], a[172:175], v[168:171]\n"
    "v_mfma_f32_16x16x32_bf16 v[172:175], v[192:195], a[176:179], v[172:175]\n"
    "v_mfma_f32_16x16x32_bf16 v[172:175], v[196:199], a[180:183], v[172:175]\n"
    "v_mfma_f32_16x16x32_bf16 v[172:175], v[200:203], a[184:187], v[172:175]\n"
    "v_mfma_f32_16x16x32_bf16 v[172:175], v[204:207], a[188:191], v[172:175]\n"
    "s_cmp_lg_u32 s35, 0\n"
    "s_cbranch_scc1 .L_dend_%=\n"
    "s_waitcnt lgkmcnt(4)\n"
    "v_mfma_f32_16x16x32_bf16 v[16:19], v[176:179], a[64:67], v[16:19]\n"
    "s_add_u32 m0, s77, 74752\n"
    "v_mfma_f32_16x16x32_bf16 v[16:19], v[180:183], a[68:71], v[16:19]\n"
    "s_waitcnt lgkmcnt(0)\n"
    "v_mfma_f32_16x16x32_bf16 v[16:19], v[184:187], a[72:75], v[16:19]\n"
    "buffer_load_dwordx4 v5, s[44:47], s18 offen offset:0 lds\n"
    "v_mfma_f32_16x16x32_bf16 v[16:19], v[188:191], a[76:79], v[16:19]\n"
    "ds_read_b64_tr_b16 v[192:193], v9 offset:0\n"
    "ds_read_b64_tr_b16 v[194:195], v9 offset:5120\n"
    "v_mfma_f32_16x16x32_bf16 v[20:23], v[176:179], a[80:83], v[20:23]\n"
    "ds_read_b64_tr_b16 v[196:197], v9 offset:10240\n"
    "v_mfma_f32_16x16x32_bf16 v[20:23], v[180:183], a[84:87], v[20:23]\n"
    "s_add_u32 m0, s77, 106496\n"
    "ds_read_b64_tr_b16 v[198:199], v9 offset:15360\n"
    "v_mfma_f32_16x16x32_bf16 v[20:23], v[184:187], a[88:91], v[20:23]\n"
    "ds_read_b64_tr_b16 v[200:201], v9 offset:20480\n"
    "v_mfma_f32_16x16x32_bf16 v[20:23], v[188:191], a[92:95], v[20:23]\n"
    "ds_read_b64_tr_b16 v[202:203], v9 offset:25600\n"
    "v_mfma_f32_16x16x32_bf16 v[24:27], v[176:179], a[96:99], v[24:27]\n"
    "buffer_load_dwordx4 v1, s[64:67], s19 offen offset:0 lds\n"
    "v_mfma_f32_16x16x32_bf16 v[24:27], v[180:183], a[100:103], v[24:27]\n"
    "ds_read_b64_tr_b16 v[204:205], v9 offset:30720\n"
    "ds_read_b64_tr_b16 v[206:207], v9 offset:35840\n"
    "v_mfma_f32_16x16x32_bf16 v[24:27], v[184:187], a[104:107], v[24:27]\n"
    "v_mfma_f32_16x16x32_bf16 v[24:27], v[188:191], a[108:111], v[24:27]\n"
    "s_add_u32 m0, s77, 107520\n"
    "v_mfma_f32_16x16x32_bf16 v[28:31], v[176:179], a[112:115], v[28:31]\n"
    "v_mfma_f32_16x16x32_bf16 v[28:31], v[180:183], a[116:119], v[28:31]\n"
    "v_mfma_f32_16x16x32_bf16 v[28:31], v[184:187], a[120:123], v[28:31]\n"
    "buffer_load_dwordx4 v2, s[64:67], s19 offen offset:0 lds\n"
    "v_mfma_f32_16x16x32_bf16 v[28:31], v[188:191], a[124:127], v[28:31]\n"
    "s_waitcnt lgkmcnt(4)\n"
    "v_mfma_f32_16x16x32_bf16 v[32:35], v[192:195], a[64:67], v[32:35]\n"
    "v_mfma_f32_16x16x32_bf16 v[32:35], v[196:199], a[68:71], v[32:35]\n"
    "s_waitcnt lgkmcnt(0)\n"
    "s_add_u32 m0, s77, 108544\n"
    "v_mfma_f32_16x16x32_bf16 v[32:35], v[200:203], a[72:75], v[32:35]\n"
    "ds_read_b64_tr_b16 v[176:177], v8 offset:64\n"
    "v_mfma_f32_16x16x32_bf16 v[32:35], v[204:207], a[76:79], v[32:35]\n"
    "ds_read_b64_tr_b16 v[178:179], v8 offset:5184\n"
    "v_mfma_f32_16x16x32_bf16 v[36:39], v[192:195], a[80:83], v[36:39]\n"
    "buffer_load_dwordx4 v3, s[64:67], s19 offen offset:0 lds\n"
    "v_mfma_f32_16x16x32_bf16 v[36:39], v[196:199], a[84:87], v[36:39]\n"
    "ds_read_b64_tr_b16 v[180:181], v8 offset:10304\n"
    "ds_read_b64_tr_b16 v[182:183], v8 offset:15424\n"
    "v_mfma_f32_16x16x32_bf16 v[36:39], v[200:203], a[88:91], v[36:39]\n"
    "ds_read_b64_tr_b16 v[184:185], v8 offset:20544\n"
    "v_mfma_f32_16x16x32_bf16 v[36:39], v[204:207], a[92:95], v[36:39]\n"
    "s_add_u32 m0, s77, 109568\n"
    "ds_read_b64_tr_b16 v[186:187], v8 offset:25664\n"
    "v_mfma_f32_16x16x32_bf16 v[40:43], v[192:195], a[96:99], v[40:43]\n"
    "ds_read_b64_tr_b16 v[188:189], v8 offset:30784\n"
    "v_mfma_f32_16x16x32_bf16 v[40:43], v[196:199], a[100:103], v[40:43]\n"
    "ds_read_b64_tr_b16 v[190:191], v8 offset:35904\n"
    "v_mfma_f32_16x16x32_bf16 v[40:43], v[200:203], a[104:107], v[40:43]\n"
    "buffer_load_dwordx4 v4, s[64:67], s19 offen offset:0 lds\n"
    "v_mfma_f32_16x16x32_bf16 v[40:43], v[204:207], a[108:111], v[40:43]\n"
    "v_mfma_f32_16x16x32_bf16 v[44:47], v[192:195], a[112:115], v[44:47]\n"
    "v_mfma_f32_16x16x32_bf16 v[44:47], v[196:199], a[116:119], v[44:47]\n"
    "s_add_u32 m0, s77, 110592\n"
    "v_mfma_f32_16x16x32_bf16 v[44:47], v[200:203], a[120:123], v[44:47]\n"
    "v_mfma_f32_16x16x32_bf16 v[44:47], v[204:207], a[124:127], v[44:47]\n"
    "s_waitcnt lgkmcnt(4)\n"
    "v_mfma_f32_16x16x32_bf16 v[48:51], v[176:179], a[64:67], v[48:51]\n"
    "buffer_load_dwordx4 v5, s[64:67], s19 offen offset:0 lds\n"
    "v_mfma_f32_16x16x32_bf16 v[48:51], v[180:183], a[68:71], v[48:51]\n"
    "s_waitcnt lgkmcnt(0)\n"
    "v_mfma_f32_16x16x32_bf16 v[48:51], v[184:187], a[72:75], v[48:51]\n"
    "s_add_u32 m0, s77, 111616\n"
    "ds_read_b64_tr_b16 v[192:193], v9 offset:64\n"
    "v_mfma_f32_16x16x32_bf16 v[48:51], v[188:191], a[76:79], v[48:51]\n"
    "ds_read_b64_tr_b16 v[194:195], v9 offset:5184\n"
    "v_mfma_f32_16x16x32_bf16 v[52:55], v[176:179], a[80:83], v[52:55]\n"
    "ds_read_b64_tr_b16 v[196:197], v9 offset:10304\n"
    "v_mfma_f32_16x16x32_bf16 v[52:55], v[180:183], a[84:87], v[52:55]\n"
    "buffer_load_dwordx4 v1, s[44:47], s19 offen offset:0 lds\n"
    "v_mfma_f32_16x16x32_bf16 v[52:55], v[184:187], a[88:91], v[52:55]\n"
    "ds_read_b64_tr_b16 v[198:199], v9 offset:15424\n"
    "ds_read_b64_tr_b16 v[200:201], v9 offset:20544\n"
    "v_mfma_f32_16x16x32_bf16 v[52:55], v[188:191], a[92:95], v[52:55]\n"
    "ds_read_b64_tr_b16 v[202:203], v9 offset:25664\n"
    "v_mfma_f32_16x16x32_bf16 v[56:59], v[176:179], a[96:99], v[56:59]\n"
    "s_add_u32 m0, s77, 112640\n"
    "ds_read_b64_tr_b16 v[204:205], v9 offset:30784\n"
    "v_mfma_f32_16x16x32_bf16 v[56:59], v[180:183], a[100:103], v[56:59]\n"
    "ds_read_b64_tr_b16 v[206:207], v9 offset:35904\n"
    "v_mfma_f32_16x16x32_bf16 v[56:59], v[184:187], a[104:107], v[56:59]\n"
    "v_mfma_f32_16x16x32_bf16 v[56:59], v[188:191], a[108:111], v[56:59]\n"
    "buffer_load_dwordx4 v2, s[44:47], s19 offen offset:0 lds\n"
    "v_mfma_f32_16x16x32_bf16 v[60:63], v[176:179], a[112:115], v[60:63]\n"
    "v_mfma_f32_16x16x32_bf16 v[60:63], v[180:183], a[116:119], v[60:63]\n"
    "v_mfma_f32_16x16x32_bf16 v[60:63], v[184:187], a[120:123], v[60:63]\n"
    "s_add_u32 m0, s77, 113664\n"
    "v_mfma_f32_16x16x32_bf16 v[60:63], v[188:191], a[124:127], v[60:63]\n"
    "s_waitcnt lgkmcnt(4)\n"
    "v_mfma_f32_16x16x32_bf16 v[64:67], v[192:195], a[64:67], v[64:67]\n"
    "v_mfma_f32_16x16x32_bf16 v[64:67], v[196:199], a[68:71], v[64:67]\n"
    "s_waitcnt lgkmcnt(0)\n"
    "v_mfma_f32_16x16x32_bf16 v[64:67], v[200:203], a[72:75], v[64:67]\n"
    "buffer_load_dwordx4 v3, s[44:47], s19 offen offset:0 lds\n"
    "v_mfma_f32_16x16x32_bf16 v[64:67], v[204:207], a[76:79], v[64:67]\n"
    "ds_read_b64_tr_b16 v[176:177], v8 offset:128\n"
    "ds_read_b64_tr_b16 v[178:179], v8 offset:5248\n"
    "v_mfma_f32_16x16x32_bf16 v[68:71], v[192:195], a[80:83], v[68:71]\n"
    "s_add_u32 m0, s77, 114688\n"
    "ds_read_b64_tr_b16 v[180:181], v8 offset:10368\n"
    "v_mfma_f32_16x16x32_bf16 v[68:71], v[196:199], a[84:87], v[68:71]\n"
    "ds_read_b64_tr_b16 v[182:183], v8 offset:15488\n"
    "v_mfma_f32_16x16x32_bf16 v[68:71], v[200:203], a[88:91], v[68:71]\n"
    "ds_read_b64_tr_b16 v[184:185], v8 offset:20608\n"
    "v_mfma_f32_16x16x32_bf16 v[68:71], v[204:207], a[92:95], v[68:71]\n"
    "buffer_load_dwordx4 v4, s[44:47], s19 offen offset:0 lds\n"
    "v_mfma_f32_16x16x32_bf16 v[72:75], v[192:195], a[96:99], v[72:75]\n"
    "ds_read_b64_tr_b16 v[186:187], v8 offset:25728\n"
    "ds_read_b64_tr_b16 v[188:189], v8 offset:30848\n"
    "v_mfma_f32_16x16x32_bf16 v[72:75], v[196:199], a[100:103], v[72:75]\n"
    "ds_read_b64_tr_b16 v[190:191], v8 offset:35968\n"
    "v_mfma_f32_16x16x32_bf16 v[72:75], v[200:203], a[104:107], v[72:75]\n"
    "s_add_u32 m0, s77, 115712\n"
    "v_mfma_f32_16x16x32_bf16 v[72:75], v[204:207], a[108:111], v[72:75]\n"
    "v_mfma_f32_16x16x32_bf16 v[76:79], v[192:195], a[112:115], v[76:79]\n"
    "v_mfma_f32_16x16x32_bf16 v[76:79], v[196:199], a[116:119], v[76:79]\n"
    "buffer_load_dwordx4 v5, s[44:47], s19 offen offset:0 lds\n"
    "v_mfma_f32_16x16x32_bf16 v[76:79], v[200:203], a[120:123], v[76:79]\n"
    "v_mfma_f32_16x16x32_bf16 v[76:79], v[204:207], a[124:127], v[76:79]\n"
    "s_waitcnt lgkmcnt(4)\n"
    "v_mfma_f32_16x16x32_bf16 v[80:83], v[176:179], a[64:67], v[80:83]\n"
    "s_add_u32 s89, s20, s81\n"
    "v_mfma_f32_16x16x32_bf16 v[80:83], v[180:183], a[68:71], v[80:83]\n"
    "s_waitcnt lgkmcnt(0)\n"
    "v_mfma_f32_16x16x32_bf16 v[80:83], v[184:187], a[72:75], v[80:83]\n"
    "ds_read_b64_tr_b16 v[192:193], v9 offset:128\n"
    "v_mfma_f32_16x16x32_bf16 v[80:83], v[188:191], a[76:79], v[80:83]\n"
    "s_add_u32 s89, s89, 28672\n"
    "ds_read_b64_tr_b16 v[194:195], v9 offset:5248\n"
    "v_mfma_f32_16x16x32_bf16 v[84:87], v[176:179], a[80:83], v[84:87]\n"
    "ds_read_b64_tr_b16 v[196:197], v9 offset:10368\n"
    "v_mfma_f32_16x16x32_bf16 v[84:87], v[180:183], a[84:87], v[84:87]\n"
    "buffer_load_dwordx4 a[128:131], v0, s[40:43], s89 offen offset:0 sc1\n"
    "v_mfma_f32_16x16x32_bf16 v[84:87], v[184:187], a[88:91], v[84:87]\n"
    "ds_read_b64_tr_b16 v[198:199], v9 offset:15488\n"
    "ds_read_b64_tr_b16 v[200:201], v9 offset:20608\n"
    "v_mfma_f32_16x16x32_bf16 v[84:87], v[188:191], a[92:95], v[84:87]\n"
    "ds_read_b64_tr_b16 v[202:203], v9 offset:25728\n"
    "v_mfma_f32_16x16x32_bf16 v[88:91], v[176:179], a[96:99], v[88:91]\n"
    "buffer_load_dwordx4 a[132:135], v0, s[40:43], s89 offen offset:1024 sc1\n"
    "v_mfma_f32_16x16x32_bf16 v[88:91], v[180:183], a[100:103], v[88:91]\n"
    "ds_read_b64_tr_b16 v[204:205], v9 offset:30848\n"
    "ds_read_b64_tr_b16 v[206:207], v9 offset:35968\n"
    "v_mfma_f32_16x16x32_bf16 v[88:91], v[184:187], a[104:107], v[88:91]\n"
    "v_mfma_f32_16x16x32_bf16 v[88:91], v[188:191], a[108:111], v[88:91]\n"
    "buffer_load_dwordx4 a[136:139], v0, s[40:43], s89 offen offset:2048 sc1\n"
    "v_mfma_f32_16x16x32_bf16 v[92:95], v[176:179], a[112:115], v[92:95]\n"
    "v_mfma_f32_16x16x32_bf16 v[92:95], v[180:183], a[116:119], v[92:95]\n"
    "v_mfma_f32_16x16x32_bf16 v[92:95], v[184:187], a[120:123], v[92:95]\n"
    "buffer_load_dwordx4 a[140:143], v0, s[40:43], s89 offen offset:3072 sc1\n"
    "v_mfma_f32_16x16x32_bf16 v[92:95], v[188:191], a[124:127], v[92:95]\n"
    "s_waitcnt lgkmcnt(4)\n"
    "v_mfma_f32_16x16x32_bf16 v[96:99], v[192:195], a[64:67], v[96:99]\n"
    "v_mfma_f32_16x16x32_bf16 v[96:99], v[196:199], a[68:71], v[96:99]\n"
    "s_waitcnt lgkmcnt(0)\n"
    "s_add_u32 s89, s20, s3\n"
    "v_mfma_f32_16x16x32_bf16 v[96:99], v[200:203], a[72:75], v[96:99]\n"
    "ds_read_b64_tr_b16 v[176:177], v8 offset:192\n"
    "v_mfma_f32_16x16x32_bf16 v[96:99], v[204:207], a[76:79], v[96:99]\n"
    "ds_read_b64_tr_b16 v[178:179], v8 offset:5312\n"
    "v_mfma_f32_16x16x32_bf16 v[100:103], v[192:195], a[80:83], v[100:103]\n"
    "s_add_u32 s89, s89, 28672\n"
    "ds_read_b64_tr_b16 v[180:181], v8 offset:10432\n"
    "v_mfma_f32_16x16x32_bf16 v[100:103], v[196:199], a[84:87], v[100:103]\n"
    "ds_read_b64_tr_b16 v[182:183], v8 offset:15552\n"
    "v_mfma_f32_16x16x32_bf16 v[100:103], v[200:203], a[88:91], v[100:103]\n"
    "ds_read_b64_tr_b16 v[184:185], v8 offset:20672\n"
    "v_mfma_f32_16x16x32_bf16 v[100:103], v[204:207], a[92:95], v[100:103]\n"
    "buffer_load_dwordx4 a[144:147], v0, s[40:43], s89 offen offset:0 sc1\n"
    "v_mfma_f32_16x16x32_bf16 v[104:107], v[192:195], a[96:99], v[104:107]\n"
    "ds_read_b64_tr_b16 v[186:187], v8 offset:25792\n"
    "ds_read_b64_tr_b16 v[188:189], v8 offset:30912\n"
    "v_mfma_f32_16x16x32_bf16 v[104:107], v[196:199], a[100:103], v[104:107]\n"
    "ds_read_b64_tr_b16 v[190:191], v8 offset:36032\n"
    "v_mfma_f32_16x16x32_bf16 v[104:107], v[200:203], a[104:107], v[104:107]\n"
    "buffer_load_dwordx4 a[148:151], v0, s[40:43], s89 offen offset:1024 sc1\n"
    "v_mfma_f32_16x16x32_bf16 v[104:107], v[204:207], a[108:111], v[104:107]\n"
    "v_mfma_f32_16x16x32_bf16 v[108:111], v[192:195], a[112:115], v[108:111]\n"
    "v_mfma_f32_16x16x32_bf16 v[108:111], v[196:199], a[116:119], v[108:111]\n"
    "buffer_load_dwordx4 a[152:155], v0, s[40:43], s89 offen offset:2048 sc1\n"
    "v_mfma_f32_16x16x32_bf16 v[108:111], v[200:203], a[120:123], v[108:111]\n"
    "v_mfma_f32_16x16x32_bf16 v[108:111], v[204:207], a[124:127], v[108:111]\n"
    "s_waitcnt lgkmcnt(4)\n"
    "v_mfma_f32_16x16x32_bf16 v[112:115], v[176:179], a[64:67], v[112:115]\n"
    "buffer_load_dwordx4 a[156:159], v0, s[40:43], s89 offen offset:3072 sc1\n"
    "v_mfma_f32_16x16x32_bf16 v[112:115], v[180:183], a[68:71], v[112:115]\n"
    "s_waitcnt lgkmcnt(0)\n"
    "v_mfma_f32_16x16x32_bf16 v[112:115], v[184:187], a[72:75], v[112:115]\n"
    "ds_read_b64_tr_b16 v[192:193], v9 offset:192\n"
    "v_mfma_f32_16x16x32_bf16 v[112:115], v[188:191], a[76:79], v[112:115]\n"
    "s_add_u32 s89, s20, s39\n"
    "ds_read_b64_tr_b16 v[194:195], v9 offset:5312\n"
    "v_mfma_f32_16x16x32_bf16 v[116:119], v[176:179], a[80:83], v[116:119]\n"
    "ds_read_b64_tr_b16 v[196:197], v9 offset:10432\n"
    "v_mfma_f32_16x16x32_bf16 v[116:119], v[180:183], a[84:87], v[116:119]\n"
    "s_add_u32 s89, s89, 28672\n"
    "ds_read_b64_tr_b16 v[198:199], v9 offset:15552\n"
    "v_mfma_f32_16x16x32_bf16 v[116:119], v[184:187], a[88:91], v[116:119]\n"
    "ds_read_b64_tr_b16 v[200:201], v9 offset:20672\n"
    "v_mfma_f32_16x16x32_bf16 v[116:119], v[188:191], a[92:95], v[116:119]\n"
    "ds_read_b64_tr_b16 v[202:203], v9 offset:25792\n"
    "v_mfma_f32_16x16x32_bf16 v[120:123], v[176:179], a[96:99], v[120:123]\n"
    "buffer_load_dwordx4 a[160:163], v0, s[40:43], s89 offen offset:0 sc1\n"
    "v_mfma_f32_16x16x32_bf16 v[120:123], v[180:183], a[100:103], v[120:123]\n"
    "ds_read_b64_tr_b16 v[204:205], v9 offset:30912\n"
    "ds_read_b64_tr_b16 v[206:207], v9 offset:36032\n"
    "v_mfma_f32_16x16x32_bf16 v[120:123], v[184:187], a[104:107], v[120:123]\n"
    "v_mfma_f32_16x16x32_bf16 v[120:123], v[188:191], a[108:111], v[120:123]\n"
    "buffer_load_dwordx4 a[164:167], v0, s[40:43], s89 offen offset:1024 sc1\n"
    "v_mfma_f32_16x16x32_bf16 v[124:127], v[176:179], a[112:115], v[124:127]\n"
    "v_mfma_f32_16x16x32_bf16 v[124:127], v[180:183], a[116:119], v[124:127]\n"
    "v_mfma_f32_16x16x32_bf16 v[124:127], v[184:187], a[120:123], v[124:127]\n"
    "buffer_load_dwordx4 a[168:171], v0, s[40:43], s89 offen offset:2048 sc1\n"
    "v_mfma_f32_16x16x32_bf16 v[124:127], v[188:191], a[124:127], v[124:127]\n"
    "s_waitcnt lgkmcnt(4)\n"
    "v_mfma_f32_16x16x32_bf16 v[128:131], v[192:195], a[64:67], v[128:131]\n"
    "v_mfma_f32_16x16x32_bf16 v[128:131], v[196:199], a[68:71], v[128:131]\n"
    "s_waitcnt lgkmcnt(0)\n"
    "v_mfma_f32_16x16x32_bf16 v[128:131], v[200:203], a[72:75], v[128:131]\n"
    "buffer_load_dwordx4 a[172:175], v0, s[40:43], s89 offen offset:3072 sc1\n"
    "v_mfma_f32_16x16x32_bf16 v[128:131], v[204:207], a[76:79], v[128:131]\n"
    "ds_read_b64_tr_b16 v[176:177], v8 offset:256\n"
    "ds_read_b64_tr_b16 v[178:179], v8 offset:5376\n"
    "v_mfma_f32_16x16x32_bf16 v[132:135], v[192:195], a[80:83], v[132:135]\n"
    "s_add_u32 s89, s20, s99\n"
    "ds_read_b64_tr_b16 v[180:181], v8 offset:10496\n"
    "v_mfma_f32_16x16x32_bf16 v[132:135], v[196:199], a[84:87], v[132:135]\n"
    "ds_read_b64_tr_b16 v[182:183], v8 offset:15616\n"
    "v_mfma_f32_16x16x32_bf16 v[132:135], v[200:203], a[88:91], v[132:135]\n"
    "ds_read_b64_tr_b16 v[184:185], v8 offset:20736\n"
    "v_mfma_f32_16x16x32_bf16 v[132:135], v[204:207], a[92:95], v[132:135]\n"
    "s_add_u32 s89, s89, 28672\n"
    "ds_read_b64_tr_b16 v[186:187], v8 offset:25856\n"
    "v_mfma_f32_16x16x32_bf16 v[136:139], v[192:195], a[96:99], v[136:139]\n"
    "ds_read_b64_tr_b16 v[188:189], v8 offset:30976\n"
    "v_mfma_f32_16x16x32_bf16 v[136:139], v[196:199], a[100:103], v[136:139]\n"
    "ds_read_b64_tr_b16 v[190:191], v8 offset:36096\n"
    "v_mfma_f32_16x16x32_bf16 v[136:139], v[200:203], a[104:107], v[136:139]\n"
    "buffer_load_dwordx4 a[176:179], v0, s[40:43], s89 offen offset:0 sc1\n"
    "v_mfma_f32_16x16x32_bf16 v[136:139], v[204:207], a[108:111], v[136:139]\n"
    "v_mfma_f32_16x16x32_bf16 v[140:143], v[192:195], a[112:115], v[140:143]\n"
    "v_mfma_f32_16x16x32_bf16 v[140:143], v[196:199], a[116:119], v[140:143]\n"
    "buffer_load_dwordx4 a[180:183], v0, s[40:43], s89 offen offset:1024 sc1\n"
    "v_mfma_f32_16x16x32_bf16 v[140:143], v[200:203], a[120:123], v[140:143]\n"
    "v_mfma_f32_16x16x32_bf16 v[140:143], v[204:207], a[124:127], v[140:143]\n"
    "s_waitcnt lgkmcnt(4)\n"
    "v_mfma_f32_16x16x32_bf16 v[144:147], v[176:179], a[64:67], v[144:147]\n"
    "buffer_load_dwordx4 a[184:187], v0, s[40:43], s89 offen offset:2048 sc1\n"
    "v_mfma_f32_16x16x32_bf16 v[144:147], v[180:183], a[68:71], v[144:147]\n"
    "s_waitcnt lgkmcnt(0)\n"
    "v_mfma_f32_16x16x32_bf16 v[144:147], v[184:187], a[72:75], v[144:147]\n"
    "ds_read_b64_tr_b16 v[192:193], v9 offset:256\n"
    "v_mfma_f32_16x16x32_bf16 v[144:147], v[188:191], a[76:79], v[144:147]\n"
    "buffer_load_dwordx4 a[188:191], v0, s[40:43], s89 offen offset:3072 sc1\n"
    "v_mfma_f32_16x16x32_bf16 v[148:151], v[176:179], a[80:83], v[148:151]\n"
    "ds_read_b64_tr_b16 v[194:195], v9 offset:5376\n"
    "ds_read_b64_tr_b16 v[196:197], v9 offset:10496\n"
    "v_mfma_f32_16x16x32_bf16 v[148:151], v[180:183], a[84:87], v[148:151]\n"
    "ds_read_b64_tr_b16 v[198:199], v9 offset:15616\n"
    "v_mfma_f32_16x16x32_bf16 v[148:151], v[184:187], a[88:91], v[148:151]\n"
    "ds_read_b64_tr_b16 v[200:201], v9 offset:20736\n"
    "v_mfma_f32_16x16x32_bf16 v[148:151], v[188:191], a[92:95], v[148:151]\n"
    "ds_read_b64_tr_b16 v[202:203], v9 offset:25856\n"
    "v_mfma_f32_16x16x32_bf16 v[152:155], v[176:179], a[96:99], v[152:155]\n"
    "ds_read_b64_tr_b16 v[204:205], v9 offset:30976\n"
    "v_mfma_f32_16x16x32_bf16 v[152:155], v[180:183], a[100:103], v[152:155]\n"
    "ds_read_b64_tr_b16 v[206:207], v9 offset:36096\n"
    "v_mfma_f32_16x16x32_bf16 v[152:155], v[184:187], a[104:107], v[152:155]\n"
    "v_mfma_f32_16x16x32_bf16 v[152:155], v[188:191], a[108:111], v[152:155]\n"
    "v_mfma_f32_16x16x32_bf16 v[156:159], v[176:179], a[112:115], v[156:159]\n"
    "v_mfma_f32_16x16x32_bf16 v[156:159], v[180:183], a[116:119], v[156:159]\n"
    "v_mfma_f32_16x16x32_bf16 v[156:159], v[184:187], a[120:123], v[156:159]\n"
    "v_mfma_f32_16x16x32_bf16 v[156:159], v[188:191], a[124:127], v[156:159]\n"
    "s_waitcnt vmcnt(16) lgkmcnt(0)\n"
    "s_barrier\n"
    "v_mfma_f32_16x16x32_bf16 v[160:163], v[192:195], a[64:67], v[160:163]\n"
    "ds_read_b64_tr_b16 v[176:177], v11 offset:0\n"
    "v_mfma_f32_16x16x32_bf16 v[160:163], v[196:199], a[68:71], v[160:163]\n"
    "ds_read_b64_tr_b16 v[178:179], v11 offset:5120\n"
    "v_mfma_f32_16x16x32_bf16 v[160:163], v[200:203], a[72:75], v[160:163]\n"
    "ds_read_b64_tr_b16 v[180:181], v11 offset:10240\n"
    "v_mfma_f32_16x16x32_bf16 v[160:163], v[204:207], a[76:79], v[160:163]\n"
    "ds_read_b64_tr_b16 v[182:183], v11 offset:15360\n"
    "v_mfma_f32_16x16x32_bf16 v[164:167], v[192:195], a[80:83], v[164:167]\n"
    "ds_read_b64_tr_b16 v[184:185], v11 offset:20480\n"
    "v_mfma_f32_16x16x32_bf16 v[164:167], v[196:199], a[84:87], v[164:167]\n"
    "ds_read_b64_tr_b16 v[186:187], v11 offset:25600\n"
    "v_mfma_f32_16x16x32_bf16 v[164:167], v[200:203], a[88:91], v[164:167]\n"
    "ds_read_b64_tr_b16 v[188:189], v11 offset:30720\n"
    "v_mfma_f32_16x16x32_bf16 v[164:167], v[204:207], a[92:95], v[164:167]\n"
    "ds_read_b64_tr_b16 v[190:191], v11 offset:35840\n"
    "v_mfma_f32_16x16x32_bf16 v[168:171], v[192:195], a[96:99], v[168:171]\n"
    "v_mfma_f32_16x16x32_bf16 v[168:171], v[196:199], a[100:103], v[168:171]\n"
    "v_mfma_f32_16x16x32_bf16 v[168:171], v[200:203], a[104:107], v[168:171]\n"
    "v_mfma_f32_16x16x32_bf16 v[168:171], v[204:207], a[108:111], v[168:171]\n"
    "v_mfma_f32_16x16x32_bf16 v[172:175], v[192:195], a[112:115], v[172:175]\n"
    "v_mfma_f32_16x16x32_bf16 v[172:175], v[196:199], a[116:119], v[172:175]\n"
    "v_mfma_f32_16x16x32_bf16 v[172:175], v[200:203], a[120:123], v[172:175]\n"
    "v_mfma_f32_16x16x32_bf16 v[172:175], v[204:207], a[124:127], v[172:175]\n"
    "ds_read_b64_tr_b16 v[192:193], v13 offset:0\n"
    "ds_read_b64_tr_b16 v[194:195], v13 offset:5120\n"
    "ds_read_b64_tr_b16 v[196:197], v13 offset:10240\n"
    "ds_read_b64_tr_b16 v[198:199], v13 offset:15360\n"
    "ds_read_b64_tr_b16 v[200:201], v13 offset:20480\n"
    "ds_read_b64_tr_b16 v[202:203], v13 offset:25600\n"
    "ds_read_b64_tr_b16 v[204:205], v13 offset:30720\n"
    "ds_read_b64_tr_b16 v[206:207], v13 offset:35840\n"
    "s_waitcnt lgkmcnt(8)\n"
    "v_mfma_f32_16x16x32_bf16 v[16:19], v[176:179], a[192:195], v[16:19]\n"
    "v_mfma_f32_16x16x32_bf16 v[16:19], v[180:183], a[196:199], v[16:19]\n"
    "v_mfma_f32_16x16x32_bf16 v[16:19], v[184:187], a[200:203], v[16:19]\n"
    "v_mfma_f32_16x16x32_bf16 v[16:19], v[188:191], a[204:207], v[16:19]\n"
    "s_waitcnt vmcnt(0) lgkmcnt(0)\n"
    "v_mfma_f32_16x16x32_bf16 v[16:19], v[192:195], a[128:131], v[16:19]\n"
    "s_mov_b32 s86, 0x3c319c2f\n"
    "ds_read_b64_tr_b16 v[208:209], v12 offset:0\n"
    "s_mov_b32 s87, 0xc3051595\n"
    "v_mfma_f32_16x16x32_bf16 v[16:19], v[196:199], a[132:135], v[16:19]\n"
    "ds_read_b64_tr_b16 v[210:211], v12 offset:5120\n"
    "v_or_b32 v246, 384, v15\n"
    "v_mfma_f32_16x16x32_bf16 v[16:19], v[200:203], a[136:139], v[16:19]\n"
    "v_mfma_f32_16x16x32_bf16 v[16:19], v[204:207], a[140:143], v[16:19]\n"
    "ds_read_b64_tr_b16 v[212:213], v12 offset:10240\n"
    "v_mfma_f32_16x16x32_bf16 v[20:23], v[176:179], a[208:211], v[20:23]\n"
    "ds_read_b64_tr_b16 v[214:215], v12 offset:15360\n"
    "v_mfma_f32_16x16x32_bf16 v[20:23], v[180:183], a[212:215], v[20:23]\n"
    "v_mfma_f32_16x16x32_bf16 v[20:23], v[184:187], a[216:219], v[20:23]\n"
    "ds_read_b64_tr_b16 v[216:217], v12 offset:20480\n"
    "v_mfma_f32_16x16x32_bf16 v[20:23], v[188:191], a[220:223], v[20:23]\n"
    "ds_read_b64_tr_b16 v[218:219], v12 offset:25600\n"
    "v_mfma_f32_16x16x32_bf16 v[20:23], v[192:195], a[144:147], v[20:23]\n"
    "v_mfma_f32_16x16x32_bf16 v[20:23], v[196:199], a[148:151], v[20:23]\n"
    "ds_read_b64_tr_b16 v[220:221], v12 offset:30720\n"
    "v_mfma_f32_16x16x32_bf16 v[20:23], v[200:203], a[152:155], v[20:23]\n"
    "ds_read_b64_tr_b16 v[222:223], v12 offset:35840\n"
    "v_mfma_f32_16x16x32_bf16 v[20:23], v[204:207], a[156:159], v[20:23]\n"
    "v_mfma_f32_16x16x32_bf16 v[24:27], v[176:179], a[224:227], v[24:27]\n"
    "ds_read_b64_tr_b16 v[224:225], v14 offset:0\n"
    "v_mfma_f32_16x16x32_bf16 v[24:27], v[180:183], a[228:231], v[24:27]\n"
    "ds_read_b64_tr_b16 v[226:227], v14 offset:5120\n"
    "v_mfma_f32_16x16x32_bf16 v[24:27], v[184:187], a[232:235], v[24:27]\n"
    "v_mfma_f32_16x16x32_bf16 v[24:27], v[188:191], a[236:239], v[24:27]\n"
    "ds_read_b64_tr_b16 v[228:229], v14 offset:10240\n"
    "v_mfma_f32_16x16x32_bf16 v[24:27], v[192:195], a[160:163], v[24:27]\n"
    "buffer_load_dwordx4 a[112:115], v10, s[68:71], s36 offen offset:0\n"
    "v_mfma_f32_16x16x32_bf16 v[24:27], v[196:199], a[164:167], v[24:27]\n"
    "ds_read_b64_tr_b16 v[230:231], v14 offset:15360\n"
    "v_mfma_f32_16x16x32_bf16 v[24:27], v[200:203], a[168:171], v[24:27]\n"
    "ds_read_b64_tr_b16 v[232:233], v14 offset:20480\n"
    "v_mfma_f32_16x16x32_bf16 v[24:27], v[204:207], a[172:175], v[24:27]\n"
    "ds_read_b64_tr_b16 v[234:235], v14 offset:25600\n"
    "v_mfma_f32_16x16x32_bf16 v[28:31], v[176:179], a[240:243], v[28:31]\n"
    "v_mfma_f32_16x16x32_bf16 v[28:31], v[180:183], a[244:247], v[28:31]\n"
    "ds_read_b64_tr_b16 v[236:237], v14 offset:30720\n"
    "v_mfma_f32_16x16x32_bf16 v[28:31], v[184:187], a[248:251], v[28:31]\n"
    "buffer_load_dwordx4 a[116:119], v10, s[68:71], s37 offen offset:0\n"
    "v_mfma_f32_16x16x32_bf16 v[28:31], v[188:191], a[252:255], v[28:31]\n"
    "ds_read_b64_tr_b16 v[238:239], v14 offset:35840\n"
    "v_mfma_f32_16x16x32_bf16 v[28:31], v[192:195], a[176:179], v[28:31]\n"
    "v_mfma_f32_16x16x32_bf16 v[28:31], v[196:199], a[180:183], v[28:31]\n"
    "v_mfma_f32_16x16x32_bf16 v[28:31], v[200:203], a[184:187], v[28:31]\n"
    "v_mfma_f32_16x16x32_bf16 v[28:31], v[204:207], a[188:191], v[28:31]\n"
    "s_waitcnt lgkmcnt(8)\n"
    "v_mfma_f32_16x16x32_bf16 v[32:35], v[208:211], a[192:195], v[32:35]\n"
    "v_mfma_f32_16x16x32_bf16 v[32:35], v[212:215], a[196:199], v[32:35]\n"
    "buffer_load_dwordx4 a[120:123], v10, s[68:71], s38 offen offset:0\n"
    "v_mfma_f32_16x16x32_bf16 v[32:35], v[216:219], a[200:203], v[32:35]\n"
    "v_mfma_f32_16x16x32_bf16 v[32:35], v[220:223], a[204:207], v[32:35]\n"
    "s_waitcnt lgkmcnt(0)\n"
    "v_mfma_f32_16x16x32_bf16 v[32:35], v[224:227], a[128:131], v[32:35]\n"
    "ds_read_b64_tr_b16 v[176:177], v11 offset:64\n"
    "v_mfma_f32_16x16x32_bf16 v[32:35], v[228:231], a[132:135], v[32:35]\n"
    "ds_read_b64_tr_b16 v[178:179], v11 offset:5184\n"
    "v_mfma_f32_16x16x32_bf16 v[32:35], v[232:235], a[136:139], v[32:35]\n"
    "v_mfma_f32_16x16x32_bf16 v[32:35], v[236:239], a[140:143], v[32:35]\n"
    "ds_read_b64_tr_b16 v[180:181], v11 offset:10304\n"
    "v_mfma_f32_16x16x32_bf16 v[36:39], v[208:211], a[208:211], v[36:39]\n"
    "ds_read_b64_tr_b16 v[182:183], v11 offset:15424\n"
    "v_mfma_f32_16x16x32_bf16 v[36:39], v[212:215], a[212:215], v[36:39]\n"
    "v_mfma_f32_16x16x32_bf16 v[36:39], v[216:219], a[216:219], v[36:39]\n"
    "ds_read_b64_tr_b16 v[184:185], v11 offset:20544\n"
    "v_cvt_pk_bf16_f32 v16, v16, v17\n"
    "v_mfma_f32_16x16x32_bf16 v[36:39], v[220:223], a[220:223], v[36:39]\n"
    "ds_read_b64_tr_b16 v[186:187], v11 offset:25664\n"
    "v_cvt_pk_bf16_f32 v17, v18, v19\n"
    "v_mfma_f32_16x16x32_bf16 v[36:39], v[224:227], a[144:147], v[36:39]\n"
    "v_cvt_pk_bf16_f32 v18, v32, v33\n"
    "v_cvt_pk_bf16_f32 v19, v34, v35\n"
    "v_mfma_f32_16x16x32_bf16 v[36:39], v[228:231], a[148:151], v[36:39]\n"
    "s_add_u32 s90, s79, 0\n"
    "ds_read_b64_tr_b16 v[188:189], v11 offset:30784\n"
    "ds_write_b128 v252, v[16:19] offset:0\n"
    "v_mfma_f32_16x16x32_bf16 v[36:39], v[232:235], a[152:155], v[36:39]\n"
    "buffer_store_dwordx4 v[16:19], v0, s[40:43], s90 offen offset:0 sc1\n"
    "v_mfma_f32_16x16x32_bf16 v[36:39], v[236:239], a[156:159], v[36:39]\n"
    "ds_read_b64_tr_b16 v[190:191], v11 offset:35904\n"
    "v_mfma_f32_16x16x32_bf16 v[40:43], v[208:211], a[224:227], v[40:43]\n"
    "ds_read_b64_tr_b16 v[192:193], v13 offset:64\n"
    "v_mfma_f32_16x16x32_bf16 v[40:43], v[212:215], a[228:231], v[40:43]\n"
    "ds_read_b64_tr_b16 v[194:195], v13 offset:5184\n"
    "v_mfma_f32_16x16x32_bf16 v[40:43], v[216:219], a[232:235], v[40:43]\n"
    "v_cvt_pk_bf16_f32 v20, v20, v21\n"
    "v_cvt_pk_bf16_f32 v21, v22, v23\n"
    "v_mfma_f32_16x16x32_bf16 v[40:43], v[220:223], a[236:239], v[40:43]\n"
    "ds_read_b64_tr_b16 v[196:197], v13 offset:10304\n"
    "v_cvt_pk_bf16_f32 v22, v36, v37\n"
    "v_mfma_f32_16x16x32_bf16 v[40:43], v[224:227], a[160:163], v[40:43]\n"
    "ds_read_b64_tr_b16 v[198:199], v13 offset:15424\n"
    "v_cvt_pk_bf16_f32 v23, v38, v39\n"
    "v_mfma_f32_16x16x32_bf16 v[40:43], v[228:231], a[164:167], v[40:43]\n"
    "s_add_u32 s90, s80, 0\n"
    "ds_write_b128 v252, v[20:23] offset:5120\n"
    "v_mfma_f32_16x16x32_bf16 v[40:43], v[232:235], a[168:171], v[40:43]\n"
    "buffer_store_dwordx4 v[20:23], v0, s[40:43], s90 offen offset:0 sc1\n"
    "v_mfma_f32_16x16x32_bf16 v[40:43], v[236:239], a[172:175], v[40:43]\n"
    "ds_read_b64_tr_b16 v[200:201], v13 offset:20544\n"
    "ds_read_b64_tr_b16 v[202:203], v13 offset:25664\n"
    "v_mfma_f32_16x16x32_bf16 v[44:47], v[208:211], a[240:243], v[44:47]\n"
    "v_mfma_f32_16x16x32_bf16 v[44:47], v[212:215], a[244:247], v[44:47]\n"
    "ds_read_b64_tr_b16 v[204:205], v13 offset:30784\n"
    "v_mfma_f32_16x16x32_bf16 v[44:47], v[216:219], a[248:251], v[44:47]\n"
    "ds_read_b64_tr_b16 v[206:207], v13 offset:35904\n"
    "v_cvt_pk_bf16_f32 v24, v24, v25\n"
    "v_mfma_f32_16x16x32_bf16 v[44:47], v[220:223], a[252:255], v[44:47]\n"
    "v_cvt_pk_bf16_f32 v25, v26, v27\n"
    "v_cvt_pk_bf16_f32 v26, v40, v41\n"
    "v_mfma_f32_16x16x32_bf16 v[44:47], v[224:227], a[176:179], v[44:47]\n"
    "v_cvt_pk_bf16_f32 v27, v42, v43\n"
    "s_add_u32 s90, s82, 0\n"
    "v_mfma_f32_16x16x32_bf16 v[44:47], v[228:231], a[180:183], v[44:47]\n"
    "ds_write_b128 v253, v[24:27] offset:0\n"
    "v_mfma_f32_16x16x32_bf16 v[44:47], v[232:235], a[184:187], v[44:47]\n"
    "buffer_store_dwordx4 v[24:27], v0, s[40:43], s90 offen offset:0 sc1\n"
    "v_mfma_f32_16x16x32_bf16 v[44:47], v[236:239], a[188:191], v[44:47]\n"
    "s_waitcnt lgkmcnt(10)\n"
    "v_mfma_f32_16x16x32_bf16 v[48:51], v[176:179], a[192:195], v[48:51]\n"
    "v_mfma_f32_16x16x32_bf16 v[48:51], v[180:183], a[196:199], v[48:51]\n"
    "v_mfma_f32_16x16x32_bf16 v[48:51], v[184:187], a[200:203], v[48:51]\n"
    "v_cvt_pk_bf16_f32 v28, v28, v29\n"
    "v_cvt_pk_bf16_f32 v29, v30, v31\n"
    "v_mfma_f32_16x16x32_bf16 v[48:51], v[188:191], a[204:207], v[48:51]\n"
    "s_waitcnt lgkmcnt(1)\n"
    "v_cvt_pk_bf16_f32 v30, v44, v45\n"
    "v_cvt_pk_bf16_f32 v31, v46, v47\n"
    "v_mfma_f32_16x16x32_bf16 v[48:51], v[192:195], a[128:131], v[48:51]\n"
    "s_add_u32 s90, s83, 0\n"
    "ds_read_b64_tr_b16 v[208:209], v12 offset:64\n"
    "ds_write_b128 v253, v[28:31] offset:5120\n"
    "v_mfma_f32_16x16x32_bf16 v[48:51], v[196:199], a[132:135], v[48:51]\n"
    "buffer_store_dwordx4 v[28:31], v0, s[40:43], s90 offen offset:0 sc1\n"
    "v_mfma_f32_16x16x32_bf16 v[48:51], v[200:203], a[136:139], v[48:51]\n"
    "ds_read_b64_tr_b16 v[210:211], v12 offset:5184\n"
    "v_mfma_f32_16x16x32_bf16 v[48:51], v[204:207], a[140:143], v[48:51]\n"
    "ds_read_b64_tr_b16 v[212:213], v12 offset:10304\n"
    "v_mfma_f32_16x16x32_bf16 v[52:55], v[176:179], a[208:211], v[52:55]\n"
    "ds_read_b64_tr_b16 v[214:215], v12 offset:15424\n"
    "v_mfma_f32_16x16x32_bf16 v[52:55], v[180:183], a[212:215], v[52:55]\n"
    "v_mfma_f32_16x16x32_bf16 v[52:55], v[184:187], a[216:219], v[52:55]\n"
    "ds_read_b64_tr_b16 v[216:217], v12 offset:20544\n"
    "v_mfma_f32_16x16x32_bf16 v[52:55], v[188:191], a[220:223], v[52:55]\n"
    "ds_read_b64_tr_b16 v[218:219], v12 offset:25664\n"
    "v_mfma_f32_16x16x32_bf16 v[52:55], v[192:195], a[144:147], v[52:55]\n"
    "v_mfma_f32_16x16x32_bf16 v[52:55], v[196:199], a[148:151], v[52:55]\n"
    "ds_read_b64_tr_b16 v[220:221], v12 offset:30784\n"
    "v_mfma_f32_16x16x32_bf16 v[52:55], v[200:203], a[152:155], v[52:55]\n"
    "ds_read_b64_tr_b16 v[222:223], v12 offset:35904\n"
    "v_mfma_f32_16x16x32_bf16 v[52:55], v[204:207], a[156:159], v[52:55]\n"
    "v_mfma_f32_16x16x32_bf16 v[56:59], v[176:179], a[224:227], v[56:59]\n"
    "ds_read_b64_tr_b16 v[224:225], v14 offset:64\n"
    "v_mfma_f32_16x16x32_bf16 v[56:59], v[180:183], a[228:231], v[56:59]\n"
    "ds_read_b64_tr_b16 v[226:227], v14 offset:5184\n"
    "v_mfma_f32_16x16x32_bf16 v[56:59], v[184:187], a[232:235], v[56:59]\n"
    "v_mfma_f32_16x16x32_bf16 v[56:59], v[188:191], a[236:239], v[56:59]\n"
    "ds_read_b64_tr_b16 v[228:229], v14 offset:10304\n"
    "v_mfma_f32_16x16x32_bf16 v[56:59], v[192:195], a[160:163], v[56:59]\n"
    "ds_read_b64_tr_b16 v[230:231], v14 offset:15424\n"
    "v_mfma_f32_16x16x32_bf16 v[56:59], v[196:199], a[164:167], v[56:59]\n"
    "v_mfma_f32_16x16x32_bf16 v[56:59], v[200:203], a[168:171], v[56:59]\n"
    "ds_read_b64_tr_b16 v[232:233], v14 offset:20544\n"
    "v_mfma_f32_16x16x32_bf16 v[56:59], v[204:207], a[172:175], v[56:59]\n"
    "ds_read_b64_tr_b16 v[234:235], v14 offset:25664\n"
    "v_mfma_f32_16x16x32_bf16 v[60:63], v[176:179], a[240:243], v[60:63]\n"
    "v_mfma_f32_16x16x32_bf16 v[60:63], v[180:183], a[244:247], v[60:63]\n"
    "ds_read_b64_tr_b16 v[236:237], v14 offset:30784\n"
    "v_mfma_f32_16x16x32_bf16 v[60:63], v[184:187], a[248:251], v[60:63]\n"
    "ds_read_b64_tr_b16 v[238:239], v14 offset:35904\n"
    "v_mfma_f32_16x16x32_bf16 v[60:63], v[188:191], a[252:255], v[60:63]\n"
    "v_mfma_f32_16x16x32_bf16 v[60:63], v[192:195], a[176:179], v[60:63]\n"
    "v_mfma_f32_16x16x32_bf16 v[60:63], v[196:199], a[180:183], v[60:63]\n"
    "v_mfma_f32_16x16x32_bf16 v[60:63], v[200:203], a[184:187], v[60:63]\n"
    "v_mfma_f32_16x16x32_bf16 v[60:63], v[204:207], a[188:191], v[60:63]\n"
    "s_waitcnt lgkmcnt(8)\n"
    "v_mfma_f32_16x16x32_bf16 v[64:67], v[208:211], a[192:195], v[64:67]\n"
    "v_mfma_f32_16x16x32_bf16 v[64:67], v[212:215], a[196:199], v[64:67]\n"
    "v_mfma_f32_16x16x32_bf16 v[64:67], v[216:219], a[200:203], v[64:67]\n"
    "v_mfma_f32_16x16x32_bf16 v[64:67], v[220:223], a[204:207], v[64:67]\n"
    "s_waitcnt lgkmcnt(0)\n"
    "v_mfma_f32_16x16x32_bf16 v[64:67], v[224:227], a[128:131], v[64:67]\n"
    "ds_read_b64_tr_b16 v[176:177], v11 offset:128\n"
    "v_mfma_f32_16x16x32_bf16 v[64:67], v[228:231], a[132:135], v[64:67]\n"
    "ds_read_b64_tr_b16 v[178:179], v11 offset:5248\n"
    "v_mfma_f32_16x16x32_bf16 v[64:67], v[232:235], a[136:139], v[64:67]\n"
    "v_mfma_f32_16x16x32_bf16 v[64:67], v[236:239], a[140:143], v[64:67]\n"
    "ds_read_b64_tr_b16 v[180:181], v11 offset:10368\n"
    "v_mfma_f32_16x16x32_bf16 v[68:71], v[208:211], a[208:211], v[68:71]\n"
    "ds_read_b64_tr_b16 v[182:183], v11 offset:15488\n"
    "v_mfma_f32_16x16x32_bf16 v[68:71], v[212:215], a[212:215], v[68:71]\n"
    "v_mfma_f32_16x16x32_bf16 v[68:71], v[216:219], a[216:219], v[68:71]\n"
    "ds_read_b64_tr_b16 v[184:185], v11 offset:20608\n"
    "v_cvt_pk_bf16_f32 v48, v48, v49\n"
    "v_mfma_f32_16x16x32_bf16 v[68:71], v[220:223], a[220:223], v[68:71]\n"
    "ds_read_b64_tr_b16 v[186:187], v11 offset:25728\n"
    "v_cvt_pk_bf16_f32 v49, v50, v51\n"
    "v_mfma_f32_16x16x32_bf16 v[68:71], v[224:227], a[144:147], v[68:71]\n"
    "v_cvt_pk_bf16_f32 v50, v64, v65\n"
    "v_cvt_pk_bf16_f32 v51, v66, v67\n"
    "v_mfma_f32_16x16x32_bf16 v[68:71], v[228:231], a[148:151], v[68:71]\n"
    "s_add_u32 s90, s79, 1024\n"
    "ds_read_b64_tr_b16 v[188:189], v11 offset:30848\n"
    "ds_write_b128 v252, v[48:51] offset:1024\n"
    "v_mfma_f32_16x16x32_bf16 v[68:71], v[232:235], a[152:155], v[68:71]\n"
    "buffer_store_dwordx4 v[48:51], v0, s[40:43], s90 offen offset:0 sc1\n"
    "v_mfma_f32_16x16x32_bf16 v[68:71], v[236:239], a[156:159], v[68:71]\n"
    "ds_read_b64_tr_b16 v[190:191], v11 offset:35968\n"
    "v_mfma_f32_16x16x32_bf16 v[72:75], v[208:211], a[224:227], v[72:75]\n"
    "ds_read_b64_tr_b16 v[192:193], v13 offset:128\n"
    "v_mfma_f32_16x16x32_bf16 v[72:75], v[212:215], a[228:231], v[72:75]\n"
    "ds_read_b64_tr_b16 v[194:195], v13 offset:5248\n"
    "v_mfma_f32_16x16x32_bf16 v[72:75], v[216:219], a[232:235], v[72:75]\n"
    "v_cvt_pk_bf16_f32 v52, v52, v53\n"
    "v_cvt_pk_bf16_f32 v53, v54, v55\n"
    "v_mfma_f32_16x16x32_bf16 v[72:75], v[220:223], a[236:239], v[72:75]\n"
    "ds_read_b64_tr_b16 v[196:197], v13 offset:10368\n"
    "v_cvt_pk_bf16_f32 v54, v68, v69\n"
    "v_mfma_f32_16x16x32_bf16 v[72:75], v[224:227], a[160:163], v[72:75]\n"
    "ds_read_b64_tr_b16 v[198:199], v13 offset:15488\n"
    "v_cvt_pk_bf16_f32 v55, v70, v71\n"
    "v_mfma_f32_16x16x32_bf16 v[72:75], v[228:231], a[164:167], v[72:75]\n"
    "s_add_u32 s90, s80, 1024\n"
    "ds_write_b128 v252, v[52:55] offset:6144\n"
    "v_mfma_f32_16x16x32_bf16 v[72:75], v[232:235], a[168:171], v[72:75]\n"
    "buffer_store_dwordx4 v[52:55], v0, s[40:43], s90 offen offset:0 sc1\n"
    "v_mfma_f32_16x16x32_bf16 v[72:75], v[236:239], a[172:175], v[72:75]\n"
    "ds_read_b64_tr_b16 v[200:201], v13 offset:20608\n"
    "ds_read_b64_tr_b16 v[202:203], v13 offset:25728\n"
    "v_mfma_f32_16x16x32_bf16 v[76:79], v[208:211], a[240:243], v[76:79]\n"
    "v_mfma_f32_16x16x32_bf16 v[76:79], v[212:215], a[244:247], v[76:79]\n"
    "ds_read_b64_tr_b16 v[204:205], v13 offset:30848\n"
    "v_mfma_f32_16x16x32_bf16 v[76:79], v[216:219], a[248:251], v[76:79]\n"
    "ds_read_b64_tr_b16 v[206:207], v13 offset:35968\n"
    "v_cvt_pk_bf16_f32 v56, v56, v57\n"
    "v_mfma_f32_16x16x32_bf16 v[76:79], v[220:223], a[252:255], v[76:79]\n"
    "v_cvt_pk_bf16_f32 v57, v58, v59\n"
    "v_cvt_pk_bf16_f32 v58, v72, v73\n"
    "v_mfma_f32_16x16x32_bf16 v[76:79], v[224:227], a[176:179], v[76:79]\n"
    "v_cvt_pk_bf16_f32 v59, v74, v75\n"
    "s_add_u32 s90, s82, 1024\n"
    "v_mfma_f32_16x16x32_bf16 v[76:79], v[228:231], a[180:183], v[76:79]\n"
    "ds_write_b128 v253, v[56:59] offset:1024\n"
    "v_mfma_f32_16x16x32_bf16 v[76:79], v[232:235], a[184:187], v[76:79]\n"
    "buffer_store_dwordx4 v[56:59], v0, s[40:43], s90 offen offset:0 sc1\n"
    "v_mfma_f32_16x16x32_bf16 v[76:79], v[236:239], a[188:191], v[76:79]\n"
    "s_waitcnt lgkmcnt(10)\n"
    "v_mfma_f32_16x16x32_bf16 v[80:83], v[176:179], a[192:195], v[80:83]\n"
    "v_mfma_f32_16x16x32_bf16 v[80:83], v[180:183], a[196:199], v[80:83]\n"
    "v_mfma_f32_16x16x32_bf16 v[80:83], v[184:187], a[200:203], v[80:83]\n"
    "v_cvt_pk_bf16_f32 v60, v60, v61\n"
    "v_cvt_pk_bf16_f32 v61, v62, v63\n"
    "v_mfma_f32_16x16x32_bf16 v[80:83], v[188:191], a[204:207], v[80:83]\n"
    "s_waitcnt lgkmcnt(1)\n"
    "v_cvt_pk_bf16_f32 v62, v76, v77\n"
    "v_cvt_pk_bf16_f32 v63, v78, v79\n"
    "v_mfma_f32_16x16x32_bf16 v[80:83], v[192:195], a[128:131], v[80:83]\n"
    "s_add_u32 s90, s83, 1024\n"
    "ds_read_b64_tr_b16 v[208:209], v12 offset:128\n"
    "ds_write_b128 v253, v[60:63] offset:6144\n"
    "v_mfma_f32_16x16x32_bf16 v[80:83], v[196:199], a[132:135], v[80:83]\n"
    "buffer_store_dwordx4 v[60:63], v0, s[40:43], s90 offen offset:0 sc1\n"
    "v_mfma_f32_16x16x32_bf16 v[80:83], v[200:203], a[136:139], v[80:83]\n"
    "ds_read_b64_tr_b16 v[210:211], v12 offset:5248\n"
    "v_mfma_f32_16x16x32_bf16 v[80:83], v[204:207], a[140:143], v[80:83]\n"
    "ds_read_b64_tr_b16 v[212:213], v12 offset:10368\n"
    "v_mfma_f32_16x16x32_bf16 v[84:87], v[176:179], a[208:211], v[84:87]\n"
    "ds_read_b64_tr_b16 v[214:215], v12 offset:15488\n"
    "v_mfma_f32_16x16x32_bf16 v[84:87], v[180:183], a[212:215], v[84:87]\n"
    "v_mfma_f32_16x16x32_bf16 v[84:87], v[184:187], a[216:219], v[84:87]\n"
    "ds_read_b64_tr_b16 v[216:217], v12 offset:20608\n"
    "v_mfma_f32_16x16x32_bf16 v[84:87], v[188:191], a[220:223], v[84:87]\n"
    "ds_read_b64_tr_b16 v[218:219], v12 offset:25728\n"
    "v_mfma_f32_16x16x32_bf16 v[84:87], v[192:195], a[144:147], v[84:87]\n"
    "v_mfma_f32_16x16x32_bf16 v[84:87], v[196:199], a[148:151], v[84:87]\n"
    "ds_read_b64_tr_b16 v[220:221], v12 offset:30848\n"
    "v_mfma_f32_16x16x32_bf16 v[84:87], v[200:203], a[152:155], v[84:87]\n"
    "ds_read_b64_tr_b16 v[222:223], v12 offset:35968\n"
    "v_mfma_f32_16x16x32_bf16 v[84:87], v[204:207], a[156:159], v[84:87]\n"
    "v_mfma_f32_16x16x32_bf16 v[88:91], v[176:179], a[224:227], v[88:91]\n"
    "ds_read_b64_tr_b16 v[224:225], v14 offset:128\n"
    "v_mfma_f32_16x16x32_bf16 v[88:91], v[180:183], a[228:231], v[88:91]\n"
    "ds_read_b64_tr_b16 v[226:227], v14 offset:5248\n"
    "v_mfma_f32_16x16x32_bf16 v[88:91], v[184:187], a[232:235], v[88:91]\n"
    "v_mfma_f32_16x16x32_bf16 v[88:91], v[188:191], a[236:239], v[88:91]\n"
    "ds_read_b64_tr_b16 v[228:229], v14 offset:10368\n"
    "v_mfma_f32_16x16x32_bf16 v[88:91], v[192:195], a[160:163], v[88:91]\n"
    "ds_read_b64_tr_b16 v[230:231], v14 offset:15488\n"
    "v_mfma_f32_16x16x32_bf16 v[88:91], v[196:199], a[164:167], v[88:91]\n"
    "v_mfma_f32_16x16x32_bf16 v[88:91], v[200:203], a[168:171], v[88:91]\n"
    "ds_read_b64_tr_b16 v[232:233], v14 offset:20608\n"
    "v_mfma_f32_16x16x32_bf16 v[88:91], v[204:207], a[172:175], v[88:91]\n"
    "ds_read_b64_tr_b16 v[234:235], v14 offset:25728\n"
    "v_mfma_f32_16x16x32_bf16 v[92:95], v[176:179], a[240:243], v[92:95]\n"
    "v_mfma_f32_16x16x32_bf16 v[92:95], v[180:183], a[244:247], v[92:95]\n"
    "ds_read_b64_tr_b16 v[236:237], v14 offset:30848\n"
    "v_mfma_f32_16x16x32_bf16 v[92:95], v[184:187], a[248:251], v[92:95]\n"
    "ds_read_b64_tr_b16 v[238:239], v14 offset:35968\n"
    "v_mfma_f32_16x16x32_bf16 v[92:95], v[188:191], a[252:255], v[92:95]\n"
    "v_mfma_f32_16x16x32_bf16 v[92:95], v[192:195], a[176:179], v[92:95]\n"
    "v_mfma_f32_16x16x32_bf16 v[92:95], v[196:199], a[180:183], v[92:95]\n"
    "v_mfma_f32_16x16x32_bf16 v[92:95], v[200:203], a[184:187], v[92:95]\n"
    "v_mfma_f32_16x16x32_bf16 v[92:95], v[204:207], a[188:191], v[92:95]\n"
    "s_waitcnt lgkmcnt(8)\n"
    "v_mfma_f32_16x16x32_bf16 v[96:99], v[208:211], a[192:195], v[96:99]\n"
    "v_mfma_f32_16x16x32_bf16 v[96:99], v[212:215], a[196:199], v[96:99]\n"
    "v_mfma_f32_16x16x32_bf16 v[96:99], v[216:219], a[200:203], v[96:99]\n"
    "v_mfma_f32_16x16x32_bf16 v[96:99], v[220:223], a[204:207], v[96:99]\n"
    "s_waitcnt lgkmcnt(0)\n"
    "v_mfma_f32_16x16x32_bf16 v[96:99], v[224:227], a[128:131], v[96:99]\n"
    "ds_read_b64_tr_b16 v[176:177], v11 offset:192\n"
    "v_mfma_f32_16x16x32_bf16 v[96:99], v[228:231], a[132:135], v[96:99]\n"
    "ds_read_b64_tr_b16 v[178:179], v11 offset:5312\n"
    "v_mfma_f32_16x16x32_bf16 v[96:99], v[232:235], a[136:139], v[96:99]\n"
    "v_mfma_f32_16x16x32_bf16 v[96:99], v[236:239], a[140:143], v[96:99]\n"
    "ds_read_b64_tr_b16 v[180:181], v11 offset:10432\n"
    "v_mfma_f32_16x16x32_bf16 v[100:103], v[208:211], a[208:211], v[100:103]\n"
    "ds_read_b64_tr_b16 v[182:183], v11 offset:15552\n"
    "v_mfma_f32_16x16x32_bf16 v[100:103], v[212:215], a[212:215], v[100:103]\n"
    "v_mfma_f32_16x16x32_bf16 v[100:103], v[216:219], a[216:219], v[100:103]\n"
    "ds_read_b64_tr_b16 v[184:185], v11 offset:20672\n"
    "v_cvt_pk_bf16_f32 v80, v80, v81\n"
    "v_mfma_f32_16x16x32_bf16 v[100:103], v[220:223], a[220:223], v[100:103]\n"
    "ds_read_b64_tr_b16 v[186:187], v11 offset:25792\n"
    "v_cvt_pk_bf16_f32 v81, v82, v83\n"
    "v_mfma_f32_16x16x32_bf16 v[100:103], v[224:227], a[144:147], v[100:103]\n"
    "v_cvt_pk_bf16_f32 v82, v96, v97\n"
    "v_cvt_pk_bf16_f32 v83, v98, v99\n"
    "v_mfma_f32_16x16x32_bf16 v[100:103], v[228:231], a[148:151], v[100:103]\n"
    "s_add_u32 s90, s79, 2048\n"
    "ds_read_b64_tr_b16 v[188:189], v11 offset:30912\n"
    "ds_write_b128 v252, v[80:83] offset:2048\n"
    "v_mfma_f32_16x16x32_bf16 v[100:103], v[232:235], a[152:155], v[100:103]\n"
    "buffer_store_dwordx4 v[80:83], v0, s[40:43], s90 offen offset:0 sc1\n"
    "v_mfma_f32_16x16x32_bf16 v[100:103], v[236:239], a[156:159], v[100:103]\n"
    "ds_read_b64_tr_b16 v[190:191], v11 offset:36032\n"
    "v_mfma_f32_16x16x32_bf16 v[104:107], v[208:211], a[224:227], v[104:107]\n"
    "ds_read_b64_tr_b16 v[192:193], v13 offset:192\n"
    "v_mfma_f32_16x16x32_bf16 v[104:107], v[212:215], a[228:231], v[104:107]\n"
    "ds_read_b64_tr_b16 v[194:195], v13 offset:5312\n"
    "v_mfma_f32_16x16x32_bf16 v[104:107], v[216:219], a[232:235], v[104:107]\n"
    "v_cvt_pk_bf16_f32 v84, v84, v85\n"
    "v_cvt_pk_bf16_f32 v85, v86, v87\n"
    "v_mfma_f32_16x16x32_bf16 v[104:107], v[220:223], a[236:239], v[104:107]\n"
    "ds_read_b64_tr_b16 v[196:197], v13 offset:10432\n"
    "v_cvt_pk_bf16_f32 v86, v100, v101\n"
    "v_mfma_f32_16x16x32_bf16 v[104:107], v[224:227], a[160:163], v[104:107]\n"
    "ds_read_b64_tr_b16 v[198:199], v13 offset:15552\n"
    "v_cvt_pk_bf16_f32 v87, v102, v103\n"
    "v_mfma_f32_16x16x32_bf16 v[104:107], v[228:231], a[164:167], v[104:107]\n"
    "s_add_u32 s90, s80, 2048\n"
    "ds_write_b128 v252, v[84:87] offset:7168\n"
    "v_mfma_f32_16x16x32_bf16 v[104:107], v[232:235], a[168:171], v[104:107]\n"
    "buffer_store_dwordx4 v[84:87], v0, s[40:43], s90 offen offset:0 sc1\n"
    "v_mfma_f32_16x16x32_bf16 v[104:107], v[236:239], a[172:175], v[104:107]\n"
    "ds_read_b64_tr_b16 v[200:201], v13 offset:20672\n"
    "ds_read_b64_tr_b16 v[202:203], v13 offset:25792\n"
    "v_mfma_f32_16x16x32_bf16 v[108:111], v[208:211], a[240:243], v[108:111]\n"
    "v_mfma_f32_16x16x32_bf16 v[108:111], v[212:215], a[244:247], v[108:111]\n"
    "ds_read_b64_tr_b16 v[204:205], v13 offset:30912\n"
    "v_mfma_f32_16x16x32_bf16 v[108:111], v[216:219], a[248:251], v[108:111]\n"
    "ds_read_b64_tr_b16 v[206:207], v13 offset:36032\n"
    "v_cvt_pk_bf16_f32 v88, v88, v89\n"
    "v_mfma_f32_16x16x32_bf16 v[108:111], v[220:223], a[252:255], v[108:111]\n"
    "v_cvt_pk_bf16_f32 v89, v90, v91\n"
    "v_cvt_pk_bf16_f32 v90, v104, v105\n"
    "v_mfma_f32_16x16x32_bf16 v[108:111], v[224:227], a[176:179], v[108:111]\n"
    "v_cvt_pk_bf16_f32 v91, v106, v107\n"
    "s_add_u32 s90, s82, 2048\n"
    "v_mfma_f32_16x16x32_bf16 v[108:111], v[228:231], a[180:183], v[108:111]\n"
    "ds_write_b128 v253, v[88:91] offset:2048\n"
    "v_mfma_f32_16x16x32_bf16 v[108:111], v[232:235], a[184:187], v[108:111]\n"
    "buffer_store_dwordx4 v[88:91], v0, s[40:43], s90 offen offset:0 sc1\n"
    "v_mfma_f32_16x16x32_bf16 v[108:111], v[236:239], a[188:191], v[108:111]\n"
    "s_waitcnt lgkmcnt(10)\n"
    "v_mfma_f32_16x16x32_bf16 v[112:115], v[176:179], a[192:195], v[112:115]\n"
    "v_mfma_f32_16x16x32_bf16 v[112:115], v[180:183], a[196:199], v[112:115]\n"
    "v_mfma_f32_16x16x32_bf16 v[112:115], v[184:187], a[200:203], v[112:115]\n"
    "v_cvt_pk_bf16_f32 v92, v92, v93\n"
    "v_cvt_pk_bf16_f32 v93, v94, v95\n"
    "v_mfma_f32_16x16x32_bf16 v[112:115], v[188:191], a[204:207], v[112:115]\n"
    "s_waitcnt lgkmcnt(1)\n"
    "v_cvt_pk_bf16_f32 v94, v108, v109\n"
    "v_cvt_pk_bf16_f32 v95, v110, v111\n"
    "v_mfma_f32_16x16x32_bf16 v[112:115], v[192:195], a[128:131], v[112:115]\n"
    "s_add_u32 s90, s83, 2048\n"
    "ds_read_b64_tr_b16 v[208:209], v12 offset:192\n"
    "ds_write_b128 v253, v[92:95] offset:7168\n"
    "v_mfma_f32_16x16x32_bf16 v[112:115], v[196:199], a[132:135], v[112:115]\n"
    "buffer_store_dwordx4 v[92:95], v0, s[40:43], s90 offen offset:0 sc1\n"
    "v_mfma_f32_16x16x32_bf16 v[112:115], v[200:203], a[136:139], v[112:115]\n"
    "ds_read_b64_tr_b16 v[210:211], v12 offset:5312\n"
    "v_mfma_f32_16x16x32_bf16 v[112:115], v[204:207], a[140:143], v[112:115]\n"
    "ds_read_b64_tr_b16 v[212:213], v12 offset:10432\n"
    "v_mfma_f32_16x16x32_bf16 v[116:119], v[176:179], a[208:211], v[116:119]\n"
    "s_add_u32 s94, s97, 2\n"
    "ds_read_b64_tr_b16 v[214:215], v12 offset:15552\n"
    "v_accvgpr_write_b32 a53, s94\n"
    "v_mfma_f32_16x16x32_bf16 v[116:119], v[180:183], a[212:215], v[116:119]\n"
    "s_waitcnt vmcnt(4)\n"
    "v_mfma_f32_16x16x32_bf16 v[116:119], v[184:187], a[216:219], v[116:119]\n"
    "buffer_store_dwordx2 a[52:53], v7, s[40:43], s28 offen offset:0 sc1\n"
    "v_mfma_f32_16x16x32_bf16 v[116:119], v[188:191], a[220:223], v[116:119]\n"
    "ds_read_b64_tr_b16 v[216:217], v12 offset:20672\n"
    "ds_read_b64_tr_b16 v[218:219], v12 offset:25792\n"
    "v_mfma_f32_16x16x32_bf16 v[116:119], v[192:195], a[144:147], v[116:119]\n"
    "v_mfma_f32_16x16x32_bf16 v[116:119], v[196:199], a[148:151], v[116:119]\n"
    "ds_read_b64_tr_b16 v[220:221], v12 offset:30912\n"
    "v_mfma_f32_16x16x32_bf16 v[116:119], v[200:203], a[152:155], v[116:119]\n"
    "ds_read_b64_tr_b16 v[222:223], v12 offset:36032\n"
    "v_mfma_f32_16x16x32_bf16 v[116:119], v[204:207], a[156:159], v[116:119]\n"
    "v_mfma_f32_16x16x32_bf16 v[120:123], v[176:179], a[224:227], v[120:123]\n"
    "ds_read_b64_tr_b16 v[224:225], v14 offset:192\n"
    "v_mfma_f32_16x16x32_bf16 v[120:123], v[180:183], a[228:231], v[120:123]\n"
    "ds_read_b64_tr_b16 v[226:227], v14 offset:5312\n"
    "v_mfma_f32_16x16x32_bf16 v[120:123], v[184:187], a[232:235], v[120:123]\n"
    "v_mfma_f32_16x16x32_bf16 v[120:123], v[188:191], a[236:239], v[120:123]\n"
    "ds_read_b64_tr_b16 v[228:229], v14 offset:10432\n"
    "v_mfma_f32_16x16x32_bf16 v[120:123], v[192:195], a[160:163], v[120:123]\n"
    "ds_read_b64_tr_b16 v[230:231], v14 offset:15552\n"
    "v_mfma_f32_16x16x32_bf16 v[120:123], v[196:199], a[164:167], v[120:123]\n"
    "v_mfma_f32_16x16x32_bf16 v[120:123], v[200:203], a[168:171], v[120:123]\n"
    "ds_read_b64_tr_b16 v[232:233], v14 offset:20672\n"
    "v_mfma_f32_16x16x32_bf16 v[120:123], v[204:207], a[172:175], v[120:123]\n"
    "ds_read_b64_tr_b16 v[234:235], v14 offset:25792\n"
    "v_mfma_f32_16x16x32_bf16 v[124:127], v[176:179], a[240:243], v[124:127]\n"
    "v_mfma_f32_16x16x32_bf16 v[124:127], v[180:183], a[244:247], v[124:127]\n"
    "ds_read_b64_tr_b16 v[236:237], v14 offset:30912\n"
    "v_mfma_f32_16x16x32_bf16 v[124:127], v[184:187], a[248:251], v[124:127]\n"
    "ds_read_b64_tr_b16 v[238:239], v14 offset:36032\n"
    "v_mfma_f32_16x16x32_bf16 v[124:127], v[188:191], a[252:255], v[124:127]\n"
    "v_mfma_f32_16x16x32_bf16 v[124:127], v[192:195], a[176:179], v[124:127]\n"
    "v_mfma_f32_16x16x32_bf16 v[124:127], v[196:199], a[180:183], v[124:127]\n"
    "v_mfma_f32_16x16x32_bf16 v[124:127], v[200:203], a[184:187], v[124:127]\n"
    "v_mfma_f32_16x16x32_bf16 v[124:127], v[204:207], a[188:191], v[124:127]\n"
    "s_waitcnt lgkmcnt(8)\n"
    "v_mfma_f32_16x16x32_bf16 v[128:131], v[208:211], a[192:195], v[128:131]\n"
    "v_mfma_f32_16x16x32_bf16 v[128:131], v[212:215], a[196:199], v[128:131]\n"
    "v_accvgpr_read_b32 v64, a112\n"
    "v_mfma_f32_16x16x32_bf16 v[128:131], v[216:219], a[200:203], v[128:131]\n"
    "v_mfma_f32_16x16x32_bf16 v[128:131], v[220:223], a[204:207], v[128:131]\n"
    "s_waitcnt lgkmcnt(0)\n"
    "v_accvgpr_read_b32 v66, a113\n"
    "v_mfma_f32_16x16x32_bf16 v[128:131], v[224:227], a[128:131], v[128:131]\n"
    "ds_read_b64_tr_b16 v[176:177], v11 offset:256\n"
    "v_mfma_f32_16x16x32_bf16 v[128:131], v[228:231], a[132:135], v[128:131]\n"
    "ds_read_b64_tr_b16 v[178:179], v11 offset:5376\n"
    "v_accvgpr_read_b32 v68, a114\n"
    "v_mfma_f32_16x16x32_bf16 v[128:131], v[232:235], a[136:139], v[128:131]\n"
    "v_accvgpr_read_b32 v70, a115\n"
    "v_mfma_f32_16x16x32_bf16 v[128:131], v[236:239], a[140:143], v[128:131]\n"
    "ds_read_b64_tr_b16 v[180:181], v11 offset:10496\n"
    "v_mfma_f32_16x16x32_bf16 v[132:135], v[208:211], a[208:211], v[132:135]\n"
    "ds_read_b64_tr_b16 v[182:183], v11 offset:15616\n"
    "v_and_b32 v65, 0xffff0000, v64\n"
    "v_mfma_f32_16x16x32_bf16 v[132:135], v[212:215], a[212:215], v[132:135]\n"
    "v_mfma_f32_16x16x32_bf16 v[132:135], v[216:219], a[216:219], v[132:135]\n"
    "ds_read_b64_tr_b16 v[184:185], v11 offset:20736\n"
    "v_cvt_pk_bf16_f32 v112, v112, v113\n"
    "v_mfma_f32_16x16x32_bf16 v[132:135], v[220:223], a[220:223], v[132:135]\n"
    "ds_read_b64_tr_b16 v[186:187], v11 offset:25856\n"
    "v_cvt_pk_bf16_f32 v113, v114, v115\n"
    "v_mfma_f32_16x16x32_bf16 v[132:135], v[224:227], a[144:147], v[132:135]\n"
    "v_cvt_pk_bf16_f32 v114, v128, v129\n"
    "v_cvt_pk_bf16_f32 v115, v130, v131\n"
    "v_mfma_f32_16x16x32_bf16 v[132:135], v[228:231], a[148:151], v[132:135]\n"
    "s_add_u32 s90, s79, 3072\n"
    "ds_read_b64_tr_b16 v[188:189], v11 offset:30976\n"
    "ds_write_b128 v252, v[112:115] offset:3072\n"
    "v_mfma_f32_16x16x32_bf16 v[132:135], v[232:235], a[152:155], v[132:135]\n"
    "ds_read_b64_tr_b16 v[190:191], v11 offset:36096\n"
    "v_lshlrev_b32 v64, 16, v64\n"
    "v_mfma_f32_16x16x32_bf16 v[132:135], v[236:239], a[156:159], v[132:135]\n"
    "buffer_store_dwordx4 v[112:115], v0, s[40:43], s90 offen offset:0 sc1\n"
    "v_mfma_f32_16x16x32_bf16 v[136:139], v[208:211], a[224:227], v[136:139]\n"
    "ds_read_b64_tr_b16 v[192:193], v13 offset:256\n"
    "v_and_b32 v67, 0xffff0000, v66\n"
    "v_mfma_f32_16x16x32_bf16 v[136:139], v[212:215], a[228:231], v[136:139]\n"
    "ds_read_b64_tr_b16 v[194:195], v13 offset:5376\n"
    "v_lshlrev_b32 v66, 16, v66\n"
    "v_mfma_f32_16x16x32_bf16 v[136:139], v[216:219], a[232:235], v[136:139]\n"
    "v_cvt_pk_bf16_f32 v116, v116, v117\n"
    "v_cvt_pk_bf16_f32 v117, v118, v119\n"
    "v_mfma_f32_16x16x32_bf16 v[136:139], v[220:223], a[236:239], v[136:139]\n"
    "ds_read_b64_tr_b16 v[196:197], v13 offset:10496\n"
    "v_cvt_pk_bf16_f32 v118, v132, v133\n"
    "v_mfma_f32_16x16x32_bf16 v[136:139], v[224:227], a[160:163], v[136:139]\n"
    "ds_read_b64_tr_b16 v[198:199], v13 offset:15616\n"
    "v_cvt_pk_bf16_f32 v119, v134, v135\n"
    "v_mfma_f32_16x16x32_bf16 v[136:139], v[228:231], a[164:167], v[136:139]\n"
    "s_add_u32 s90, s80, 3072\n"
    "ds_write_b128 v252, v[116:119] offset:8192\n"
    "v_and_b32 v69, 0xffff0000, v68\n"
    "v_mfma_f32_16x16x32_bf16 v[136:139], v[232:235], a[168:171], v[136:139]\n"
    "ds_read_b64_tr_b16 v[200:201], v13 offset:20736\n"
    "v_lshlrev_b32 v68, 16, v68\n"
    "v_mfma_f32_16x16x32_bf16 v[136:139], v[236:239], a[172:175], v[136:139]\n"
    "ds_read_b64_tr_b16 v[202:203], v13 offset:25856\n"
    "v_and_b32 v71, 0xffff0000, v70\n"
    "v_mfma_f32_16x16x32_bf16 v[140:143], v[208:211], a[240:243], v[140:143]\n"
    "buffer_store_dwordx4 v[116:119], v0, s[40:43], s90 offen offset:0 sc1\n"
    "v_mfma_f32_16x16x32_bf16 v[140:143], v[212:215], a[244:247], v[140:143]\n"
    "ds_read_b64_tr_b16 v[204:205], v13 offset:30976\n"
    "v_lshlrev_b32 v70, 16, v70\n"
    "v_mfma_f32_16x16x32_bf16 v[140:143], v[216:219], a[248:251], v[140:143]\n"
    "ds_read_b64_tr_b16 v[206:207], v13 offset:36096\n"
    "v_cvt_pk_bf16_f32 v120, v120, v121\n"
    "v_mfma_f32_16x16x32_bf16 v[140:143], v[220:223], a[252:255], v[140:143]\n"
    "v_cvt_pk_bf16_f32 v121, v122, v123\n"
    "v_cvt_pk_bf16_f32 v122, v136, v137\n"
    "v_mfma_f32_16x16x32_bf16 v[140:143], v[224:227], a[176:179], v[140:143]\n"
    "v_cvt_pk_bf16_f32 v123, v138, v139\n"
    "v_accvgpr_read_b32 v72, a116\n"
    "v_mfma_f32_16x16x32_bf16 v[140:143], v[228:231], a[180:183], v[140:143]\n"
    "s_add_u32 s90, s82, 3072\n"
    "ds_write_b128 v253, v[120:123] offset:3072\n"
    "v_accvgpr_read_b32 v74, a117\n"
    "v_mfma_f32_16x16x32_bf16 v[140:143], v[232:235], a[184:187], v[140:143]\n"
    "buffer_store_dwordx4 v[120:123], v0, s[40:43], s90 offen offset:0 sc1\n"
    "v_mfma_f32_16x16x32_bf16 v[140:143], v[236:239], a[188:191], v[140:143]\n"
    "s_waitcnt lgkmcnt(10)\n"
    "v_accvgpr_read_b32 v76, a118\n"
    "v_accvgpr_read_b32 v78, a119\n"
    "v_mfma_f32_16x16x32_bf16 v[144:147], v[176:179], a[192:195], v[144:147]\n"
    "v_and_b32 v73, 0xffff0000, v72\n"
    "v_lshlrev_b32 v72, 16, v72\n"
    "v_mfma_f32_16x16x32_bf16 v[144:147], v[180:183], a[196:199], v[144:147]\n"
    "v_and_b32 v75, 0xffff0000, v74\n"
    "v_mfma_f32_16x16x32_bf16 v[144:147], v[184:187], a[200:203], v[144:147]\n"
    "v_cvt_pk_bf16_f32 v124, v124, v125\n"
    "v_cvt_pk_bf16_f32 v125, v126, v127\n"
    "v_mfma_f32_16x16x32_bf16 v[144:147], v[188:191], a[204:207], v[144:147]\n"
    "s_waitcnt lgkmcnt(1)\n"
    "v_cvt_pk_bf16_f32 v126, v140, v141\n"
    "v_cvt_pk_bf16_f32 v127, v142, v143\n"
    "v_mfma_f32_16x16x32_bf16 v[144:147], v[192:195], a[128:131], v[144:147]\n"
    "s_add_u32 s90, s83, 3072\n"
    "ds_read_b64_tr_b16 v[208:209], v12 offset:256\n"
    "ds_write_b128 v253, v[124:127] offset:8192\n"
    "v_mfma_f32_16x16x32_bf16 v[144:147], v[196:199], a[132:135], v[144:147]\n"
    "ds_read_b64_tr_b16 v[210:211], v12 offset:5376\n"
    "v_lshlrev_b32 v74, 16, v74\n"
    "v_mfma_f32_16x16x32_bf16 v[144:147], v[200:203], a[136:139], v[144:147]\n"
    "buffer_store_dwordx4 v[124:127], v0, s[40:43], s90 offen offset:0 sc1\n"
    "v_mfma_f32_16x16x32_bf16 v[144:147], v[204:207], a[140:143], v[144:147]\n"
    "ds_read_b64_tr_b16 v[212:213], v12 offset:10496\n"
    "v_and_b32 v77, 0xffff0000, v76\n"
    "v_mfma_f32_16x16x32_bf16 v[148:151], v[176:179], a[208:211], v[148:151]\n"
    "ds_read_b64_tr_b16 v[214:215], v12 offset:15616\n"
    "v_lshlrev_b32 v76, 16, v76\n"
    "v_mfma_f32_16x16x32_bf16 v[148:151], v[180:183], a[212:215], v[148:151]\n"
    "v_and_b32 v79, 0xffff0000, v78\n"
    "v_lshlrev_b32 v78, 16, v78\n"
    "v_mfma_f32_16x16x32_bf16 v[148:151], v[184:187], a[216:219], v[148:151]\n"
    "ds_read_b64_tr_b16 v[216:217], v12 offset:20736\n"
    "v_mfma_f32_16x16x32_bf16 v[148:151], v[188:191], a[220:223], v[148:151]\n"
    "ds_read_b64_tr_b16 v[218:219], v12 offset:25856\n"
    "v_accvgpr_read_b32 v48, a120\n"
    "v_mfma_f32_16x16x32_bf16 v[148:151], v[192:195], a[144:147], v[148:151]\n"
    "v_accvgpr_read_b32 v50, a121\n"
    "v_mfma_f32_16x16x32_bf16 v[148:151], v[196:199], a[148:151], v[148:151]\n"
    "ds_read_b64_tr_b16 v[220:221], v12 offset:30976\n"
    "v_mfma_f32_16x16x32_bf16 v[148:151], v[200:203], a[152:155], v[148:151]\n"
    "ds_read_b64_tr_b16 v[222:223], v12 offset:36096\n"
    "v_accvgpr_read_b32 v52, a122\n"
    "v_mfma_f32_16x16x32_bf16 v[148:151], v[204:207], a[156:159], v[148:151]\n"
    "v_mfma_f32_16x16x32_bf16 v[152:155], v[176:179], a[224:227], v[152:155]\n"
    "ds_read_b64_tr_b16 v[224:225], v14 offset:256\n"
    "v_accvgpr_read_b32 v54, a123\n"
    "v_mfma_f32_16x16x32_bf16 v[152:155], v[180:183], a[228:231], v[152:155]\n"
    "ds_read_b64_tr_b16 v[226:227], v14 offset:5376\n"
    "v_mfma_f32_16x16x32_bf16 v[152:155], v[184:187], a[232:235], v[152:155]\n"
    "v_and_b32 v49, 0xffff0000, v48\n"
    "v_mfma_f32_16x16x32_bf16 v[152:155], v[188:191], a[236:239], v[152:155]\n"
    "ds_read_b64_tr_b16 v[228:229], v14 offset:10496\n"
    "v_lshlrev_b32 v48, 16, v48\n"
    "v_mfma_f32_16x16x32_bf16 v[152:155], v[192:195], a[160:163], v[152:155]\n"
    "ds_read_b64_tr_b16 v[230:231], v14 offset:15616\n"
    "v_mfma_f32_16x16x32_bf16 v[152:155], v[196:199], a[164:167], v[152:155]\n"
    "v_and_b32 v51, 0xffff0000, v50\n"
    "v_mfma_f32_16x16x32_bf16 v[152:155], v[200:203], a[168:171], v[152:155]\n"
    "ds_read_b64_tr_b16 v[232:233], v14 offset:20736\n"
    "v_mfma_f32_16x16x32_bf16 v[152:155], v[204:207], a[172:175], v[152:155]\n"
    "ds_read_b64_tr_b16 v[234:235], v14 offset:25856\n"
    "v_lshlrev_b32 v50, 16, v50\n"
    "v_mfma_f32_16x16x32_bf16 v[156:159], v[176:179], a[240:243], v[156:159]\n"
    "v_mfma_f32_16x16x32_bf16 v[156:159], v[180:183], a[244:247], v[156:159]\n"
    "ds_read_b64_tr_b16 v[236:237], v14 offset:30976\n"
    "v_and_b32 v53, 0xffff0000, v52\n"
    "v_mfma_f32_16x16x32_bf16 v[156:159], v[184:187], a[248:251], v[156:159]\n"
    "ds_read_b64_tr_b16 v[238:239], v14 offset:36096\n"
    "v_lshlrev_b32 v52, 16, v52\n"
    "v_mfma_f32_16x16x32_bf16 v[156:159], v[188:191], a[252:255], v[156:159]\n"
    "v_mfma_f32_16x16x32_bf16 v[156:159], v[192:195], a[176:179], v[156:159]\n"
    "v_and_b32 v55, 0xffff0000, v54\n"
    "v_mfma_f32_16x16x32_bf16 v[156:159], v[196:199], a[180:183], v[156:159]\n"
    "v_mfma_f32_16x16x32_bf16 v[156:159], v[200:203], a[184:187], v[156:159]\n"
    "v_lshlrev_b32 v54, 16, v54\n"
    "v_mfma_f32_16x16x32_bf16 v[156:159], v[204:207], a[188:191], v[156:159]\n"
    "s_waitcnt lgkmcnt(8)\n"
    "v_mfma_f32_16x16x32_bf16 v[160:163], v[208:211], a[192:195], v[160:163]\n"
    "v_mfma_f32_16x16x32_bf16 v[160:163], v[212:215], a[196:199], v[160:163]\n"
    "v_mfma_f32_16x16x32_bf16 v[160:163], v[216:219], a[200:203], v[160:163]\n"
    "v_mfma_f32_16x16x32_bf16 v[160:163], v[220:223], a[204:207], v[160:163]\n"
    "s_waitcnt lgkmcnt(0)\n"
    "v_mfma_f32_16x16x32_bf16 v[160:163], v[224:227], a[128:131], v[160:163]\n"
    "v_mfma_f32_16x16x32_bf16 v[160:163], v[228:231], a[132:135], v[160:163]\n"
    "v_mfma_f32_16x16x32_bf16 v[160:163], v[232:235], a[136:139], v[160:163]\n"
    "v_mfma_f32_16x16x32_bf16 v[160:163], v[236:239], a[140:143], v[160:163]\n"
    "v_mfma_f32_16x16x32_bf16 v[164:167], v[208:211], a[208:211], v[164:167]\n"
    "v_mfma_f32_16x16x32_bf16 v[164:167], v[212:215], a[212:215], v[164:167]\n"
    "v_mfma_f32_16x16x32_bf16 v[164:167], v[216:219], a[216:219], v[164:167]\n"
    "v_cvt_pk_bf16_f32 v144, v144, v145\n"
    "v_cvt_pk_bf16_f32 v145, v146, v147\n"
    "v_mfma_f32_16x16x32_bf16 v[164:167], v[220:223], a[220:223], v[164:167]\n"
    "v_cvt_pk_bf16_f32 v146, v160, v161\n"
    "v_cvt_pk_bf16_f32 v147, v162, v163\n"
    "v_mfma_f32_16x16x32_bf16 v[164:167], v[224:227], a[144:147], v[164:167]\n"
    "s_add_u32 s90, s79, 4096\n"
    "ds_write_b128 v252, v[144:147] offset:4096\n"
    "v_mfma_f32_16x16x32_bf16 v[164:167], v[228:231], a[148:151], v[164:167]\n"
    "buffer_store_dwordx4 v[144:147], v0, s[40:43], s90 offen offset:0 sc1\n"
    "v_mfma_f32_16x16x32_bf16 v[164:167], v[232:235], a[152:155], v[164:167]\n"
    "v_mfma_f32_16x16x32_bf16 v[164:167], v[236:239], a[156:159], v[164:167]\n"
    "v_mfma_f32_16x16x32_bf16 v[168:171], v[208:211], a[224:227], v[168:171]\n"
    "v_mfma_f32_16x16x32_bf16 v[168:171], v[212:215], a[228:231], v[168:171]\n"
    "v_mfma_f32_16x16x32_bf16 v[168:171], v[216:219], a[232:235], v[168:171]\n"
    "v_cvt_pk_bf16_f32 v148, v148, v149\n"
    "v_cvt_pk_bf16_f32 v149, v150, v151\n"
    "v_mfma_f32_16x16x32_bf16 v[168:171], v[220:223], a[236:239], v[168:171]\n"
    "v_cvt_pk_bf16_f32 v150, v164, v165\n"
    "v_cvt_pk_bf16_f32 v151, v166, v167\n"
    "v_mfma_f32_16x16x32_bf16 v[168:171], v[224:227], a[160:163], v[168:171]\n"
    "s_add_u32 s90, s80, 4096\n"
    "ds_write_b128 v252, v[148:151] offset:9216\n"
    "v_mfma_f32_16x16x32_bf16 v[168:171], v[228:231], a[164:167], v[168:171]\n"
    "buffer_store_dwordx4 v[148:151], v0, s[40:43], s90 offen offset:0 sc1\n"
    "v_mfma_f32_16x16x32_bf16 v[168:171], v[232:235], a[168:171], v[168:171]\n"
    "s_add_u32 s94, s97, 4\n"
    "v_accvgpr_write_b32 a53, s94\n"
    "s_waitcnt vmcnt(2)\n"
    "v_mfma_f32_16x16x32_bf16 v[168:171], v[236:239], a[172:175], v[168:171]\n"
    "buffer_store_dwordx2 a[52:53], v7, s[40:43], s28 offen offset:0 sc1\n"
    "v_mfma_f32_16x16x32_bf16 v[172:175], v[208:211], a[240:243], v[172:175]\n"
    "v_mfma_f32_16x16x32_bf16 v[172:175], v[212:215], a[244:247], v[172:175]\n"
    "v_mfma_f32_16x16x32_bf16 v[172:175], v[216:219], a[248:251], v[172:175]\n"
    "v_cvt_pk_bf16_f32 v152, v152, v153\n"
    "v_cvt_pk_bf16_f32 v153, v154, v155\n"
    "v_mfma_f32_16x16x32_bf16 v[172:175], v[220:223], a[252:255], v[172:175]\n"
    "v_cvt_pk_bf16_f32 v154, v168, v169\n"
    "v_cvt_pk_bf16_f32 v155, v170, v171\n"
    "v_mfma_f32_16x16x32_bf16 v[172:175], v[224:227], a[176:179], v[172:175]\n"
    "buffer_load_dwordx2 v[250:251], v15, s[40:43], s29 offen offset:0 sc1\n"
    "v_mfma_f32_16x16x32_bf16 v[172:175], v[228:231], a[180:183], v[172:175]\n"
    "s_add_u32 s90, s82, 4096\n"
    "ds_write_b128 v253, v[152:155] offset:4096\n"
    "v_mfma_f32_16x16x32_bf16 v[172:175], v[232:235], a[184:187], v[172:175]\n"
    "buffer_store_dwordx4 v[152:155], v0, s[40:43], s90 offen offset:0 sc1\n"
    "v_mfma_f32_16x16x32_bf16 v[172:175], v[236:239], a[188:191], v[172:175]\n"
    "v_cvt_pk_bf16_f32 v156, v156, v157\n"
    "v_cvt_pk_bf16_f32 v157, v158, v159\n"
    "s_nop 5\n"
    "v_cvt_pk_bf16_f32 v158, v172, v173\n"
    "v_cvt_pk_bf16_f32 v159, v174, v175\n"
    "s_add_u32 s90, s83, 4096\n"
    "ds_write_b128 v253, v[156:159] offset:9216\n"
    "buffer_store_dwordx4 v[156:159], v0, s[40:43], s90 offen offset:0 sc1\n"
    ".L_fin_%=:\n"
    "s_add_u32 s94, s97, 5\n"
    "v_accvgpr_write_b32 a53, s94\n"
    "s_waitcnt vmcnt(0)\n"
    "buffer_store_dwordx2 a[52:53], v7, s[40:43], s28 offen offset:0 sc1\n"
    "s_waitcnt lgkmcnt(0)\n"
    "s_barrier\n"
    "ds_read_b128 a[24:27], v254 offset:40960\n"
    "ds_read_b128 a[28:31], v254 offset:43008\n"
    "ds_read_b128 a[32:35], v254 offset:45056\n"
    "v_bfe_u32 v246, v0, 4, 3\n"
    "v_cmp_eq_u32 vcc, s85, v246\n"
    "v_mov_b32 v246, s96\n"
    "v_mov_b32 v247, s94\n"
    "v_cndmask_b32 v250, v250, v246, vcc\n"
    "v_cndmask_b32 v251, v251, v247, vcc\n"
    "s_add_u32 s94, s97, 2\n"
    "v_xor_b32 v248, s96, v250\n"
    "v_subrev_u32 v249, s94, v251\n"
    "v_lshrrev_b32 v249, 4, v249\n"
    "v_or_b32 v248, v248, v249\n"
    "v_cmp_ne_u32 vcc, 0, v248\n"
    "s_nop 0\n"
    "s_cbranch_vccnz .L_spin5_%=\n"
    ".L_back5_%=:\n"
    "s_add_u32 s89, s32, s10\n"
    "buffer_load_dwordx4 a[64:67], v0, s[40:43], s89 offen offset:0 sc1\n"
    "s_add_u32 s89, s32, s11\n"
    "buffer_load_dwordx4 a[68:71], v0, s[40:43], s89 offen offset:0 sc1\n"
    "s_add_u32 s89, s33, s10\n"
    "buffer_load_dwordx4 a[72:75], v0, s[40:43], s89 offen offset:0 sc1\n"
    "s_add_u32 s89, s33, s11\n"
    "buffer_load_dwordx4 a[76:79], v0, s[40:43], s89 offen offset:0 sc1\n"
    "s_add_u32 s89, s34, s10\n"
    "buffer_load_dwordx4 a[80:83], v0, s[40:43], s89 offen offset:0 sc1\n"
    "s_add_u32 s89, s34, s11\n"
    "buffer_load_dwordx4 a[84:87], v0, s[40:43], s89 offen offset:0 sc1\n"
    "s_add_u32 s89, s32, s4\n"
    "buffer_load_dwordx4 a[88:91], v0, s[40:43], s89 offen offset:0 sc1\n"
    "s_add_u32 s89, s32, s5\n"
    "buffer_load_dwordx4 a[92:95], v0, s[40:43], s89 offen offset:0 sc1\n"
    "s_add_u32 s89, s32, s6\n"
    "buffer_load_dwordx4 a[96:99], v0, s[40:43], s89 offen offset:0 sc1\n"
    "s_add_u32 s89, s32, s7\n"
    "buffer_load_dwordx4 a[100:103], v0, s[40:43], s89 offen offset:0 sc1\n"
    "s_add_u32 s89, s32, s8\n"
    "buffer_load_dwordx4 a[104:107], v0, s[40:43], s89 offen offset:0 sc1\n"
    "s_add_u32 s89, s32, s9\n"
    "buffer_load_dwordx4 a[108:111], v0, s[40:43], s89 offen offset:0 sc1\n"
    "s_add_u32 s94, s97, 4\n"
    "v_xor_b32 v248, s96, v250\n"
    "v_subrev_u32 v249, s94, v251\n"
    "v_lshrrev_b32 v249, 4, v249\n"
    "v_or_b32 v248, v248, v249\n"
    "v_cmp_ne_u32 vcc, 0, v248\n"
    "s_nop 0\n"
    "s_cbranch_vccnz .L_spin8_%=\n"
    ".L_back8_%=:\n"
    "s_add_u32 s89, s33, s4\n"
    "buffer_load_dwordx4 v[80:83], v0, s[40:43], s89 offen offset:0 sc1\n"
    "s_add_u32 s89, s33, s5\n"
    "buffer_load_dwordx4 v[84:87], v0, s[40:43], s89 offen offset:0 sc1\n"
    "s_add_u32 s89, s33, s6\n"
    "buffer_load_dwordx4 v[88:91], v0, s[40:43], s89 offen offset:0 sc1\n"
    "s_add_u32 s89, s33, s7\n"
    "buffer_load_dwordx4 v[92:95], v0, s[40:43], s89 offen offset:0 sc1\n"
    "s_add_u32 s89, s33, s8\n"
    "buffer_load_dwordx4 v[96:99], v0, s[40:43], s89 offen offset:0 sc1\n"
    "s_add_u32 s89, s33, s9\n"
    "buffer_load_dwordx4 v[100:103], v0, s[40:43], s89 offen offset:0 sc1\n"
    "s_add_u32 s94, s97, 5\n"
    "v_xor_b32 v248, s96, v250\n"
    "v_subrev_u32 v249, s94, v251\n"
    "v_lshrrev_b32 v249, 4, v249\n"
    "v_or_b32 v248, v248, v249\n"
    "v_cmp_ne_u32 vcc, 0, v248\n"
    "s_nop 0\n"
    "s_cbranch_vccnz .L_spin6_%=\n"
    ".L_back6_%=:\n"
    "s_add_u32 s89, s34, s4\n"
    "buffer_load_dwordx4 v[112:115], v0, s[40:43], s89 offen offset:0 sc1\n"
    "s_add_u32 s89, s34, s5\n"
    "buffer_load_dwordx4 v[116:119], v0, s[40:43], s89 offen offset:0 sc1\n"
    "s_add_u32 s89, s34, s6\n"
    "buffer_load_dwordx4 v[120:123], v0, s[40:43], s89 offen offset:0 sc1\n"
    "s_add_u32 s89, s34, s7\n"
    "buffer_load_dwordx4 v[124:127], v0, s[40:43], s89 offen offset:0 sc1\n"
    "s_add_u32 s89, s34, s8\n"
    "buffer_load_dwordx4 v[128:131], v0, s[40:43], s89 offen offset:0 sc1\n"
    "s_add_u32 s89, s34, s9\n"
    "buffer_load_dwordx4 v[132:135], v0, s[40:43], s89 offen offset:0 sc1\n"
    "buffer_store_dword a52, v7, s[40:43], s84 offen offset:0 sc1\n"
    "s_waitcnt lgkmcnt(2)\n"
    "v_mfma_f32_16x16x32_bf16 v[32:35], a[16:19], a[24:27], 0\n"
    "s_waitcnt vmcnt(24)\n"
    "v_mfma_f32_16x16x32_bf16 v[32:35], a[0:3], a[64:67], v[32:35]\n"
    "s_waitcnt vmcnt(23)\n"
    "v_mfma_f32_16x16x32_bf16 v[32:35], a[0:3], a[68:71], v[32:35]\n"
    "v_mfma_f32_16x16x32_bf16 v[36:39], a[20:23], a[24:27], 0\n"
    "v_mfma_f32_16x16x32_bf16 v[36:39], a[4:7], a[64:67], v[36:39]\n"
    "v_mfma_f32_16x16x32_bf16 v[36:39], a[4:7], a[68:71], v[36:39]\n"
    "s_waitcnt lgkmcnt(1)\n"
    "v_mfma_f32_16x16x32_bf16 v[40:43], a[16:19], a[28:31], 0\n"
    "s_waitcnt vmcnt(22)\n"
    "v_mfma_f32_16x16x32_bf16 v[40:43], a[0:3], a[72:75], v[40:43]\n"
    "s_waitcnt vmcnt(21)\n"
    "v_mfma_f32_16x16x32_bf16 v[40:43], a[0:3], a[76:79], v[40:43]\n"
    "v_mfma_f32_16x16x32_bf16 v[44:47], a[20:23], a[28:31], 0\n"
    "v_mfma_f32_16x16x32_bf16 v[44:47], a[4:7], a[72:75], v[44:47]\n"
    "v_mfma_f32_16x16x32_bf16 v[44:47], a[4:7], a[76:79], v[44:47]\n"
    "s_waitcnt lgkmcnt(0)\n"
    "v_mfma_f32_16x16x32_bf16 v[16:19], a[16:19], a[32:35], 0\n"
    "s_waitcnt vmcnt(20)\n"
    "v_mfma_f32_16x16x32_bf16 v[16:19], a[0:3], a[80:83], v[16:19]\n"
    "s_waitcnt vmcnt(19)\n"
    "v_mfma_f32_16x16x32_bf16 v[16:19], a[0:3], a[84:87], v[16:19]\n"
    "v_mfma_f32_16x16x32_bf16 v[20:23], a[20:23], a[32:35], 0\n"
    "v_mfma_f32_16x16x32_bf16 v[20:23], a[4:7], a[80:83], v[20:23]\n"
    "v_mfma_f32_16x16x32_bf16 v[20:23], a[4:7], a[84:87], v[20:23]\n"
    "v_mul_f32 v24, s87, v32\n"
    "v_mul_f32 v25, s87, v33\n"
    "v_mul_f32 v26, s87, v34\n"
    "v_mul_f32 v27, s87, v35\n"
    "v_mul_f32 v28, s87, v36\n"
    "v_mul_f32 v29, s87, v37\n"
    "v_mul_f32 v30, s87, v38\n"
    "v_mul_f32 v31, s87, v39\n"
    "v_exp_f32 v24, v24\n"
    "v_exp_f32 v25, v25\n"
    "v_exp_f32 v26, v26\n"
    "v_exp_f32 v27, v27\n"
    "v_exp_f32 v28, v28\n"
    "v_exp_f32 v29, v29\n"
    "v_exp_f32 v30, v30\n"
    "v_exp_f32 v31, v31\n"
    "v_fma_f32 v24, v24, s86, s86\n"
    "v_fma_f32 v25, v25, s86, s86\n"
    "v_fma_f32 v26, v26, s86, s86\n"
    "v_fma_f32 v27, v27, s86, s86\n"
    "v_fma_f32 v28, v28, s86, s86\n"
    "v_fma_f32 v29, v29, s86, s86\n"
    "v_fma_f32 v30, v30, s86, s86\n"
    "v_fma_f32 v31, v31, s86, s86\n"
    "v_rcp_f32 v24, v24\n"
    "v_rcp_f32 v25, v25\n"
    "v_rcp_f32 v26, v26\n"
    "v_rcp_f32 v27, v27\n"
    "v_rcp_f32 v28, v28\n"
    "v_rcp_f32 v29, v29\n"
    "v_rcp_f32 v30, v30\n"
    "v_rcp_f32 v31, v31\n"
    "v_mul_f32 v32, v32, v24\n"
    "v_mul_f32 v33, v33, v25\n"
    "v_mul_f32 v34, v34, v26\n"
    "v_mul_f32 v35, v35, v27\n"
    "v_mul_f32 v36, v36, v28\n"
    "v_mul_f32 v37, v37, v29\n"
    "v_mul_f32 v38, v38, v30\n"
    "v_mul_f32 v39, v39, v31\n"
    "v_mul_f32 v24, s87, v40\n"
    "v_mul_f32 v25, s87, v41\n"
    "v_mul_f32 v26, s87, v42\n"
    "v_mul_f32 v27, s87, v43\n"
    "v_mul_f32 v28, s87, v44\n"
    "v_mul_f32 v29, s87, v45\n"
    "v_mul_f32 v30, s87, v46\n"
    "v_mul_f32 v31, s87, v47\n"
    "v_exp_f32 v24, v24\n"
    "v_exp_f32 v25, v25\n"
    "v_exp_f32 v26, v26\n"
    "v_exp_f32 v27, v27\n"
    "v_exp_f32 v28, v28\n"
    "v_exp_f32 v29, v29\n"
    "v_exp_f32 v30, v30\n"
    "v_exp_f32 v31, v31\n"
    "v_fma_f32 v24, v24, s86, s86\n"
    "v_fma_f32 v25, v25, s86, s86\n"
    "v_fma_f32 v26, v26, s86, s86\n"
    "v_fma_f32 v27, v27, s86, s86\n"
    "v_fma_f32 v28, v28, s86, s86\n"
    "v_fma_f32 v29, v29, s86, s86\n"
    "v_fma_f32 v30, v30, s86, s86\n"
    "v_fma_f32 v31, v31, s86, s86\n"
    "v_rcp_f32 v24, v24\n"
    "v_rcp_f32 v25, v25\n"
    "v_rcp_f32 v26, v26\n"
    "v_rcp_f32 v27, v27\n"
    "v_rcp_f32 v28, v28\n"
    "v_rcp_f32 v29, v29\n"
    "v_rcp_f32 v30, v30\n"
    "v_rcp_f32 v31, v31\n"
    "v_mul_f32 v40, v40, v24\n"
    "v_mul_f32 v41, v41, v25\n"
    "v_mul_f32 v42, v42, v26\n"
    "v_mul_f32 v43, v43, v27\n"
    "v_mul_f32 v44, v44, v28\n"
    "v_mul_f32 v45, v45, v29\n"
    "v_mul_f32 v46, v46, v30\n"
    "v_mul_f32 v47, v47, v31\n"
    "v_mul_f32 v24, s87, v16\n"
    "v_mul_f32 v25, s87, v17\n"
    "v_mul_f32 v26, s87, v18\n"
    "v_mul_f32 v27, s87, v19\n"
    "v_mul_f32 v28, s87, v20\n"
    "v_mul_f32 v29, s87, v21\n"
    "v_mul_f32 v30, s87, v22\n"
    "v_mul_f32 v31, s87, v23\n"
    "v_exp_f32 v24, v24\n"
    "v_exp_f32 v25, v25\n"
    "v_exp_f32 v26, v26\n"
    "v_exp_f32 v27, v27\n"
    "v_exp_f32 v28, v28\n"
    "v_exp_f32 v29, v29\n"
    "v_exp_f32 v30, v30\n"
    "v_exp_f32 v31, v31\n"
    "v_fma_f32 v24, v24, s86, s86\n"
    "v_fma_f32 v25, v25, s86, s86\n"
    "v_fma_f32 v26, v26, s86, s86\n"
    "v_fma_f32 v27, v27, s86, s86\n"
    "v_fma_f32 v28, v28, s86, s86\n"
    "v_fma_f32 v29, v29, s86, s86\n"
    "v_fma_f32 v30, v30, s86, s86\n"
    "v_fma_f32 v31, v31, s86, s86\n"
    "v_rcp_f32 v24, v24\n"
    "v_rcp_f32 v25, v25\n"
    "v_rcp_f32 v26, v26\n"
    "v_rcp_f32 v27, v27\n"
    "v_rcp_f32 v28, v28\n"
    "v_rcp_f32 v29, v29\n"
    "v_rcp_f32 v30, v30\n"
    "v_rcp_f32 v31, v31\n"
    "v_mul_f32 v16, v16, v24\n"
    "v_mul_f32 v17, v17, v25\n"
    "v_mul_f32 v18, v18, v26\n"
    "v_mul_f32 v19, v19, v27\n"
    "v_mul_f32 v20, v20, v28\n"
    "v_mul_f32 v21, v21, v29\n"
    "v_mul_f32 v22, v22, v30\n"
    "v_mul_f32 v23, v23, v31\n"
    "s_nop 1\n"
    "v_mfma_f32_16x16x32_bf16 v[56:59], a[8:11], a[24:27], 0\n"
    "s_waitcnt vmcnt(18)\n"
    "v_mfma_f32_16x16x32_bf16 v[56:59], a[0:3], a[88:91], v[56:59]\n"
    "s_waitcnt vmcnt(17)\n"
    "v_mfma_f32_16x16x32_bf16 v[56:59], a[0:3], a[92:95], v[56:59]\n"
    "s_waitcnt vmcnt(16)\n"
    "v_mfma_f32_16x16x32_bf16 v[56:59], a[0:3], a[96:99], v[56:59]\n"
    "s_waitcnt vmcnt(15)\n"
    "v_mfma_f32_16x16x32_bf16 v[56:59], a[0:3], a[100:103], v[56:59]\n"
    "s_waitcnt vmcnt(14)\n"
    "v_mfma_f32_16x16x32_bf16 v[56:59], a[0:3], a[104:107], v[56:59]\n"
    "s_waitcnt vmcnt(13)\n"
    "v_mfma_f32_16x16x32_bf16 v[56:59], a[0:3], a[108:111], v[56:59]\n"
    "v_mfma_f32_16x16x32_bf16 v[60:63], a[12:15], a[24:27], 0\n"
    "v_mfma_f32_16x16x32_bf16 v[60:63], a[4:7], a[88:91], v[60:63]\n"
    "v_mfma_f32_16x16x32_bf16 v[60:63], a[4:7], a[92:95], v[60:63]\n"
    "v_mfma_f32_16x16x32_bf16 v[60:63], a[4:7], a[96:99], v[60:63]\n"
    "v_mfma_f32_16x16x32_bf16 v[60:63], a[4:7], a[100:103], v[60:63]\n"
    "v_mfma_f32_16x16x32_bf16 v[60:63], a[4:7], a[104:107], v[60:63]\n"
    "v_mfma_f32_16x16x32_bf16 v[60:63], a[4:7], a[108:111], v[60:63]\n"
    "v_fmac_f32 v64, v56, v32\n"
    "v_fmac_f32 v65, v57, v33\n"
    "v_fmac_f32 v66, v58, v34\n"
    "v_fmac_f32 v67, v59, v35\n"
    "s_nop 3\n"
    "v_fmac_f32 v68, v60, v36\n"
    "v_fmac_f32 v69, v61, v37\n"
    "v_fmac_f32 v70, v62, v38\n"
    "v_fmac_f32 v71, v63, v39\n"
    "v_cvt_pk_bf16_f32 v24, v64, v65\n"
    "v_cvt_pk_bf16_f32 v25, v66, v67\n"
    "v_cvt_pk_bf16_f32 v26, v68, v69\n"
    "v_cvt_pk_bf16_f32 v27, v70, v71\n"
    "buffer_store_dwordx4 v[24:27], v10, s[72:75], s36 offen offset:0 sc1\n"
    "v_mfma_f32_16x16x32_bf16 v[104:107], a[8:11], a[28:31], 0\n"
    "s_waitcnt vmcnt(13)\n"
    "v_mfma_f32_16x16x32_bf16 v[104:107], a[0:3], v[80:83], v[104:107]\n"
    "s_waitcnt vmcnt(12)\n"
    "v_mfma_f32_16x16x32_bf16 v[104:107], a[0:3], v[84:87], v[104:107]\n"
    "s_waitcnt vmcnt(11)\n"
    "v_mfma_f32_16x16x32_bf16 v[104:107], a[0:3], v[88:91], v[104:107]\n"
    "s_waitcnt vmcnt(10)\n"
    "v_mfma_f32_16x16x32_bf16 v[104:107], a[0:3], v[92:95], v[104:107]\n"
    "s_waitcnt vmcnt(9)\n"
    "v_mfma_f32_16x16x32_bf16 v[104:107], a[0:3], v[96:99], v[104:107]\n"
    "s_waitcnt vmcnt(8)\n"
    "v_mfma_f32_16x16x32_bf16 v[104:107], a[0:3], v[100:103], v[104:107]\n"
    "v_mfma_f32_16x16x32_bf16 v[108:111], a[12:15], a[28:31], 0\n"
    "v_mfma_f32_16x16x32_bf16 v[108:111], a[4:7], v[80:83], v[108:111]\n"
    "v_mfma_f32_16x16x32_bf16 v[108:111], a[4:7], v[84:87], v[108:111]\n"
    "v_mfma_f32_16x16x32_bf16 v[108:111], a[4:7], v[88:91], v[108:111]\n"
    "v_mfma_f32_16x16x32_bf16 v[108:111], a[4:7], v[92:95], v[108:111]\n"
    "v_mfma_f32_16x16x32_bf16 v[108:111], a[4:7], v[96:99], v[108:111]\n"
    "v_mfma_f32_16x16x32_bf16 v[108:111], a[4:7], v[100:103], v[108:111]\n"
    "v_fmac_f32 v72, v104, v40\n"
    "v_fmac_f32 v73, v105, v41\n"
    "v_fmac_f32 v74, v106, v42\n"
    "v_fmac_f32 v75, v107, v43\n"
    "s_nop 3\n"
    "v_fmac_f32 v76, v108, v44\n"
    "v_fmac_f32 v77, v109, v45\n"
    "v_fmac_f32 v78, v110, v46\n"
    "v_fmac_f32 v79, v111, v47\n"
    "v_cvt_pk_bf16_f32 v28, v72, v73\n"
    "v_cvt_pk_bf16_f32 v29, v74, v75\n"
    "v_cvt_pk_bf16_f32 v30, v76, v77\n"
    "v_cvt_pk_bf16_f32 v31, v78, v79\n"
    "buffer_store_dwordx4 v[28:31], v10, s[72:75], s37 offen offset:0 sc1\n"
    "v_mfma_f32_16x16x32_bf16 v[136:139], a[8:11], a[32:35], 0\n"
    "s_waitcnt vmcnt(8)\n"
    "v_mfma_f32_16x16x32_bf16 v[136:139], a[0:3], v[112:115], v[136:139]\n"
    "s_waitcnt vmcnt(7)\n"
    "v_mfma_f32_16x16x32_bf16 v[136:139], a[0:3], v[116:119], v[136:139]\n"
    "s_waitcnt vmcnt(6)\n"
    "v_mfma_f32_16x16x32_bf16 v[136:139], a[0:3], v[120:123], v[136:139]\n"
    "s_waitcnt vmcnt(5)\n"
    "v_mfma_f32_16x16x32_bf16 v[136:139], a[0:3], v[124:127], v[136:139]\n"
    "s_waitcnt vmcnt(4)\n"
    "v_mfma_f32_16x16x32_bf16 v[136:139], a[0:3], v[128:131], v[136:139]\n"
    "s_waitcnt vmcnt(3)\n"
    "v_mfma_f32_16x16x32_bf16 v[136:139], a[0:3], v[132:135], v[136:139]\n"
    "v_mfma_f32_16x16x32_bf16 v[140:143], a[12:15], a[32:35], 0\n"
    "v_mfma_f32_16x16x32_bf16 v[140:143], a[4:7], v[112:115], v[140:143]\n"
    "v_mfma_f32_16x16x32_bf16 v[140:143], a[4:7], v[116:119], v[140:143]\n"
    "v_mfma_f32_16x16x32_bf16 v[140:143], a[4:7], v[120:123], v[140:143]\n"
    "v_mfma_f32_16x16x32_bf16 v[140:143], a[4:7], v[124:127], v[140:143]\n"
    "v_mfma_f32_16x16x32_bf16 v[140:143], a[4:7], v[128:131], v[140:143]\n"
    "v_mfma_f32_16x16x32_bf16 v[140:143], a[4:7], v[132:135], v[140:143]\n"
    "v_fmac_f32 v48, v136, v16\n"
    "v_fmac_f32 v49, v137, v17\n"
    "v_fmac_f32 v50, v138, v18\n"
    "v_fmac_f32 v51, v139, v19\n"
    "s_nop 3\n"
    "v_fmac_f32 v52, v140, v20\n"
    "v_fmac_f32 v53, v141, v21\n"
    "v_fmac_f32 v54, v142, v22\n"
    "v_fmac_f32 v55, v143, v23\n"
    "v_cvt_pk_bf16_f32 v24, v48, v49\n"
    "v_cvt_pk_bf16_f32 v25, v50, v51\n"
    "v_cvt_pk_bf16_f32 v26, v52, v53\n"
    "v_cvt_pk_bf16_f32 v27, v54, v55\n"
    "buffer_store_dwordx4 v[24:27], v10, s[72:75], s38 offen offset:0 sc1\n"
    "s_endpgm\n"
    ".L_dend_%=:\n"
    "s_waitcnt vmcnt(0) lgkmcnt(0)\n"
    "v_mfma_f32_16x16x32_bf16 v[16:19], v[176:179], a[64:67], v[16:19]\n"
    "v_mfma_f32_16x16x32_bf16 v[16:19], v[180:183], a[68:71], v[16:19]\n"
    "v_mfma_f32_16x16x32_bf16 v[16:19], v[184:187], a[72:75], v[16:19]\n"
    "ds_read_b64_tr_b16 v[192:193], v9 offset:0\n"
    "v_mfma_f32_16x16x32_bf16 v[16:19], v[188:191], a[76:79], v[16:19]\n"
    "ds_read_b64_tr_b16 v[194:195], v9 offset:5120\n"
    "v_mfma_f32_16x16x32_bf16 v[20:23], v[176:179], a[80:83], v[20:23]\n"
    "ds_read_b64_tr_b16 v[196:197], v9 offset:10240\n"
    "v_mfma_f32_16x16x32_bf16 v[20:23], v[180:183], a[84:87], v[20:23]\n"
    "ds_read_b64_tr_b16 v[198:199], v9 offset:15360\n"
    "v_mfma_f32_16x16x32_bf16 v[20:23], v[184:187], a[88:91], v[20:23]\n"
    "ds_read_b64_tr_b16 v[200:201], v9 offset:20480\n"
    "v_mfma_f32_16x16x32_bf16 v[20:23], v[188:191], a[92:95], v[20:23]\n"
    "ds_read_b64_tr_b16 v[202:203], v9 offset:25600\n"
    "v_mfma_f32_16x16x32_bf16 v[24:27], v[176:179], a[96:99], v[24:27]\n"
    "ds_read_b64_tr_b16 v[204:205], v9 offset:30720\n"
    "v_mfma_f32_16x16x32_bf16 v[24:27], v[180:183], a[100:103], v[24:27]\n"
    "ds_read_b64_tr_b16 v[206:207], v9 offset:35840\n"
    "v_mfma_f32_16x16x32_bf16 v[24:27], v[184:187], a[104:107], v[24:27]\n"
    "v_mfma_f32_16x16x32_bf16 v[24:27], v[188:191], a[108:111], v[24:27]\n"
    "v_mfma_f32_16x16x32_bf16 v[28:31], v[176:179], a[112:115], v[28:31]\n"
    "v_mfma_f32_16x16x32_bf16 v[28:31], v[180:183], a[116:119], v[28:31]\n"
    "v_mfma_f32_16x16x32_bf16 v[28:31], v[184:187], a[120:123], v[28:31]\n"
    "v_mfma_f32_16x16x32_bf16 v[28:31], v[188:191], a[124:127], v[28:31]\n"
    "s_waitcnt lgkmcnt(4)\n"
    "v_mfma_f32_16x16x32_bf16 v[32:35], v[192:195], a[64:67], v[32:35]\n"
    "v_mfma_f32_16x16x32_bf16 v[32:35], v[196:199], a[68:71], v[32:35]\n"
    "s_waitcnt lgkmcnt(0)\n"
    "v_mfma_f32_16x16x32_bf16 v[32:35], v[200:203], a[72:75], v[32:35]\n"
    "ds_read_b64_tr_b16 v[176:177], v8 offset:64\n"
    "v_mfma_f32_16x16x32_bf16 v[32:35], v[204:207], a[76:79], v[32:35]\n"
    "ds_read_b64_tr_b16 v[178:179], v8 offset:5184\n"
    "v_mfma_f32_16x16x32_bf16 v[36:39], v[192:195], a[80:83], v[36:39]\n"
    "ds_read_b64_tr_b16 v[180:181], v8 offset:10304\n"
    "v_mfma_f32_16x16x32_bf16 v[36:39], v[196:199], a[84:87], v[36:39]\n"
    "ds_read_b64_tr_b16 v[182:183], v8 offset:15424\n"
    "v_mfma_f32_16x16x32_bf16 v[36:39], v[200:203], a[88:91], v[36:39]\n"
    "ds_read_b64_tr_b16 v[184:185], v8 offset:20544\n"
    "v_cvt_pk_bf16_f32 v16, v16, v17\n"
    "v_mfma_f32_16x16x32_bf16 v[36:39], v[204:207], a[92:95], v[36:39]\n"
    "ds_read_b64_tr_b16 v[186:187], v8 offset:25664\n"
    "v_cvt_pk_bf16_f32 v17, v18, v19\n"
    "v_mfma_f32_16x16x32_bf16 v[40:43], v[192:195], a[96:99], v[40:43]\n"
    "ds_read_b64_tr_b16 v[188:189], v8 offset:30784\n"
    "v_cvt_pk_bf16_f32 v18, v32, v33\n"
    "v_mfma_f32_16x16x32_bf16 v[40:43], v[196:199], a[100:103], v[40:43]\n"
    "ds_read_b64_tr_b16 v[190:191], v8 offset:35904\n"
    "v_cvt_pk_bf16_f32 v19, v34, v35\n"
    "v_mfma_f32_16x16x32_bf16 v[40:43], v[200:203], a[104:107], v[40:43]\n"
    "s_add_u32 s90, s79, 0\n"
    "ds_write_b128 v252, v[16:19] offset:0\n"
    "v_mfma_f32_16x16x32_bf16 v[40:43], v[204:207], a[108:111], v[40:43]\n"
    "buffer_store_dwordx4 v[16:19], v0, s[40:43], s90 offen offset:0 sc1\n"
    "v_mfma_f32_16x16x32_bf16 v[44:47], v[192:195], a[112:115], v[44:47]\n"
    "v_cvt_pk_bf16_f32 v20, v20, v21\n"
    "v_cvt_pk_bf16_f32 v21, v22, v23\n"
    "v_mfma_f32_16x16x32_bf16 v[44:47], v[196:199], a[116:119], v[44:47]\n"
    "v_cvt_pk_bf16_f32 v22, v36, v37\n"
    "v_cvt_pk_bf16_f32 v23, v38, v39\n"
    "v_mfma_f32_16x16x32_bf16 v[44:47], v[200:203], a[120:123], v[44:47]\n"
    "s_add_u32 s90, s80, 0\n"
    "ds_write_b128 v252, v[20:23] offset:5120\n"
    "v_mfma_f32_16x16x32_bf16 v[44:47], v[204:207], a[124:127], v[44:47]\n"
    "s_waitcnt lgkmcnt(6)\n"
    "v_mfma_f32_16x16x32_bf16 v[48:51], v[176:179], a[64:67], v[48:51]\n"
    "buffer_store_dwordx4 v[20:23], v0, s[40:43], s90 offen offset:0 sc1\n"
    "v_mfma_f32_16x16x32_bf16 v[48:51], v[180:183], a[68:71], v[48:51]\n"
    "s_waitcnt lgkmcnt(2)\n"
    "v_cvt_pk_bf16_f32 v24, v24, v25\n"
    "v_cvt_pk_bf16_f32 v25, v26, v27\n"
    "v_mfma_f32_16x16x32_bf16 v[48:51], v[184:187], a[72:75], v[48:51]\n"
    "ds_read_b64_tr_b16 v[192:193], v9 offset:64\n"
    "v_cvt_pk_bf16_f32 v26, v40, v41\n"
    "v_mfma_f32_16x16x32_bf16 v[48:51], v[188:191], a[76:79], v[48:51]\n"
    "ds_read_b64_tr_b16 v[194:195], v9 offset:5184\n"
    "v_cvt_pk_bf16_f32 v27, v42, v43\n"
    "v_mfma_f32_16x16x32_bf16 v[52:55], v[176:179], a[80:83], v[52:55]\n"
    "s_add_u32 s90, s82, 0\n"
    "ds_read_b64_tr_b16 v[196:197], v9 offset:10304\n"
    "ds_write_b128 v253, v[24:27] offset:0\n"
    "v_mfma_f32_16x16x32_bf16 v[52:55], v[180:183], a[84:87], v[52:55]\n"
    "buffer_store_dwordx4 v[24:27], v0, s[40:43], s90 offen offset:0 sc1\n"
    "v_mfma_f32_16x16x32_bf16 v[52:55], v[184:187], a[88:91], v[52:55]\n"
    "ds_read_b64_tr_b16 v[198:199], v9 offset:15424\n"
    "ds_read_b64_tr_b16 v[200:201], v9 offset:20544\n"
    "v_mfma_f32_16x16x32_bf16 v[52:55], v[188:191], a[92:95], v[52:55]\n"
    "ds_read_b64_tr_b16 v[202:203], v9 offset:25664\n"
    "v_cvt_pk_bf16_f32 v28, v28, v29\n"
    "v_mfma_f32_16x16x32_bf16 v[56:59], v[176:179], a[96:99], v[56:59]\n"
    "ds_read_b64_tr_b16 v[204:205], v9 offset:30784\n"
    "v_cvt_pk_bf16_f32 v29, v30, v31\n"
    "v_mfma_f32_16x16x32_bf16 v[56:59], v[180:183], a[100:103], v[56:59]\n"
    "ds_read_b64_tr_b16 v[206:207], v9 offset:35904\n"
    "v_cvt_pk_bf16_f32 v30, v44, v45\n"
    "v_mfma_f32_16x16x32_bf16 v[56:59], v[184:187], a[104:107], v[56:59]\n"
    "v_cvt_pk_bf16_f32 v31, v46, v47\n"
    "s_add_u32 s90, s83, 0\n"
    "v_mfma_f32_16x16x32_bf16 v[56:59], v[188:191], a[108:111], v[56:59]\n"
    "ds_write_b128 v253, v[28:31] offset:5120\n"
    "v_mfma_f32_16x16x32_bf16 v[60:63], v[176:179], a[112:115], v[60:63]\n"
    "buffer_store_dwordx4 v[28:31], v0, s[40:43], s90 offen offset:0 sc1\n"
    "v_mfma_f32_16x16x32_bf16 v[60:63], v[180:183], a[116:119], v[60:63]\n"
    "v_mfma_f32_16x16x32_bf16 v[60:63], v[184:187], a[120:123], v[60:63]\n"
    "v_mfma_f32_16x16x32_bf16 v[60:63], v[188:191], a[124:127], v[60:63]\n"
    "s_waitcnt lgkmcnt(5)\n"
    "v_mfma_f32_16x16x32_bf16 v[64:67], v[192:195], a[64:67], v[64:67]\n"
    "v_mfma_f32_16x16x32_bf16 v[64:67], v[196:199], a[68:71], v[64:67]\n"
    "s_waitcnt lgkmcnt(1)\n"
    "v_mfma_f32_16x16x32_bf16 v[64:67], v[200:203], a[72:75], v[64:67]\n"
    "ds_read_b64_tr_b16 v[176:177], v8 offset:128\n"
    "v_mfma_f32_16x16x32_bf16 v[64:67], v[204:207], a[76:79], v[64:67]\n"
    "ds_read_b64_tr_b16 v[178:179], v8 offset:5248\n"
    "v_mfma_f32_16x16x32_bf16 v[68:71], v[192:195], a[80:83], v[68:71]\n"
    "ds_read_b64_tr_b16 v[180:181], v8 offset:10368\n"
    "v_mfma_f32_16x16x32_bf16 v[68:71], v[196:199], a[84:87], v[68:71]\n"
    "ds_read_b64_tr_b16 v[182:183], v8 offset:15488\n"
    "v_mfma_f32_16x16x32_bf16 v[68:71], v[200:203], a[88:91], v[68:71]\n"
    "ds_read_b64_tr_b16 v[184:185], v8 offset:20608\n"
    "v_cvt_pk_bf16_f32 v48, v48, v49\n"
    "v_mfma_f32_16x16x32_bf16 v[68:71], v[204:207], a[92:95], v[68:71]\n"
    "ds_read_b64_tr_b16 v[186:187], v8 offset:25728\n"
    "v_cvt_pk_bf16_f32 v49, v50, v51\n"
    "v_mfma_f32_16x16x32_bf16 v[72:75], v[192:195], a[96:99], v[72:75]\n"
    "ds_read_b64_tr_b16 v[188:189], v8 offset:30848\n"
    "v_cvt_pk_bf16_f32 v50, v64, v65\n"
    "v_mfma_f32_16x16x32_bf16 v[72:75], v[196:199], a[100:103], v[72:75]\n"
    "ds_read_b64_tr_b16 v[190:191], v8 offset:35968\n"
    "v_cvt_pk_bf16_f32 v51, v66, v67\n"
    "v_mfma_f32_16x16x32_bf16 v[72:75], v[200:203], a[104:107], v[72:75]\n"
    "s_add_u32 s90, s79, 1024\n"
    "ds_write_b128 v252, v[48:51] offset:1024\n"
    "v_mfma_f32_16x16x32_bf16 v[72:75], v[204:207], a[108:111], v[72:75]\n"
    "buffer_store_dwordx4 v[48:51], v0, s[40:43], s90 offen offset:0 sc1\n"
    "v_mfma_f32_16x16x32_bf16 v[76:79], v[192:195], a[112:115], v[76:79]\n"
    "v_cvt_pk_bf16_f32 v52, v52, v53\n"
    "v_cvt_pk_bf16_f32 v53, v54, v55\n"
    "v_mfma_f32_16x16x32_bf16 v[76:79], v[196:199], a[116:119], v[76:79]\n"
    "v_cvt_pk_bf16_f32 v54, v68, v69\n"
    "v_cvt_pk_bf16_f32 v55, v70, v71\n"
    "v_mfma_f32_16x16x32_bf16 v[76:79], v[200:203], a[120:123], v[76:79]\n"
    "s_add_u32 s90, s80, 1024\n"
    "ds_write_b128 v252, v[52:55] offset:6144\n"
    "v_mfma_f32_16x16x32_bf16 v[76:79], v[204:207], a[124:127], v[76:79]\n"
    "s_waitcnt lgkmcnt(6)\n"
    "v_mfma_f32_16x16x32_bf16 v[80:83], v[176:179], a[64:67], v[80:83]\n"
    "buffer_store_dwordx4 v[52:55], v0, s[40:43], s90 offen offset:0 sc1\n"
    "v_mfma_f32_16x16x32_bf16 v[80:83], v[180:183], a[68:71], v[80:83]\n"
    "s_waitcnt lgkmcnt(2)\n"
    "v_cvt_pk_bf16_f32 v56, v56, v57\n"
    "v_cvt_pk_bf16_f32 v57, v58, v59\n"
    "v_mfma_f32_16x16x32_bf16 v[80:83], v[184:187], a[72:75], v[80:83]\n"
    "ds_read_b64_tr_b16 v[192:193], v9 offset:128\n"
    "v_cvt_pk_bf16_f32 v58, v72, v73\n"
    "v_mfma_f32_16x16x32_bf16 v[80:83], v[188:191], a[76:79], v[80:83]\n"
    "ds_read_b64_tr_b16 v[194:195], v9 offset:5248\n"
    "v_cvt_pk_bf16_f32 v59, v74, v75\n"
    "v_mfma_f32_16x16x32_bf16 v[84:87], v[176:179], a[80:83], v[84:87]\n"
    "s_add_u32 s90, s82, 1024\n"
    "ds_read_b64_tr_b16 v[196:197], v9 offset:10368\n"
    "ds_write_b128 v253, v[56:59] offset:1024\n"
    "v_mfma_f32_16x16x32_bf16 v[84:87], v[180:183], a[84:87], v[84:87]\n"
    "buffer_store_dwordx4 v[56:59], v0, s[40:43], s90 offen offset:0 sc1\n"
    "v_mfma_f32_16x16x32_bf16 v[84:87], v[184:187], a[88:91], v[84:87]\n"
    "ds_read_b64_tr_b16 v[198:199], v9 offset:15488\n"
    "ds_read_b64_tr_b16 v[200:201], v9 offset:20608\n"
    "v_mfma_f32_16x16x32_bf16 v[84:87], v[188:191], a[92:95], v[84:87]\n"
    "ds_read_b64_tr_b16 v[202:203], v9 offset:25728\n"
    "v_cvt_pk_bf16_f32 v60, v60, v61\n"
    "v_mfma_f32_16x16x32_bf16 v[88:91], v[176:179], a[96:99], v[88:91]\n"
    "ds_read_b64_tr_b16 v[204:205], v9 offset:30848\n"
    "v_cvt_pk_bf16_f32 v61, v62, v63\n"
    "v_mfma_f32_16x16x32_bf16 v[88:91], v[180:183], a[100:103], v[88:91]\n"
    "ds_read_b64_tr_b16 v[206:207], v9 offset:35968\n"
    "v_cvt_pk_bf16_f32 v62, v76, v77\n"
    "v_mfma_f32_16x16x32_bf16 v[88:91], v[184:187], a[104:107], v[88:91]\n"
    "v_cvt_pk_bf16_f32 v63, v78, v79\n"
    "s_add_u32 s90, s83, 1024\n"
    "v_mfma_f32_16x16x32_bf16 v[88:91], v[188:191], a[108:111], v[88:91]\n"
    "ds_write_b128 v253, v[60:63] offset:6144\n"
    "v_mfma_f32_16x16x32_bf16 v[92:95], v[176:179], a[112:115], v[92:95]\n"
    "buffer_store_dwordx4 v[60:63], v0, s[40:43], s90 offen offset:0 sc1\n"
    "v_mfma_f32_16x16x32_bf16 v[92:95], v[180:183], a[116:119], v[92:95]\n"
    "v_mfma_f32_16x16x32_bf16 v[92:95], v[184:187], a[120:123], v[92:95]\n"
    "v_mfma_f32_16x16x32_bf16 v[92:95], v[188:191], a[124:127], v[92:95]\n"
    "s_waitcnt lgkmcnt(5)\n"
    "v_mfma_f32_16x16x32_bf16 v[96:99], v[192:195], a[64:67], v[96:99]\n"
    "v_mfma_f32_16x16x32_bf16 v[96:99], v[196:199], a[68:71], v[96:99]\n"
    "s_waitcnt lgkmcnt(1)\n"
    "v_mfma_f32_16x16x32_bf16 v[96:99], v[200:203], a[72:75], v[96:99]\n"
    "ds_read_b64_tr_b16 v[176:177], v8 offset:192\n"
    "v_mfma_f32_16x16x32_bf16 v[96:99], v[204:207], a[76:79], v[96:99]\n"
    "ds_read_b64_tr_b16 v[178:179], v8 offset:5312\n"
    "v_mfma_f32_16x16x32_bf16 v[100:103], v[192:195], a[80:83], v[100:103]\n"
    "ds_read_b64_tr_b16 v[180:181], v8 offset:10432\n"
    "v_mfma_f32_16x16x32_bf16 v[100:103], v[196:199], a[84:87], v[100:103]\n"
    "ds_read_b64_tr_b16 v[182:183], v8 offset:15552\n"
    "v_mfma_f32_16x16x32_bf16 v[100:103], v[200:203], a[88:91], v[100:103]\n"
    "ds_read_b64_tr_b16 v[184:185], v8 offset:20672\n"
    "v_cvt_pk_bf16_f32 v80, v80, v81\n"
    "v_mfma_f32_16x16x32_bf16 v[100:103], v[204:207], a[92:95], v[100:103]\n"
    "ds_read_b64_tr_b16 v[186:187], v8 offset:25792\n"
    "v_cvt_pk_bf16_f32 v81, v82, v83\n"
    "v_mfma_f32_16x16x32_bf16 v[104:107], v[192:195], a[96:99], v[104:107]\n"
    "ds_read_b64_tr_b16 v[188:189], v8 offset:30912\n"
    "v_cvt_pk_bf16_f32 v82, v96, v97\n"
    "v_mfma_f32_16x16x32_bf16 v[104:107], v[196:199], a[100:103], v[104:107]\n"
    "ds_read_b64_tr_b16 v[190:191], v8 offset:36032\n"
    "v_cvt_pk_bf16_f32 v83, v98, v99\n"
    "v_mfma_f32_16x16x32_bf16 v[104:107], v[200:203], a[104:107], v[104:107]\n"
    "s_add_u32 s90, s79, 2048\n"
    "ds_write_b128 v252, v[80:83] offset:2048\n"
    "v_mfma_f32_16x16x32_bf16 v[104:107], v[204:207], a[108:111], v[104:107]\n"
    "buffer_store_dwordx4 v[80:83], v0, s[40:43], s90 offen offset:0 sc1\n"
    "v_mfma_f32_16x16x32_bf16 v[108:111], v[192:195], a[112:115], v[108:111]\n"
    "v_cvt_pk_bf16_f32 v84, v84, v85\n"
    "v_cvt_pk_bf16_f32 v85, v86, v87\n"
    "v_mfma_f32_16x16x32_bf16 v[108:111], v[196:199], a[116:119], v[108:111]\n"
    "v_cvt_pk_bf16_f32 v86, v100, v101\n"
    "v_cvt_pk_bf16_f32 v87, v102, v103\n"
    "v_mfma_f32_16x16x32_bf16 v[108:111], v[200:203], a[120:123], v[108:111]\n"
    "s_add_u32 s90, s80, 2048\n"
    "ds_write_b128 v252, v[84:87] offset:7168\n"
    "v_mfma_f32_16x16x32_bf16 v[108:111], v[204:207], a[124:127], v[108:111]\n"
    "s_waitcnt lgkmcnt(6)\n"
    "v_mfma_f32_16x16x32_bf16 v[112:115], v[176:179], a[64:67], v[112:115]\n"
    "buffer_store_dwordx4 v[84:87], v0, s[40:43], s90 offen offset:0 sc1\n"
    "v_mfma_f32_16x16x32_bf16 v[112:115], v[180:183], a[68:71], v[112:115]\n"
    "s_waitcnt lgkmcnt(2)\n"
    "v_cvt_pk_bf16_f32 v88, v88, v89\n"
    "v_cvt_pk_bf16_f32 v89, v90, v91\n"
    "v_mfma_f32_16x16x32_bf16 v[112:115], v[184:187], a[72:75], v[112:115]\n"
    "ds_read_b64_tr_b16 v[192:193], v9 offset:192\n"
    "v_cvt_pk_bf16_f32 v90, v104, v105\n"
    "v_mfma_f32_16x16x32_bf16 v[112:115], v[188:191], a[76:79], v[112:115]\n"
    "ds_read_b64_tr_b16 v[194:195], v9 offset:5312\n"
    "v_cvt_pk_bf16_f32 v91, v106, v107\n"
    "v_mfma_f32_16x16x32_bf16 v[116:119], v[176:179], a[80:83], v[116:119]\n"
    "s_add_u32 s90, s82, 2048\n"
    "ds_read_b64_tr_b16 v[196:197], v9 offset:10432\n"
    "ds_write_b128 v253, v[88:91] offset:2048\n"
    "v_mfma_f32_16x16x32_bf16 v[116:119], v[180:183], a[84:87], v[116:119]\n"
    "buffer_store_dwordx4 v[88:91], v0, s[40:43], s90 offen offset:0 sc1\n"
    "v_mfma_f32_16x16x32_bf16 v[116:119], v[184:187], a[88:91], v[116:119]\n"
    "ds_read_b64_tr_b16 v[198:199], v9 offset:15552\n"
    "ds_read_b64_tr_b16 v[200:201], v9 offset:20672\n"
    "v_mfma_f32_16x16x32_bf16 v[116:119], v[188:191], a[92:95], v[116:119]\n"
    "ds_read_b64_tr_b16 v[202:203], v9 offset:25792\n"
    "v_cvt_pk_bf16_f32 v92, v92, v93\n"
    "v_mfma_f32_16x16x32_bf16 v[120:123], v[176:179], a[96:99], v[120:123]\n"
    "ds_read_b64_tr_b16 v[204:205], v9 offset:30912\n"
    "v_cvt_pk_bf16_f32 v93, v94, v95\n"
    "v_mfma_f32_16x16x32_bf16 v[120:123], v[180:183], a[100:103], v[120:123]\n"
    "ds_read_b64_tr_b16 v[206:207], v9 offset:36032\n"
    "v_cvt_pk_bf16_f32 v94, v108, v109\n"
    "v_mfma_f32_16x16x32_bf16 v[120:123], v[184:187], a[104:107], v[120:123]\n"
    "v_cvt_pk_bf16_f32 v95, v110, v111\n"
    "s_add_u32 s90, s83, 2048\n"
    "v_mfma_f32_16x16x32_bf16 v[120:123], v[188:191], a[108:111], v[120:123]\n"
    "ds_write_b128 v253, v[92:95] offset:7168\n"
    "v_mfma_f32_16x16x32_bf16 v[124:127], v[176:179], a[112:115], v[124:127]\n"
    "buffer_store_dwordx4 v[92:95], v0, s[40:43], s90 offen offset:0 sc1\n"
    "v_mfma_f32_16x16x32_bf16 v[124:127], v[180:183], a[116:119], v[124:127]\n"
    "v_mfma_f32_16x16x32_bf16 v[124:127], v[184:187], a[120:123], v[124:127]\n"
    "v_mfma_f32_16x16x32_bf16 v[124:127], v[188:191], a[124:127], v[124:127]\n"
    "s_waitcnt lgkmcnt(5)\n"
    "v_mfma_f32_16x16x32_bf16 v[128:131], v[192:195], a[64:67], v[128:131]\n"
    "v_mfma_f32_16x16x32_bf16 v[128:131], v[196:199], a[68:71], v[128:131]\n"
    "s_waitcnt lgkmcnt(1)\n"
    "v_mfma_f32_16x16x32_bf16 v[128:131], v[200:203], a[72:75], v[128:131]\n"
    "ds_read_b64_tr_b16 v[176:177], v8 offset:256\n"
    "v_mfma_f32_16x16x32_bf16 v[128:131], v[204:207], a[76:79], v[128:131]\n"
    "ds_read_b64_tr_b16 v[178:179], v8 offset:5376\n"
    "v_mfma_f32_16x16x32_bf16 v[132:135], v[192:195], a[80:83], v[132:135]\n"
    "ds_read_b64_tr_b16 v[180:181], v8 offset:10496\n"
    "v_mfma_f32_16x16x32_bf16 v[132:135], v[196:199], a[84:87], v[132:135]\n"
    "ds_read_b64_tr_b16 v[182:183], v8 offset:15616\n"
    "v_mfma_f32_16x16x32_bf16 v[132:135], v[200:203], a[88:91], v[132:135]\n"
    "ds_read_b64_tr_b16 v[184:185], v8 offset:20736\n"
    "v_cvt_pk_bf16_f32 v112, v112, v113\n"
    "v_mfma_f32_16x16x32_bf16 v[132:135], v[204:207], a[92:95], v[132:135]\n"
    "ds_read_b64_tr_b16 v[186:187], v8 offset:25856\n"
    "v_cvt_pk_bf16_f32 v113, v114, v115\n"
    "v_mfma_f32_16x16x32_bf16 v[136:139], v[192:195], a[96:99], v[136:139]\n"
    "ds_read_b64_tr_b16 v[188:189], v8 offset:30976\n"
    "v_cvt_pk_bf16_f32 v114, v128, v129\n"
    "v_mfma_f32_16x16x32_bf16 v[136:139], v[196:199], a[100:103], v[136:139]\n"
    "ds_read_b64_tr_b16 v[190:191], v8 offset:36096\n"
    "v_cvt_pk_bf16_f32 v115, v130, v131\n"
    "v_mfma_f32_16x16x32_bf16 v[136:139], v[200:203], a[104:107], v[136:139]\n"
    "s_add_u32 s90, s79, 3072\n"
    "ds_write_b128 v252, v[112:115] offset:3072\n"
    "v_mfma_f32_16x16x32_bf16 v[136:139], v[204:207], a[108:111], v[136:139]\n"
    "buffer_store_dwordx4 v[112:115], v0, s[40:43], s90 offen offset:0 sc1\n"
    "v_mfma_f32_16x16x32_bf16 v[140:143], v[192:195], a[112:115], v[140:143]\n"
    "v_cvt_pk_bf16_f32 v116, v116, v117\n"
    "v_cvt_pk_bf16_f32 v117, v118, v119\n"
    "v_mfma_f32_16x16x32_bf16 v[140:143], v[196:199], a[116:119], v[140:143]\n"
    "v_cvt_pk_bf16_f32 v118, v132, v133\n"
    "v_cvt_pk_bf16_f32 v119, v134, v135\n"
    "v_mfma_f32_16x16x32_bf16 v[140:143], v[200:203], a[120:123], v[140:143]\n"
    "s_add_u32 s90, s80, 3072\n"
    "ds_write_b128 v252, v[116:119] offset:8192\n"
    "v_mfma_f32_16x16x32_bf16 v[140:143], v[204:207], a[124:127], v[140:143]\n"
    "s_waitcnt lgkmcnt(6)\n"
    "v_mfma_f32_16x16x32_bf16 v[144:147], v[176:179], a[64:67], v[144:147]\n"
    "buffer_store_dwordx4 v[116:119], v0, s[40:43], s90 offen offset:0 sc1\n"
    "v_mfma_f32_16x16x32_bf16 v[144:147], v[180:183], a[68:71], v[144:147]\n"
    "s_waitcnt lgkmcnt(2)\n"
    "v_cvt_pk_bf16_f32 v120, v120, v121\n"
    "v_cvt_pk_bf16_f32 v121, v122, v123\n"
    "v_mfma_f32_16x16x32_bf16 v[144:147], v[184:187], a[72:75], v[144:147]\n"
    "ds_read_b64_tr_b16 v[192:193], v9 offset:256\n"
    "v_cvt_pk_bf16_f32 v122, v136, v137\n"
    "v_mfma_f32_16x16x32_bf16 v[144:147], v[188:191], a[76:79], v[144:147]\n"
    "ds_read_b64_tr_b16 v[194:195], v9 offset:5376\n"
    "v_cvt_pk_bf16_f32 v123, v138, v139\n"
    "v_mfma_f32_16x16x32_bf16 v[148:151], v[176:179], a[80:83], v[148:151]\n"
    "s_add_u32 s90, s82, 3072\n"
    "ds_read_b64_tr_b16 v[196:197], v9 offset:10496\n"
    "ds_write_b128 v253, v[120:123] offset:3072\n"
    "v_mfma_f32_16x16x32_bf16 v[148:151], v[180:183], a[84:87], v[148:151]\n"
    "buffer_store_dwordx4 v[120:123], v0, s[40:43], s90 offen offset:0 sc1\n"
    "v_mfma_f32_16x16x32_bf16 v[148:151], v[184:187], a[88:91], v[148:151]\n"
    "ds_read_b64_tr_b16 v[198:199], v9 offset:15616\n"
    "ds_read_b64_tr_b16 v[200:201], v9 offset:20736\n"
    "v_mfma_f32_16x16x32_bf16 v[148:151], v[188:191], a[92:95], v[148:151]\n"
    "ds_read_b64_tr_b16 v[202:203], v9 offset:25856\n"
    "v_cvt_pk_bf16_f32 v124, v124, v125\n"
    "v_mfma_f32_16x16x32_bf16 v[152:155], v[176:179], a[96:99], v[152:155]\n"
    "ds_read_b64_tr_b16 v[204:205], v9 offset:30976\n"
    "v_cvt_pk_bf16_f32 v125, v126, v127\n"
    "v_mfma_f32_16x16x32_bf16 v[152:155], v[180:183], a[100:103], v[152:155]\n"
    "ds_read_b64_tr_b16 v[206:207], v9 offset:36096\n"
    "v_cvt_pk_bf16_f32 v126, v140, v141\n"
    "v_mfma_f32_16x16x32_bf16 v[152:155], v[184:187], a[104:107], v[152:155]\n"
    "v_cvt_pk_bf16_f32 v127, v142, v143\n"
    "s_add_u32 s90, s83, 3072\n"
    "v_mfma_f32_16x16x32_bf16 v[152:155], v[188:191], a[108:111], v[152:155]\n"
    "ds_write_b128 v253, v[124:127] offset:8192\n"
    "v_mfma_f32_16x16x32_bf16 v[156:159], v[176:179], a[112:115], v[156:159]\n"
    "buffer_store_dwordx4 v[124:127], v0, s[40:43], s90 offen offset:0 sc1\n"
    "v_mfma_f32_16x16x32_bf16 v[156:159], v[180:183], a[116:119], v[156:159]\n"
    "v_mfma_f32_16x16x32_bf16 v[156:159], v[184:187], a[120:123], v[156:159]\n"
    "v_mfma_f32_16x16x32_bf16 v[156:159], v[188:191], a[124:127], v[156:159]\n"
    "s_waitcnt lgkmcnt(5)\n"
    "v_mfma_f32_16x16x32_bf16 v[160:163], v[192:195], a[64:67], v[160:163]\n"
    "v_mfma_f32_16x16x32_bf16 v[160:163], v[196:199], a[68:71], v[160:163]\n"
    "s_waitcnt lgkmcnt(1)\n"
    "v_mfma_f32_16x16x32_bf16 v[160:163], v[200:203], a[72:75], v[160:163]\n"
    "v_mfma_f32_16x16x32_bf16 v[160:163], v[204:207], a[76:79], v[160:163]\n"
    "v_mfma_f32_16x16x32_bf16 v[164:167], v[192:195], a[80:83], v[164:167]\n"
    "v_mfma_f32_16x16x32_bf16 v[164:167], v[196:199], a[84:87], v[164:167]\n"
    "v_mfma_f32_16x16x32_bf16 v[164:167], v[200:203], a[88:91], v[164:167]\n"
    "v_cvt_pk_bf16_f32 v144, v144, v145\n"
    "v_cvt_pk_bf16_f32 v145, v146, v147\n"
    "v_mfma_f32_16x16x32_bf16 v[164:167], v[204:207], a[92:95], v[164:167]\n"
    "v_cvt_pk_bf16_f32 v146, v160, v161\n"
    "v_cvt_pk_bf16_f32 v147, v162, v163\n"
    "v_mfma_f32_16x16x32_bf16 v[168:171], v[192:195], a[96:99], v[168:171]\n"
    "s_add_u32 s90, s79, 4096\n"
    "ds_write_b128 v252, v[144:147] offset:4096\n"
    "v_mfma_f32_16x16x32_bf16 v[168:171], v[196:199], a[100:103], v[168:171]\n"
    "buffer_store_dwordx4 v[144:147], v0, s[40:43], s90 offen offset:0 sc1\n"
    "v_mfma_f32_16x16x32_bf16 v[168:171], v[200:203], a[104:107], v[168:171]\n"
    "v_cvt_pk_bf16_f32 v148, v148, v149\n"
    "v_cvt_pk_bf16_f32 v149, v150, v151\n"
    "v_mfma_f32_16x16x32_bf16 v[168:171], v[204:207], a[108:111], v[168:171]\n"
    "v_cvt_pk_bf16_f32 v150, v164, v165\n"
    "v_cvt_pk_bf16_f32 v151, v166, v167\n"
    "v_mfma_f32_16x16x32_bf16 v[172:175], v[192:195], a[112:115], v[172:175]\n"
    "s_add_u32 s90, s80, 4096\n"
    "ds_write_b128 v252, v[148:151] offset:9216\n"
    "v_mfma_f32_16x16x32_bf16 v[172:175], v[196:199], a[116:119], v[172:175]\n"
    "buffer_store_dwordx4 v[148:151], v0, s[40:43], s90 offen offset:0 sc1\n"
    "v_mfma_f32_16x16x32_bf16 v[172:175], v[200:203], a[120:123], v[172:175]\n"
    "v_cvt_pk_bf16_f32 v152, v152, v153\n"
    "v_cvt_pk_bf16_f32 v153, v154, v155\n"
    "v_mfma_f32_16x16x32_bf16 v[172:175], v[204:207], a[124:127], v[172:175]\n"
    "v_cvt_pk_bf16_f32 v154, v168, v169\n"
    "v_cvt_pk_bf16_f32 v155, v170, v171\n"
    "s_add_u32 s90, s82, 4096\n"
    "ds_write_b128 v253, v[152:155] offset:4096\n"
    "buffer_store_dwordx4 v[152:155], v0, s[40:43], s90 offen offset:0 sc1\n"
    "v_cvt_pk_bf16_f32 v156, v156, v157\n"
    "v_cvt_pk_bf16_f32 v157, v158, v159\n"
    "s_nop 0\n"
    "v_cvt_pk_bf16_f32 v158, v172, v173\n"
    "v_cvt_pk_bf16_f32 v159, v174, v175\n"
    "s_add_u32 s90, s83, 4096\n"
    "ds_write_b128 v253, v[156:159] offset:9216\n"
    "buffer_store_dwordx4 v[156:159], v0, s[40:43], s90 offen offset:0 sc1\n"
    "s_add_u32 s94, s97, 5\n"
    "v_accvgpr_write_b32 a53, s94\n"
    "s_waitcnt vmcnt(0)\n"
    "buffer_store_dwordx2 a[52:53], v7, s[40:43], s28 offen offset:0 sc1\n"
    "s_mov_b32 s86, 0x3c319c2f\n"
    "s_mov_b32 s87, 0xc3051595\n"
    "v_or_b32 v246, 384, v15\n"
    "buffer_load_dwordx4 a[112:115], v10, s[68:71], s36 offen offset:0\n"
    "buffer_load_dwordx4 a[116:119], v10, s[68:71], s37 offen offset:0\n"
    "buffer_load_dwordx4 a[120:123], v10, s[68:71], s38 offen offset:0\n"
    "s_waitcnt vmcnt(2)\n"
    "v_accvgpr_read_b32 v64, a112\n"
    "v_accvgpr_read_b32 v66, a113\n"
    "v_accvgpr_read_b32 v68, a114\n"
    "v_accvgpr_read_b32 v70, a115\n"
    "v_and_b32 v65, 0xffff0000, v64\n"
    "v_lshlrev_b32 v64, 16, v64\n"
    "v_and_b32 v67, 0xffff0000, v66\n"
    "v_lshlrev_b32 v66, 16, v66\n"
    "v_and_b32 v69, 0xffff0000, v68\n"
    "v_lshlrev_b32 v68, 16, v68\n"
    "v_and_b32 v71, 0xffff0000, v70\n"
    "v_lshlrev_b32 v70, 16, v70\n"
    "s_waitcnt vmcnt(1)\n"
    "v_accvgpr_read_b32 v72, a116\n"
    "v_accvgpr_read_b32 v74, a117\n"
    "v_accvgpr_read_b32 v76, a118\n"
    "v_accvgpr_read_b32 v78, a119\n"
    "v_and_b32 v73, 0xffff0000, v72\n"
    "v_lshlrev_b32 v72, 16, v72\n"
    "v_and_b32 v75, 0xffff0000, v74\n"
    "v_lshlrev_b32 v74, 16, v74\n"
    "v_and_b32 v77, 0xffff0000, v76\n"
    "v_lshlrev_b32 v76, 16, v76\n"
    "v_and_b32 v79, 0xffff0000, v78\n"
    "v_lshlrev_b32 v78, 16, v78\n"
    "s_waitcnt vmcnt(0)\n"
    "v_accvgpr_read_b32 v48, a120\n"
    "v_accvgpr_read_b32 v50, a121\n"
    "v_accvgpr_read_b32 v52, a122\n"
    "v_accvgpr_read_b32 v54, a123\n"
    "v_and_b32 v49, 0xffff0000, v48\n"
    "v_lshlrev_b32 v48, 16, v48\n"
    "v_and_b32 v51, 0xffff0000, v50\n"
    "v_lshlrev_b32 v50, 16, v50\n"
    "v_and_b32 v53, 0xffff0000, v52\n"
    "v_lshlrev_b32 v52, 16, v52\n"
    "v_and_b32 v55, 0xffff0000, v54\n"
    "v_lshlrev_b32 v54, 16, v54\n"
    "buffer_load_dwordx2 v[250:251], v15, s[40:43], s29 offen offset:0 sc1\n"
    "s_waitcnt vmcnt(0) lgkmcnt(0)\n"
    "s_branch .L_fin_%=\n"
    ".L_spin7_%=:\n"
    "s_mov_b32 s95, 0\n"
    ".L_spinl7_%=:\n"
    "s_sleep 1\n"
    "buffer_load_dwordx2 v[40:41], v18, s[40:43], s91 offen offset:0 sc1\n"
    "s_waitcnt vmcnt(0) lgkmcnt(0)\n"
    "s_add_u32 s95, s95, 1\n"
    "v_xor_b32 v42, s96, v40\n"
    "v_subrev_u32 v43, s94, v41\n"
    "v_lshrrev_b32 v43, 4, v43\n"
    "v_or_b32 v42, v42, v43\n"
    "v_cmp_ne_u32 vcc, 0, v42\n"
    "s_cmp_gt_u32 s95, 400000\n"
    "s_nop 1\n"
    "s_cbranch_scc1 .L_back7_%=\n"
    "s_cbranch_vccnz .L_spinl7_%=\n"
    "s_branch .L_back7_%=\n"
    ".L_spin0_%=:\n"
    "s_mov_b32 s95, 0\n"
    ".L_spinl0_%=:\n"
    "s_sleep 1\n"
    "buffer_load_dwordx2 v[250:251], v15, s[40:43], s24 offen offset:0 sc1\n"
    "s_waitcnt vmcnt(0) lgkmcnt(0)\n"
    "s_add_u32 s95, s95, 1\n"
    "v_xor_b32 v248, s96, v250\n"
    "v_subrev_u32 v249, s94, v251\n"
    "v_lshrrev_b32 v249, 4, v249\n"
    "v_or_b32 v248, v248, v249\n"
    "v_cmp_ne_u32 vcc, 0, v248\n"
    "s_cmp_gt_u32 s95, 400000\n"
    "s_nop 1\n"
    "s_cbranch_scc1 .L_back0_%=\n"
    "s_cbranch_vccnz .L_spinl0_%=\n"
    "s_branch .L_back0_%=\n"
    ".L_spin1_%=:\n"
    "s_mov_b32 s95, 0\n"
    ".L_spinl1_%=:\n"
    "s_sleep 1\n"
    "buffer_load_dwordx2 v[250:251], v15, s[40:43], s24 offen offset:0 sc1\n"
    "s_waitcnt vmcnt(0) lgkmcnt(0)\n"
    "s_add_u32 s95, s95, 1\n"
    "v_xor_b32 v248, s96, v250\n"
    "v_subrev_u32 v249, s94, v251\n"
    "v_lshrrev_b32 v249, 4, v249\n"
    "v_or_b32 v248, v248, v249\n"
    "v_cmp_ne_u32 vcc, 0, v248\n"
    "s_cmp_gt_u32 s95, 400000\n"
    "s_nop 1\n"
    "s_cbranch_scc1 .L_back1_%=\n"
    "s_cbranch_vccnz .L_spinl1_%=\n"
    "s_branch .L_back1_%=\n"
    ".L_spin3_%=:\n"
    "s_mov_b32 s95, 0\n"
    ".L_spinl3_%=:\n"
    "s_sleep 1\n"
    "buffer_load_dwordx2 v[250:251], v15, s[40:43], s24 offen offset:0 sc1\n"
    "s_waitcnt vmcnt(0) lgkmcnt(0)\n"
    "s_add_u32 s95, s95, 1\n"
    "v_xor_b32 v248, s96, v250\n"
    "v_subrev_u32 v249, s94, v251\n"
    "v_lshrrev_b32 v249, 4, v249\n"
    "v_or_b32 v248, v248, v249\n"
    "v_cmp_ne_u32 vcc, 0, v248\n"
    "s_cmp_gt_u32 s95, 400000\n"
    "s_nop 1\n"
    "s_cbranch_scc1 .L_back3_%=\n"
    "s_cbranch_vccnz .L_spinl3_%=\n"
    "s_branch .L_back3_%=\n"
    ".L_spin4_%=:\n"
    "s_mov_b32 s95, 0\n"
    ".L_spinl4_%=:\n"
    "s_sleep 1\n"
    "buffer_load_dwordx2 v[250:251], v15, s[40:43], s24 offen offset:0 sc1\n"
    "s_waitcnt vmcnt(0) lgkmcnt(0)\n"
    "s_add_u32 s95, s95, 1\n"
    "v_xor_b32 v248, s96, v250\n"
    "v_subrev_u32 v249, s94, v251\n"
    "v_lshrrev_b32 v249, 4, v249\n"
    "v_or_b32 v248, v248, v249\n"
    "v_cmp_ne_u32 vcc, 0, v248\n"
    "s_cmp_gt_u32 s95, 400000\n"
    "s_nop 1\n"
    "s_cbranch_scc1 .L_back4_%=\n"
    "s_cbranch_vccnz .L_spinl4_%=\n"
    "s_branch .L_back4_%=\n"
    ".L_spin5_%=:\n"
    "s_mov_b32 s95, 0\n"
    ".L_spinl5_%=:\n"
    "s_sleep 1\n"
    "buffer_load_dwordx2 v[250:251], v15, s[40:43], s29 offen offset:0 sc1\n"
    "s_waitcnt vmcnt(0) lgkmcnt(0)\n"
    "s_add_u32 s95, s95, 1\n"
    "v_xor_b32 v248, s96, v250\n"
    "v_subrev_u32 v249, s94, v251\n"
    "v_lshrrev_b32 v249, 4, v249\n"
    "v_or_b32 v248, v248, v249\n"
    "v_cmp_ne_u32 vcc, 0, v248\n"
    "s_cmp_gt_u32 s95, 400000\n"
    "s_nop 1\n"
    "s_cbranch_scc1 .L_back5_%=\n"
    "s_cbranch_vccnz .L_spinl5_%=\n"
    "s_branch .L_back5_%=\n"
    ".L_spin8_%=:\n"
    "s_mov_b32 s95, 0\n"
    ".L_spinl8_%=:\n"
    "s_sleep 1\n"
    "buffer_load_dwordx2 v[250:251], v15, s[40:43], s29 offen offset:0 sc1\n"
    "s_waitcnt vmcnt(0) lgkmcnt(0)\n"
    "s_add_u32 s95, s95, 1\n"
    "v_xor_b32 v248, s96, v250\n"
    "v_subrev_u32 v249, s94, v251\n"
    "v_lshrrev_b32 v249, 4, v249\n"
    "v_or_b32 v248, v248, v249\n"
    "v_cmp_ne_u32 vcc, 0, v248\n"
    "s_cmp_gt_u32 s95, 400000\n"
    "s_nop 1\n"
    "s_cbranch_scc1 .L_back8_%=\n"
    "s_cbranch_vccnz .L_spinl8_%=\n"
    "s_branch .L_back8_%=\n"
    ".L_spin6_%=:\n"
    "s_mov_b32 s95, 0\n"
    ".L_spinl6_%=:\n"
    "s_sleep 1\n"
    "buffer_load_dwordx2 v[250:251], v15, s[40:43], s29 offen offset:0 sc1\n"
    "s_waitcnt vmcnt(0) lgkmcnt(0)\n"
    "s_add_u32 s95, s95, 1\n"
    "v_xor_b32 v248, s96, v250\n"
    "v_subrev_u32 v249, s94, v251\n"
    "v_lshrrev_b32 v249, 4, v249\n"
    "v_or_b32 v248, v248, v249\n"
    "v_cmp_ne_u32 vcc, 0, v248\n"
    "s_cmp_gt_u32 s95, 400000\n"
    "s_nop 1\n"
    "s_cbranch_scc1 .L_back6_%=\n"
    "s_cbranch_vccnz .L_spinl6_%=\n"
    "s_branch .L_back6_%=\n"
    ".L_end_%=:\n"
    ".L_redo_%=:\n"
    "s_waitcnt vmcnt(0) lgkmcnt(0)\n"
    "s_barrier\n"
    "s_mov_b32 s92, -1\n"
    "s_lshl_b32 s86, s76, 6\n"
    "v_lshrrev_b32 v0, 4, v0\n"
    "v_add_u32 v0, s86, v0\n"
    "s_nop 1\n"
    "s_branch .L_start_%=\n"
    ".L_max_%=:\n"
    "s_waitcnt vmcnt(0) lgkmcnt(0)\n"
    "s_mov_b32 s86, 0\n"
    "v_mov_b32 v16, 0xff800000\n"
    "v_mov_b32 v17, 0xff800000\n"
    "v_mov_b32 v18, 0xff800000\n"
    "v_mov_b32 v19, 0xff800000\n"
    ".L_maxloop_%=:\n"
    "s_barrier\n"
    "s_mov_b32 m0, s83\n"
    "s_add_u32 s88, s82, s86\n"
    "buffer_load_dwordx4 v11, s[56:59], s88 offen offset:0 lds\n"
    "buffer_load_dwordx4 v12, s[56:59], s88 offen offset:1024 lds\n"
    "buffer_load_dwordx4 v13, s[56:59], s88 offen offset:2048 lds\n"
    "s_waitcnt vmcnt(0) lgkmcnt(0)\n"
    "s_barrier\n"
    "ds_read_b128 a[160:163], v14 offset:0\n"
    "ds_read_b128 a[164:167], v15 offset:0\n"
    "ds_read_b128 a[168:171], v14 offset:1024\n"
    "ds_read_b128 a[172:175], v15 offset:1024\n"
    "ds_read_b128 a[176:179], v14 offset:2048\n"
    "ds_read_b128 a[180:183], v15 offset:2048\n"
    "ds_read_b128 a[184:187], v14 offset:6144\n"
    "ds_read_b128 a[188:191], v15 offset:6144\n"
    "ds_read_b128 a[192:195], v14 offset:7168\n"
    "ds_read_b128 a[196:199], v15 offset:7168\n"
    "ds_read_b128 a[200:203], v14 offset:8192\n"
    "ds_read_b128 a[204:207], v15 offset:8192\n"
    "s_waitcnt vmcnt(0) lgkmcnt(0)\n"
    "v_mfma_f32_16x16x32_bf16 v[224:227], a[160:163], a[64:67], 0\n"
    "v_mfma_f32_16x16x32_bf16 v[224:227], a[164:167], a[80:83], v[224:227]\n"
    "v_mfma_f32_16x16x32_bf16 v[224:227], a[168:171], a[96:99], v[224:227]\n"
    "v_mfma_f32_16x16x32_bf16 v[224:227], a[172:175], a[112:115], v[224:227]\n"
    "v_mfma_f32_16x16x32_bf16 v[224:227], a[176:179], a[208:211], v[224:227]\n"
    "v_mfma_f32_16x16x32_bf16 v[224:227], a[180:183], a[224:227], v[224:227]\n"
    "v_mfma_f32_16x16x32_bf16 v[228:231], a[160:163], a[68:71], 0\n"
    "v_mfma_f32_16x16x32_bf16 v[228:231], a[164:167], a[84:87], v[228:231]\n"
    "v_mfma_f32_16x16x32_bf16 v[228:231], a[168:171], a[100:103], v[228:231]\n"
    "v_mfma_f32_16x16x32_bf16 v[228:231], a[172:175], a[116:119], v[228:231]\n"
    "v_mfma_f32_16x16x32_bf16 v[228:231], a[176:179], a[212:215], v[228:231]\n"
    "v_mfma_f32_16x16x32_bf16 v[228:231], a[180:183], a[228:231], v[228:231]\n"
    "v_mfma_f32_16x16x32_bf16 v[232:235], a[160:163], a[72:75], 0\n"
    "v_mfma_f32_16x16x32_bf16 v[232:235], a[164:167], a[88:91], v[232:235]\n"
    "v_mfma_f32_16x16x32_bf16 v[232:235], a[168:171], a[104:107], v[232:235]\n"
    "v_mfma_f32_16x16x32_bf16 v[232:235], a[172:175], a[120:123], v[232:235]\n"
    "v_mfma_f32_16x16x32_bf16 v[232:235], a[176:179], a[216:219], v[232:235]\n"
    "v_mfma_f32_16x16x32_bf16 v[232:235], a[180:183], a[232:235], v[232:235]\n"
    "v_mfma_f32_16x16x32_bf16 v[236:239], a[160:163], a[76:79], 0\n"
    "v_mfma_f32_16x16x32_bf16 v[236:239], a[164:167], a[92:95], v[236:239]\n"
    "v_mfma_f32_16x16x32_bf16 v[236:239], a[168:171], a[108:111], v[236:239]\n"
    "v_mfma_f32_16x16x32_bf16 v[236:239], a[172:175], a[124:127], v[236:239]\n"
    "v_mfma_f32_16x16x32_bf16 v[236:239], a[176:179], a[220:223], v[236:239]\n"
    "v_mfma_f32_16x16x32_bf16 v[236:239], a[180:183], a[236:239], v[236:239]\n"
    "s_nop 7\n"
    "v_max3_f32 v16, v16, v224, v225\n"
    "v_max3_f32 v16, v16, v226, v227\n"
    "v_max3_f32 v17, v17, v228, v229\n"
    "v_max3_f32 v17, v17, v230, v231\n"
    "v_max3_f32 v18, v18, v232, v233\n"
    "v_max3_f32 v18, v18, v234, v235\n"
    "v_max3_f32 v19, v19, v236, v237\n"
    "v_max3_f32 v19, v19, v238, v239\n"
    "s_nop 1\n"
    "v_mfma_f32_16x16x32_bf16 v[224:227], a[184:187], a[64:67], 0\n"
    "v_mfma_f32_16x16x32_bf16 v[224:227], a[188:191], a[80:83], v[224:227]\n"
    "v_mfma_f32_16x16x32_bf16 v[224:227], a[192:195], a[96:99], v[224:227]\n"
    "v_mfma_f32_16x16x32_bf16 v[224:227], a[196:199], a[112:115], v[224:227]\n"
    "v_mfma_f32_16x16x32_bf16 v[224:227], a[200:203], a[208:211], v[224:227]\n"
    "v_mfma_f32_16x16x32_bf16 v[224:227], a[204:207], a[224:227], v[224:227]\n"
    "v_mfma_f32_16x16x32_bf16 v[228:231], a[184:187], a[68:71], 0\n"
    "v_mfma_f32_16x16x32_bf16 v[228:231], a[188:191], a[84:87], v[228:231]\n"
    "v_mfma_f32_16x16x32_bf16 v[228:231], a[192:195], a[100:103], v[228:231]\n"
    "v_mfma_f32_16x16x32_bf16 v[228:231], a[196:199], a[116:119], v[228:231]\n"
    "v_mfma_f32_16x16x32_bf16 v[228:231], a[200:203], a[212:215], v[228:231]\n"
    "v_mfma_f32_16x16x32_bf16 v[228:231], a[204:207], a[228:231], v[228:231]\n"
    "v_mfma_f32_16x16x32_bf16 v[232:235], a[184:187], a[72:75], 0\n"
    "v_mfma_f32_16x16x32_bf16 v[232:235], a[188:191], a[88:91], v[232:235]\n"
    "v_mfma_f32_16x16x32_bf16 v[232:235], a[192:195], a[104:107], v[232:235]\n"
    "v_mfma_f32_16x16x32_bf16 v[232:235], a[196:199], a[120:123], v[232:235]\n"
    "v_mfma_f32_16x16x32_bf16 v[232:235], a[200:203], a[216:219], v[232:235]\n"
    "v_mfma_f32_16x16x32_bf16 v[232:235], a[204:207], a[232:235], v[232:235]\n"
    "v_mfma_f32_16x16x32_bf16 v[236:239], a[184:187], a[76:79], 0\n"
    "v_mfma_f32_16x16x32_bf16 v[236:239], a[188:191], a[92:95], v[236:239]\n"
    "v_mfma_f32_16x16x32_bf16 v[236:239], a[192:195], a[108:111], v[236:239]\n"
    "v_mfma_f32_16x16x32_bf16 v[236:239], a[196:199], a[124:127], v[236:239]\n"
    "v_mfma_f32_16x16x32_bf16 v[236:239], a[200:203], a[220:223], v[236:239]\n"
    "v_mfma_f32_16x16x32_bf16 v[236:239], a[204:207], a[236:239], v[236:239]\n"
    "s_nop 7\n"
    "v_max3_f32 v16, v16, v224, v225\n"
    "v_max3_f32 v16, v16, v226, v227\n"
    "v_max3_f32 v17, v17, v228, v229\n"
    "v_max3_f32 v17, v17, v230, v231\n"
    "v_max3_f32 v18, v18, v232, v233\n"
    "v_max3_f32 v18, v18, v234, v235\n"
    "v_max3_f32 v19, v19, v236, v237\n"
    "v_max3_f32 v19, v19, v238, v239\n"
    "s_nop 1\n"
    "s_add_u32 s86, s86, 12288\n"
    "s_cmp_lt_u32 s86, 196608\n"
    "s_cbranch_scc1 .L_maxloop_%=\n"
    "v_mov_b32 v20, v16\n"
    "s_nop 1\n"
    "v_permlane16_swap_b32 v16, v20\n"
    "s_nop 0\n"
    "v_max_f32 v16, v16, v20\n"
    "v_mov_b32 v20, v16\n"
    "s_nop 1\n"
    "v_permlane32_swap_b32 v16, v20\n"
    "s_nop 0\n"
    "v_max_f32 v16, v16, v20\n"
    "v_xor_b32 v240, 0x80000000, v16\n"
    "v_xor_b32 v241, 0x80000000, v16\n"
    "v_xor_b32 v242, 0x80000000, v16\n"
    "v_xor_b32 v243, 0x80000000, v16\n"
    "v_mov_b32 v20, v17\n"
    "s_nop 1\n"
    "v_permlane16_swap_b32 v17, v20\n"
    "s_nop 0\n"
    "v_max_f32 v17, v17, v20\n"
    "v_mov_b32 v20, v17\n"
    "s_nop 1\n"
    "v_permlane32_swap_b32 v17, v20\n"
    "s_nop 0\n"
    "v_max_f32 v17, v17, v20\n"
    "v_xor_b32 v244, 0x80000000, v17\n"
    "v_xor_b32 v245, 0x80000000, v17\n"
    "v_xor_b32 v246, 0x80000000, v17\n"
    "v_xor_b32 v247, 0x80000000, v17\n"
    "v_mov_b32 v20, v18\n"
    "s_nop 1\n"
    "v_permlane16_swap_b32 v18, v20\n"
    "s_nop 0\n"
    "v_max_f32 v18, v18, v20\n"
    "v_mov_b32 v20, v18\n"
    "s_nop 1\n"
    "v_permlane32_swap_b32 v18, v20\n"
    "s_nop 0\n"
    "v_max_f32 v18, v18, v20\n"
    "v_xor_b32 v248, 0x80000000, v18\n"
    "v_xor_b32 v249, 0x80000000, v18\n"
    "v_xor_b32 v250, 0x80000000, v18\n"
    "v_xor_b32 v251, 0x80000000, v18\n"
    "v_mov_b32 v20, v19\n"
    "s_nop 1\n"
    "v_permlane16_swap_b32 v19, v20\n"
    "s_nop 0\n"
    "v_max_f32 v19, v19, v20\n"
    "v_mov_b32 v20, v19\n"
    "s_nop 1\n"
    "v_permlane32_swap_b32 v19, v20\n"
    "s_nop 0\n"
    "v_max_f32 v19, v19, v20\n"
    "v_xor_b32 v252, 0x80000000, v19\n"
    "v_xor_b32 v253, 0x80000000, v19\n"
    "v_xor_b32 v254, 0x80000000, v19\n"
    "v_xor_b32 v255, 0x80000000, v19\n"
    "s_barrier\n"
    "s_add_u32 m0, s83, 0\n"
    "s_add_u32 s88, s82, 0\n"
    "buffer_load_dwordx4 v11, s[56:59], s88 offen offset:0 lds\n"
    "buffer_load_dwordx4 v12, s[56:59], s88 offen offset:1024 lds\n"
    "buffer_load_dwordx4 v13, s[56:59], s88 offen offset:2048 lds\n"
    "s_add_u32 m0, s83, 12288\n"
    "s_add_u32 s88, s82, 12288\n"
    "buffer_load_dwordx4 v11, s[56:59], s88 offen offset:0 lds\n"
    "buffer_load_dwordx4 v12, s[56:59], s88 offen offset:1024 lds\n"
    "buffer_load_dwordx4 v13, s[56:59], s88 offen offset:2048 lds\n"
    "s_add_u32 m0, s83, 24576\n"
    "s_add_u32 s88, s82, 24576\n"
    "buffer_load_dwordx4 v11, s[56:59], s88 offen offset:0 lds\n"
    "buffer_load_dwordx4 v12, s[56:59], s88 offen offset:1024 lds\n"
    "buffer_load_dwordx4 v13, s[56:59], s88 offen offset:2048 lds\n"
    "s_add_u32 m0, s83, 36864\n"
    "s_add_u32 s88, s82, 36864\n"
    "buffer_load_dwordx4 v11, s[56:59], s88 offen offset:0 lds\n"
    "buffer_load_dwordx4 v12, s[56:59], s88 offen offset:1024 lds\n"
    "buffer_load_dwordx4 v13, s[56:59], s88 offen offset:2048 lds\n"
    "s_waitcnt vmcnt(0) lgkmcnt(0)\n"
    "s_barrier\n"
    "ds_read_b128 a[160:163], v14 offset:0\n"
    "ds_read_b128 a[164:167], v15 offset:0\n"
    "ds_read_b128 a[168:171], v14 offset:1024\n"
    "ds_read_b128 a[172:175], v15 offset:1024\n"
    "ds_read_b128 a[176:179], v14 offset:2048\n"
    "ds_read_b128 a[180:183], v15 offset:2048\n"
    "s_waitcnt vmcnt(0) lgkmcnt(0)\n"
    "s_branch .L_sgo_%=\n"
    : "+v"(tid)
    : [ka] "s"(__builtin_amdgcn_kernarg_segment_ptr()), [bid] "s"(blockIdx.x)
    :
    "v1","v2","v3","v4","v5","v6","v7","v8","v9","v10","v11","v12","v13","v14","v15","v16","v17","v18","v19","v20","v21","v22","v23","v24",
    "v25","v26","v27","v28","v29","v30","v31","v32","v33","v34","v35","v36","v37","v38","v39","v40","v41","v42","v43","v44","v45","v46","v47","v48",
    "v49","v50","v51","v52","v53","v54","v55","v56","v57","v58","v59","v60","v61","v62","v63","v64","v65","v66","v67","v68","v69","v70","v71","v72",
    "v73","v74","v75","v76","v77","v78","v79","v80","v81","v82","v83","v84","v85","v86","v87","v88","v89","v90","v91","v92","v93","v94","v95","v96",
    "v97","v98","v99","v100","v101","v102","v103","v104","v105","v106","v107","v108","v109","v110","v111","v112","v113","v114","v115","v116","v117","v118","v119","v120",
    "v121","v122","v123","v124","v125","v126","v127","v128","v129","v130","v131","v132","v133","v134","v135","v136","v137","v138","v139","v140","v141","v142","v143","v144",
    "v145","v146","v147","v148","v149","v150","v151","v152","v153","v154","v155","v156","v157","v158","v159","v160","v161","v162","v163","v164","v165","v166","v167","v168",
    "v169","v170","v171","v172","v173","v174","v175","v176","v177","v178","v179","v180","v181","v182","v183","v184","v185","v186","v187","v188","v189","v190","v191","v192",
    "v193","v194","v195","v196","v197","v198","v199","v200","v201","v202","v203","v204","v205","v206","v207","v208","v209","v210","v211","v212","v213","v214","v215","v216",
    "v217","v218","v219","v220","v221","v222","v223","v224","v225","v226","v227","v228","v229","v230","v231","v232","v233","v234","v235","v236","v237","v238","v239","v240",
    "v241","v242","v243","v244","v245","v246","v247","v248","v249","v250","v251","v252","v253","v254","v255","a0","a1","a2","a3","a4","a5","a6","a7","a8",
    "a9","a10","a11","a12","a13","a14","a15","a16","a17","a18","a19","a20","a21","a22","a23","a24","a25","a26","a27","a28","a29","a30","a31","a32",
    "a33","a34","a35","a36","a37","a38","a39","a40","a41","a42","a43","a44","a45","a46","a47","a48","a49","a50","a51","a52","a53","a54","a55","a56",
    "a57","a58","a59","a60","a61","a62","a63","a64","a65","a66","a67","a68","a69","a70","a71","a72","a73","a74","a75","a76","a77","a78","a79","a80",
    "a81","a82","a83","a84","a85","a86","a87","a88","a89","a90","a91","a92","a93","a94","a95","a96","a97","a98","a99","a100","a101","a102","a103","a104",
    "a105","a106","a107","a108","a109","a110","a111","a112","a113","a114","a115","a116","a117","a118","a119","a120","a121","a122","a123","a124","a125","a126","a127","a128",
    "a129","a130","a131","a132","a133","a134","a135","a136","a137","a138","a139","a140","a141","a142","a143","a144","a145","a146","a147","a148","a149","a150","a151","a152",
    "a153","a154","a155","a156","a157","a158","a159","a160","a161","a162","a163","a164","a165","a166","a167","a168","a169","a170","a171","a172","a173","a174","a175","a176",
    "a177","a178","a179","a180","a181","a182","a183","a184","a185","a186","a187","a188","a189","a190","a191","a192","a193","a194","a195","a196","a197","a198","a199","a200",
    "a201","a202","a203","a204","a205","a206","a207","a208","a209","a210","a211","a212","a213","a214","a215","a216","a217","a218","a219","a220","a221","a222","a223","a224",
    "a225","a226","a227","a228","a229","a230","a231","a232","a233","a234","a235","a236","a237","a238","a239","a240","a241","a242","a243","a244","a245","a246","a247","a248",
    "a249","a250","a251","a252","a253","a254","a255","s3","s4","s5","s6","s7","s8","s9","s10","s11","s12","s13","s14","s15","s16","s17","s18","s19",
    "s20","s21","s22","s23","s24","s25","s26","s27","s28","s29","s30","s31","s33","s34","s35","s36","s37","s38","s39","s40","s41","s42","s43","s44",
    "s45","s46","s47","s48","s49","s50","s51","s52","s53","s54","s55","s56","s57","s58","s59","s60","s61","s62","s63","s64","s65","s66","s67","s68",
    "s69","s70","s71","s72","s73","s74","s75","s76","s77","s78","s79","s80","s81","s82","s83","s84","s85","s86","s87","s88","s89","s90","s91","s92",
    "s93","s94","s95","s96","s97","s98","s99","vcc","scc","memory"
  );
}

extern "C" void kernel_launch(void* const* d_in, const int* in_sizes, int n_in, void* d_out, int out_size, void* d_ws, size_t ws_size,
                              hipStream_t stream) {
  if (n_in != 9 || out_size != 2048 * 640 || ws_size < WS_USED) return;
  km_main<<<256, 256, 0, stream>>>((char*)d_ws, (const u16*)d_in[3], (const u16*)d_in[6], (const u16*)d_in[7], (const u16*)d_in[1],
                                   (const u16*)d_in[2], (const u16*)d_in[4], (const u16*)d_in[8], (const u16*)d_in[0], (u16*)d_out);
}
```
